```python
import torch

def _softmax(x, axis=-1):
    return torch.softmax(x, dim=axis)
def _gelu(x):
    return torch.nn.functional.gelu(x, approximate="tanh")
def _silu(x):
    return torch.nn.functional.silu(x)
def _sigmoid(x):
    return torch.sigmoid(x)
def _layernorm(x, axis=-1, eps=1e-5):
    m = x.mean(dim=axis, keepdim=True)
    v = x.var(dim=axis, keepdim=True, unbiased=False)
    return (x - m) / torch.sqrt(v + eps)
def _scatter_add(tgt, idx, src, axis=0):
    return tgt.clone().index_add_(axis, idx, src)
def _segment_sum(src, seg, n):
    out = torch.zeros((n,) + src.shape[1:], dtype=src.dtype)
    return out.index_add_(0, seg, src)
def _pad(x, pw):
    flat = tuple(v for pair in reversed(pw) for v in pair)
    return torch.nn.functional.pad(x, flat)

kernel_name = 'recurrent_scan__compressed_kv_cla__mixer_s_mlp__prompt'

def setup_inputs():

    _w = (lambda a: (lambda t: t.to(torch.bfloat16) if t.is_floating_point() else t)(torch.as_tensor(a)))
    def _A_setup():
        B, H, Sq, Sk, D, M = 1, 32, 2048, 2048, 128, 640
        Dc = 4 * D
        return {
            "x": _w((lambda *s: torch.randn(*s, dtype=torch.bfloat16) * 0.05)(B, Sq, M)),
            "c_kv": _w((lambda *s: torch.randn(*s, dtype=torch.bfloat16) * 0.2)(B, Sk, Dc)),
            "w_q": _w((lambda *s: torch.randn(*s, dtype=torch.bfloat16) * 3.2)(M, H, D) * (M ** -0.5)),
            "w_uk": _w((lambda *s: torch.randn(*s, dtype=torch.bfloat16) * 3.2)(Dc, H, D) * (Dc ** -0.5)),
            "w_uv": _w((lambda *s: torch.randn(*s, dtype=torch.bfloat16) * 0.2)(Dc, H, D) * (Dc ** -0.5)),
            "w_o": _w((lambda *s: torch.randn(*s, dtype=torch.bfloat16) * 0.2)(H, D, M) * ((H * D) ** -0.5)),
        }
    _A_in = _A_setup()

    def _B_setup():
        B, S, D, F = 1, 2048, 640, 1728
        return {
            "x":  _w((lambda *s: torch.randn(*s, dtype=torch.bfloat16) * 0.05)(B, S, D)),
            "w1": _w((lambda *s: torch.randn(*s, dtype=torch.bfloat16) * 3.2)(D, D) * (D ** -0.5)),
            "w2": _w((lambda *s: torch.randn(*s, dtype=torch.bfloat16) * 3.2)(D, D) * (D ** -0.5)),
            "w3": _w((lambda *s: torch.randn(*s, dtype=torch.bfloat16) * 3.2)(D, F) * (D ** -0.5)),
            "w4": _w((lambda *s: torch.randn(*s, dtype=torch.bfloat16) * 1.6)(F, D) * (F ** -0.5)),
        }
    _B_in = _B_setup()

    return {
        "A_x": _A_in["x"],
        "A_c_kv": _A_in["c_kv"],
        "A_w_q": _A_in["w_q"],
        "A_w_uk": _A_in["w_uk"],
        "A_w_uv": _A_in["w_uv"],
        "A_w_o": _A_in["w_o"],
        "B_x": _B_in["x"],
        "B_w1": _B_in["w1"],
        "B_w2": _B_in["w2"],
        "B_w3": _B_in["w3"],
        "B_w4": _B_in["w4"],
    }

def reference(A_x, A_c_kv, A_w_q, A_w_uk, A_w_uv, A_w_o, B_x, B_w1, B_w2, B_w3, B_w4):
    def _A_ref(x, c_kv, w_q, w_uk, w_uv, w_o):
        B, Sq, M = x.shape[0], x.shape[1], x.shape[2]
        Sk = c_kv.shape[1]
        H, D = w_q.shape[1], w_q.shape[2]
        q = torch.einsum("bqm,mhd->bhqd", x, w_q)
        k = torch.einsum("bkc,chd->bhkd", c_kv, w_uk)
        v = torch.einsum("bkc,chd->bhkd", c_kv, w_uv)
        scores = torch.einsum("bhqd,bhkd->bhqk", q, k) * (D ** -0.5)
        off = Sk - Sq
        qi = torch.arange(Sq)[:, None]
        kj = torch.arange(Sk)[None, :]
        cmask = kj <= (qi + off)
        scores = torch.where(cmask[None, None], scores, -1e9)
        p = _softmax(scores, axis=-1)
        ctx = torch.einsum("bhqk,bhkd->bhqd", p, v)
        return torch.einsum("bhqd,hdm->bqm", ctx, w_o)

    def _B_ref(x, w1, w2, w3, w4):
        B, S, D = x.shape
        h = _gelu(_layernorm(x, axis=-1) @ w1)
        c = D // 4
        g0 = _pad(h[:, :, 0*c:1*c], ((0, 0), (1, 0), (0, 0)))[:, :S, :]
        g1 = _pad(h[:, :, 1*c:2*c], ((0, 0), (0, 1), (0, 0)))[:, 1:, :]
        g2 = _pad(h[:, :, 2*c:3*c], ((0, 0), (2, 0), (0, 0)))[:, :S, :]
        g3 = _pad(h[:, :, 3*c:   ], ((0, 0), (0, 2), (0, 0)))[:, 2:, :]
        hs = torch.concatenate([g0, g1, g2, g3], axis=-1) @ w2
        x1 = x + hs
        h2 = _gelu(_layernorm(x1, axis=-1) @ w3) @ w4
        return x1 + h2

    x = A_x
    xn = _layernorm(x, axis=-1)
    s = _A_ref(xn, A_c_kv, A_w_q, A_w_uk, A_w_uv, A_w_o)
    g = _silu(_B_ref(xn, B_w1, B_w2, B_w3, B_w4))
    return x + s * g
```

```cpp
#include <hip/hip_runtime.h>
#include <stdint.h>

struct Params { const void* p[12]; };

__global__ __launch_bounds__(256, 1) void fused_kernel(Params prm) {
  __shared__ __attribute__((aligned(1024))) char lds[163840];
  const unsigned wave = __builtin_amdgcn_readfirstlane(threadIdx.x >> 6);
  const unsigned ldsbase = (unsigned)(uintptr_t)lds;
  asm volatile(
".macro MF a:vararg\nv_mfma_f32_16x16x32_bf16 \\a\n.endm\n"
".macro TR a:vararg\nds_read_b64_tr_b16 \\a\n.endm\n"
".macro RD a:vararg\nds_read_b128 \\a\n.endm\n"
".macro LD a:vararg\nbuffer_load_dwordx4 \\a\n.endm\n"
".macro ST a:vararg\nbuffer_store_dwordx4 \\a\n.endm\n"
".macro CV a:vararg\nv_cvt_pk_bf16_f32 \\a\n.endm\n"
".macro WT a:vararg\ns_waitcnt \\a\n.endm\n"
".macro EX a:vararg\nv_exp_f32 \\a\n.endm\n"
".macro AW a:vararg\nv_accvgpr_write_b32 \\a\n.endm\n"
".macro AR a:vararg\nv_accvgpr_read_b32 \\a\n.endm\n"
"s_load_dwordx8 s[8:15], %0, 0\n"
"s_load_dwordx8 s[16:23], %0, 32\n"
"s_load_dwordx8 s[24:31], %0, 64\n"
"s_mov_b32 s33, %1\n"
"s_mov_b32 s34, %2\n"
"v_mbcnt_lo_u32_b32 v0, -1, 0\n"
"v_mbcnt_hi_u32_b32 v0, -1, v0\n"
"v_lshlrev_b32 v1, 4, v0\n"
"s_and_b32 s72, s33, 7\n"
"s_lshr_b32 s37, s33, 3\n"
"s_and_b32 s58, s37, 3\n"
"s_lshl_b32 s56, s72, 2\n"
"s_add_u32 s35, s56, s58\n"
"s_lshr_b32 s36, s37, 2\n"
"s_getpc_b64 s[56:57]\n"
"L_pc_1:\n"
"s_and_b32 s57, s57, 0xffff\n"
"s_mov_b32 s58, L_codeend-L_pc_1\n"
"s_mov_b32 s59, 0x20000\n"
"v_and_b32 v23, 7, v0\n"
"v_lshlrev_b32 v23, 7, v23\n"
"s_sub_u32 s73, s37, 0\n"
"s_lshl_b32 s73, s73, 2\n"
"s_add_u32 s73, s73, s34\n"
"s_lshl_b32 s73, s73, 10\n"
"buffer_load_dword v31, v23, s[56:59], s73 offen\n"
"v_lshrrev_b32 v21, 3, v0\n"
"v_and_b32 v22, 7, v0\n"
"v_lshrrev_b32 v23, 1, v21\n"
"v_xor_b32 v24, v22, v23\n"
"v_lshlrev_b32 v24, 4, v24\n"
"v_lshl_add_u32 v2, v21, 10, v24\n"
"v_xor_b32 v3, 64, v2\n"
"v_add_u32 v3, 7168, v3\n"
"v_add_u32 v4, 14336, v2\n"
"v_add_u32 v5, 14336, v3\n"
"v_lshlrev_b32 v23, 1, v23\n"
"v_xor_b32 v24, v22, v23\n"
"v_lshlrev_b32 v24, 4, v24\n"
"v_lshrrev_b32 v25, 2, v21\n"
"v_lshlrev_b32 v25, 3, v25\n"
"v_and_b32 v26, 3, v21\n"
"v_add_u32 v25, v25, v26\n"
"v_lshl_add_u32 v6, v25, 13, v24\n"
"v_add_u32 v7, 130048, v6\n"
"v_add_u32 v8, 30720, v6\n"
"v_add_u32 v9, 160768, v6\n"
"s_mov_b32 s64, s35\n"
"s_mov_b32 s81, s36\n"
"s_lshl_b32 s60, s81, 18\n"
"s_lshl_b32 s56, s34, 16\n"
"s_add_u32 s60, s60, s56\n"
"s_mul_i32 s62, s34, 20480\n"
"s_lshr_b32 s56, s34, 1\n"
"s_and_b32 s57, s34, 1\n"
"s_mul_i32 s65, s56, 262144\n"
"s_lshl_b32 s58, s57, 7\n"
"s_add_u32 s65, s65, s58\n"
"s_lshl_b32 s58, s64, 8\n"
"s_add_u32 s65, s65, s58\n"
"s_lshl_b32 s66, s34, 12\n"
"s_mov_b32 s67, s65\n"
"WT lgkmcnt(0)\n"
"s_mov_b32 s44, s10\n"
"s_and_b32 s45, s11, 0xffff\n"
"s_mov_b32 s46, 0x200000\n"
"s_mov_b32 s47, 0x20000\n"
"s_mov_b32 s48, s14\n"
"s_and_b32 s49, s15, 0xffff\n"
"s_mov_b32 s50, 0x400000\n"
"s_mov_b32 s51, 0x20000\n"
"s_mov_b32 s40, s30\n"
"s_and_b32 s41, s31, 0xffff\n"
"s_mov_b32 s42, 0x5c00000\n"
"s_mov_b32 s43, 0x20000\n"
"v_mov_b32 v21, 0\n"
"buffer_load_dwordx2 v[18:19], v21, s[40:43], 0 offen sc1\n"
"s_cmp_ge_u32 s33, 96\n"
"s_cbranch_scc1 L_nox_5\n"
"s_mov_b32 s52, s8\n"
"s_and_b32 s53, s9, 0xffff\n"
"s_mov_b32 s54, 0x280000\n"
"s_mov_b32 s55, 0x20000\n"
"s_mul_i32 s57, s33, 20480\n"
"s_mul_i32 s56, s34, 320\n"
"s_add_u32 s58, s57, s56\n"
"s_mul_i32 s59, s34, 5120\n"
"s_add_u32 s59, s59, 114688\n"
"s_movk_i32 s56, 3277\n"
"s_movk_i32 s57, 960\n"
"v_add_u32 v22, 0, v0\n"
"v_mul_u32_u24 v23, s56, v22\n"
"v_lshrrev_b32 v23, 16, v23\n"
"v_lshlrev_b32 v22, 4, v22\n"
"v_mad_u32_u24 v112, v23, s57, v22\n"
"v_add_u32 v22, 64, v0\n"
"v_mul_u32_u24 v23, s56, v22\n"
"v_lshrrev_b32 v23, 16, v23\n"
"v_lshlrev_b32 v22, 4, v22\n"
"v_mad_u32_u24 v113, v23, s57, v22\n"
"v_add_u32 v22, 128, v0\n"
"v_mul_u32_u24 v23, s56, v22\n"
"v_lshrrev_b32 v23, 16, v23\n"
"v_lshlrev_b32 v22, 4, v22\n"
"v_mad_u32_u24 v114, v23, s57, v22\n"
"v_add_u32 v22, 192, v0\n"
"v_mul_u32_u24 v23, s56, v22\n"
"v_lshrrev_b32 v23, 16, v23\n"
"v_lshlrev_b32 v22, 4, v22\n"
"v_mad_u32_u24 v115, v23, s57, v22\n"
"v_add_u32 v22, 256, v0\n"
"v_mul_u32_u24 v23, s56, v22\n"
"v_lshrrev_b32 v23, 16, v23\n"
"v_lshlrev_b32 v22, 4, v22\n"
"v_mad_u32_u24 v116, v23, s57, v22\n"
"s_add_u32 m0, s59, 0\n"
"s_nop 0\n"
"LD v112,s[52:55],s58 offen lds\n"
"s_add_u32 m0, s59, 1024\n"
"s_nop 0\n"
"LD v113,s[52:55],s58 offen lds\n"
"s_add_u32 m0, s59, 2048\n"
"s_nop 0\n"
"LD v114,s[52:55],s58 offen lds\n"
"s_add_u32 m0, s59, 3072\n"
"s_nop 0\n"
"LD v115,s[52:55],s58 offen lds\n"
"s_add_u32 m0, s59, 4096\n"
"s_nop 0\n"
"LD v116,s[52:55],s58 offen lds\n"
"s_cmp_lt_u32 s33, 64\n"
"s_cbranch_scc1 L_csl_6\n"
"s_add_u32 s58, s58, 655360\n"
"s_add_u32 m0, s59, 20480\n"
"s_nop 0\n"
"LD v112,s[52:55],s58 offen lds\n"
"s_add_u32 m0, s59, 21504\n"
"s_nop 0\n"
"LD v113,s[52:55],s58 offen lds\n"
"s_add_u32 m0, s59, 22528\n"
"s_nop 0\n"
"LD v114,s[52:55],s58 offen lds\n"
"s_add_u32 m0, s59, 23552\n"
"s_nop 0\n"
"LD v115,s[52:55],s58 offen lds\n"
"s_add_u32 m0, s59, 24576\n"
"s_nop 0\n"
"LD v116,s[52:55],s58 offen lds\n"
"s_branch L_nox_5\n"
"L_csl_6:\n"
"s_cmp_ge_u32 s33, 59\n"
"s_cbranch_scc1 L_nox_5\n"
"s_cmp_lt_u32 s33, 32\n"
"s_cbranch_scc1 L_nox_5\n"
"s_mov_b32 s52, s24\n"
"s_and_b32 s53, s25, 0xffff\n"
"s_mov_b32 s54, 0x21c000\n"
"s_mov_b32 s55, 0x20000\n"
"v_lshrrev_b32 v22, 3, v0\n"
"v_mul_u32_u24 v22, 3456, v22\n"
"v_and_b32 v23, 7, v0\n"
"v_lshl_add_u32 v22, v23, 4, v22\n"
"s_sub_u32 s58, s33, 32\n"
"s_lshl_b32 s58, s58, 7\n"
"s_mul_i32 s56, s34, 552960\n"
"s_add_u32 s58, s58, s56\n"
"LD v[140:143],v22,s[52:55],s58 offen\n"
"s_add_u32 s58, s58, 27648\n"
"LD v[144:147],v22,s[52:55],s58 offen\n"
"s_add_u32 s58, s58, 27648\n"
"LD v[148:151],v22,s[52:55],s58 offen\n"
"s_add_u32 s58, s58, 27648\n"
"LD v[152:155],v22,s[52:55],s58 offen\n"
"s_add_u32 s58, s58, 27648\n"
"LD v[156:159],v22,s[52:55],s58 offen\n"
"s_add_u32 s58, s58, 27648\n"
"LD v[160:163],v22,s[52:55],s58 offen\n"
"s_add_u32 s58, s58, 27648\n"
"LD v[164:167],v22,s[52:55],s58 offen\n"
"s_add_u32 s58, s58, 27648\n"
"LD v[168:171],v22,s[52:55],s58 offen\n"
"s_add_u32 s58, s58, 27648\n"
"LD v[172:175],v22,s[52:55],s58 offen\n"
"s_add_u32 s58, s58, 27648\n"
"LD v[176:179],v22,s[52:55],s58 offen\n"
"s_add_u32 s58, s58, 27648\n"
"LD v[180:183],v22,s[52:55],s58 offen\n"
"s_add_u32 s58, s58, 27648\n"
"LD v[184:187],v22,s[52:55],s58 offen\n"
"s_add_u32 s58, s58, 27648\n"
"LD v[188:191],v22,s[52:55],s58 offen\n"
"s_add_u32 s58, s58, 27648\n"
"LD v[192:195],v22,s[52:55],s58 offen\n"
"s_add_u32 s58, s58, 27648\n"
"LD v[196:199],v22,s[52:55],s58 offen\n"
"s_add_u32 s58, s58, 27648\n"
"LD v[200:203],v22,s[52:55],s58 offen\n"
"s_add_u32 s58, s58, 27648\n"
"LD v[204:207],v22,s[52:55],s58 offen\n"
"s_add_u32 s58, s58, 27648\n"
"LD v[208:211],v22,s[52:55],s58 offen\n"
"s_add_u32 s58, s58, 27648\n"
"LD v[212:215],v22,s[52:55],s58 offen\n"
"s_add_u32 s58, s58, 27648\n"
"LD v[216:219],v22,s[52:55],s58 offen\n"
"s_add_u32 s58, s58, 27648\n"
"L_nox_5:\n"
"s_mov_b32 s65, s67\n"
"s_add_u32 m0, s66, 81920\n"
"s_nop 0\n"
"LD v6,s[48:51],s65 offen offset:0 lds\n"
"LD v7,s[48:51],s65 offen offset:1024 lds\n"
"LD v8,s[48:51],s65 offen offset:2048 lds\n"
"LD v9,s[48:51],s65 offen offset:3072 lds\n"
"s_add_u32 s61, s60, 0\n"
"s_add_u32 m0, s62, 0\n"
"s_nop 0\n"
"LD v2,s[44:47],s61 offen offset:0 lds\n"
"LD v3,s[44:47],s61 offen offset:1024 lds\n"
"LD v4,s[44:47],s61 offen offset:2048 lds\n"
"LD v5,s[44:47],s61 offen offset:3072 lds\n"
"s_add_u32 s61, s60, 32768\n"
"s_add_u32 m0, s62, 4096\n"
"s_nop 0\n"
"LD v2,s[44:47],s61 offen offset:0 lds\n"
"LD v3,s[44:47],s61 offen offset:1024 lds\n"
"LD v4,s[44:47],s61 offen offset:2048 lds\n"
"LD v5,s[44:47],s61 offen offset:3072 lds\n"
"s_cmp_lt_u32 s33, 96\n"
"s_cbranch_scc1 L_p0_2\n"
"L_p0back_3:\n"
"s_add_u32 s61, s60, 128\n"
"s_add_u32 m0, s62, 8192\n"
"s_nop 0\n"
"LD v2,s[44:47],s61 offen offset:0 lds\n"
"LD v3,s[44:47],s61 offen offset:1024 lds\n"
"LD v4,s[44:47],s61 offen offset:2048 lds\n"
"LD v5,s[44:47],s61 offen offset:3072 lds\n"
"s_add_u32 s61, s60, 32896\n"
"s_add_u32 m0, s62, 12288\n"
"s_nop 0\n"
"LD v2,s[44:47],s61 offen offset:0 lds\n"
"LD v3,s[44:47],s61 offen offset:1024 lds\n"
"LD v4,s[44:47],s61 offen offset:2048 lds\n"
"LD v5,s[44:47],s61 offen offset:3072 lds\n"
"s_add_u32 s65, s65, 524288\n"
"s_add_u32 m0, s66, 98304\n"
"s_nop 0\n"
"LD v6,s[48:51],s65 offen offset:0 lds\n"
"LD v7,s[48:51],s65 offen offset:1024 lds\n"
"LD v8,s[48:51],s65 offen offset:2048 lds\n"
"LD v9,s[48:51],s65 offen offset:3072 lds\n"
"s_add_u32 s61, s60, 256\n"
"s_add_u32 m0, s62, 16384\n"
"s_nop 0\n"
"LD v2,s[44:47],s61 offen offset:0 lds\n"
"LD v3,s[44:47],s61 offen offset:1024 lds\n"
"LD v4,s[44:47],s61 offen offset:2048 lds\n"
"LD v5,s[44:47],s61 offen offset:3072 lds\n"
"s_mov_b32 s56, s10\n"
"s_and_b32 s57, s11, 0xffff\n"
"s_mov_b32 s58, 0x200000\n"
"s_mov_b32 s59, 0x20000\n"
"v_min_u32 v23, 15, v0\n"
"v_lshlrev_b32 v23, 7, v23\n"
"s_lshl_b32 s73, s33, 2\n"
"s_add_u32 s73, s73, s34\n"
"s_mul_i32 s73, s73, 2048\n"
"buffer_load_dword a255, v23, s[56:59], s73 offen\n"
"s_mov_b32 s56, s14\n"
"s_and_b32 s57, s15, 0xffff\n"
"s_mov_b32 s58, 0x400000\n"
"s_mov_b32 s59, 0x20000\n"
"v_lshrrev_b32 v23, 3, v0\n"
"v_min_u32 v23, 3, v23\n"
"v_lshlrev_b32 v23, 13, v23\n"
"v_and_b32 v22, 7, v0\n"
"v_lshl_add_u32 v23, v22, 7, v23\n"
"s_lshl_b32 s73, s37, 2\n"
"s_add_u32 s73, s73, s34\n"
"s_mul_i32 s73, s73, 32\n"
"s_add_u32 s73, s73, s72\n"
"s_lshl_b32 s73, s73, 10\n"
"buffer_load_dword a255, v23, s[56:59], s73 offen\n"
"s_lshl_b32 s73, s33, 2\n"
"s_add_u32 s73, s73, s34\n"
"s_mul_i32 s56, s73, 2560\n"
"s_add_u32 s56, s56, 0x700000\n"
"v_min_u32 v23, 19, v0\n"
"v_lshlrev_b32 v23, 7, v23\n"
"v_add_u32 v23, s56, v23\n"
"s_lshl_b32 s56, s33, 7\n"
"s_add_u32 s56, s56, 0xfff40000\n"
"v_lshlrev_b32 v22, 15, v0\n"
"v_add_u32 v22, s56, v22\n"
"v_cmp_lt_u32 vcc, 31, v0\n"
"s_nop 1\n"
"v_cndmask_b32 v23, v23, v22, vcc\n"
"s_lshl_b32 s56, s73, 7\n"
"s_add_u32 s56, s56, 0xffbe0000\n"
"v_min_u32 v22, 39, v0\n"
"v_lshlrev_b32 v22, 17, v22\n"
"v_add_u32 v22, s56, v22\n"
"v_cmp_lt_u32 vcc, 36, v0\n"
"s_nop 1\n"
"v_cndmask_b32 v23, v23, v22, vcc\n"
"buffer_load_dword a255, v23, s[40:43], 0 offen sc1\n"
"v_and_b32 v21, 15, v0\n"
"v_lshrrev_b32 v22, 4, v0\n"
"v_lshrrev_b32 v23, 1, v21\n"
"v_xor_b32 v23, v22, v23\n"
"v_lshlrev_b32 v23, 4, v23\n"
"v_lshl_add_u32 v10, v21, 7, v23\n"
"v_add_u32 v10, s62, v10\n"
"v_xor_b32 v11, 64, v10\n"
"s_sub_u32 s59, s37, 12\n"
"s_max_i32 s59, s59, 0\n"
"s_cmp_ge_u32 s59, 10\n"
"s_cselect_b32 s57, 1, 0\n"
"s_cselect_b32 s58, 10, 0\n"
"s_sub_u32 s71, s59, s58\n"
"s_lshl_b32 s70, s72, 1\n"
"s_add_u32 s70, s70, s57\n"
"s_cmp_ge_u32 s33, 96\n"
"s_cselect_b32 s69, 0xc8000, 0\n"
"s_cselect_b32 s78, 0x5c00000, 0\n"
"s_mov_b32 s76, s40\n"
"s_mov_b32 s77, s41\n"
"s_mov_b32 s79, 0x20000\n"
"s_lshl_b32 s56, s36, 3\n"
"s_sub_u32 s57, 120, s56\n"
"s_lshl_b32 s58, s70, 3\n"
"v_mov_b32 v22, s56\n"
"v_mov_b32 v23, s57\n"
"v_cmp_gt_u32 vcc, 8, v0\n"
"s_nop 1\n"
"v_cndmask_b32 v22, v23, v22, vcc\n"
"v_mov_b32 v23, s58\n"
"v_cmp_gt_u32 vcc, 16, v0\n"
"s_nop 1\n"
"v_cndmask_b32 v22, v23, v22, vcc\n"
"v_and_b32 v21, 7, v0\n"
"v_add_u32 v21, v21, v22\n"
"v_subrev_u32 v22, 32, v21\n"
"v_cmp_gt_u32 vcc, 96, v21\n"
"s_nop 1\n"
"v_cndmask_b32 v21, v22, v21, vcc\n"
"v_lshlrev_b32 v21, 7, v21\n"
"v_add_u32 v20, 0x40000, v21\n"
"s_mov_b32 s74, 0xbdd2d3e8\n"
"s_mov_b32 s75, 0xc0135761\n"
"s_mul_i32 s56, s64, 1092352\n"
"s_lshl_b32 s57, s81, 2\n"
"s_add_u32 s57, s57, s34\n"
"s_mul_i32 s57, s57, 34048\n"
"s_add_u32 s56, s56, s57\n"
"s_add_u32 s80, s56, 0x1c00000\n"
"WT vmcnt(31)\n"
"v_lshrrev_b32 v21, 2, v0\n"
"v_and_b32 v21, 7, v21\n"
"v_and_b32 v22, 3, v0\n"
"v_lshlrev_b32 v22, 3, v22\n"
"v_lshl_add_u32 v22, v21, 7, v22\n"
"v_lshrrev_b32 v23, 5, v0\n"
"v_lshl_add_u32 v22, v23, 10, v22\n"
"v_add_u32 v22, 0, v22\n"
"v_lshrrev_b32 v21, 1, v21\n"
"v_lshlrev_b32 v21, 5, v21\n"
"v_xor_b32 v23, 0, v21\n"
"v_add_u32 v12, v22, v23\n"
"v_xor_b32 v23, 32, v21\n"
"v_add_u32 v13, v22, v23\n"
"v_xor_b32 v23, 64, v21\n"
"v_add_u32 v14, v22, v23\n"
"v_xor_b32 v23, 96, v21\n"
"v_add_u32 v15, v22, v23\n"
"v_add_u32 v24, 0x10000, v12\n"
"v_add_u32 v28, 0x20000, v12\n"
"v_add_u32 v25, 0x10000, v13\n"
"v_add_u32 v29, 0x20000, v13\n"
"v_add_u32 v26, 0x10000, v14\n"
"v_add_u32 v30, 0x20000, v14\n"
"v_add_u32 v27, 0x10000, v15\n"
"v_add_u32 v31, 0x20000, v15\n"
"v_readfirstlane_b32 s38, v18\n"
"v_readfirstlane_b32 s39, v19\n"
"s_nop 0\n"
"s_add_u32 s38, s38, 1\n"
"s_addc_u32 s39, s39, 0\n"
"v_mov_b32 v16, s38\n"
"v_mov_b32 v17, s39\n"
"WT vmcnt(19)\n"
"s_barrier\n"
"RD a[0:3],v10 offset:0\n"
"RD a[16:19],v11 offset:0\n"
"TR v[96:97],v24 offset:16384\n"
"TR v[98:99],v24 offset:18432\n"
"TR v[100:101],v24 offset:24576\n"
"TR v[102:103],v24 offset:26624\n"
"RD a[4:7],v10 offset:2048\n"
"RD a[20:23],v11 offset:2048\n"
"RD a[8:11],v10 offset:4096\n"
"RD a[12:15],v10 offset:6144\n"
"RD a[24:27],v11 offset:4096\n"
"RD a[28:31],v11 offset:6144\n"
"TR v[104:105],v25 offset:16384\n"
"TR v[106:107],v25 offset:18432\n"
"TR v[108:109],v25 offset:24576\n"
"TR v[110:111],v25 offset:26624\n"
"TR v[112:113],v26 offset:16384\n"
"TR v[114:115],v26 offset:18432\n"
"TR v[116:117],v26 offset:24576\n"
"TR v[118:119],v26 offset:26624\n"
"TR v[120:121],v27 offset:16384\n"
"TR v[122:123],v27 offset:18432\n"
"TR v[124:125],v27 offset:24576\n"
"TR v[126:127],v27 offset:26624\n"
"s_cmp_lg_u32 s34, 0\n"
"s_cbranch_scc1 L_pub_7\n"
"s_mov_b64 exec, 1\n"
"s_lshl_b32 s56, s33, 7\n"
"v_lshl_add_u32 v21, v0, 3, s56\n"
"s_mov_b32 s57, 0x40000\n"
"buffer_store_dwordx2 v[16:17], v21, s[40:43], s57 offen sc1\n"
"s_mov_b64 exec, -1\n"
"L_pub_7:\n"
"WT lgkmcnt(0)\n"
"MF v[128:131],v[96:99],a[0:3],0\n"
"MF v[128:131],v[100:103],a[16:19],v[128:131]\n"
"s_add_u32 s65, s65, 524288\n"
"s_add_u32 m0, s66, 114688\n"
"MF v[160:163],v[96:99],a[4:7],0\n"
"LD v6,s[48:51],s65 offen offset:0 lds\n"
"MF v[160:163],v[100:103],a[20:23],v[160:163]\n"
"MF v[192:195],v[96:99],a[8:11],0\n"
"MF v[192:195],v[100:103],a[24:27],v[192:195]\n"
"MF v[224:227],v[96:99],a[12:15],0\n"
"LD v7,s[48:51],s65 offen offset:1024 lds\n"
"MF v[224:227],v[100:103],a[28:31],v[224:227]\n"
"TR v[96:97],v24 offset:20480\n"
"MF v[132:135],v[104:107],a[0:3],0\n"
"MF v[132:135],v[108:111],a[16:19],v[132:135]\n"
"TR v[98:99],v24 offset:22528\n"
"MF v[164:167],v[104:107],a[4:7],0\n"
"LD v8,s[48:51],s65 offen offset:2048 lds\n"
"MF v[164:167],v[108:111],a[20:23],v[164:167]\n"
"TR v[100:101],v24 offset:28672\n"
"MF v[196:199],v[104:107],a[8:11],0\n"
"MF v[196:199],v[108:111],a[24:27],v[196:199]\n"
"TR v[102:103],v24 offset:30720\n"
"MF v[228:231],v[104:107],a[12:15],0\n"
"LD v9,s[48:51],s65 offen offset:3072 lds\n"
"MF v[228:231],v[108:111],a[28:31],v[228:231]\n"
"TR v[104:105],v25 offset:20480\n"
"MF v[136:139],v[112:115],a[0:3],0\n"
"MF v[136:139],v[116:119],a[16:19],v[136:139]\n"
"s_add_u32 s61, s60, 33024\n"
"s_add_u32 m0, s62, 0\n"
"TR v[106:107],v25 offset:22528\n"
"MF v[168:171],v[112:115],a[4:7],0\n"
"LD v2,s[44:47],s61 offen offset:0 lds\n"
"MF v[168:171],v[116:119],a[20:23],v[168:171]\n"
"TR v[108:109],v25 offset:28672\n"
"MF v[200:203],v[112:115],a[8:11],0\n"
"MF v[200:203],v[116:119],a[24:27],v[200:203]\n"
"TR v[110:111],v25 offset:30720\n"
"MF v[232:235],v[112:115],a[12:15],0\n"
"LD v3,s[44:47],s61 offen offset:1024 lds\n"
"MF v[232:235],v[116:119],a[28:31],v[232:235]\n"
"TR v[112:113],v26 offset:20480\n"
"MF v[140:143],v[120:123],a[0:3],0\n"
"MF v[140:143],v[124:127],a[16:19],v[140:143]\n"
"TR v[114:115],v26 offset:22528\n"
"MF v[172:175],v[120:123],a[4:7],0\n"
"LD v4,s[44:47],s61 offen offset:2048 lds\n"
"MF v[172:175],v[124:127],a[20:23],v[172:175]\n"
"TR v[116:117],v26 offset:28672\n"
"MF v[204:207],v[120:123],a[8:11],0\n"
"MF v[204:207],v[124:127],a[24:27],v[204:207]\n"
"TR v[118:119],v26 offset:30720\n"
"MF v[236:239],v[120:123],a[12:15],0\n"
"LD v5,s[44:47],s61 offen offset:3072 lds\n"
"MF v[236:239],v[124:127],a[28:31],v[236:239]\n"
"WT lgkmcnt(10)\n"
"TR v[120:121],v27 offset:20480\n"
"MF v[144:147],v[96:99],a[0:3],0\n"
"WT lgkmcnt(9)\n"
"WT vmcnt(15)\n"
"s_barrier\n"
"MF v[144:147],v[100:103],a[16:19],v[144:147]\n"
"s_add_u32 s65, s65, 524288\n"
"s_add_u32 m0, s66, 131072\n"
"RD a[32:35],v10 offset:8192\n"
"RD a[36:39],v10 offset:10240\n"
"MF v[176:179],v[96:99],a[4:7],0\n"
"LD v6,s[48:51],s65 offen offset:0 lds\n"
"MF v[176:179],v[100:103],a[20:23],v[176:179]\n"
"TR v[122:123],v27 offset:22528\n"
"MF v[208:211],v[96:99],a[8:11],0\n"
"MF v[208:211],v[100:103],a[24:27],v[208:211]\n"
"RD a[48:51],v11 offset:8192\n"
"RD a[52:55],v11 offset:10240\n"
"MF v[240:243],v[96:99],a[12:15],0\n"
"LD v7,s[48:51],s65 offen offset:1024 lds\n"
"MF v[240:243],v[100:103],a[28:31],v[240:243]\n"
"WT lgkmcnt(12)\n"
"TR v[124:125],v27 offset:28672\n"
"MF v[148:151],v[104:107],a[0:3],0\n"
"WT lgkmcnt(11)\n"
"MF v[148:151],v[108:111],a[16:19],v[148:151]\n"
"s_add_u32 s61, s60, 384\n"
"s_add_u32 m0, s62, 4096\n"
"RD a[40:43],v10 offset:12288\n"
"RD a[44:47],v10 offset:14336\n"
"MF v[180:183],v[104:107],a[4:7],0\n"
"LD v2,s[44:47],s61 offen offset:0 lds\n"
"MF v[180:183],v[108:111],a[20:23],v[180:183]\n"
"TR v[126:127],v27 offset:30720\n"
"MF v[212:215],v[104:107],a[8:11],0\n"
"MF v[212:215],v[108:111],a[24:27],v[212:215]\n"
"RD a[56:59],v11 offset:12288\n"
"RD a[60:63],v11 offset:14336\n"
"MF v[244:247],v[104:107],a[12:15],0\n"
"LD v3,s[44:47],s61 offen offset:1024 lds\n"
"MF v[244:247],v[108:111],a[28:31],v[244:247]\n"
"WT lgkmcnt(14)\n"
"TR v[96:97],v24 offset:32768\n"
"MF v[152:155],v[112:115],a[0:3],0\n"
"WT lgkmcnt(13)\n"
"TR v[98:99],v24 offset:34816\n"
"MF v[152:155],v[116:119],a[16:19],v[152:155]\n"
"TR v[100:101],v24 offset:40960\n"
"MF v[184:187],v[112:115],a[4:7],0\n"
"LD v4,s[44:47],s61 offen offset:2048 lds\n"
"MF v[184:187],v[116:119],a[20:23],v[184:187]\n"
"TR v[102:103],v24 offset:43008\n"
"MF v[216:219],v[112:115],a[8:11],0\n"
"MF v[216:219],v[116:119],a[24:27],v[216:219]\n"
"TR v[104:105],v25 offset:32768\n"
"MF v[248:251],v[112:115],a[12:15],0\n"
"LD v5,s[44:47],s61 offen offset:3072 lds\n"
"MF v[248:251],v[116:119],a[28:31],v[248:251]\n"
"WT lgkmcnt(13)\n"
"TR v[106:107],v25 offset:34816\n"
"MF v[156:159],v[120:123],a[0:3],0\n"
"WT lgkmcnt(8)\n"
"MF v[156:159],v[124:127],a[16:19],v[156:159]\n"
"s_add_u32 m0, s66, 131072\n"
"TR v[108:109],v25 offset:40960\n"
"MF v[188:191],v[120:123],a[4:7],0\n"
"LD v8,s[48:51],s65 offen offset:2048 lds\n"
"MF v[188:191],v[124:127],a[20:23],v[188:191]\n"
"TR v[110:111],v25 offset:43008\n"
"MF v[220:223],v[120:123],a[8:11],0\n"
"MF v[220:223],v[124:127],a[24:27],v[220:223]\n"
"TR v[112:113],v26 offset:32768\n"
"MF v[252:255],v[120:123],a[12:15],0\n"
"LD v9,s[48:51],s65 offen offset:3072 lds\n"
"MF v[252:255],v[124:127],a[28:31],v[252:255]\n"
"WT lgkmcnt(7)\n"
"TR v[114:115],v26 offset:34816\n"
"MF v[128:131],v[96:99],a[32:35],v[128:131]\n"
"WT lgkmcnt(6)\n"
"MF v[128:131],v[100:103],a[48:51],v[128:131]\n"
"s_add_u32 s61, s60, 33152\n"
"s_add_u32 m0, s62, 8192\n"
"TR v[116:117],v26 offset:40960\n"
"MF v[160:163],v[96:99],a[36:39],v[160:163]\n"
"LD v2,s[44:47],s61 offen offset:0 lds\n"
"MF v[160:163],v[100:103],a[52:55],v[160:163]\n"
"TR v[118:119],v26 offset:43008\n"
"MF v[192:195],v[96:99],a[40:43],v[192:195]\n"
"MF v[192:195],v[100:103],a[56:59],v[192:195]\n"
"TR v[120:121],v27 offset:32768\n"
"MF v[224:227],v[96:99],a[44:47],v[224:227]\n"
"LD v3,s[44:47],s61 offen offset:1024 lds\n"
"MF v[224:227],v[100:103],a[60:63],v[224:227]\n"
"WT lgkmcnt(7)\n"
"TR v[122:123],v27 offset:34816\n"
"MF v[132:135],v[104:107],a[32:35],v[132:135]\n"
"WT lgkmcnt(6)\n"
"MF v[132:135],v[108:111],a[48:51],v[132:135]\n"
"TR v[124:125],v27 offset:40960\n"
"MF v[164:167],v[104:107],a[36:39],v[164:167]\n"
"LD v4,s[44:47],s61 offen offset:2048 lds\n"
"MF v[164:167],v[108:111],a[52:55],v[164:167]\n"
"TR v[126:127],v27 offset:43008\n"
"MF v[196:199],v[104:107],a[40:43],v[196:199]\n"
"MF v[196:199],v[108:111],a[56:59],v[196:199]\n"
"TR v[96:97],v24 offset:36864\n"
"MF v[228:231],v[104:107],a[44:47],v[228:231]\n"
"MF v[228:231],v[108:111],a[60:63],v[228:231]\n"
"WT lgkmcnt(7)\n"
"TR v[98:99],v24 offset:38912\n"
"MF v[136:139],v[112:115],a[32:35],v[136:139]\n"
"WT lgkmcnt(6)\n"
"MF v[136:139],v[116:119],a[48:51],v[136:139]\n"
"TR v[100:101],v24 offset:45056\n"
"MF v[168:171],v[112:115],a[36:39],v[168:171]\n"
"LD v5,s[44:47],s61 offen offset:3072 lds\n"
"MF v[168:171],v[116:119],a[52:55],v[168:171]\n"
"TR v[102:103],v24 offset:47104\n"
"MF v[200:203],v[112:115],a[40:43],v[200:203]\n"
"MF v[200:203],v[116:119],a[56:59],v[200:203]\n"
"s_add_u32 s65, s65, 524288\n"
"s_add_u32 m0, s66, 147456\n"
"TR v[104:105],v25 offset:36864\n"
"MF v[232:235],v[112:115],a[44:47],v[232:235]\n"
"LD v6,s[48:51],s65 offen offset:0 lds\n"
"MF v[232:235],v[116:119],a[60:63],v[232:235]\n"
"WT lgkmcnt(7)\n"
"TR v[106:107],v25 offset:38912\n"
"MF v[140:143],v[120:123],a[32:35],v[140:143]\n"
"WT lgkmcnt(6)\n"
"MF v[140:143],v[124:127],a[48:51],v[140:143]\n"
"TR v[108:109],v25 offset:45056\n"
"MF v[172:175],v[120:123],a[36:39],v[172:175]\n"
"LD v7,s[48:51],s65 offen offset:1024 lds\n"
"MF v[172:175],v[124:127],a[52:55],v[172:175]\n"
"TR v[110:111],v25 offset:47104\n"
"MF v[204:207],v[120:123],a[40:43],v[204:207]\n"
"MF v[204:207],v[124:127],a[56:59],v[204:207]\n"
"TR v[112:113],v26 offset:36864\n"
"MF v[236:239],v[120:123],a[44:47],v[236:239]\n"
"MF v[236:239],v[124:127],a[60:63],v[236:239]\n"
"WT lgkmcnt(7)\n"
"TR v[114:115],v26 offset:38912\n"
"MF v[144:147],v[96:99],a[32:35],v[144:147]\n"
"WT lgkmcnt(6)\n"
"WT vmcnt(18)\n"
"s_barrier\n"
"MF v[144:147],v[100:103],a[48:51],v[144:147]\n"
"s_add_u32 s61, s60, 512\n"
"s_add_u32 m0, s62, 12288\n"
"RD a[64:67],v10 offset:16384\n"
"RD a[68:71],v10 offset:18432\n"
"MF v[176:179],v[96:99],a[36:39],v[176:179]\n"
"LD v2,s[44:47],s61 offen offset:0 lds\n"
"MF v[176:179],v[100:103],a[52:55],v[176:179]\n"
"TR v[116:117],v26 offset:45056\n"
"MF v[208:211],v[96:99],a[40:43],v[208:211]\n"
"MF v[208:211],v[100:103],a[56:59],v[208:211]\n"
"RD a[80:83],v11 offset:16384\n"
"RD a[84:87],v11 offset:18432\n"
"MF v[240:243],v[96:99],a[44:47],v[240:243]\n"
"LD v3,s[44:47],s61 offen offset:1024 lds\n"
"MF v[240:243],v[100:103],a[60:63],v[240:243]\n"
"WT lgkmcnt(9)\n"
"TR v[118:119],v26 offset:47104\n"
"MF v[148:151],v[104:107],a[32:35],v[148:151]\n"
"WT lgkmcnt(8)\n"
"TR v[120:121],v27 offset:36864\n"
"MF v[148:151],v[108:111],a[48:51],v[148:151]\n"
"WT vmcnt(16)\n"
"RD a[72:75],v10 offset:0\n"
"RD a[76:79],v10 offset:2048\n"
"MF v[180:183],v[104:107],a[36:39],v[180:183]\n"
"LD v4,s[44:47],s61 offen offset:2048 lds\n"
"MF v[180:183],v[108:111],a[52:55],v[180:183]\n"
"TR v[122:123],v27 offset:38912\n"
"MF v[212:215],v[104:107],a[40:43],v[212:215]\n"
"MF v[212:215],v[108:111],a[56:59],v[212:215]\n"
"RD a[88:91],v11 offset:0\n"
"RD a[92:95],v11 offset:2048\n"
"MF v[244:247],v[104:107],a[44:47],v[244:247]\n"
"TR v[124:125],v27 offset:45056\n"
"MF v[244:247],v[108:111],a[60:63],v[244:247]\n"
"WT lgkmcnt(13)\n"
"TR v[126:127],v27 offset:47104\n"
"MF v[152:155],v[112:115],a[32:35],v[152:155]\n"
"WT lgkmcnt(8)\n"
"TR v[96:97],v24 offset:49152\n"
"MF v[152:155],v[116:119],a[48:51],v[152:155]\n"
"TR v[98:99],v24 offset:51200\n"
"MF v[184:187],v[112:115],a[36:39],v[184:187]\n"
"LD v5,s[44:47],s61 offen offset:3072 lds\n"
"MF v[184:187],v[116:119],a[52:55],v[184:187]\n"
"TR v[100:101],v24 offset:57344\n"
"MF v[216:219],v[112:115],a[40:43],v[216:219]\n"
"MF v[216:219],v[116:119],a[56:59],v[216:219]\n"
"s_add_u32 m0, s66, 147456\n"
"TR v[102:103],v24 offset:59392\n"
"MF v[248:251],v[112:115],a[44:47],v[248:251]\n"
"LD v8,s[48:51],s65 offen offset:2048 lds\n"
"MF v[248:251],v[116:119],a[60:63],v[248:251]\n"
"WT lgkmcnt(8)\n"
"TR v[104:105],v25 offset:49152\n"
"MF v[156:159],v[120:123],a[32:35],v[156:159]\n"
"WT lgkmcnt(5)\n"
"TR v[106:107],v25 offset:51200\n"
"MF v[156:159],v[124:127],a[48:51],v[156:159]\n"
"TR v[108:109],v25 offset:57344\n"
"MF v[188:191],v[120:123],a[36:39],v[188:191]\n"
"LD v9,s[48:51],s65 offen offset:3072 lds\n"
"MF v[188:191],v[124:127],a[52:55],v[188:191]\n"
"TR v[110:111],v25 offset:59392\n"
"MF v[220:223],v[120:123],a[40:43],v[220:223]\n"
"MF v[220:223],v[124:127],a[56:59],v[220:223]\n"
"TR v[112:113],v26 offset:49152\n"
"MF v[252:255],v[120:123],a[44:47],v[252:255]\n"
"MF v[252:255],v[124:127],a[60:63],v[252:255]\n"
"WT lgkmcnt(7)\n"
"TR v[114:115],v26 offset:51200\n"
"MF v[128:131],v[96:99],a[64:67],v[128:131]\n"
"WT lgkmcnt(6)\n"
"MF v[128:131],v[100:103],a[80:83],v[128:131]\n"
"s_add_u32 s61, s60, 33280\n"
"s_add_u32 m0, s62, 16384\n"
"TR v[116:117],v26 offset:57344\n"
"MF v[160:163],v[96:99],a[68:71],v[160:163]\n"
"LD v2,s[44:47],s61 offen offset:0 lds\n"
"MF v[160:163],v[100:103],a[84:87],v[160:163]\n"
"TR v[118:119],v26 offset:59392\n"
"MF v[192:195],v[96:99],a[72:75],v[192:195]\n"
"MF v[192:195],v[100:103],a[88:91],v[192:195]\n"
"TR v[120:121],v27 offset:49152\n"
"MF v[224:227],v[96:99],a[76:79],v[224:227]\n"
"LD v3,s[44:47],s61 offen offset:1024 lds\n"
"MF v[224:227],v[100:103],a[92:95],v[224:227]\n"
"WT lgkmcnt(7)\n"
"TR v[122:123],v27 offset:51200\n"
"MF v[132:135],v[104:107],a[64:67],v[132:135]\n"
"WT lgkmcnt(6)\n"
"MF v[132:135],v[108:111],a[80:83],v[132:135]\n"
"TR v[124:125],v27 offset:57344\n"
"MF v[164:167],v[104:107],a[68:71],v[164:167]\n"
"LD v4,s[44:47],s61 offen offset:2048 lds\n"
"MF v[164:167],v[108:111],a[84:87],v[164:167]\n"
"TR v[126:127],v27 offset:59392\n"
"MF v[196:199],v[104:107],a[72:75],v[196:199]\n"
"MF v[196:199],v[108:111],a[88:91],v[196:199]\n"
"TR v[96:97],v24 offset:53248\n"
"MF v[228:231],v[104:107],a[76:79],v[228:231]\n"
"MF v[228:231],v[108:111],a[92:95],v[228:231]\n"
"WT lgkmcnt(7)\n"
"TR v[98:99],v24 offset:55296\n"
"MF v[136:139],v[112:115],a[64:67],v[136:139]\n"
"WT lgkmcnt(6)\n"
"MF v[136:139],v[116:119],a[80:83],v[136:139]\n"
"TR v[100:101],v24 offset:61440\n"
"MF v[168:171],v[112:115],a[68:71],v[168:171]\n"
"LD v5,s[44:47],s61 offen offset:3072 lds\n"
"MF v[168:171],v[116:119],a[84:87],v[168:171]\n"
"TR v[102:103],v24 offset:63488\n"
"MF v[200:203],v[112:115],a[72:75],v[200:203]\n"
"MF v[200:203],v[116:119],a[88:91],v[200:203]\n"
"s_add_u32 s65, s65, 524288\n"
"s_add_u32 m0, s66, 81920\n"
"TR v[104:105],v25 offset:53248\n"
"MF v[232:235],v[112:115],a[76:79],v[232:235]\n"
"LD v6,s[48:51],s65 offen offset:0 lds\n"
"MF v[232:235],v[116:119],a[92:95],v[232:235]\n"
"WT lgkmcnt(7)\n"
"TR v[106:107],v25 offset:55296\n"
"MF v[140:143],v[120:123],a[64:67],v[140:143]\n"
"WT lgkmcnt(6)\n"
"MF v[140:143],v[124:127],a[80:83],v[140:143]\n"
"TR v[108:109],v25 offset:61440\n"
"MF v[172:175],v[120:123],a[68:71],v[172:175]\n"
"LD v7,s[48:51],s65 offen offset:1024 lds\n"
"MF v[172:175],v[124:127],a[84:87],v[172:175]\n"
"TR v[110:111],v25 offset:63488\n"
"MF v[204:207],v[120:123],a[72:75],v[204:207]\n"
"MF v[204:207],v[124:127],a[88:91],v[204:207]\n"
"TR v[112:113],v26 offset:53248\n"
"MF v[236:239],v[120:123],a[76:79],v[236:239]\n"
"MF v[236:239],v[124:127],a[92:95],v[236:239]\n"
"WT lgkmcnt(7)\n"
"TR v[114:115],v26 offset:55296\n"
"MF v[144:147],v[96:99],a[64:67],v[144:147]\n"
"WT lgkmcnt(6)\n"
"WT vmcnt(18)\n"
"s_barrier\n"
"MF v[144:147],v[100:103],a[80:83],v[144:147]\n"
"s_add_u32 s61, s60, 640\n"
"s_add_u32 m0, s62, 0\n"
"RD a[96:99],v10 offset:4096\n"
"RD a[100:103],v10 offset:6144\n"
"MF v[176:179],v[96:99],a[68:71],v[176:179]\n"
"LD v2,s[44:47],s61 offen offset:0 lds\n"
"MF v[176:179],v[100:103],a[84:87],v[176:179]\n"
"TR v[116:117],v26 offset:61440\n"
"MF v[208:211],v[96:99],a[72:75],v[208:211]\n"
"MF v[208:211],v[100:103],a[88:91],v[208:211]\n"
"RD a[112:115],v11 offset:4096\n"
"RD a[116:119],v11 offset:6144\n"
"MF v[240:243],v[96:99],a[76:79],v[240:243]\n"
"LD v3,s[44:47],s61 offen offset:1024 lds\n"
"MF v[240:243],v[100:103],a[92:95],v[240:243]\n"
"WT lgkmcnt(9)\n"
"TR v[118:119],v26 offset:63488\n"
"MF v[148:151],v[104:107],a[64:67],v[148:151]\n"
"WT lgkmcnt(8)\n"
"TR v[120:121],v27 offset:53248\n"
"MF v[148:151],v[108:111],a[80:83],v[148:151]\n"
"WT vmcnt(16)\n"
"RD a[104:107],v10 offset:8192\n"
"RD a[108:111],v10 offset:10240\n"
"MF v[180:183],v[104:107],a[68:71],v[180:183]\n"
"LD v4,s[44:47],s61 offen offset:2048 lds\n"
"MF v[180:183],v[108:111],a[84:87],v[180:183]\n"
"TR v[122:123],v27 offset:55296\n"
"MF v[212:215],v[104:107],a[72:75],v[212:215]\n"
"MF v[212:215],v[108:111],a[88:91],v[212:215]\n"
"RD a[120:123],v11 offset:8192\n"
"RD a[124:127],v11 offset:10240\n"
"MF v[244:247],v[104:107],a[76:79],v[244:247]\n"
"LD v5,s[44:47],s61 offen offset:3072 lds\n"
"MF v[244:247],v[108:111],a[92:95],v[244:247]\n"
"WT lgkmcnt(12)\n"
"TR v[124:125],v27 offset:61440\n"
"MF v[152:155],v[112:115],a[64:67],v[152:155]\n"
"WT lgkmcnt(7)\n"
"TR v[126:127],v27 offset:63488\n"
"MF v[152:155],v[116:119],a[80:83],v[152:155]\n"
"s_add_u32 m0, s66, 81920\n"
"TR v[96:97],v28 offset:0\n"
"MF v[184:187],v[112:115],a[68:71],v[184:187]\n"
"LD v8,s[48:51],s65 offen offset:2048 lds\n"
"MF v[184:187],v[116:119],a[84:87],v[184:187]\n"
"TR v[98:99],v28 offset:2048\n"
"MF v[216:219],v[112:115],a[72:75],v[216:219]\n"
"MF v[216:219],v[116:119],a[88:91],v[216:219]\n"
"TR v[100:101],v28 offset:8192\n"
"MF v[248:251],v[112:115],a[76:79],v[248:251]\n"
"LD v9,s[48:51],s65 offen offset:3072 lds\n"
"MF v[248:251],v[116:119],a[92:95],v[248:251]\n"
"WT lgkmcnt(7)\n"
"TR v[102:103],v28 offset:10240\n"
"MF v[156:159],v[120:123],a[64:67],v[156:159]\n"
"WT lgkmcnt(4)\n"
"TR v[104:105],v29 offset:0\n"
"MF v[156:159],v[124:127],a[80:83],v[156:159]\n"
"TR v[106:107],v29 offset:2048\n"
"MF v[188:191],v[120:123],a[68:71],v[188:191]\n"
"s_mov_b32 s56, s16\n"
"s_and_b32 s57, s17, 0xffff\n"
"s_mov_b32 s58, 0x400000\n"
"s_mov_b32 s59, 0x20000\n"
"v_lshrrev_b32 v23, 3, v0\n"
"v_min_u32 v23, 3, v23\n"
"v_lshlrev_b32 v23, 13, v23\n"
"v_and_b32 v22, 7, v0\n"
"v_lshl_add_u32 v23, v22, 7, v23\n"
"s_lshl_b32 s73, s37, 2\n"
"s_add_u32 s73, s73, s34\n"
"s_mul_i32 s73, s73, 32\n"
"s_add_u32 s73, s73, s72\n"
"s_lshl_b32 s73, s73, 10\n"
"buffer_load_dword v34, v23, s[56:59], s73 offen\n"
"MF v[188:191],v[124:127],a[84:87],v[188:191]\n"
"TR v[108:109],v29 offset:8192\n"
"MF v[220:223],v[120:123],a[72:75],v[220:223]\n"
"MF v[220:223],v[124:127],a[88:91],v[220:223]\n"
"TR v[110:111],v29 offset:10240\n"
"MF v[252:255],v[120:123],a[76:79],v[252:255]\n"
"s_mov_b32 s56, s20\n"
"s_and_b32 s57, s21, 0xffff\n"
"s_mov_b32 s58, 0xc8000\n"
"s_mov_b32 s59, 0x20000\n"
"v_min_u32 v23, 6, v0\n"
"v_lshlrev_b32 v23, 7, v23\n"
"s_lshl_b32 s73, s33, 2\n"
"s_add_u32 s73, s73, s34\n"
"s_mul_i32 s73, s73, 896\n"
"buffer_load_dword v34, v23, s[56:59], s73 offen\n"
"MF v[252:255],v[124:127],a[92:95],v[252:255]\n"
"WT lgkmcnt(6)\n"
"TR v[112:113],v30 offset:0\n"
"MF v[128:131],v[96:99],a[96:99],v[128:131]\n"
"WT lgkmcnt(5)\n"
"TR v[114:115],v30 offset:2048\n"
"MF v[128:131],v[100:103],a[112:115],v[128:131]\n"
"s_add_u32 s61, s60, 33408\n"
"s_add_u32 m0, s62, 4096\n"
"TR v[116:117],v30 offset:8192\n"
"MF v[160:163],v[96:99],a[100:103],v[160:163]\n"
"LD v2,s[44:47],s61 offen offset:0 lds\n"
"MF v[160:163],v[100:103],a[116:119],v[160:163]\n"
"TR v[118:119],v30 offset:10240\n"
"MF v[192:195],v[96:99],a[104:107],v[192:195]\n"
"MF v[192:195],v[100:103],a[120:123],v[192:195]\n"
"TR v[120:121],v31 offset:0\n"
"MF v[224:227],v[96:99],a[108:111],v[224:227]\n"
"LD v3,s[44:47],s61 offen offset:1024 lds\n"
"MF v[224:227],v[100:103],a[124:127],v[224:227]\n"
"WT lgkmcnt(7)\n"
"TR v[122:123],v31 offset:2048\n"
"MF v[132:135],v[104:107],a[96:99],v[132:135]\n"
"WT lgkmcnt(6)\n"
"MF v[132:135],v[108:111],a[112:115],v[132:135]\n"
"TR v[124:125],v31 offset:8192\n"
"MF v[164:167],v[104:107],a[100:103],v[164:167]\n"
"LD v4,s[44:47],s61 offen offset:2048 lds\n"
"MF v[164:167],v[108:111],a[116:119],v[164:167]\n"
"TR v[126:127],v31 offset:10240\n"
"MF v[196:199],v[104:107],a[104:107],v[196:199]\n"
"MF v[196:199],v[108:111],a[120:123],v[196:199]\n"
"TR v[96:97],v28 offset:4096\n"
"MF v[228:231],v[104:107],a[108:111],v[228:231]\n"
"MF v[228:231],v[108:111],a[124:127],v[228:231]\n"
"WT lgkmcnt(7)\n"
"TR v[98:99],v28 offset:6144\n"
"MF v[136:139],v[112:115],a[96:99],v[136:139]\n"
"WT lgkmcnt(6)\n"
"MF v[136:139],v[116:119],a[112:115],v[136:139]\n"
"TR v[100:101],v28 offset:12288\n"
"MF v[168:171],v[112:115],a[100:103],v[168:171]\n"
"LD v5,s[44:47],s61 offen offset:3072 lds\n"
"MF v[168:171],v[116:119],a[116:119],v[168:171]\n"
"TR v[102:103],v28 offset:14336\n"
"MF v[200:203],v[112:115],a[104:107],v[200:203]\n"
"MF v[200:203],v[116:119],a[120:123],v[200:203]\n"
"s_add_u32 s65, s65, 524288\n"
"s_add_u32 m0, s66, 98304\n"
"TR v[104:105],v29 offset:4096\n"
"MF v[232:235],v[112:115],a[108:111],v[232:235]\n"
"LD v6,s[48:51],s65 offen offset:0 lds\n"
"MF v[232:235],v[116:119],a[124:127],v[232:235]\n"
"WT lgkmcnt(7)\n"
"TR v[106:107],v29 offset:6144\n"
"MF v[140:143],v[120:123],a[96:99],v[140:143]\n"
"WT lgkmcnt(6)\n"
"MF v[140:143],v[124:127],a[112:115],v[140:143]\n"
"TR v[108:109],v29 offset:12288\n"
"MF v[172:175],v[120:123],a[100:103],v[172:175]\n"
"LD v7,s[48:51],s65 offen offset:1024 lds\n"
"MF v[172:175],v[124:127],a[116:119],v[172:175]\n"
"TR v[110:111],v29 offset:14336\n"
"MF v[204:207],v[120:123],a[104:107],v[204:207]\n"
"MF v[204:207],v[124:127],a[120:123],v[204:207]\n"
"TR v[112:113],v30 offset:4096\n"
"MF v[236:239],v[120:123],a[108:111],v[236:239]\n"
"MF v[236:239],v[124:127],a[124:127],v[236:239]\n"
"WT lgkmcnt(7)\n"
"TR v[114:115],v30 offset:6144\n"
"MF v[144:147],v[96:99],a[96:99],v[144:147]\n"
"WT lgkmcnt(6)\n"
"WT vmcnt(20)\n"
"s_barrier\n"
"MF v[144:147],v[100:103],a[112:115],v[144:147]\n"
"s_add_u32 s61, s60, 768\n"
"s_add_u32 m0, s62, 8192\n"
"RD a[128:131],v10 offset:12288\n"
"RD a[132:135],v10 offset:14336\n"
"MF v[176:179],v[96:99],a[100:103],v[176:179]\n"
"LD v2,s[44:47],s61 offen offset:0 lds\n"
"MF v[176:179],v[100:103],a[116:119],v[176:179]\n"
"TR v[116:117],v30 offset:12288\n"
"MF v[208:211],v[96:99],a[104:107],v[208:211]\n"
"MF v[208:211],v[100:103],a[120:123],v[208:211]\n"
"RD a[144:147],v11 offset:12288\n"
"RD a[148:151],v11 offset:14336\n"
"MF v[240:243],v[96:99],a[108:111],v[240:243]\n"
"LD v3,s[44:47],s61 offen offset:1024 lds\n"
"MF v[240:243],v[100:103],a[124:127],v[240:243]\n"
"WT lgkmcnt(9)\n"
"TR v[118:119],v30 offset:14336\n"
"MF v[148:151],v[104:107],a[96:99],v[148:151]\n"
"WT lgkmcnt(8)\n"
"TR v[120:121],v31 offset:4096\n"
"MF v[148:151],v[108:111],a[112:115],v[148:151]\n"
"WT vmcnt(18)\n"
"RD a[136:139],v10 offset:16384\n"
"RD a[140:143],v10 offset:18432\n"
"MF v[180:183],v[104:107],a[100:103],v[180:183]\n"
"LD v4,s[44:47],s61 offen offset:2048 lds\n"
"MF v[180:183],v[108:111],a[116:119],v[180:183]\n"
"TR v[122:123],v31 offset:6144\n"
"MF v[212:215],v[104:107],a[104:107],v[212:215]\n"
"MF v[212:215],v[108:111],a[120:123],v[212:215]\n"
"RD a[152:155],v11 offset:16384\n"
"RD a[156:159],v11 offset:18432\n"
"MF v[244:247],v[104:107],a[108:111],v[244:247]\n"
"TR v[124:125],v31 offset:12288\n"
"MF v[244:247],v[108:111],a[124:127],v[244:247]\n"
"WT lgkmcnt(13)\n"
"TR v[126:127],v31 offset:14336\n"
"MF v[152:155],v[112:115],a[96:99],v[152:155]\n"
"WT lgkmcnt(8)\n"
"TR v[96:97],v28 offset:16384\n"
"MF v[152:155],v[116:119],a[112:115],v[152:155]\n"
"TR v[98:99],v28 offset:18432\n"
"MF v[184:187],v[112:115],a[100:103],v[184:187]\n"
"LD v5,s[44:47],s61 offen offset:3072 lds\n"
"MF v[184:187],v[116:119],a[116:119],v[184:187]\n"
"TR v[100:101],v28 offset:24576\n"
"MF v[216:219],v[112:115],a[104:107],v[216:219]\n"
"MF v[216:219],v[116:119],a[120:123],v[216:219]\n"
"s_add_u32 m0, s66, 98304\n"
"TR v[102:103],v28 offset:26624\n"
"MF v[248:251],v[112:115],a[108:111],v[248:251]\n"
"LD v8,s[48:51],s65 offen offset:2048 lds\n"
"MF v[248:251],v[116:119],a[124:127],v[248:251]\n"
"WT lgkmcnt(8)\n"
"TR v[104:105],v29 offset:16384\n"
"MF v[156:159],v[120:123],a[96:99],v[156:159]\n"
"WT lgkmcnt(5)\n"
"TR v[106:107],v29 offset:18432\n"
"MF v[156:159],v[124:127],a[112:115],v[156:159]\n"
"TR v[108:109],v29 offset:24576\n"
"MF v[188:191],v[120:123],a[100:103],v[188:191]\n"
"LD v9,s[48:51],s65 offen offset:3072 lds\n"
"MF v[188:191],v[124:127],a[116:119],v[188:191]\n"
"TR v[110:111],v29 offset:26624\n"
"MF v[220:223],v[120:123],a[104:107],v[220:223]\n"
"MF v[220:223],v[124:127],a[120:123],v[220:223]\n"
"TR v[112:113],v30 offset:16384\n"
"MF v[252:255],v[120:123],a[108:111],v[252:255]\n"
"MF v[252:255],v[124:127],a[124:127],v[252:255]\n"
"WT lgkmcnt(7)\n"
"TR v[114:115],v30 offset:18432\n"
"MF v[128:131],v[96:99],a[128:131],v[128:131]\n"
"WT lgkmcnt(6)\n"
"MF v[128:131],v[100:103],a[144:147],v[128:131]\n"
"s_add_u32 s61, s60, 33536\n"
"s_add_u32 m0, s62, 12288\n"
"TR v[116:117],v30 offset:24576\n"
"MF v[160:163],v[96:99],a[132:135],v[160:163]\n"
"LD v2,s[44:47],s61 offen offset:0 lds\n"
"MF v[160:163],v[100:103],a[148:151],v[160:163]\n"
"TR v[118:119],v30 offset:26624\n"
"MF v[192:195],v[96:99],a[136:139],v[192:195]\n"
"MF v[192:195],v[100:103],a[152:155],v[192:195]\n"
"TR v[120:121],v31 offset:16384\n"
"MF v[224:227],v[96:99],a[140:143],v[224:227]\n"
"LD v3,s[44:47],s61 offen offset:1024 lds\n"
"MF v[224:227],v[100:103],a[156:159],v[224:227]\n"
"WT lgkmcnt(7)\n"
"TR v[122:123],v31 offset:18432\n"
"MF v[132:135],v[104:107],a[128:131],v[132:135]\n"
"WT lgkmcnt(6)\n"
"MF v[132:135],v[108:111],a[144:147],v[132:135]\n"
"TR v[124:125],v31 offset:24576\n"
"MF v[164:167],v[104:107],a[132:135],v[164:167]\n"
"LD v4,s[44:47],s61 offen offset:2048 lds\n"
"MF v[164:167],v[108:111],a[148:151],v[164:167]\n"
"TR v[126:127],v31 offset:26624\n"
"MF v[196:199],v[104:107],a[136:139],v[196:199]\n"
"MF v[196:199],v[108:111],a[152:155],v[196:199]\n"
"TR v[96:97],v28 offset:20480\n"
"MF v[228:231],v[104:107],a[140:143],v[228:231]\n"
"MF v[228:231],v[108:111],a[156:159],v[228:231]\n"
"WT lgkmcnt(7)\n"
"TR v[98:99],v28 offset:22528\n"
"MF v[136:139],v[112:115],a[128:131],v[136:139]\n"
"WT lgkmcnt(6)\n"
"MF v[136:139],v[116:119],a[144:147],v[136:139]\n"
"TR v[100:101],v28 offset:28672\n"
"MF v[168:171],v[112:115],a[132:135],v[168:171]\n"
"LD v5,s[44:47],s61 offen offset:3072 lds\n"
"MF v[168:171],v[116:119],a[148:151],v[168:171]\n"
"TR v[102:103],v28 offset:30720\n"
"MF v[200:203],v[112:115],a[136:139],v[200:203]\n"
"MF v[200:203],v[116:119],a[152:155],v[200:203]\n"
"s_add_u32 s65, s65, 524288\n"
"s_add_u32 m0, s66, 114688\n"
"TR v[104:105],v29 offset:20480\n"
"MF v[232:235],v[112:115],a[140:143],v[232:235]\n"
"LD v6,s[48:51],s65 offen offset:0 lds\n"
"MF v[232:235],v[116:119],a[156:159],v[232:235]\n"
"WT lgkmcnt(7)\n"
"TR v[106:107],v29 offset:22528\n"
"MF v[140:143],v[120:123],a[128:131],v[140:143]\n"
"WT lgkmcnt(6)\n"
"MF v[140:143],v[124:127],a[144:147],v[140:143]\n"
"TR v[108:109],v29 offset:28672\n"
"MF v[172:175],v[120:123],a[132:135],v[172:175]\n"
"LD v7,s[48:51],s65 offen offset:1024 lds\n"
"MF v[172:175],v[124:127],a[148:151],v[172:175]\n"
"TR v[110:111],v29 offset:30720\n"
"MF v[204:207],v[120:123],a[136:139],v[204:207]\n"
"MF v[204:207],v[124:127],a[152:155],v[204:207]\n"
"TR v[112:113],v30 offset:20480\n"
"MF v[236:239],v[120:123],a[140:143],v[236:239]\n"
"MF v[236:239],v[124:127],a[156:159],v[236:239]\n"
"WT lgkmcnt(7)\n"
"TR v[114:115],v30 offset:22528\n"
"MF v[144:147],v[96:99],a[128:131],v[144:147]\n"
"WT lgkmcnt(6)\n"
"WT vmcnt(20)\n"
"s_barrier\n"
"MF v[144:147],v[100:103],a[144:147],v[144:147]\n"
"s_add_u32 s61, s60, 896\n"
"s_add_u32 m0, s62, 16384\n"
"RD a[160:163],v10 offset:0\n"
"RD a[164:167],v10 offset:2048\n"
"MF v[176:179],v[96:99],a[132:135],v[176:179]\n"
"LD v2,s[44:47],s61 offen offset:0 lds\n"
"MF v[176:179],v[100:103],a[148:151],v[176:179]\n"
"TR v[116:117],v30 offset:28672\n"
"MF v[208:211],v[96:99],a[136:139],v[208:211]\n"
"MF v[208:211],v[100:103],a[152:155],v[208:211]\n"
"RD a[176:179],v11 offset:0\n"
"RD a[180:183],v11 offset:2048\n"
"MF v[240:243],v[96:99],a[140:143],v[240:243]\n"
"LD v3,s[44:47],s61 offen offset:1024 lds\n"
"MF v[240:243],v[100:103],a[156:159],v[240:243]\n"
"WT lgkmcnt(9)\n"
"TR v[118:119],v30 offset:30720\n"
"MF v[148:151],v[104:107],a[128:131],v[148:151]\n"
"WT lgkmcnt(8)\n"
"TR v[120:121],v31 offset:20480\n"
"MF v[148:151],v[108:111],a[144:147],v[148:151]\n"
"WT vmcnt(16)\n"
"RD a[168:171],v10 offset:4096\n"
"RD a[172:175],v10 offset:6144\n"
"MF v[180:183],v[104:107],a[132:135],v[180:183]\n"
"LD v4,s[44:47],s61 offen offset:2048 lds\n"
"MF v[180:183],v[108:111],a[148:151],v[180:183]\n"
"TR v[122:123],v31 offset:22528\n"
"MF v[212:215],v[104:107],a[136:139],v[212:215]\n"
"MF v[212:215],v[108:111],a[152:155],v[212:215]\n"
"RD a[184:187],v11 offset:4096\n"
"RD a[188:191],v11 offset:6144\n"
"MF v[244:247],v[104:107],a[140:143],v[244:247]\n"
"TR v[124:125],v31 offset:28672\n"
"MF v[244:247],v[108:111],a[156:159],v[244:247]\n"
"WT lgkmcnt(13)\n"
"TR v[126:127],v31 offset:30720\n"
"MF v[152:155],v[112:115],a[128:131],v[152:155]\n"
"WT lgkmcnt(8)\n"
"TR v[96:97],v24 offset:16384\n"
"MF v[152:155],v[116:119],a[144:147],v[152:155]\n"
"TR v[98:99],v24 offset:18432\n"
"MF v[184:187],v[112:115],a[132:135],v[184:187]\n"
"LD v5,s[44:47],s61 offen offset:3072 lds\n"
"MF v[184:187],v[116:119],a[148:151],v[184:187]\n"
"TR v[100:101],v24 offset:24576\n"
"MF v[216:219],v[112:115],a[136:139],v[216:219]\n"
"MF v[216:219],v[116:119],a[152:155],v[216:219]\n"
"s_add_u32 m0, s66, 114688\n"
"TR v[102:103],v24 offset:26624\n"
"MF v[248:251],v[112:115],a[140:143],v[248:251]\n"
"LD v8,s[48:51],s65 offen offset:2048 lds\n"
"MF v[248:251],v[116:119],a[156:159],v[248:251]\n"
"WT lgkmcnt(8)\n"
"TR v[104:105],v25 offset:16384\n"
"MF v[156:159],v[120:123],a[128:131],v[156:159]\n"
"WT lgkmcnt(5)\n"
"TR v[106:107],v25 offset:18432\n"
"MF v[156:159],v[124:127],a[144:147],v[156:159]\n"
"TR v[108:109],v25 offset:24576\n"
"MF v[188:191],v[120:123],a[132:135],v[188:191]\n"
"LD v9,s[48:51],s65 offen offset:3072 lds\n"
"MF v[188:191],v[124:127],a[148:151],v[188:191]\n"
"TR v[110:111],v25 offset:26624\n"
"MF v[220:223],v[120:123],a[136:139],v[220:223]\n"
"MF v[220:223],v[124:127],a[152:155],v[220:223]\n"
"TR v[112:113],v26 offset:16384\n"
"MF v[252:255],v[120:123],a[140:143],v[252:255]\n"
"MF v[252:255],v[124:127],a[156:159],v[252:255]\n"
"WT lgkmcnt(7)\n"
"TR v[114:115],v26 offset:18432\n"
"MF v[128:131],v[96:99],a[160:163],v[128:131]\n"
"WT lgkmcnt(6)\n"
"MF v[128:131],v[100:103],a[176:179],v[128:131]\n"
"s_add_u32 s61, s60, 33664\n"
"s_add_u32 m0, s62, 0\n"
"TR v[116:117],v26 offset:24576\n"
"MF v[160:163],v[96:99],a[164:167],v[160:163]\n"
"LD v2,s[44:47],s61 offen offset:0 lds\n"
"MF v[160:163],v[100:103],a[180:183],v[160:163]\n"
"TR v[118:119],v26 offset:26624\n"
"MF v[192:195],v[96:99],a[168:171],v[192:195]\n"
"MF v[192:195],v[100:103],a[184:187],v[192:195]\n"
"TR v[120:121],v27 offset:16384\n"
"MF v[224:227],v[96:99],a[172:175],v[224:227]\n"
"LD v3,s[44:47],s61 offen offset:1024 lds\n"
"MF v[224:227],v[100:103],a[188:191],v[224:227]\n"
"WT lgkmcnt(7)\n"
"TR v[122:123],v27 offset:18432\n"
"MF v[132:135],v[104:107],a[160:163],v[132:135]\n"
"WT lgkmcnt(6)\n"
"MF v[132:135],v[108:111],a[176:179],v[132:135]\n"
"TR v[124:125],v27 offset:24576\n"
"MF v[164:167],v[104:107],a[164:167],v[164:167]\n"
"LD v4,s[44:47],s61 offen offset:2048 lds\n"
"MF v[164:167],v[108:111],a[180:183],v[164:167]\n"
"TR v[126:127],v27 offset:26624\n"
"MF v[196:199],v[104:107],a[168:171],v[196:199]\n"
"MF v[196:199],v[108:111],a[184:187],v[196:199]\n"
"TR v[96:97],v24 offset:20480\n"
"MF v[228:231],v[104:107],a[172:175],v[228:231]\n"
"MF v[228:231],v[108:111],a[188:191],v[228:231]\n"
"WT lgkmcnt(7)\n"
"TR v[98:99],v24 offset:22528\n"
"MF v[136:139],v[112:115],a[160:163],v[136:139]\n"
"WT lgkmcnt(6)\n"
"MF v[136:139],v[116:119],a[176:179],v[136:139]\n"
"TR v[100:101],v24 offset:28672\n"
"MF v[168:171],v[112:115],a[164:167],v[168:171]\n"
"LD v5,s[44:47],s61 offen offset:3072 lds\n"
"MF v[168:171],v[116:119],a[180:183],v[168:171]\n"
"TR v[102:103],v24 offset:30720\n"
"MF v[200:203],v[112:115],a[168:171],v[200:203]\n"
"MF v[200:203],v[116:119],a[184:187],v[200:203]\n"
"s_mov_b32 s65, s67\n"
"s_mov_b32 s48, s16\n"
"s_and_b32 s49, s17, 0xffff\n"
"s_add_u32 m0, s66, 131072\n"
"TR v[104:105],v25 offset:20480\n"
"MF v[232:235],v[112:115],a[172:175],v[232:235]\n"
"LD v6,s[48:51],s65 offen offset:0 lds\n"
"MF v[232:235],v[116:119],a[188:191],v[232:235]\n"
"WT lgkmcnt(7)\n"
"TR v[106:107],v25 offset:22528\n"
"MF v[140:143],v[120:123],a[160:163],v[140:143]\n"
"WT lgkmcnt(6)\n"
"MF v[140:143],v[124:127],a[176:179],v[140:143]\n"
"TR v[108:109],v25 offset:28672\n"
"MF v[172:175],v[120:123],a[164:167],v[172:175]\n"
"LD v7,s[48:51],s65 offen offset:1024 lds\n"
"MF v[172:175],v[124:127],a[180:183],v[172:175]\n"
"TR v[110:111],v25 offset:30720\n"
"MF v[204:207],v[120:123],a[168:171],v[204:207]\n"
"MF v[204:207],v[124:127],a[184:187],v[204:207]\n"
"TR v[112:113],v26 offset:20480\n"
"MF v[236:239],v[120:123],a[172:175],v[236:239]\n"
"MF v[236:239],v[124:127],a[188:191],v[236:239]\n"
"WT lgkmcnt(7)\n"
"TR v[114:115],v26 offset:22528\n"
"MF v[144:147],v[96:99],a[160:163],v[144:147]\n"
"WT lgkmcnt(6)\n"
"WT vmcnt(18)\n"
"s_barrier\n"
"MF v[144:147],v[100:103],a[176:179],v[144:147]\n"
"s_add_u32 m0, s66, 131072\n"
"RD a[192:195],v10 offset:8192\n"
"RD a[196:199],v10 offset:10240\n"
"MF v[176:179],v[96:99],a[164:167],v[176:179]\n"
"LD v8,s[48:51],s65 offen offset:2048 lds\n"
"MF v[176:179],v[100:103],a[180:183],v[176:179]\n"
"TR v[116:117],v26 offset:28672\n"
"MF v[208:211],v[96:99],a[168:171],v[208:211]\n"
"MF v[208:211],v[100:103],a[184:187],v[208:211]\n"
"RD a[208:211],v11 offset:8192\n"
"RD a[212:215],v11 offset:10240\n"
"MF v[240:243],v[96:99],a[172:175],v[240:243]\n"
"TR v[118:119],v26 offset:30720\n"
"MF v[240:243],v[100:103],a[188:191],v[240:243]\n"
"WT lgkmcnt(10)\n"
"TR v[120:121],v27 offset:20480\n"
"MF v[148:151],v[104:107],a[160:163],v[148:151]\n"
"WT lgkmcnt(9)\n"
"TR v[122:123],v27 offset:22528\n"
"MF v[148:151],v[108:111],a[176:179],v[148:151]\n"
"WT vmcnt(15)\n"
"RD a[200:203],v10 offset:12288\n"
"RD a[204:207],v10 offset:14336\n"
"MF v[180:183],v[104:107],a[164:167],v[180:183]\n"
"LD v9,s[48:51],s65 offen offset:3072 lds\n"
"MF v[180:183],v[108:111],a[180:183],v[180:183]\n"
"TR v[124:125],v27 offset:28672\n"
"MF v[212:215],v[104:107],a[168:171],v[212:215]\n"
"MF v[212:215],v[108:111],a[184:187],v[212:215]\n"
"RD a[216:219],v11 offset:12288\n"
"RD a[220:223],v11 offset:14336\n"
"MF v[244:247],v[104:107],a[172:175],v[244:247]\n"
"TR v[126:127],v27 offset:30720\n"
"MF v[244:247],v[108:111],a[188:191],v[244:247]\n"
"WT lgkmcnt(14)\n"
"TR v[96:97],v24 offset:32768\n"
"MF v[152:155],v[112:115],a[160:163],v[152:155]\n"
"WT lgkmcnt(9)\n"
"TR v[98:99],v24 offset:34816\n"
"MF v[152:155],v[116:119],a[176:179],v[152:155]\n"
"TR v[100:101],v24 offset:40960\n"
"MF v[184:187],v[112:115],a[164:167],v[184:187]\n"
"s_mov_b32 s56, s12\n"
"s_and_b32 s57, s13, 0xffff\n"
"s_mov_b32 s58, 0x500000\n"
"s_mov_b32 s59, 0x20000\n"
"v_lshrrev_b32 v23, 3, v0\n"
"v_min_u32 v23, 4, v23\n"
"v_lshlrev_b32 v23, 13, v23\n"
"v_and_b32 v22, 7, v0\n"
"v_lshl_add_u32 v23, v22, 7, v23\n"
"s_lshl_b32 s73, s37, 2\n"
"s_add_u32 s73, s73, s34\n"
"s_mul_i32 s73, s73, 40\n"
"s_add_u32 s73, s73, s72\n"
"s_lshl_b32 s73, s73, 10\n"
"buffer_load_dword v34, v23, s[56:59], s73 offen\n"
"MF v[184:187],v[116:119],a[180:183],v[184:187]\n"
"TR v[102:103],v24 offset:43008\n"
"MF v[216:219],v[112:115],a[168:171],v[216:219]\n"
"MF v[216:219],v[116:119],a[184:187],v[216:219]\n"
"TR v[104:105],v25 offset:32768\n"
"MF v[248:251],v[112:115],a[172:175],v[248:251]\n"
"MF v[248:251],v[116:119],a[188:191],v[248:251]\n"
"WT lgkmcnt(11)\n"
"TR v[106:107],v25 offset:34816\n"
"MF v[156:159],v[120:123],a[160:163],v[156:159]\n"
"WT lgkmcnt(6)\n"
"MF v[156:159],v[124:127],a[176:179],v[156:159]\n"
"TR v[108:109],v25 offset:40960\n"
"MF v[188:191],v[120:123],a[164:167],v[188:191]\n"
"s_mov_b32 s56, s22\n"
"s_and_b32 s57, s23, 0xffff\n"
"s_mov_b32 s58, 0xc8000\n"
"s_mov_b32 s59, 0x20000\n"
"v_min_u32 v23, 6, v0\n"
"v_lshlrev_b32 v23, 7, v23\n"
"s_lshl_b32 s73, s33, 2\n"
"s_add_u32 s73, s73, s34\n"
"s_mul_i32 s73, s73, 896\n"
"buffer_load_dword v34, v23, s[56:59], s73 offen\n"
"MF v[188:191],v[124:127],a[180:183],v[188:191]\n"
"TR v[110:111],v25 offset:43008\n"
"MF v[220:223],v[120:123],a[168:171],v[220:223]\n"
"MF v[220:223],v[124:127],a[184:187],v[220:223]\n"
"TR v[112:113],v26 offset:32768\n"
"MF v[252:255],v[120:123],a[172:175],v[252:255]\n"
"MF v[252:255],v[124:127],a[188:191],v[252:255]\n"
"WT lgkmcnt(7)\n"
"TR v[114:115],v26 offset:34816\n"
"MF v[128:131],v[96:99],a[192:195],v[128:131]\n"
"WT lgkmcnt(6)\n"
"MF v[128:131],v[100:103],a[208:211],v[128:131]\n"
"s_add_u32 s65, s65, 524288\n"
"s_add_u32 m0, s66, 147456\n"
"TR v[116:117],v26 offset:40960\n"
"MF v[160:163],v[96:99],a[196:199],v[160:163]\n"
"LD v6,s[48:51],s65 offen offset:0 lds\n"
"MF v[160:163],v[100:103],a[212:215],v[160:163]\n"
"TR v[118:119],v26 offset:43008\n"
"MF v[192:195],v[96:99],a[200:203],v[192:195]\n"
"v_lshrrev_b32 v21, 3, v0\n"
"v_and_b32 v22, 7, v0\n"
"MF v[192:195],v[100:103],a[216:219],v[192:195]\n"
"TR v[120:121],v27 offset:32768\n"
"MF v[224:227],v[96:99],a[204:207],v[224:227]\n"
"MF v[224:227],v[100:103],a[220:223],v[224:227]\n"
"WT lgkmcnt(7)\n"
"TR v[122:123],v27 offset:34816\n"
"MF v[132:135],v[104:107],a[192:195],v[132:135]\n"
"WT lgkmcnt(6)\n"
"v_and_b32 v23, 6, v21\n"
"v_xor_b32 v22, v22, v23\n"
"MF v[132:135],v[108:111],a[208:211],v[132:135]\n"
"TR v[124:125],v27 offset:40960\n"
"MF v[164:167],v[104:107],a[196:199],v[164:167]\n"
"LD v7,s[48:51],s65 offen offset:1024 lds\n"
"MF v[164:167],v[108:111],a[212:215],v[164:167]\n"
"TR v[126:127],v27 offset:43008\n"
"MF v[196:199],v[104:107],a[200:203],v[196:199]\n"
"v_lshlrev_b32 v22, 4, v22\n"
"s_mov_b32 s56, 1280\n"
"MF v[196:199],v[108:111],a[216:219],v[196:199]\n"
"TR v[96:97],v24 offset:36864\n"
"MF v[228:231],v[104:107],a[204:207],v[228:231]\n"
"MF v[228:231],v[108:111],a[220:223],v[228:231]\n"
"WT lgkmcnt(7)\n"
"TR v[98:99],v24 offset:38912\n"
"MF v[136:139],v[112:115],a[192:195],v[136:139]\n"
"WT lgkmcnt(6)\n"
"v_mad_u32_u24 v2, v21, s56, v22\n"
"v_add_u32 v3, 9216, v2\n"
"MF v[136:139],v[116:119],a[208:211],v[136:139]\n"
"s_add_u32 m0, s66, 147456\n"
"TR v[100:101],v24 offset:45056\n"
"MF v[168:171],v[112:115],a[196:199],v[168:171]\n"
"LD v8,s[48:51],s65 offen offset:2048 lds\n"
"MF v[168:171],v[116:119],a[212:215],v[168:171]\n"
"TR v[102:103],v24 offset:47104\n"
"MF v[200:203],v[112:115],a[200:203],v[200:203]\n"
"v_add_u32 v4, 18432, v2\n"
"v_add_u32 v5, 27648, v2\n"
"MF v[200:203],v[116:119],a[216:219],v[200:203]\n"
"TR v[104:105],v25 offset:36864\n"
"MF v[232:235],v[112:115],a[204:207],v[232:235]\n"
"MF v[232:235],v[116:119],a[220:223],v[232:235]\n"
"WT lgkmcnt(7)\n"
"TR v[106:107],v25 offset:38912\n"
"MF v[140:143],v[120:123],a[192:195],v[140:143]\n"
"WT lgkmcnt(6)\n"
"s_mov_b32 s52, s20\n"
"s_and_b32 s53, s21, 0xffff\n"
"MF v[140:143],v[124:127],a[208:211],v[140:143]\n"
"TR v[108:109],v25 offset:45056\n"
"MF v[172:175],v[120:123],a[196:199],v[172:175]\n"
"LD v9,s[48:51],s65 offen offset:3072 lds\n"
"MF v[172:175],v[124:127],a[212:215],v[172:175]\n"
"TR v[110:111],v25 offset:47104\n"
"MF v[204:207],v[120:123],a[200:203],v[204:207]\n"
"s_mov_b32 s54, s69\n"
"s_mov_b32 s55, 0x20000\n"
"MF v[204:207],v[124:127],a[216:219],v[204:207]\n"
"TR v[112:113],v26 offset:36864\n"
"MF v[236:239],v[120:123],a[204:207],v[236:239]\n"
"MF v[236:239],v[124:127],a[220:223],v[236:239]\n"
"WT lgkmcnt(7)\n"
"TR v[114:115],v26 offset:38912\n"
"MF v[144:147],v[96:99],a[192:195],v[144:147]\n"
"WT lgkmcnt(6)\n"
"WT vmcnt(14)\n"
"s_barrier\n"
"MF v[144:147],v[100:103],a[208:211],v[144:147]\n"
"s_add_u32 s65, s65, 524288\n"
"s_add_u32 m0, s66, 81920\n"
"RD a[224:227],v10 offset:16384\n"
"RD a[228:231],v10 offset:18432\n"
"MF v[176:179],v[96:99],a[196:199],v[176:179]\n"
"LD v6,s[48:51],s65 offen offset:0 lds\n"
"MF v[176:179],v[100:103],a[212:215],v[176:179]\n"
"TR v[116:117],v26 offset:45056\n"
"MF v[208:211],v[96:99],a[200:203],v[208:211]\n"
"s_mul_i32 s10, s34, 40960\n"
"s_mul_i32 s56, s71, 128\n"
"MF v[208:211],v[100:103],a[216:219],v[208:211]\n"
"RD a[240:243],v11 offset:16384\n"
"RD a[244:247],v11 offset:18432\n"
"MF v[240:243],v[96:99],a[204:207],v[240:243]\n"
"TR v[118:119],v26 offset:47104\n"
"MF v[240:243],v[100:103],a[220:223],v[240:243]\n"
"WT lgkmcnt(10)\n"
"TR v[120:121],v27 offset:36864\n"
"MF v[148:151],v[104:107],a[192:195],v[148:151]\n"
"WT lgkmcnt(9)\n"
"TR v[122:123],v27 offset:38912\n"
"s_add_u32 s10, s10, s56\n"
"s_lshl_b32 s11, s34, 12\n"
"MF v[148:151],v[108:111],a[208:211],v[148:151]\n"
"WT vmcnt(11)\n"
"RD a[232:235],v10 offset:0\n"
"RD a[236:239],v10 offset:2048\n"
"MF v[180:183],v[104:107],a[196:199],v[180:183]\n"
"LD v7,s[48:51],s65 offen offset:1024 lds\n"
"MF v[180:183],v[108:111],a[212:215],v[180:183]\n"
"TR v[124:125],v27 offset:45056\n"
"MF v[212:215],v[104:107],a[200:203],v[212:215]\n"
"s_add_u32 s11, s11, 0\n"
"s_lshl_b32 s14, s70, 2\n"
"MF v[212:215],v[108:111],a[216:219],v[212:215]\n"
"RD a[248:251],v11 offset:0\n"
"RD a[252:255],v11 offset:2048\n"
"MF v[244:247],v[104:107],a[204:207],v[244:247]\n"
"TR v[126:127],v27 offset:47104\n"
"MF v[244:247],v[108:111],a[220:223],v[244:247]\n"
"WT lgkmcnt(14)\n"
"TR v[96:97],v24 offset:49152\n"
"MF v[152:155],v[112:115],a[192:195],v[152:155]\n"
"WT lgkmcnt(9)\n"
"TR v[98:99],v24 offset:51200\n"
"s_add_u32 s14, s14, s34\n"
"s_mul_i32 s14, s14, 40960\n"
"MF v[152:155],v[116:119],a[208:211],v[152:155]\n"
"s_add_u32 m0, s66, 81920\n"
"TR v[100:101],v24 offset:57344\n"
"MF v[184:187],v[112:115],a[196:199],v[184:187]\n"
"LD v8,s[48:51],s65 offen offset:2048 lds\n"
"MF v[184:187],v[116:119],a[212:215],v[184:187]\n"
"TR v[102:103],v24 offset:59392\n"
"MF v[216:219],v[112:115],a[200:203],v[216:219]\n"
"s_add_u32 s14, s14, 0x400000\n"
"MF v[216:219],v[116:119],a[216:219],v[216:219]\n"
"TR v[104:105],v25 offset:49152\n"
"MF v[248:251],v[112:115],a[204:207],v[248:251]\n"
"MF v[248:251],v[116:119],a[220:223],v[248:251]\n"
"WT lgkmcnt(11)\n"
"TR v[106:107],v25 offset:51200\n"
"MF v[156:159],v[120:123],a[192:195],v[156:159]\n"
"WT lgkmcnt(6)\n"
"MF v[156:159],v[124:127],a[208:211],v[156:159]\n"
"TR v[108:109],v25 offset:57344\n"
"MF v[188:191],v[120:123],a[196:199],v[188:191]\n"
"LD v9,s[48:51],s65 offen offset:3072 lds\n"
"MF v[188:191],v[124:127],a[212:215],v[188:191]\n"
"TR v[110:111],v25 offset:59392\n"
"MF v[220:223],v[120:123],a[200:203],v[220:223]\n"
"MF v[220:223],v[124:127],a[216:219],v[220:223]\n"
"TR v[112:113],v26 offset:49152\n"
"MF v[252:255],v[120:123],a[204:207],v[252:255]\n"
"MF v[252:255],v[124:127],a[220:223],v[252:255]\n"
"WT lgkmcnt(7)\n"
"TR v[114:115],v26 offset:51200\n"
"MF v[128:131],v[96:99],a[224:227],v[128:131]\n"
"WT lgkmcnt(6)\n"
"WT vmcnt(4)\n"
"MF v[128:131],v[100:103],a[240:243],v[128:131]\n"
"TR v[116:117],v26 offset:57344\n"
"MF v[160:163],v[96:99],a[228:231],v[160:163]\n"
"MF v[160:163],v[100:103],a[244:247],v[160:163]\n"
"TR v[118:119],v26 offset:59392\n"
"MF v[192:195],v[96:99],a[232:235],v[192:195]\n"
"MF v[192:195],v[100:103],a[248:251],v[192:195]\n"
"TR v[120:121],v27 offset:49152\n"
"MF v[224:227],v[96:99],a[236:239],v[224:227]\n"
"MF v[224:227],v[100:103],a[252:255],v[224:227]\n"
"WT lgkmcnt(7)\n"
"TR v[122:123],v27 offset:51200\n"
"MF v[132:135],v[104:107],a[224:227],v[132:135]\n"
"WT lgkmcnt(6)\n"
"MF v[132:135],v[108:111],a[240:243],v[132:135]\n"
"TR v[124:125],v27 offset:57344\n"
"MF v[164:167],v[104:107],a[228:231],v[164:167]\n"
"MF v[164:167],v[108:111],a[244:247],v[164:167]\n"
"TR v[126:127],v27 offset:59392\n"
"MF v[196:199],v[104:107],a[232:235],v[196:199]\n"
"MF v[196:199],v[108:111],a[248:251],v[196:199]\n"
"TR v[96:97],v24 offset:53248\n"
"MF v[228:231],v[104:107],a[236:239],v[228:231]\n"
"CV v32,v128,v129\n"
"CV v33,v130,v131\n"
"MF v[228:231],v[108:111],a[252:255],v[228:231]\n"
"WT lgkmcnt(7)\n"
"TR v[98:99],v24 offset:55296\n"
"CV v34,v132,v133\n"
"MF v[136:139],v[112:115],a[224:227],v[136:139]\n"
"WT lgkmcnt(6)\n"
"CV v35,v134,v135\n"
"CV v48,v160,v161\n"
"MF v[136:139],v[116:119],a[240:243],v[136:139]\n"
"TR v[100:101],v24 offset:61440\n"
"CV v49,v162,v163\n"
"MF v[168:171],v[112:115],a[228:231],v[168:171]\n"
"CV v64,v192,v193\n"
"CV v65,v194,v195\n"
"MF v[168:171],v[116:119],a[244:247],v[168:171]\n"
"TR v[102:103],v24 offset:63488\n"
"CV v66,v196,v197\n"
"MF v[200:203],v[112:115],a[232:235],v[200:203]\n"
"CV v67,v198,v199\n"
"CV v50,v164,v165\n"
"MF v[200:203],v[116:119],a[248:251],v[200:203]\n"
"TR v[104:105],v25 offset:53248\n"
"CV v51,v166,v167\n"
"MF v[232:235],v[112:115],a[236:239],v[232:235]\n"
"CV v80,v224,v225\n"
"CV v81,v226,v227\n"
"MF v[232:235],v[116:119],a[252:255],v[232:235]\n"
"WT lgkmcnt(7)\n"
"TR v[106:107],v25 offset:55296\n"
"CV v82,v228,v229\n"
"MF v[140:143],v[120:123],a[224:227],v[140:143]\n"
"WT lgkmcnt(6)\n"
"CV v83,v230,v231\n"
"MF v[140:143],v[124:127],a[240:243],v[140:143]\n"
"TR v[108:109],v25 offset:61440\n"
"MF v[172:175],v[120:123],a[228:231],v[172:175]\n"
"MF v[172:175],v[124:127],a[244:247],v[172:175]\n"
"TR v[110:111],v25 offset:63488\n"
"MF v[204:207],v[120:123],a[232:235],v[204:207]\n"
"MF v[204:207],v[124:127],a[248:251],v[204:207]\n"
"TR v[112:113],v26 offset:53248\n"
"MF v[236:239],v[120:123],a[236:239],v[236:239]\n"
"CV v36,v136,v137\n"
"CV v37,v138,v139\n"
"MF v[236:239],v[124:127],a[252:255],v[236:239]\n"
"WT lgkmcnt(7)\n"
"TR v[114:115],v26 offset:55296\n"
"CV v38,v140,v141\n"
"MF v[144:147],v[96:99],a[224:227],v[144:147]\n"
"WT lgkmcnt(6)\n"
"s_barrier\n"
"CV v39,v142,v143\n"
"CV v52,v168,v169\n"
"MF v[144:147],v[100:103],a[240:243],v[144:147]\n"
"s_add_u32 s65, s65, 524288\n"
"s_add_u32 m0, s66, 98304\n"
"TR v[116:117],v26 offset:61440\n"
"CV v53,v170,v171\n"
"MF v[176:179],v[96:99],a[228:231],v[176:179]\n"
"LD v6,s[48:51],s65 offen offset:0 lds\n"
"MF v[176:179],v[100:103],a[244:247],v[176:179]\n"
"TR v[118:119],v26 offset:63488\n"
"CV v68,v200,v201\n"
"MF v[208:211],v[96:99],a[232:235],v[208:211]\n"
"CV v69,v202,v203\n"
"CV v70,v204,v205\n"
"MF v[208:211],v[100:103],a[248:251],v[208:211]\n"
"TR v[120:121],v27 offset:53248\n"
"CV v71,v206,v207\n"
"MF v[240:243],v[96:99],a[236:239],v[240:243]\n"
"CV v54,v172,v173\n"
"CV v55,v174,v175\n"
"MF v[240:243],v[100:103],a[252:255],v[240:243]\n"
"WT lgkmcnt(7)\n"
"TR v[122:123],v27 offset:55296\n"
"CV v84,v232,v233\n"
"MF v[148:151],v[104:107],a[224:227],v[148:151]\n"
"WT lgkmcnt(6)\n"
"CV v85,v234,v235\n"
"CV v86,v236,v237\n"
"MF v[148:151],v[108:111],a[240:243],v[148:151]\n"
"TR v[124:125],v27 offset:61440\n"
"CV v87,v238,v239\n"
"MF v[180:183],v[104:107],a[228:231],v[180:183]\n"
"LD v7,s[48:51],s65 offen offset:1024 lds\n"
"MF v[180:183],v[108:111],a[244:247],v[180:183]\n"
"TR v[126:127],v27 offset:63488\n"
"MF v[212:215],v[104:107],a[232:235],v[212:215]\n"
"MF v[212:215],v[108:111],a[248:251],v[212:215]\n"
"TR v[96:97],v28 offset:0\n"
"MF v[244:247],v[104:107],a[236:239],v[244:247]\n"
"CV v40,v144,v145\n"
"CV v41,v146,v147\n"
"MF v[244:247],v[108:111],a[252:255],v[244:247]\n"
"WT lgkmcnt(7)\n"
"TR v[98:99],v28 offset:2048\n"
"CV v42,v148,v149\n"
"MF v[152:155],v[112:115],a[224:227],v[152:155]\n"
"WT lgkmcnt(6)\n"
"CV v43,v150,v151\n"
"CV v56,v176,v177\n"
"MF v[152:155],v[116:119],a[240:243],v[152:155]\n"
"s_add_u32 m0, s66, 98304\n"
"TR v[100:101],v28 offset:8192\n"
"CV v57,v178,v179\n"
"MF v[184:187],v[112:115],a[228:231],v[184:187]\n"
"LD v8,s[48:51],s65 offen offset:2048 lds\n"
"MF v[184:187],v[116:119],a[244:247],v[184:187]\n"
"TR v[102:103],v28 offset:10240\n"
"CV v72,v208,v209\n"
"MF v[216:219],v[112:115],a[232:235],v[216:219]\n"
"CV v73,v210,v211\n"
"CV v74,v212,v213\n"
"MF v[216:219],v[116:119],a[248:251],v[216:219]\n"
"TR v[104:105],v28 offset:16384\n"
"CV v75,v214,v215\n"
"MF v[248:251],v[112:115],a[236:239],v[248:251]\n"
"CV v58,v180,v181\n"
"CV v59,v182,v183\n"
"MF v[248:251],v[116:119],a[252:255],v[248:251]\n"
"WT lgkmcnt(7)\n"
"TR v[106:107],v28 offset:18432\n"
"CV v88,v240,v241\n"
"MF v[156:159],v[120:123],a[224:227],v[156:159]\n"
"WT lgkmcnt(6)\n"
"CV v89,v242,v243\n"
"CV v90,v244,v245\n"
"MF v[156:159],v[124:127],a[240:243],v[156:159]\n"
"TR v[108:109],v28 offset:24576\n"
"CV v91,v246,v247\n"
"MF v[188:191],v[120:123],a[228:231],v[188:191]\n"
"LD v9,s[48:51],s65 offen offset:3072 lds\n"
"MF v[188:191],v[124:127],a[244:247],v[188:191]\n"
"TR v[110:111],v28 offset:26624\n"
"MF v[220:223],v[120:123],a[232:235],v[220:223]\n"
"MF v[220:223],v[124:127],a[248:251],v[220:223]\n"
"TR v[112:113],v29 offset:0\n"
"MF v[252:255],v[120:123],a[236:239],v[252:255]\n"
"CV v44,v152,v153\n"
"CV v45,v154,v155\n"
"MF v[252:255],v[124:127],a[252:255],v[252:255]\n"
"WT lgkmcnt(7)\n"
"TR v[114:115],v29 offset:2048\n"
"CV v46,v156,v157\n"
"MF v[128:131],a[0:3],v[96:99],0\n"
"WT lgkmcnt(6)\n"
"s_barrier\n"
"CV v47,v158,v159\n"
"CV v60,v184,v185\n"
"MF v[128:131],a[16:19],v[100:103],v[128:131]\n"
"WT lgkmcnt(4)\n"
"s_add_u32 s65, s65, 524288\n"
"s_add_u32 m0, s66, 114688\n"
"TR v[116:117],v29 offset:8192\n"
"CV v61,v186,v187\n"
"MF v[128:131],a[32:35],v[104:107],v[128:131]\n"
"WT lgkmcnt(3)\n"
"LD v6,s[48:51],s65 offen offset:0 lds\n"
"MF v[128:131],a[48:51],v[108:111],v[128:131]\n"
"TR v[118:119],v29 offset:10240\n"
"CV v76,v216,v217\n"
"MF v[132:135],a[4:7],v[96:99],0\n"
"CV v77,v218,v219\n"
"CV v78,v220,v221\n"
"MF v[132:135],a[20:23],v[100:103],v[132:135]\n"
"TR v[120:121],v29 offset:16384\n"
"CV v79,v222,v223\n"
"MF v[132:135],a[36:39],v[104:107],v[132:135]\n"
"LD v7,s[48:51],s65 offen offset:1024 lds\n"
"MF v[132:135],a[52:55],v[108:111],v[132:135]\n"
"TR v[122:123],v29 offset:18432\n"
"CV v62,v188,v189\n"
"MF v[192:195],a[8:11],v[96:99],0\n"
"CV v63,v190,v191\n"
"CV v92,v248,v249\n"
"MF v[192:195],a[24:27],v[100:103],v[192:195]\n"
"TR v[124:125],v29 offset:24576\n"
"CV v93,v250,v251\n"
"MF v[192:195],a[40:43],v[104:107],v[192:195]\n"
"LD v8,s[48:51],s65 offen offset:2048 lds\n"
"MF v[192:195],a[56:59],v[108:111],v[192:195]\n"
"TR v[126:127],v29 offset:26624\n"
"CV v94,v252,v253\n"
"MF v[196:199],a[12:15],v[96:99],0\n"
"CV v95,v254,v255\n"
"MF v[196:199],a[28:31],v[100:103],v[196:199]\n"
"TR v[96:97],v30 offset:0\n"
"MF v[196:199],a[44:47],v[104:107],v[196:199]\n"
"LD v9,s[48:51],s65 offen offset:3072 lds\n"
"MF v[196:199],a[60:63],v[108:111],v[196:199]\n"
"WT lgkmcnt(7)\n"
"TR v[98:99],v30 offset:2048\n"
"MF v[136:139],a[0:3],v[112:115],0\n"
"WT lgkmcnt(6)\n"
"MF v[136:139],a[16:19],v[116:119],v[136:139]\n"
"WT lgkmcnt(4)\n"
"s_add_u32 m0, s11, 0\n"
"TR v[100:101],v30 offset:8192\n"
"MF v[136:139],a[32:35],v[120:123],v[136:139]\n"
"WT lgkmcnt(3)\n"
"LD v2,s[52:55],s10 offen offset:0 lds\n"
"MF v[136:139],a[48:51],v[124:127],v[136:139]\n"
"TR v[102:103],v30 offset:10240\n"
"MF v[140:143],a[4:7],v[112:115],0\n"
"MF v[140:143],a[20:23],v[116:119],v[140:143]\n"
"TR v[104:105],v30 offset:16384\n"
"MF v[140:143],a[36:39],v[120:123],v[140:143]\n"
"LD v3,s[52:55],s10 offen offset:1024 lds\n"
"MF v[140:143],a[52:55],v[124:127],v[140:143]\n"
"TR v[106:107],v30 offset:18432\n"
"MF v[200:203],a[8:11],v[112:115],0\n"
"MF v[200:203],a[24:27],v[116:119],v[200:203]\n"
"TR v[108:109],v30 offset:24576\n"
"MF v[200:203],a[40:43],v[120:123],v[200:203]\n"
"LD v4,s[52:55],s10 offen offset:2048 lds\n"
"MF v[200:203],a[56:59],v[124:127],v[200:203]\n"
"TR v[110:111],v30 offset:26624\n"
"MF v[204:207],a[12:15],v[112:115],0\n"
"MF v[204:207],a[28:31],v[116:119],v[204:207]\n"
"TR v[112:113],v31 offset:0\n"
"MF v[204:207],a[44:47],v[120:123],v[204:207]\n"
"LD v5,s[52:55],s10 offen offset:3072 lds\n"
"MF v[204:207],a[60:63],v[124:127],v[204:207]\n"
"WT lgkmcnt(7)\n"
"TR v[114:115],v31 offset:2048\n"
"MF v[144:147],a[0:3],v[96:99],0\n"
"WT lgkmcnt(6)\n"
"MF v[144:147],a[16:19],v[100:103],v[144:147]\n"
"WT lgkmcnt(4)\n"
"s_add_u32 s10, s10, 163840\n"
"s_add_u32 m0, s11, 16384\n"
"TR v[116:117],v31 offset:8192\n"
"MF v[144:147],a[32:35],v[104:107],v[144:147]\n"
"WT lgkmcnt(3)\n"
"LD v2,s[52:55],s10 offen offset:0 lds\n"
"MF v[144:147],a[48:51],v[108:111],v[144:147]\n"
"TR v[118:119],v31 offset:10240\n"
"MF v[148:151],a[4:7],v[96:99],0\n"
"MF v[148:151],a[20:23],v[100:103],v[148:151]\n"
"TR v[120:121],v31 offset:16384\n"
"MF v[148:151],a[36:39],v[104:107],v[148:151]\n"
"LD v3,s[52:55],s10 offen offset:1024 lds\n"
"MF v[148:151],a[52:55],v[108:111],v[148:151]\n"
"TR v[122:123],v31 offset:18432\n"
"MF v[208:211],a[8:11],v[96:99],0\n"
"MF v[208:211],a[24:27],v[100:103],v[208:211]\n"
"TR v[124:125],v31 offset:24576\n"
"MF v[208:211],a[40:43],v[104:107],v[208:211]\n"
"LD v4,s[52:55],s10 offen offset:2048 lds\n"
"MF v[208:211],a[56:59],v[108:111],v[208:211]\n"
"TR v[126:127],v31 offset:26624\n"
"MF v[212:215],a[12:15],v[96:99],0\n"
"MF v[212:215],a[28:31],v[100:103],v[212:215]\n"
"TR v[96:97],v28 offset:4096\n"
"MF v[212:215],a[44:47],v[104:107],v[212:215]\n"
"MF v[212:215],a[60:63],v[108:111],v[212:215]\n"
"WT lgkmcnt(7)\n"
"TR v[98:99],v28 offset:6144\n"
"MF v[152:155],a[0:3],v[112:115],0\n"
"WT lgkmcnt(6)\n"
"MF v[152:155],a[16:19],v[116:119],v[152:155]\n"
"WT lgkmcnt(4)\n"
"TR v[100:101],v28 offset:12288\n"
"MF v[152:155],a[32:35],v[120:123],v[152:155]\n"
"WT lgkmcnt(3)\n"
"LD v5,s[52:55],s10 offen offset:3072 lds\n"
"MF v[152:155],a[48:51],v[124:127],v[152:155]\n"
"TR v[102:103],v28 offset:14336\n"
"MF v[156:159],a[4:7],v[112:115],0\n"
"MF v[156:159],a[20:23],v[116:119],v[156:159]\n"
"s_add_u32 s64, s80, 0\n"
"TR v[104:105],v28 offset:20480\n"
"MF v[156:159],a[36:39],v[120:123],v[156:159]\n"
"ST v[32:35],v1,s[40:43],s64 offen offset:0 sc1\n"
"MF v[156:159],a[52:55],v[124:127],v[156:159]\n"
"TR v[106:107],v28 offset:22528\n"
"MF v[216:219],a[8:11],v[112:115],0\n"
"MF v[216:219],a[24:27],v[116:119],v[216:219]\n"
"TR v[108:109],v28 offset:28672\n"
"MF v[216:219],a[40:43],v[120:123],v[216:219]\n"
"ST v[36:39],v1,s[40:43],s64 offen offset:1024 sc1\n"
"MF v[216:219],a[56:59],v[124:127],v[216:219]\n"
"TR v[110:111],v28 offset:30720\n"
"MF v[220:223],a[12:15],v[112:115],0\n"
"MF v[220:223],a[28:31],v[116:119],v[220:223]\n"
"TR v[112:113],v29 offset:4096\n"
"MF v[220:223],a[44:47],v[120:123],v[220:223]\n"
"buffer_load_dwordx2 v[18:19], v20, s[40:43], 0 offen sc1\n"
"MF v[220:223],a[60:63],v[124:127],v[220:223]\n"
"WT lgkmcnt(7)\n"
"TR v[114:115],v29 offset:6144\n"
"MF v[160:163],a[0:3],v[96:99],0\n"
"WT lgkmcnt(6)\n"
"MF v[160:163],a[16:19],v[100:103],v[160:163]\n"
"WT lgkmcnt(4)\n"
"s_add_u32 s10, s10, 163840\n"
"s_add_u32 m0, s11, 32768\n"
"TR v[116:117],v29 offset:12288\n"
"MF v[160:163],a[32:35],v[104:107],v[160:163]\n"
"WT lgkmcnt(3)\n"
"LD v2,s[52:55],s10 offen offset:0 lds\n"
"MF v[160:163],a[48:51],v[108:111],v[160:163]\n"
"TR v[118:119],v29 offset:14336\n"
"MF v[164:167],a[4:7],v[96:99],0\n"
"s_lshl_b32 s56, s36, 3\n"
"s_add_u32 s57, s56, s34\n"
"MF v[164:167],a[20:23],v[100:103],v[164:167]\n"
"TR v[120:121],v29 offset:20480\n"
"MF v[164:167],a[36:39],v[104:107],v[164:167]\n"
"LD v3,s[52:55],s10 offen offset:1024 lds\n"
"MF v[164:167],a[52:55],v[108:111],v[164:167]\n"
"TR v[122:123],v29 offset:22528\n"
"MF v[224:227],a[8:11],v[96:99],0\n"
"s_sub_u32 s58, s34, s56\n"
"s_add_u32 s58, s58, 120\n"
"MF v[224:227],a[24:27],v[100:103],v[224:227]\n"
"TR v[124:125],v29 offset:28672\n"
"MF v[224:227],a[40:43],v[104:107],v[224:227]\n"
"LD v4,s[52:55],s10 offen offset:2048 lds\n"
"MF v[224:227],a[56:59],v[108:111],v[224:227]\n"
"TR v[126:127],v29 offset:30720\n"
"MF v[228:231],a[12:15],v[96:99],0\n"
"s_mul_i32 s60, s57, 20480\n"
"s_add_u32 s60, s60, 1048576\n"
"MF v[228:231],a[28:31],v[100:103],v[228:231]\n"
"TR v[96:97],v30 offset:4096\n"
"MF v[228:231],a[44:47],v[104:107],v[228:231]\n"
"MF v[228:231],a[60:63],v[108:111],v[228:231]\n"
"WT lgkmcnt(7)\n"
"TR v[98:99],v30 offset:6144\n"
"MF v[168:171],a[0:3],v[112:115],0\n"
"WT lgkmcnt(6)\n"
"s_add_u32 s61, s60, 81920\n"
"s_mul_i32 s62, s58, 20480\n"
"MF v[168:171],a[16:19],v[116:119],v[168:171]\n"
"WT lgkmcnt(4)\n"
"TR v[100:101],v30 offset:12288\n"
"MF v[168:171],a[32:35],v[120:123],v[168:171]\n"
"WT lgkmcnt(3)\n"
"LD v5,s[52:55],s10 offen offset:3072 lds\n"
"MF v[168:171],a[48:51],v[124:127],v[168:171]\n"
"TR v[102:103],v30 offset:14336\n"
"MF v[172:175],a[4:7],v[112:115],0\n"
"s_add_u32 s62, s62, 1048576\n"
"s_add_u32 s63, s62, 81920\n"
"MF v[172:175],a[20:23],v[116:119],v[172:175]\n"
"TR v[104:105],v30 offset:20480\n"
"MF v[172:175],a[36:39],v[120:123],v[172:175]\n"
"ST v[40:43],v1,s[40:43],s64 offen offset:2048 sc1\n"
"MF v[172:175],a[52:55],v[124:127],v[172:175]\n"
"TR v[106:107],v30 offset:22528\n"
"MF v[232:235],a[8:11],v[112:115],0\n"
"s_add_u32 s56, s14, 20480\n"
"s_add_u32 s57, s14, 10240\n"
"MF v[232:235],a[24:27],v[116:119],v[232:235]\n"
"TR v[108:109],v30 offset:28672\n"
"MF v[232:235],a[40:43],v[120:123],v[232:235]\n"
"ST v[44:47],v1,s[40:43],s64 offen offset:3072 sc1\n"
"MF v[232:235],a[56:59],v[124:127],v[232:235]\n"
"TR v[110:111],v30 offset:30720\n"
"MF v[236:239],a[12:15],v[112:115],0\n"
"s_add_u32 s58, s14, 30720\n"
"v_mov_b32 v10, v1\n"
"MF v[236:239],a[28:31],v[116:119],v[236:239]\n"
"TR v[112:113],v31 offset:4096\n"
"MF v[236:239],a[44:47],v[120:123],v[236:239]\n"
"MF v[236:239],a[60:63],v[124:127],v[236:239]\n"
"WT lgkmcnt(7)\n"
"TR v[114:115],v31 offset:6144\n"
"MF v[176:179],a[0:3],v[96:99],0\n"
"WT lgkmcnt(6)\n"
"WT vmcnt(21)\n"
"s_barrier\n"
"MF v[176:179],a[16:19],v[100:103],v[176:179]\n"
"WT lgkmcnt(4)\n"
"s_add_u32 s10, s10, 163840\n"
"s_add_u32 m0, s11, 49152\n"
"TR v[116:117],v31 offset:12288\n"
"MF v[176:179],a[32:35],v[104:107],v[176:179]\n"
"WT lgkmcnt(3)\n"
"LD v2,s[52:55],s10 offen offset:0 lds\n"
"MF v[176:179],a[48:51],v[108:111],v[176:179]\n"
"TR v[118:119],v31 offset:14336\n"
"MF v[180:183],a[4:7],v[96:99],0\n"
"v_add_u32 v11, 4096, v1\n"
"MF v[180:183],a[20:23],v[100:103],v[180:183]\n"
"TR v[120:121],v31 offset:20480\n"
"MF v[180:183],a[36:39],v[104:107],v[180:183]\n"
"LD v3,s[52:55],s10 offen offset:1024 lds\n"
"MF v[180:183],a[52:55],v[108:111],v[180:183]\n"
"TR v[122:123],v31 offset:22528\n"
"MF v[240:243],a[8:11],v[96:99],0\n"
"s_cmp_ge_u32 s33, 96\n"
"s_cselect_b32 s60, s14, s60\n"
"s_cselect_b32 s61, s56, s61\n"
"s_cselect_b32 s62, s57, s62\n"
"s_cselect_b32 s63, s58, s63\n"
"MF v[240:243],a[24:27],v[100:103],v[240:243]\n"
"TR v[124:125],v31 offset:28672\n"
"MF v[240:243],a[40:43],v[104:107],v[240:243]\n"
"LD v4,s[52:55],s10 offen offset:2048 lds\n"
"MF v[240:243],a[56:59],v[108:111],v[240:243]\n"
"TR v[126:127],v31 offset:30720\n"
"MF v[244:247],a[12:15],v[96:99],0\n"
"MF v[244:247],a[28:31],v[100:103],v[244:247]\n"
"TR v[96:97],v24 offset:16384\n"
"MF v[244:247],a[44:47],v[104:107],v[244:247]\n"
"MF v[244:247],a[60:63],v[108:111],v[244:247]\n"
"WT lgkmcnt(7)\n"
"TR v[98:99],v24 offset:18432\n"
"MF v[184:187],a[0:3],v[112:115],0\n"
"WT lgkmcnt(6)\n"
"MF v[184:187],a[16:19],v[116:119],v[184:187]\n"
"WT lgkmcnt(4)\n"
"TR v[100:101],v24 offset:24576\n"
"MF v[184:187],a[32:35],v[120:123],v[184:187]\n"
"WT lgkmcnt(3)\n"
"LD v5,s[52:55],s10 offen offset:3072 lds\n"
"MF v[184:187],a[48:51],v[124:127],v[184:187]\n"
"TR v[102:103],v24 offset:26624\n"
"MF v[188:191],a[4:7],v[112:115],0\n"
"MF v[188:191],a[20:23],v[116:119],v[188:191]\n"
"s_add_u32 s64, s80, 4096\n"
"TR v[104:105],v24 offset:32768\n"
"MF v[188:191],a[36:39],v[120:123],v[188:191]\n"
"ST v[48:51],v1,s[40:43],s64 offen offset:0 sc1\n"
"MF v[188:191],a[52:55],v[124:127],v[188:191]\n"
"TR v[106:107],v24 offset:34816\n"
"MF v[248:251],a[8:11],v[112:115],0\n"
"MF v[248:251],a[24:27],v[116:119],v[248:251]\n"
"TR v[108:109],v24 offset:40960\n"
"MF v[248:251],a[40:43],v[120:123],v[248:251]\n"
"ST v[52:55],v1,s[40:43],s64 offen offset:1024 sc1\n"
"MF v[248:251],a[56:59],v[124:127],v[248:251]\n"
"TR v[110:111],v24 offset:43008\n"
"MF v[252:255],a[12:15],v[112:115],0\n"
"WT vmcnt(12)\n"
"v_cmp_ne_u64 vcc, s[38:39], v[18:19]\n"
"s_nop 1\n"
"s_cmp_eq_u64 vcc, 0\n"
"s_cbranch_scc0 L_psp_9\n"
"L_pok_8:\n"
"MF v[252:255],a[28:31],v[116:119],v[252:255]\n"
"TR v[112:113],v25 offset:16384\n"
"MF v[252:255],a[44:47],v[120:123],v[252:255]\n"
"MF v[252:255],a[60:63],v[124:127],v[252:255]\n"
"WT lgkmcnt(7)\n"
"TR v[114:115],v25 offset:18432\n"
"MF v[128:131],a[64:67],v[96:99],v[128:131]\n"
"WT lgkmcnt(6)\n"
"s_barrier\n"
"MF v[128:131],a[80:83],v[100:103],v[128:131]\n"
"WT lgkmcnt(4)\n"
"s_add_u32 s65, s65, 524288\n"
"s_add_u32 m0, s66, 131072\n"
"TR v[116:117],v25 offset:24576\n"
"MF v[128:131],a[96:99],v[104:107],v[128:131]\n"
"WT lgkmcnt(3)\n"
"LD v6,s[48:51],s65 offen offset:0 lds\n"
"MF v[128:131],a[112:115],v[108:111],v[128:131]\n"
"TR v[118:119],v25 offset:26624\n"
"MF v[132:135],a[68:71],v[96:99],v[132:135]\n"
"MF v[132:135],a[84:87],v[100:103],v[132:135]\n"
"TR v[120:121],v25 offset:32768\n"
"MF v[132:135],a[100:103],v[104:107],v[132:135]\n"
"LD v7,s[48:51],s65 offen offset:1024 lds\n"
"MF v[132:135],a[116:119],v[108:111],v[132:135]\n"
"TR v[122:123],v25 offset:34816\n"
"MF v[192:195],a[72:75],v[96:99],v[192:195]\n"
"MF v[192:195],a[88:91],v[100:103],v[192:195]\n"
"TR v[124:125],v25 offset:40960\n"
"MF v[192:195],a[104:107],v[104:107],v[192:195]\n"
"LD v8,s[48:51],s65 offen offset:2048 lds\n"
"MF v[192:195],a[120:123],v[108:111],v[192:195]\n"
"TR v[126:127],v25 offset:43008\n"
"MF v[196:199],a[76:79],v[96:99],v[196:199]\n"
"MF v[196:199],a[92:95],v[100:103],v[196:199]\n"
"TR v[96:97],v26 offset:16384\n"
"MF v[196:199],a[108:111],v[104:107],v[196:199]\n"
"LD v9,s[48:51],s65 offen offset:3072 lds\n"
"MF v[196:199],a[124:127],v[108:111],v[196:199]\n"
"WT lgkmcnt(7)\n"
"TR v[98:99],v26 offset:18432\n"
"MF v[136:139],a[64:67],v[112:115],v[136:139]\n"
"WT lgkmcnt(6)\n"
"MF v[136:139],a[80:83],v[116:119],v[136:139]\n"
"WT lgkmcnt(4)\n"
"TR v[100:101],v26 offset:24576\n"
"MF v[136:139],a[96:99],v[120:123],v[136:139]\n"
"WT lgkmcnt(3)\n"
"LD a[0:3],v10,s[40:43],s60 offen offset:0 sc1\n"
"MF v[136:139],a[112:115],v[124:127],v[136:139]\n"
"TR v[102:103],v26 offset:26624\n"
"MF v[140:143],a[68:71],v[112:115],v[140:143]\n"
"MF v[140:143],a[84:87],v[116:119],v[140:143]\n"
"TR v[104:105],v26 offset:32768\n"
"MF v[140:143],a[100:103],v[120:123],v[140:143]\n"
"LD a[4:7],v10,s[40:43],s61 offen offset:0 sc1\n"
"MF v[140:143],a[116:119],v[124:127],v[140:143]\n"
"TR v[106:107],v26 offset:34816\n"
"MF v[200:203],a[72:75],v[112:115],v[200:203]\n"
"MF v[200:203],a[88:91],v[116:119],v[200:203]\n"
"TR v[108:109],v26 offset:40960\n"
"MF v[200:203],a[104:107],v[120:123],v[200:203]\n"
"ST v[56:59],v1,s[40:43],s64 offen offset:2048 sc1\n"
"MF v[200:203],a[120:123],v[124:127],v[200:203]\n"
"TR v[110:111],v26 offset:43008\n"
"MF v[204:207],a[76:79],v[112:115],v[204:207]\n"
"MF v[204:207],a[92:95],v[116:119],v[204:207]\n"
"TR v[112:113],v27 offset:16384\n"
"MF v[204:207],a[108:111],v[120:123],v[204:207]\n"
"MF v[204:207],a[124:127],v[124:127],v[204:207]\n"
"WT lgkmcnt(7)\n"
"TR v[114:115],v27 offset:18432\n"
"MF v[144:147],a[64:67],v[96:99],v[144:147]\n"
"WT lgkmcnt(6)\n"
"s_getpc_b64 s[56:57]\n"
"L_pc_10:\n"
"s_and_b32 s57, s57, 0xffff\n"
"s_mov_b32 s58, L_codeend-L_pc_10\n"
"s_mov_b32 s59, 0x20000\n"
"v_and_b32 v23, 7, v0\n"
"v_lshlrev_b32 v23, 7, v23\n"
"s_sub_u32 s73, s37, 0\n"
"s_lshl_b32 s73, s73, 2\n"
"s_add_u32 s73, s73, s34\n"
"s_lshl_b32 s73, s73, 10\n"
"buffer_load_dword v18, v23, s[56:59], s73 offen\n"
"MF v[144:147],a[80:83],v[100:103],v[144:147]\n"
"WT lgkmcnt(4)\n"
"s_add_u32 s10, s10, 163840\n"
"s_add_u32 m0, s11, 65536\n"
"TR v[116:117],v27 offset:24576\n"
"MF v[144:147],a[96:99],v[104:107],v[144:147]\n"
"WT lgkmcnt(3)\n"
"LD v2,s[52:55],s10 offen offset:0 lds\n"
"MF v[144:147],a[112:115],v[108:111],v[144:147]\n"
"TR v[118:119],v27 offset:26624\n"
"MF v[148:151],a[68:71],v[96:99],v[148:151]\n"
"MF v[148:151],a[84:87],v[100:103],v[148:151]\n"
"TR v[120:121],v27 offset:32768\n"
"MF v[148:151],a[100:103],v[104:107],v[148:151]\n"
"LD v3,s[52:55],s10 offen offset:1024 lds\n"
"MF v[148:151],a[116:119],v[108:111],v[148:151]\n"
"TR v[122:123],v27 offset:34816\n"
"MF v[208:211],a[72:75],v[96:99],v[208:211]\n"
"MF v[208:211],a[88:91],v[100:103],v[208:211]\n"
"TR v[124:125],v27 offset:40960\n"
"MF v[208:211],a[104:107],v[104:107],v[208:211]\n"
"LD v4,s[52:55],s10 offen offset:2048 lds\n"
"MF v[208:211],a[120:123],v[108:111],v[208:211]\n"
"TR v[126:127],v27 offset:43008\n"
"MF v[212:215],a[76:79],v[96:99],v[212:215]\n"
"MF v[212:215],a[92:95],v[100:103],v[212:215]\n"
"TR v[96:97],v24 offset:20480\n"
"MF v[212:215],a[108:111],v[104:107],v[212:215]\n"
"LD v5,s[52:55],s10 offen offset:3072 lds\n"
"MF v[212:215],a[124:127],v[108:111],v[212:215]\n"
"WT lgkmcnt(7)\n"
"TR v[98:99],v24 offset:22528\n"
"MF v[152:155],a[64:67],v[112:115],v[152:155]\n"
"WT lgkmcnt(6)\n"
"MF v[152:155],a[80:83],v[116:119],v[152:155]\n"
"WT lgkmcnt(4)\n"
"TR v[100:101],v24 offset:28672\n"
"MF v[152:155],a[96:99],v[120:123],v[152:155]\n"
"WT lgkmcnt(3)\n"
"LD a[8:11],v10,s[40:43],s62 offen offset:0 sc1\n"
"MF v[152:155],a[112:115],v[124:127],v[152:155]\n"
"TR v[102:103],v24 offset:30720\n"
"MF v[156:159],a[68:71],v[112:115],v[156:159]\n"
"MF v[156:159],a[84:87],v[116:119],v[156:159]\n"
"TR v[104:105],v24 offset:36864\n"
"MF v[156:159],a[100:103],v[120:123],v[156:159]\n"
"LD a[12:15],v10,s[40:43],s63 offen offset:0 sc1\n"
"MF v[156:159],a[116:119],v[124:127],v[156:159]\n"
"TR v[106:107],v24 offset:38912\n"
"MF v[216:219],a[72:75],v[112:115],v[216:219]\n"
"MF v[216:219],a[88:91],v[116:119],v[216:219]\n"
"TR v[108:109],v24 offset:45056\n"
"MF v[216:219],a[104:107],v[120:123],v[216:219]\n"
"ST v[60:63],v1,s[40:43],s64 offen offset:3072 sc1\n"
"MF v[216:219],a[120:123],v[124:127],v[216:219]\n"
"TR v[110:111],v24 offset:47104\n"
"MF v[220:223],a[76:79],v[112:115],v[220:223]\n"
"MF v[220:223],a[92:95],v[116:119],v[220:223]\n"
"TR v[112:113],v25 offset:20480\n"
"MF v[220:223],a[108:111],v[120:123],v[220:223]\n"
"MF v[220:223],a[124:127],v[124:127],v[220:223]\n"
"WT lgkmcnt(7)\n"
"TR v[114:115],v25 offset:22528\n"
"MF v[160:163],a[64:67],v[96:99],v[160:163]\n"
"WT lgkmcnt(6)\n"
"MF v[160:163],a[80:83],v[100:103],v[160:163]\n"
"WT lgkmcnt(4)\n"
"s_add_u32 s65, s65, 524288\n"
"s_add_u32 m0, s66, 147456\n"
"TR v[116:117],v25 offset:28672\n"
"MF v[160:163],a[96:99],v[104:107],v[160:163]\n"
"WT lgkmcnt(3)\n"
"LD v6,s[48:51],s65 offen offset:0 lds\n"
"MF v[160:163],a[112:115],v[108:111],v[160:163]\n"
"TR v[118:119],v25 offset:30720\n"
"MF v[164:167],a[68:71],v[96:99],v[164:167]\n"
"MF v[164:167],a[84:87],v[100:103],v[164:167]\n"
"TR v[120:121],v25 offset:36864\n"
"MF v[164:167],a[100:103],v[104:107],v[164:167]\n"
"LD v7,s[48:51],s65 offen offset:1024 lds\n"
"MF v[164:167],a[116:119],v[108:111],v[164:167]\n"
"TR v[122:123],v25 offset:38912\n"
"MF v[224:227],a[72:75],v[96:99],v[224:227]\n"
"MF v[224:227],a[88:91],v[100:103],v[224:227]\n"
"TR v[124:125],v25 offset:45056\n"
"MF v[224:227],a[104:107],v[104:107],v[224:227]\n"
"LD v8,s[48:51],s65 offen offset:2048 lds\n"
"MF v[224:227],a[120:123],v[108:111],v[224:227]\n"
"TR v[126:127],v25 offset:47104\n"
"MF v[228:231],a[76:79],v[96:99],v[228:231]\n"
"MF v[228:231],a[92:95],v[100:103],v[228:231]\n"
"TR v[96:97],v26 offset:20480\n"
"MF v[228:231],a[108:111],v[104:107],v[228:231]\n"
"LD v9,s[48:51],s65 offen offset:3072 lds\n"
"MF v[228:231],a[124:127],v[108:111],v[228:231]\n"
"WT lgkmcnt(7)\n"
"TR v[98:99],v26 offset:22528\n"
"MF v[168:171],a[64:67],v[112:115],v[168:171]\n"
"WT lgkmcnt(6)\n"
"MF v[168:171],a[80:83],v[116:119],v[168:171]\n"
"WT lgkmcnt(4)\n"
"TR v[100:101],v26 offset:28672\n"
"MF v[168:171],a[96:99],v[120:123],v[168:171]\n"
"WT lgkmcnt(3)\n"
"LD a[16:19],v10,s[40:43],s60 offen offset:1024 sc1\n"
"MF v[168:171],a[112:115],v[124:127],v[168:171]\n"
"TR v[102:103],v26 offset:30720\n"
"MF v[172:175],a[68:71],v[112:115],v[172:175]\n"
"MF v[172:175],a[84:87],v[116:119],v[172:175]\n"
"TR v[104:105],v26 offset:36864\n"
"MF v[172:175],a[100:103],v[120:123],v[172:175]\n"
"LD a[20:23],v10,s[40:43],s61 offen offset:1024 sc1\n"
"MF v[172:175],a[116:119],v[124:127],v[172:175]\n"
"TR v[106:107],v26 offset:38912\n"
"MF v[232:235],a[72:75],v[112:115],v[232:235]\n"
"MF v[232:235],a[88:91],v[116:119],v[232:235]\n"
"s_add_u32 s64, s80, 16384\n"
"TR v[108:109],v26 offset:45056\n"
"MF v[232:235],a[104:107],v[120:123],v[232:235]\n"
"ST v[64:67],v1,s[40:43],s64 offen offset:0 sc1\n"
"MF v[232:235],a[120:123],v[124:127],v[232:235]\n"
"TR v[110:111],v26 offset:47104\n"
"MF v[236:239],a[76:79],v[112:115],v[236:239]\n"
"MF v[236:239],a[92:95],v[116:119],v[236:239]\n"
"TR v[112:113],v27 offset:20480\n"
"MF v[236:239],a[108:111],v[120:123],v[236:239]\n"
"MF v[236:239],a[124:127],v[124:127],v[236:239]\n"
"WT lgkmcnt(7)\n"
"TR v[114:115],v27 offset:22528\n"
"MF v[176:179],a[64:67],v[96:99],v[176:179]\n"
"WT lgkmcnt(6)\n"
"WT vmcnt(17)\n"
"s_barrier\n"
"MF v[176:179],a[80:83],v[100:103],v[176:179]\n"
"WT lgkmcnt(4)\n"
"TR v[116:117],v27 offset:28672\n"
"MF v[176:179],a[96:99],v[104:107],v[176:179]\n"
"WT lgkmcnt(3)\n"
"LD a[24:27],v10,s[40:43],s62 offen offset:1024 sc1\n"
"MF v[176:179],a[112:115],v[108:111],v[176:179]\n"
"TR v[118:119],v27 offset:30720\n"
"MF v[180:183],a[68:71],v[96:99],v[180:183]\n"
"MF v[180:183],a[84:87],v[100:103],v[180:183]\n"
"TR v[120:121],v27 offset:36864\n"
"MF v[180:183],a[100:103],v[104:107],v[180:183]\n"
"LD a[28:31],v10,s[40:43],s63 offen offset:1024 sc1\n"
"MF v[180:183],a[116:119],v[108:111],v[180:183]\n"
"TR v[122:123],v27 offset:38912\n"
"MF v[240:243],a[72:75],v[96:99],v[240:243]\n"
"MF v[240:243],a[88:91],v[100:103],v[240:243]\n"
"TR v[124:125],v27 offset:45056\n"
"MF v[240:243],a[104:107],v[104:107],v[240:243]\n"
"LD a[32:35],v10,s[40:43],s60 offen offset:2048 sc1\n"
"MF v[240:243],a[120:123],v[108:111],v[240:243]\n"
"TR v[126:127],v27 offset:47104\n"
"MF v[244:247],a[76:79],v[96:99],v[244:247]\n"
"MF v[244:247],a[92:95],v[100:103],v[244:247]\n"
"TR v[96:97],v24 offset:49152\n"
"MF v[244:247],a[108:111],v[104:107],v[244:247]\n"
"LD a[36:39],v10,s[40:43],s61 offen offset:2048 sc1\n"
"MF v[244:247],a[124:127],v[108:111],v[244:247]\n"
"WT lgkmcnt(7)\n"
"TR v[98:99],v24 offset:51200\n"
"MF v[184:187],a[64:67],v[112:115],v[184:187]\n"
"WT lgkmcnt(6)\n"
"MF v[184:187],a[80:83],v[116:119],v[184:187]\n"
"WT lgkmcnt(4)\n"
"TR v[100:101],v24 offset:57344\n"
"MF v[184:187],a[96:99],v[120:123],v[184:187]\n"
"WT lgkmcnt(3)\n"
"LD a[40:43],v10,s[40:43],s62 offen offset:2048 sc1\n"
"MF v[184:187],a[112:115],v[124:127],v[184:187]\n"
"TR v[102:103],v24 offset:59392\n"
"MF v[188:191],a[68:71],v[112:115],v[188:191]\n"
"MF v[188:191],a[84:87],v[116:119],v[188:191]\n"
"TR v[104:105],v28 offset:0\n"
"MF v[188:191],a[100:103],v[120:123],v[188:191]\n"
"LD a[44:47],v10,s[40:43],s63 offen offset:2048 sc1\n"
"MF v[188:191],a[116:119],v[124:127],v[188:191]\n"
"TR v[106:107],v28 offset:2048\n"
"MF v[248:251],a[72:75],v[112:115],v[248:251]\n"
"MF v[248:251],a[88:91],v[116:119],v[248:251]\n"
"TR v[108:109],v28 offset:8192\n"
"MF v[248:251],a[104:107],v[120:123],v[248:251]\n"
"ST v[68:71],v1,s[40:43],s64 offen offset:1024 sc1\n"
"MF v[248:251],a[120:123],v[124:127],v[248:251]\n"
"TR v[110:111],v28 offset:10240\n"
"MF v[252:255],a[76:79],v[112:115],v[252:255]\n"
"MF v[252:255],a[92:95],v[116:119],v[252:255]\n"
"TR v[112:113],v25 offset:49152\n"
"MF v[252:255],a[108:111],v[120:123],v[252:255]\n"
"MF v[252:255],a[124:127],v[124:127],v[252:255]\n"
"WT lgkmcnt(7)\n"
"TR v[114:115],v25 offset:51200\n"
"MF v[128:131],a[128:131],v[96:99],v[128:131]\n"
"WT lgkmcnt(6)\n"
"s_barrier\n"
"MF v[128:131],a[144:147],v[100:103],v[128:131]\n"
"WT lgkmcnt(4)\n"
"s_add_u32 s65, s65, 524288\n"
"s_add_u32 m0, s66, 81920\n"
"TR v[116:117],v25 offset:57344\n"
"MF v[128:131],a[160:163],v[104:107],v[128:131]\n"
"WT lgkmcnt(3)\n"
"LD v6,s[48:51],s65 offen offset:0 lds\n"
"MF v[128:131],a[176:179],v[108:111],v[128:131]\n"
"TR v[118:119],v25 offset:59392\n"
"MF v[132:135],a[132:135],v[96:99],v[132:135]\n"
"MF v[132:135],a[148:151],v[100:103],v[132:135]\n"
"TR v[120:121],v29 offset:0\n"
"MF v[132:135],a[164:167],v[104:107],v[132:135]\n"
"LD v7,s[48:51],s65 offen offset:1024 lds\n"
"MF v[132:135],a[180:183],v[108:111],v[132:135]\n"
"TR v[122:123],v29 offset:2048\n"
"MF v[192:195],a[136:139],v[96:99],v[192:195]\n"
"MF v[192:195],a[152:155],v[100:103],v[192:195]\n"
"TR v[124:125],v29 offset:8192\n"
"MF v[192:195],a[168:171],v[104:107],v[192:195]\n"
"LD v8,s[48:51],s65 offen offset:2048 lds\n"
"MF v[192:195],a[184:187],v[108:111],v[192:195]\n"
"TR v[126:127],v29 offset:10240\n"
"MF v[196:199],a[140:143],v[96:99],v[196:199]\n"
"MF v[196:199],a[156:159],v[100:103],v[196:199]\n"
"TR v[96:97],v26 offset:49152\n"
"MF v[196:199],a[172:175],v[104:107],v[196:199]\n"
"LD v9,s[48:51],s65 offen offset:3072 lds\n"
"MF v[196:199],a[188:191],v[108:111],v[196:199]\n"
"WT lgkmcnt(7)\n"
"TR v[98:99],v26 offset:51200\n"
"MF v[136:139],a[128:131],v[112:115],v[136:139]\n"
"WT lgkmcnt(6)\n"
"MF v[136:139],a[144:147],v[116:119],v[136:139]\n"
"WT lgkmcnt(4)\n"
"TR v[100:101],v26 offset:57344\n"
"MF v[136:139],a[160:163],v[120:123],v[136:139]\n"
"WT lgkmcnt(3)\n"
"LD a[48:51],v10,s[40:43],s60 offen offset:3072 sc1\n"
"MF v[136:139],a[176:179],v[124:127],v[136:139]\n"
"TR v[102:103],v26 offset:59392\n"
"MF v[140:143],a[132:135],v[112:115],v[140:143]\n"
"v_lshrrev_b32 v21, 3, v0\n"
"v_and_b32 v22, 7, v0\n"
"v_and_b32 v23, 6, v21\n"
"MF v[140:143],a[148:151],v[116:119],v[140:143]\n"
"TR v[104:105],v30 offset:0\n"
"MF v[140:143],a[164:167],v[120:123],v[140:143]\n"
"LD a[52:55],v10,s[40:43],s61 offen offset:3072 sc1\n"
"MF v[140:143],a[180:183],v[124:127],v[140:143]\n"
"TR v[106:107],v30 offset:2048\n"
"MF v[200:203],a[136:139],v[112:115],v[200:203]\n"
"v_xor_b32 v22, v22, v23\n"
"v_lshlrev_b32 v22, 4, v22\n"
"s_mov_b32 s56, 8192\n"
"MF v[200:203],a[152:155],v[116:119],v[200:203]\n"
"TR v[108:109],v30 offset:8192\n"
"MF v[200:203],a[168:171],v[120:123],v[200:203]\n"
"ST v[72:75],v1,s[40:43],s64 offen offset:2048 sc1\n"
"MF v[200:203],a[184:187],v[124:127],v[200:203]\n"
"TR v[110:111],v30 offset:10240\n"
"MF v[204:207],a[140:143],v[112:115],v[204:207]\n"
"v_mad_u32_u24 v6, v21, s56, v22\n"
"v_add_u32 v7, 64512, v6\n"
"v_add_u32 v8, 129024, v6\n"
"MF v[204:207],a[156:159],v[116:119],v[204:207]\n"
"TR v[112:113],v27 offset:49152\n"
"MF v[204:207],a[172:175],v[120:123],v[204:207]\n"
"MF v[204:207],a[188:191],v[124:127],v[204:207]\n"
"WT lgkmcnt(7)\n"
"TR v[114:115],v27 offset:51200\n"
"MF v[144:147],a[128:131],v[96:99],v[144:147]\n"
"WT lgkmcnt(6)\n"
"v_add_u32 v9, 193536, v6\n"
"s_mov_b32 s48, s12\n"
"s_and_b32 s49, s13, 0xffff\n"
"MF v[144:147],a[144:147],v[100:103],v[144:147]\n"
"WT lgkmcnt(4)\n"
"TR v[116:117],v27 offset:57344\n"
"MF v[144:147],a[160:163],v[104:107],v[144:147]\n"
"WT lgkmcnt(3)\n"
"LD a[56:59],v10,s[40:43],s62 offen offset:3072 sc1\n"
"MF v[144:147],a[176:179],v[108:111],v[144:147]\n"
"TR v[118:119],v27 offset:59392\n"
"MF v[148:151],a[132:135],v[96:99],v[148:151]\n"
"s_mov_b32 s50, 0x500000\n"
"s_mov_b32 s51, 0x20000\n"
"s_lshr_b32 s56, s34, 1\n"
"MF v[148:151],a[148:151],v[100:103],v[148:151]\n"
"TR v[120:121],v31 offset:0\n"
"MF v[148:151],a[164:167],v[104:107],v[148:151]\n"
"LD a[60:63],v10,s[40:43],s63 offen offset:3072 sc1\n"
"MF v[148:151],a[180:183],v[108:111],v[148:151]\n"
"TR v[122:123],v31 offset:2048\n"
"MF v[208:211],a[136:139],v[96:99],v[208:211]\n"
"s_and_b32 s57, s34, 1\n"
"s_mul_i32 s65, s56, 262144\n"
"s_lshl_b32 s58, s57, 7\n"
"MF v[208:211],a[152:155],v[100:103],v[208:211]\n"
"TR v[124:125],v31 offset:8192\n"
"MF v[208:211],a[168:171],v[104:107],v[208:211]\n"
"LD a[64:67],v11,s[40:43],s60 offen offset:0 sc1\n"
"MF v[208:211],a[184:187],v[108:111],v[208:211]\n"
"TR v[126:127],v31 offset:10240\n"
"MF v[212:215],a[140:143],v[96:99],v[212:215]\n"
"s_add_u32 s65, s65, s58\n"
"s_lshl_b32 s58, s35, 8\n"
"s_add_u32 s65, s65, s58\n"
"MF v[212:215],a[156:159],v[100:103],v[212:215]\n"
"TR v[96:97],v24 offset:53248\n"
"MF v[212:215],a[172:175],v[104:107],v[212:215]\n"
"LD a[68:71],v11,s[40:43],s61 offen offset:0 sc1\n"
"MF v[212:215],a[188:191],v[108:111],v[212:215]\n"
"WT lgkmcnt(7)\n"
"TR v[98:99],v24 offset:55296\n"
"MF v[152:155],a[128:131],v[112:115],v[152:155]\n"
"WT lgkmcnt(6)\n"
"s_lshl_b32 s66, s34, 12\n"
"s_mov_b32 s67, s65\n"
"MF v[152:155],a[144:147],v[116:119],v[152:155]\n"
"WT lgkmcnt(4)\n"
"TR v[100:101],v24 offset:61440\n"
"MF v[152:155],a[160:163],v[120:123],v[152:155]\n"
"WT lgkmcnt(3)\n"
"LD a[72:75],v11,s[40:43],s62 offen offset:0 sc1\n"
"MF v[152:155],a[176:179],v[124:127],v[152:155]\n"
"TR v[102:103],v24 offset:63488\n"
"MF v[156:159],a[132:135],v[112:115],v[156:159]\n"
"MF v[156:159],a[148:151],v[116:119],v[156:159]\n"
"TR v[104:105],v28 offset:4096\n"
"MF v[156:159],a[164:167],v[120:123],v[156:159]\n"
"LD a[76:79],v11,s[40:43],s63 offen offset:0 sc1\n"
"MF v[156:159],a[180:183],v[124:127],v[156:159]\n"
"TR v[106:107],v28 offset:6144\n"
"MF v[216:219],a[136:139],v[112:115],v[216:219]\n"
"MF v[216:219],a[152:155],v[116:119],v[216:219]\n"
"TR v[108:109],v28 offset:12288\n"
"MF v[216:219],a[168:171],v[120:123],v[216:219]\n"
"ST v[76:79],v1,s[40:43],s64 offen offset:3072 sc1\n"
"MF v[216:219],a[184:187],v[124:127],v[216:219]\n"
"TR v[110:111],v28 offset:14336\n"
"MF v[220:223],a[140:143],v[112:115],v[220:223]\n"
"MF v[220:223],a[156:159],v[116:119],v[220:223]\n"
"TR v[112:113],v25 offset:53248\n"
"MF v[220:223],a[172:175],v[120:123],v[220:223]\n"
"MF v[220:223],a[188:191],v[124:127],v[220:223]\n"
"WT lgkmcnt(7)\n"
"TR v[114:115],v25 offset:55296\n"
"MF v[160:163],a[128:131],v[96:99],v[160:163]\n"
"WT lgkmcnt(6)\n"
"MF v[160:163],a[144:147],v[100:103],v[160:163]\n"
"WT lgkmcnt(4)\n"
"s_mov_b32 s65, s67\n"
"s_add_u32 m0, s66, 98304\n"
"TR v[116:117],v25 offset:61440\n"
"MF v[160:163],a[160:163],v[104:107],v[160:163]\n"
"WT lgkmcnt(3)\n"
"LD v6,s[48:51],s65 offen offset:0 lds\n"
"MF v[160:163],a[176:179],v[108:111],v[160:163]\n"
"TR v[118:119],v25 offset:63488\n"
"MF v[164:167],a[132:135],v[96:99],v[164:167]\n"
"MF v[164:167],a[148:151],v[100:103],v[164:167]\n"
"TR v[120:121],v29 offset:4096\n"
"MF v[164:167],a[164:167],v[104:107],v[164:167]\n"
"LD v7,s[48:51],s65 offen offset:1024 lds\n"
"MF v[164:167],a[180:183],v[108:111],v[164:167]\n"
"TR v[122:123],v29 offset:6144\n"
"MF v[224:227],a[136:139],v[96:99],v[224:227]\n"
"MF v[224:227],a[152:155],v[100:103],v[224:227]\n"
"TR v[124:125],v29 offset:12288\n"
"MF v[224:227],a[168:171],v[104:107],v[224:227]\n"
"LD v8,s[48:51],s65 offen offset:2048 lds\n"
"MF v[224:227],a[184:187],v[108:111],v[224:227]\n"
"TR v[126:127],v29 offset:14336\n"
"MF v[228:231],a[140:143],v[96:99],v[228:231]\n"
"MF v[228:231],a[156:159],v[100:103],v[228:231]\n"
"TR v[96:97],v26 offset:53248\n"
"MF v[228:231],a[172:175],v[104:107],v[228:231]\n"
"LD v9,s[48:51],s65 offen offset:3072 lds\n"
"MF v[228:231],a[188:191],v[108:111],v[228:231]\n"
"WT lgkmcnt(7)\n"
"TR v[98:99],v26 offset:55296\n"
"MF v[168:171],a[128:131],v[112:115],v[168:171]\n"
"WT lgkmcnt(6)\n"
"MF v[168:171],a[144:147],v[116:119],v[168:171]\n"
"WT lgkmcnt(4)\n"
"TR v[100:101],v26 offset:61440\n"
"MF v[168:171],a[160:163],v[120:123],v[168:171]\n"
"WT lgkmcnt(3)\n"
"LD a[80:83],v11,s[40:43],s60 offen offset:1024 sc1\n"
"MF v[168:171],a[176:179],v[124:127],v[168:171]\n"
"TR v[102:103],v26 offset:63488\n"
"MF v[172:175],a[132:135],v[112:115],v[172:175]\n"
"MF v[172:175],a[148:151],v[116:119],v[172:175]\n"
"TR v[104:105],v30 offset:4096\n"
"MF v[172:175],a[164:167],v[120:123],v[172:175]\n"
"LD a[84:87],v11,s[40:43],s61 offen offset:1024 sc1\n"
"MF v[172:175],a[180:183],v[124:127],v[172:175]\n"
"TR v[106:107],v30 offset:6144\n"
"MF v[232:235],a[136:139],v[112:115],v[232:235]\n"
"MF v[232:235],a[152:155],v[116:119],v[232:235]\n"
"s_add_u32 s64, s80, 20480\n"
"TR v[108:109],v30 offset:12288\n"
"MF v[232:235],a[168:171],v[120:123],v[232:235]\n"
"ST v[80:83],v1,s[40:43],s64 offen offset:0 sc1\n"
"MF v[232:235],a[184:187],v[124:127],v[232:235]\n"
"TR v[110:111],v30 offset:14336\n"
"MF v[236:239],a[140:143],v[112:115],v[236:239]\n"
"MF v[236:239],a[156:159],v[116:119],v[236:239]\n"
"TR v[112:113],v27 offset:53248\n"
"MF v[236:239],a[172:175],v[120:123],v[236:239]\n"
"MF v[236:239],a[188:191],v[124:127],v[236:239]\n"
"WT lgkmcnt(7)\n"
"TR v[114:115],v27 offset:55296\n"
"MF v[176:179],a[128:131],v[96:99],v[176:179]\n"
"WT lgkmcnt(6)\n"
"WT vmcnt(17)\n"
"s_barrier\n"
"MF v[176:179],a[144:147],v[100:103],v[176:179]\n"
"WT lgkmcnt(4)\n"
"TR v[116:117],v27 offset:61440\n"
"MF v[176:179],a[160:163],v[104:107],v[176:179]\n"
"WT lgkmcnt(3)\n"
"LD a[88:91],v11,s[40:43],s62 offen offset:1024 sc1\n"
"MF v[176:179],a[176:179],v[108:111],v[176:179]\n"
"TR v[118:119],v27 offset:63488\n"
"MF v[180:183],a[132:135],v[96:99],v[180:183]\n"
"MF v[180:183],a[148:151],v[100:103],v[180:183]\n"
"TR v[120:121],v31 offset:4096\n"
"MF v[180:183],a[164:167],v[104:107],v[180:183]\n"
"LD a[92:95],v11,s[40:43],s63 offen offset:1024 sc1\n"
"MF v[180:183],a[180:183],v[108:111],v[180:183]\n"
"TR v[122:123],v31 offset:6144\n"
"MF v[240:243],a[136:139],v[96:99],v[240:243]\n"
"MF v[240:243],a[152:155],v[100:103],v[240:243]\n"
"TR v[124:125],v31 offset:12288\n"
"MF v[240:243],a[168:171],v[104:107],v[240:243]\n"
"LD a[96:99],v11,s[40:43],s60 offen offset:2048 sc1\n"
"MF v[240:243],a[184:187],v[108:111],v[240:243]\n"
"TR v[126:127],v31 offset:14336\n"
"MF v[244:247],a[140:143],v[96:99],v[244:247]\n"
"MF v[244:247],a[156:159],v[100:103],v[244:247]\n"
"TR v[96:97],v28 offset:16384\n"
"MF v[244:247],a[172:175],v[104:107],v[244:247]\n"
"LD a[100:103],v11,s[40:43],s61 offen offset:2048 sc1\n"
"MF v[244:247],a[188:191],v[108:111],v[244:247]\n"
"WT lgkmcnt(7)\n"
"TR v[98:99],v28 offset:18432\n"
"MF v[184:187],a[128:131],v[112:115],v[184:187]\n"
"WT lgkmcnt(6)\n"
"MF v[184:187],a[144:147],v[116:119],v[184:187]\n"
"WT lgkmcnt(4)\n"
"TR v[100:101],v28 offset:24576\n"
"MF v[184:187],a[160:163],v[120:123],v[184:187]\n"
"WT lgkmcnt(3)\n"
"LD a[104:107],v11,s[40:43],s62 offen offset:2048 sc1\n"
"MF v[184:187],a[176:179],v[124:127],v[184:187]\n"
"TR v[102:103],v28 offset:26624\n"
"MF v[188:191],a[132:135],v[112:115],v[188:191]\n"
"MF v[188:191],a[148:151],v[116:119],v[188:191]\n"
"TR v[104:105],v24 offset:16384\n"
"MF v[188:191],a[164:167],v[120:123],v[188:191]\n"
"LD a[108:111],v11,s[40:43],s63 offen offset:2048 sc1\n"
"MF v[188:191],a[180:183],v[124:127],v[188:191]\n"
"TR v[106:107],v24 offset:18432\n"
"MF v[248:251],a[136:139],v[112:115],v[248:251]\n"
"MF v[248:251],a[152:155],v[116:119],v[248:251]\n"
"TR v[108:109],v24 offset:24576\n"
"MF v[248:251],a[168:171],v[120:123],v[248:251]\n"
"ST v[84:87],v1,s[40:43],s64 offen offset:1024 sc1\n"
"MF v[248:251],a[184:187],v[124:127],v[248:251]\n"
"TR v[110:111],v24 offset:26624\n"
"MF v[252:255],a[140:143],v[112:115],v[252:255]\n"
"MF v[252:255],a[156:159],v[116:119],v[252:255]\n"
"TR v[112:113],v29 offset:16384\n"
"MF v[252:255],a[172:175],v[120:123],v[252:255]\n"
"MF v[252:255],a[188:191],v[124:127],v[252:255]\n"
"WT lgkmcnt(7)\n"
"TR v[114:115],v29 offset:18432\n"
"MF v[128:131],a[192:195],v[96:99],v[128:131]\n"
"WT lgkmcnt(6)\n"
"s_barrier\n"
"MF v[128:131],a[208:211],v[100:103],v[128:131]\n"
"WT lgkmcnt(4)\n"
"s_add_u32 s65, s65, 524288\n"
"s_add_u32 m0, s66, 114688\n"
"TR v[116:117],v29 offset:24576\n"
"MF v[128:131],a[224:227],v[104:107],v[128:131]\n"
"WT lgkmcnt(3)\n"
"LD v6,s[48:51],s65 offen offset:0 lds\n"
"MF v[128:131],a[240:243],v[108:111],v[128:131]\n"
"TR v[118:119],v29 offset:26624\n"
"MF v[132:135],a[196:199],v[96:99],v[132:135]\n"
"MF v[132:135],a[212:215],v[100:103],v[132:135]\n"
"TR v[120:121],v25 offset:16384\n"
"MF v[132:135],a[228:231],v[104:107],v[132:135]\n"
"LD v7,s[48:51],s65 offen offset:1024 lds\n"
"MF v[132:135],a[244:247],v[108:111],v[132:135]\n"
"TR v[122:123],v25 offset:18432\n"
"MF v[192:195],a[200:203],v[96:99],v[192:195]\n"
"MF v[192:195],a[216:219],v[100:103],v[192:195]\n"
"TR v[124:125],v25 offset:24576\n"
"MF v[192:195],a[232:235],v[104:107],v[192:195]\n"
"LD v8,s[48:51],s65 offen offset:2048 lds\n"
"MF v[192:195],a[248:251],v[108:111],v[192:195]\n"
"TR v[126:127],v25 offset:26624\n"
"MF v[196:199],a[204:207],v[96:99],v[196:199]\n"
"CV v32,v128,v129\n"
"CV v33,v130,v131\n"
"MF v[196:199],a[220:223],v[100:103],v[196:199]\n"
"TR v[96:97],v30 offset:16384\n"
"CV v34,v132,v133\n"
"MF v[196:199],a[236:239],v[104:107],v[196:199]\n"
"LD v9,s[48:51],s65 offen offset:3072 lds\n"
"MF v[196:199],a[252:255],v[108:111],v[196:199]\n"
"WT lgkmcnt(7)\n"
"TR v[98:99],v30 offset:18432\n"
"CV v35,v134,v135\n"
"MF v[136:139],a[192:195],v[112:115],v[136:139]\n"
"WT lgkmcnt(6)\n"
"MF v[136:139],a[208:211],v[116:119],v[136:139]\n"
"WT lgkmcnt(4)\n"
"TR v[100:101],v30 offset:24576\n"
"MF v[136:139],a[224:227],v[120:123],v[136:139]\n"
"WT lgkmcnt(3)\n"
"LD a[112:115],v11,s[40:43],s60 offen offset:3072 sc1\n"
"MF v[136:139],a[240:243],v[124:127],v[136:139]\n"
"TR v[102:103],v30 offset:26624\n"
"MF v[140:143],a[196:199],v[112:115],v[140:143]\n"
"CV v64,v192,v193\n"
"CV v65,v194,v195\n"
"MF v[140:143],a[212:215],v[116:119],v[140:143]\n"
"TR v[104:105],v26 offset:16384\n"
"CV v66,v196,v197\n"
"MF v[140:143],a[228:231],v[120:123],v[140:143]\n"
"LD a[116:119],v11,s[40:43],s61 offen offset:3072 sc1\n"
"MF v[140:143],a[244:247],v[124:127],v[140:143]\n"
"TR v[106:107],v26 offset:18432\n"
"CV v67,v198,v199\n"
"MF v[200:203],a[200:203],v[112:115],v[200:203]\n"
"MF v[200:203],a[216:219],v[116:119],v[200:203]\n"
"TR v[108:109],v26 offset:24576\n"
"MF v[200:203],a[232:235],v[120:123],v[200:203]\n"
"ST v[88:91],v1,s[40:43],s64 offen offset:2048 sc1\n"
"MF v[200:203],a[248:251],v[124:127],v[200:203]\n"
"TR v[110:111],v26 offset:26624\n"
"MF v[204:207],a[204:207],v[112:115],v[204:207]\n"
"CV v36,v136,v137\n"
"CV v37,v138,v139\n"
"MF v[204:207],a[220:223],v[116:119],v[204:207]\n"
"TR v[112:113],v31 offset:16384\n"
"CV v38,v140,v141\n"
"MF v[204:207],a[236:239],v[120:123],v[204:207]\n"
"CV v39,v142,v143\n"
"MF v[204:207],a[252:255],v[124:127],v[204:207]\n"
"WT lgkmcnt(7)\n"
"TR v[114:115],v31 offset:18432\n"
"MF v[144:147],a[192:195],v[96:99],v[144:147]\n"
"WT lgkmcnt(6)\n"
"MF v[144:147],a[208:211],v[100:103],v[144:147]\n"
"WT lgkmcnt(4)\n"
"TR v[116:117],v31 offset:24576\n"
"MF v[144:147],a[224:227],v[104:107],v[144:147]\n"
"WT lgkmcnt(3)\n"
"LD a[120:123],v11,s[40:43],s62 offen offset:3072 sc1\n"
"MF v[144:147],a[240:243],v[108:111],v[144:147]\n"
"TR v[118:119],v31 offset:26624\n"
"MF v[148:151],a[196:199],v[96:99],v[148:151]\n"
"CV v68,v200,v201\n"
"CV v69,v202,v203\n"
"MF v[148:151],a[212:215],v[100:103],v[148:151]\n"
"TR v[120:121],v27 offset:16384\n"
"CV v70,v204,v205\n"
"MF v[148:151],a[228:231],v[104:107],v[148:151]\n"
"LD a[124:127],v11,s[40:43],s63 offen offset:3072 sc1\n"
"MF v[148:151],a[244:247],v[108:111],v[148:151]\n"
"TR v[122:123],v27 offset:18432\n"
"CV v71,v206,v207\n"
"MF v[208:211],a[200:203],v[96:99],v[208:211]\n"
"MF v[208:211],a[216:219],v[100:103],v[208:211]\n"
"TR v[124:125],v27 offset:24576\n"
"v_add_u32 v10, 8192, v10\n"
"MF v[208:211],a[232:235],v[104:107],v[208:211]\n"
"LD a[128:131],v10,s[40:43],s60 offen offset:0 sc1\n"
"MF v[208:211],a[248:251],v[108:111],v[208:211]\n"
"TR v[126:127],v27 offset:26624\n"
"MF v[212:215],a[204:207],v[96:99],v[212:215]\n"
"CV v40,v144,v145\n"
"CV v41,v146,v147\n"
"MF v[212:215],a[220:223],v[100:103],v[212:215]\n"
"TR v[96:97],v28 offset:20480\n"
"CV v42,v148,v149\n"
"MF v[212:215],a[236:239],v[104:107],v[212:215]\n"
"LD a[132:135],v10,s[40:43],s61 offen offset:0 sc1\n"
"MF v[212:215],a[252:255],v[108:111],v[212:215]\n"
"WT lgkmcnt(7)\n"
"TR v[98:99],v28 offset:22528\n"
"CV v43,v150,v151\n"
"MF v[152:155],a[192:195],v[112:115],v[152:155]\n"
"WT lgkmcnt(6)\n"
"MF v[152:155],a[208:211],v[116:119],v[152:155]\n"
"WT lgkmcnt(4)\n"
"TR v[100:101],v28 offset:28672\n"
"MF v[152:155],a[224:227],v[120:123],v[152:155]\n"
"WT lgkmcnt(3)\n"
"LD a[136:139],v10,s[40:43],s62 offen offset:0 sc1\n"
"MF v[152:155],a[240:243],v[124:127],v[152:155]\n"
"TR v[102:103],v28 offset:30720\n"
"MF v[156:159],a[196:199],v[112:115],v[156:159]\n"
"CV v72,v208,v209\n"
"CV v73,v210,v211\n"
"MF v[156:159],a[212:215],v[116:119],v[156:159]\n"
"TR v[104:105],v24 offset:20480\n"
"CV v74,v212,v213\n"
"MF v[156:159],a[228:231],v[120:123],v[156:159]\n"
"LD a[140:143],v10,s[40:43],s63 offen offset:0 sc1\n"
"MF v[156:159],a[244:247],v[124:127],v[156:159]\n"
"TR v[106:107],v24 offset:22528\n"
"CV v75,v214,v215\n"
"MF v[216:219],a[200:203],v[112:115],v[216:219]\n"
"MF v[216:219],a[216:219],v[116:119],v[216:219]\n"
"TR v[108:109],v24 offset:28672\n"
"MF v[216:219],a[232:235],v[120:123],v[216:219]\n"
"ST v[92:95],v1,s[40:43],s64 offen offset:3072 sc1\n"
"MF v[216:219],a[248:251],v[124:127],v[216:219]\n"
"TR v[110:111],v24 offset:30720\n"
"MF v[220:223],a[204:207],v[112:115],v[220:223]\n"
"CV v44,v152,v153\n"
"CV v45,v154,v155\n"
"MF v[220:223],a[220:223],v[116:119],v[220:223]\n"
"TR v[112:113],v29 offset:20480\n"
"CV v46,v156,v157\n"
"MF v[220:223],a[236:239],v[120:123],v[220:223]\n"
"CV v47,v158,v159\n"
"MF v[220:223],a[252:255],v[124:127],v[220:223]\n"
"WT lgkmcnt(7)\n"
"TR v[114:115],v29 offset:22528\n"
"MF v[160:163],a[192:195],v[96:99],v[160:163]\n"
"WT lgkmcnt(6)\n"
"MF v[160:163],a[208:211],v[100:103],v[160:163]\n"
"WT lgkmcnt(4)\n"
"s_add_u32 s65, s65, 524288\n"
"s_add_u32 m0, s66, 131072\n"
"TR v[116:117],v29 offset:28672\n"
"MF v[160:163],a[224:227],v[104:107],v[160:163]\n"
"WT lgkmcnt(3)\n"
"LD v6,s[48:51],s65 offen offset:0 lds\n"
"MF v[160:163],a[240:243],v[108:111],v[160:163]\n"
"TR v[118:119],v29 offset:30720\n"
"MF v[164:167],a[196:199],v[96:99],v[164:167]\n"
"CV v76,v216,v217\n"
"CV v77,v218,v219\n"
"MF v[164:167],a[212:215],v[100:103],v[164:167]\n"
"TR v[120:121],v25 offset:20480\n"
"CV v78,v220,v221\n"
"MF v[164:167],a[228:231],v[104:107],v[164:167]\n"
"LD v7,s[48:51],s65 offen offset:1024 lds\n"
"MF v[164:167],a[244:247],v[108:111],v[164:167]\n"
"TR v[122:123],v25 offset:22528\n"
"CV v79,v222,v223\n"
"MF v[224:227],a[200:203],v[96:99],v[224:227]\n"
"MF v[224:227],a[216:219],v[100:103],v[224:227]\n"
"TR v[124:125],v25 offset:28672\n"
"MF v[224:227],a[232:235],v[104:107],v[224:227]\n"
"LD v8,s[48:51],s65 offen offset:2048 lds\n"
"MF v[224:227],a[248:251],v[108:111],v[224:227]\n"
"TR v[126:127],v25 offset:30720\n"
"MF v[228:231],a[204:207],v[96:99],v[228:231]\n"
"CV v48,v160,v161\n"
"CV v49,v162,v163\n"
"MF v[228:231],a[220:223],v[100:103],v[228:231]\n"
"TR v[96:97],v30 offset:20480\n"
"CV v50,v164,v165\n"
"MF v[228:231],a[236:239],v[104:107],v[228:231]\n"
"LD v9,s[48:51],s65 offen offset:3072 lds\n"
"MF v[228:231],a[252:255],v[108:111],v[228:231]\n"
"WT lgkmcnt(7)\n"
"TR v[98:99],v30 offset:22528\n"
"CV v51,v166,v167\n"
"MF v[168:171],a[192:195],v[112:115],v[168:171]\n"
"WT lgkmcnt(6)\n"
"MF v[168:171],a[208:211],v[116:119],v[168:171]\n"
"WT lgkmcnt(4)\n"
"TR v[100:101],v30 offset:28672\n"
"MF v[168:171],a[224:227],v[120:123],v[168:171]\n"
"WT lgkmcnt(3)\n"
"LD a[144:147],v10,s[40:43],s60 offen offset:1024 sc1\n"
"MF v[168:171],a[240:243],v[124:127],v[168:171]\n"
"TR v[102:103],v30 offset:30720\n"
"MF v[172:175],a[196:199],v[112:115],v[172:175]\n"
"CV v80,v224,v225\n"
"CV v81,v226,v227\n"
"MF v[172:175],a[212:215],v[116:119],v[172:175]\n"
"TR v[104:105],v26 offset:20480\n"
"CV v82,v228,v229\n"
"MF v[172:175],a[228:231],v[120:123],v[172:175]\n"
"LD a[148:151],v10,s[40:43],s61 offen offset:1024 sc1\n"
"MF v[172:175],a[244:247],v[124:127],v[172:175]\n"
"TR v[106:107],v26 offset:22528\n"
"CV v83,v230,v231\n"
"MF v[232:235],a[200:203],v[112:115],v[232:235]\n"
"MF v[232:235],a[216:219],v[116:119],v[232:235]\n"
"TR v[108:109],v26 offset:28672\n"
"MF v[232:235],a[232:235],v[120:123],v[232:235]\n"
"LD a[152:155],v10,s[40:43],s62 offen offset:1024 sc1\n"
"MF v[232:235],a[248:251],v[124:127],v[232:235]\n"
"TR v[110:111],v26 offset:30720\n"
"MF v[236:239],a[204:207],v[112:115],v[236:239]\n"
"CV v52,v168,v169\n"
"CV v53,v170,v171\n"
"MF v[236:239],a[220:223],v[116:119],v[236:239]\n"
"TR v[112:113],v31 offset:20480\n"
"CV v54,v172,v173\n"
"MF v[236:239],a[236:239],v[120:123],v[236:239]\n"
"CV v55,v174,v175\n"
"MF v[236:239],a[252:255],v[124:127],v[236:239]\n"
"WT lgkmcnt(7)\n"
"TR v[114:115],v31 offset:22528\n"
"MF v[176:179],a[192:195],v[96:99],v[176:179]\n"
"WT lgkmcnt(6)\n"
"WT vmcnt(17)\n"
"MF v[176:179],a[208:211],v[100:103],v[176:179]\n"
"WT lgkmcnt(4)\n"
"TR v[116:117],v31 offset:28672\n"
"MF v[176:179],a[224:227],v[104:107],v[176:179]\n"
"WT lgkmcnt(3)\n"
"LD a[156:159],v10,s[40:43],s63 offen offset:1024 sc1\n"
"MF v[176:179],a[240:243],v[108:111],v[176:179]\n"
"TR v[118:119],v31 offset:30720\n"
"MF v[180:183],a[196:199],v[96:99],v[180:183]\n"
"CV v84,v232,v233\n"
"CV v85,v234,v235\n"
"MF v[180:183],a[212:215],v[100:103],v[180:183]\n"
"TR v[120:121],v27 offset:20480\n"
"CV v86,v236,v237\n"
"MF v[180:183],a[228:231],v[104:107],v[180:183]\n"
"CV v87,v238,v239\n"
"MF v[180:183],a[244:247],v[108:111],v[180:183]\n"
"TR v[122:123],v27 offset:22528\n"
"MF v[240:243],a[200:203],v[96:99],v[240:243]\n"
"MF v[240:243],a[216:219],v[100:103],v[240:243]\n"
"TR v[124:125],v27 offset:28672\n"
"MF v[240:243],a[232:235],v[104:107],v[240:243]\n"
"MF v[240:243],a[248:251],v[108:111],v[240:243]\n"
"TR v[126:127],v27 offset:30720\n"
"MF v[244:247],a[204:207],v[96:99],v[244:247]\n"
"CV v56,v176,v177\n"
"CV v57,v178,v179\n"
"MF v[244:247],a[220:223],v[100:103],v[244:247]\n"
"CV v58,v180,v181\n"
"CV v59,v182,v183\n"
"MF v[244:247],a[236:239],v[104:107],v[244:247]\n"
"MF v[244:247],a[252:255],v[108:111],v[244:247]\n"
"WT lgkmcnt(6)\n"
"MF v[184:187],a[192:195],v[112:115],v[184:187]\n"
"WT lgkmcnt(4)\n"
"MF v[184:187],a[208:211],v[116:119],v[184:187]\n"
"WT lgkmcnt(2)\n"
"MF v[184:187],a[224:227],v[120:123],v[184:187]\n"
"WT lgkmcnt(0)\n"
"MF v[184:187],a[240:243],v[124:127],v[184:187]\n"
"MF v[188:191],a[196:199],v[112:115],v[188:191]\n"
"CV v88,v240,v241\n"
"CV v89,v242,v243\n"
"MF v[188:191],a[212:215],v[116:119],v[188:191]\n"
"CV v90,v244,v245\n"
"CV v91,v246,v247\n"
"MF v[188:191],a[228:231],v[120:123],v[188:191]\n"
"MF v[188:191],a[244:247],v[124:127],v[188:191]\n"
"MF v[248:251],a[200:203],v[112:115],v[248:251]\n"
"MF v[248:251],a[216:219],v[116:119],v[248:251]\n"
"MF v[248:251],a[232:235],v[120:123],v[248:251]\n"
"MF v[248:251],a[248:251],v[124:127],v[248:251]\n"
"MF v[252:255],a[204:207],v[112:115],v[252:255]\n"
"CV v60,v184,v185\n"
"CV v61,v186,v187\n"
"MF v[252:255],a[220:223],v[116:119],v[252:255]\n"
"CV v62,v188,v189\n"
"CV v63,v190,v191\n"
"MF v[252:255],a[236:239],v[120:123],v[252:255]\n"
"MF v[252:255],a[252:255],v[124:127],v[252:255]\n"
"s_barrier\n"
"TR v[96:97],v12 offset:0\n"
"TR v[98:99],v12 offset:2048\n"
"TR v[100:101],v12 offset:40960\n"
"TR v[102:103],v12 offset:43008\n"
"TR v[104:105],v13 offset:0\n"
"TR v[106:107],v13 offset:2048\n"
"TR v[108:109],v13 offset:40960\n"
"TR v[110:111],v13 offset:43008\n"
"TR v[112:113],v14 offset:0\n"
"TR v[114:115],v14 offset:2048\n"
"TR v[116:117],v14 offset:40960\n"
"TR v[118:119],v14 offset:43008\n"
"TR v[120:121],v15 offset:0\n"
"TR v[122:123],v15 offset:2048\n"
"TR v[124:125],v15 offset:40960\n"
"TR v[126:127],v15 offset:43008\n"
"s_lshl_b32 s56, s36, 3\n"
"s_add_u32 s57, s56, s34\n"
"s_sub_u32 s58, s34, s56\n"
"s_add_u32 s58, s58, 120\n"
"s_mul_i32 s60, s57, 20480\n"
"s_add_u32 s60, s60, 1048576\n"
"s_add_u32 s61, s60, 81920\n"
"s_mul_i32 s62, s58, 20480\n"
"s_add_u32 s62, s62, 1048576\n"
"s_add_u32 s63, s62, 81920\n"
"CV v92,v248,v249\n"
"CV v93,v250,v251\n"
"s_add_u32 s65, s65, 524288\n"
"s_add_u32 m0, s66, 147456\n"
"CV v94,v252,v253\n"
"LD v6,s[48:51],s65 offen offset:0 lds\n"
"CV v95,v254,v255\n"
"LD v7,s[48:51],s65 offen offset:1024 lds\n"
"LD v8,s[48:51],s65 offen offset:2048 lds\n"
"LD v9,s[48:51],s65 offen offset:3072 lds\n"
"s_add_u32 s65, s65, 524288\n"
"s_add_u32 m0, s66, 81920\n"
"s_nop 0\n"
"LD v6,s[48:51],s65 offen offset:0 lds\n"
"LD v7,s[48:51],s65 offen offset:1024 lds\n"
"LD v8,s[48:51],s65 offen offset:2048 lds\n"
"LD v9,s[48:51],s65 offen offset:3072 lds\n"
"s_cmp_lt_u32 s33, 96\n"
"s_cbranch_scc1 L_p2free_11\n"
"v_mov_b32 v10, v1\n"
"WT lgkmcnt(14)\n"
"MF v[128:131],v[96:99],a[0:3],0\n"
"WT lgkmcnt(12)\n"
"MF v[128:131],v[100:103],a[8:11],v[128:131]\n"
"MF v[144:147],v[96:99],a[4:7],0\n"
"TR v[96:97],v12 offset:4096\n"
"MF v[144:147],v[100:103],a[12:15],v[144:147]\n"
"WT lgkmcnt(11)\n"
"s_add_u32 s64, s80, 8192\n"
"TR v[98:99],v12 offset:6144\n"
"TR v[100:101],v12 offset:45056\n"
"MF v[132:135],v[104:107],a[0:3],0\n"
"WT lgkmcnt(11)\n"
"ST v[32:35],v1,s[40:43],s64 offen offset:0 sc1\n"
"MF v[132:135],v[108:111],a[8:11],v[132:135]\n"
"TR v[102:103],v12 offset:47104\n"
"MF v[148:151],v[104:107],a[4:7],0\n"
"TR v[104:105],v13 offset:4096\n"
"MF v[148:151],v[108:111],a[12:15],v[148:151]\n"
"WT lgkmcnt(11)\n"
"TR v[106:107],v13 offset:6144\n"
"TR v[108:109],v13 offset:45056\n"
"MF v[136:139],v[112:115],a[0:3],0\n"
"WT lgkmcnt(11)\n"
"TR v[110:111],v13 offset:47104\n"
"MF v[136:139],v[116:119],a[8:11],v[136:139]\n"
"MF v[152:155],v[112:115],a[4:7],0\n"
"TR v[112:113],v14 offset:4096\n"
"MF v[152:155],v[116:119],a[12:15],v[152:155]\n"
"WT lgkmcnt(11)\n"
"TR v[114:115],v14 offset:6144\n"
"TR v[116:117],v14 offset:45056\n"
"MF v[140:143],v[120:123],a[0:3],0\n"
"WT lgkmcnt(11)\n"
"ST v[36:39],v1,s[40:43],s64 offen offset:1024 sc1\n"
"MF v[140:143],v[124:127],a[8:11],v[140:143]\n"
"TR v[118:119],v14 offset:47104\n"
"MF v[156:159],v[120:123],a[4:7],0\n"
"TR v[120:121],v15 offset:4096\n"
"MF v[156:159],v[124:127],a[12:15],v[156:159]\n"
"WT lgkmcnt(11)\n"
"TR v[122:123],v15 offset:6144\n"
"TR v[124:125],v15 offset:45056\n"
"MF v[128:131],v[96:99],a[16:19],v[128:131]\n"
"WT lgkmcnt(11)\n"
"TR v[126:127],v15 offset:47104\n"
"MF v[128:131],v[100:103],a[24:27],v[128:131]\n"
"MF v[144:147],v[96:99],a[20:23],v[144:147]\n"
"LD a[0:3],v10,s[40:43],s60 offen offset:0 sc1\n"
"MF v[144:147],v[100:103],a[28:31],v[144:147]\n"
"WT lgkmcnt(10)\n"
"TR v[96:97],v12 offset:8192\n"
"TR v[98:99],v12 offset:10240\n"
"MF v[132:135],v[104:107],a[16:19],v[132:135]\n"
"WT lgkmcnt(10)\n"
"TR v[100:101],v12 offset:49152\n"
"MF v[132:135],v[108:111],a[24:27],v[132:135]\n"
"TR v[102:103],v12 offset:51200\n"
"MF v[148:151],v[104:107],a[20:23],v[148:151]\n"
"LD a[4:7],v10,s[40:43],s61 offen offset:0 sc1\n"
"MF v[148:151],v[108:111],a[28:31],v[148:151]\n"
"WT lgkmcnt(10)\n"
"TR v[104:105],v13 offset:8192\n"
"TR v[106:107],v13 offset:10240\n"
"MF v[136:139],v[112:115],a[16:19],v[136:139]\n"
"WT lgkmcnt(10)\n"
"TR v[108:109],v13 offset:49152\n"
"MF v[136:139],v[116:119],a[24:27],v[136:139]\n"
"TR v[110:111],v13 offset:51200\n"
"MF v[152:155],v[112:115],a[20:23],v[152:155]\n"
"LD a[8:11],v10,s[40:43],s62 offen offset:0 sc1\n"
"MF v[152:155],v[116:119],a[28:31],v[152:155]\n"
"WT lgkmcnt(10)\n"
"TR v[112:113],v14 offset:8192\n"
"TR v[114:115],v14 offset:10240\n"
"MF v[140:143],v[120:123],a[16:19],v[140:143]\n"
"WT lgkmcnt(10)\n"
"TR v[116:117],v14 offset:49152\n"
"MF v[140:143],v[124:127],a[24:27],v[140:143]\n"
"TR v[118:119],v14 offset:51200\n"
"MF v[156:159],v[120:123],a[20:23],v[156:159]\n"
"LD a[12:15],v10,s[40:43],s63 offen offset:0 sc1\n"
"MF v[156:159],v[124:127],a[28:31],v[156:159]\n"
"WT lgkmcnt(10)\n"
"TR v[120:121],v15 offset:8192\n"
"TR v[122:123],v15 offset:10240\n"
"MF v[128:131],v[96:99],a[32:35],v[128:131]\n"
"WT lgkmcnt(10)\n"
"TR v[124:125],v15 offset:49152\n"
"MF v[128:131],v[100:103],a[40:43],v[128:131]\n"
"TR v[126:127],v15 offset:51200\n"
"MF v[144:147],v[96:99],a[36:39],v[144:147]\n"
"LD a[16:19],v10,s[40:43],s60 offen offset:1024 sc1\n"
"MF v[144:147],v[100:103],a[44:47],v[144:147]\n"
"WT lgkmcnt(10)\n"
"TR v[96:97],v12 offset:12288\n"
"TR v[98:99],v12 offset:14336\n"
"MF v[132:135],v[104:107],a[32:35],v[132:135]\n"
"WT lgkmcnt(10)\n"
"TR v[100:101],v12 offset:53248\n"
"MF v[132:135],v[108:111],a[40:43],v[132:135]\n"
"TR v[102:103],v12 offset:55296\n"
"MF v[148:151],v[104:107],a[36:39],v[148:151]\n"
"LD a[20:23],v10,s[40:43],s61 offen offset:1024 sc1\n"
"MF v[148:151],v[108:111],a[44:47],v[148:151]\n"
"WT lgkmcnt(10)\n"
"TR v[104:105],v13 offset:12288\n"
"TR v[106:107],v13 offset:14336\n"
"MF v[136:139],v[112:115],a[32:35],v[136:139]\n"
"WT lgkmcnt(10)\n"
"TR v[108:109],v13 offset:53248\n"
"MF v[136:139],v[116:119],a[40:43],v[136:139]\n"
"TR v[110:111],v13 offset:55296\n"
"MF v[152:155],v[112:115],a[36:39],v[152:155]\n"
"LD a[24:27],v10,s[40:43],s62 offen offset:1024 sc1\n"
"MF v[152:155],v[116:119],a[44:47],v[152:155]\n"
"WT lgkmcnt(10)\n"
"TR v[112:113],v14 offset:12288\n"
"TR v[114:115],v14 offset:14336\n"
"MF v[140:143],v[120:123],a[32:35],v[140:143]\n"
"WT lgkmcnt(10)\n"
"ST v[40:43],v1,s[40:43],s64 offen offset:2048 sc1\n"
"MF v[140:143],v[124:127],a[40:43],v[140:143]\n"
"TR v[116:117],v14 offset:53248\n"
"MF v[156:159],v[120:123],a[36:39],v[156:159]\n"
"LD a[28:31],v10,s[40:43],s63 offen offset:1024 sc1\n"
"MF v[156:159],v[124:127],a[44:47],v[156:159]\n"
"WT lgkmcnt(9)\n"
"TR v[118:119],v14 offset:55296\n"
"TR v[120:121],v15 offset:12288\n"
"MF v[128:131],v[96:99],a[48:51],v[128:131]\n"
"WT lgkmcnt(9)\n"
"TR v[122:123],v15 offset:14336\n"
"TR v[124:125],v15 offset:53248\n"
"MF v[128:131],v[100:103],a[56:59],v[128:131]\n"
"TR v[126:127],v15 offset:55296\n"
"MF v[144:147],v[96:99],a[52:55],v[144:147]\n"
"LD a[32:35],v10,s[40:43],s60 offen offset:2048 sc1\n"
"MF v[144:147],v[100:103],a[60:63],v[144:147]\n"
"WT lgkmcnt(10)\n"
"TR v[96:97],v12 offset:16384\n"
"TR v[98:99],v12 offset:18432\n"
"MF v[132:135],v[104:107],a[48:51],v[132:135]\n"
"WT lgkmcnt(10)\n"
"TR v[100:101],v12 offset:57344\n"
"MF v[132:135],v[108:111],a[56:59],v[132:135]\n"
"TR v[102:103],v12 offset:59392\n"
"MF v[148:151],v[104:107],a[52:55],v[148:151]\n"
"LD a[36:39],v10,s[40:43],s61 offen offset:2048 sc1\n"
"MF v[148:151],v[108:111],a[60:63],v[148:151]\n"
"WT lgkmcnt(10)\n"
"TR v[104:105],v13 offset:16384\n"
"TR v[106:107],v13 offset:18432\n"
"MF v[136:139],v[112:115],a[48:51],v[136:139]\n"
"WT lgkmcnt(10)\n"
"TR v[108:109],v13 offset:57344\n"
"MF v[136:139],v[116:119],a[56:59],v[136:139]\n"
"TR v[110:111],v13 offset:59392\n"
"MF v[152:155],v[112:115],a[52:55],v[152:155]\n"
"LD a[40:43],v10,s[40:43],s62 offen offset:2048 sc1\n"
"MF v[152:155],v[116:119],a[60:63],v[152:155]\n"
"WT lgkmcnt(10)\n"
"TR v[112:113],v14 offset:16384\n"
"TR v[114:115],v14 offset:18432\n"
"MF v[140:143],v[120:123],a[48:51],v[140:143]\n"
"WT lgkmcnt(10)\n"
"TR v[116:117],v14 offset:57344\n"
"MF v[140:143],v[124:127],a[56:59],v[140:143]\n"
"TR v[118:119],v14 offset:59392\n"
"MF v[156:159],v[120:123],a[52:55],v[156:159]\n"
"LD a[44:47],v10,s[40:43],s63 offen offset:2048 sc1\n"
"MF v[156:159],v[124:127],a[60:63],v[156:159]\n"
"WT lgkmcnt(10)\n"
"TR v[120:121],v15 offset:16384\n"
"TR v[122:123],v15 offset:18432\n"
"MF v[128:131],v[96:99],a[64:67],v[128:131]\n"
"WT lgkmcnt(10)\n"
"TR v[124:125],v15 offset:57344\n"
"MF v[128:131],v[100:103],a[72:75],v[128:131]\n"
"TR v[126:127],v15 offset:59392\n"
"MF v[144:147],v[96:99],a[68:71],v[144:147]\n"
"LD a[48:51],v10,s[40:43],s60 offen offset:3072 sc1\n"
"MF v[144:147],v[100:103],a[76:79],v[144:147]\n"
"WT lgkmcnt(10)\n"
"TR v[96:97],v12 offset:20480\n"
"TR v[98:99],v12 offset:22528\n"
"MF v[132:135],v[104:107],a[64:67],v[132:135]\n"
"WT lgkmcnt(10)\n"
"TR v[100:101],v12 offset:61440\n"
"MF v[132:135],v[108:111],a[72:75],v[132:135]\n"
"TR v[102:103],v12 offset:63488\n"
"MF v[148:151],v[104:107],a[68:71],v[148:151]\n"
"LD a[52:55],v10,s[40:43],s61 offen offset:3072 sc1\n"
"MF v[148:151],v[108:111],a[76:79],v[148:151]\n"
"WT lgkmcnt(10)\n"
"TR v[104:105],v13 offset:20480\n"
"TR v[106:107],v13 offset:22528\n"
"MF v[136:139],v[112:115],a[64:67],v[136:139]\n"
"WT lgkmcnt(10)\n"
"TR v[108:109],v13 offset:61440\n"
"MF v[136:139],v[116:119],a[72:75],v[136:139]\n"
"TR v[110:111],v13 offset:63488\n"
"MF v[152:155],v[112:115],a[68:71],v[152:155]\n"
"LD a[56:59],v10,s[40:43],s62 offen offset:3072 sc1\n"
"MF v[152:155],v[116:119],a[76:79],v[152:155]\n"
"WT lgkmcnt(10)\n"
"TR v[112:113],v14 offset:20480\n"
"TR v[114:115],v14 offset:22528\n"
"MF v[140:143],v[120:123],a[64:67],v[140:143]\n"
"WT lgkmcnt(10)\n"
"TR v[116:117],v14 offset:61440\n"
"MF v[140:143],v[124:127],a[72:75],v[140:143]\n"
"TR v[118:119],v14 offset:63488\n"
"MF v[156:159],v[120:123],a[68:71],v[156:159]\n"
"LD a[60:63],v10,s[40:43],s63 offen offset:3072 sc1\n"
"MF v[156:159],v[124:127],a[76:79],v[156:159]\n"
"WT lgkmcnt(10)\n"
"TR v[120:121],v15 offset:20480\n"
"TR v[122:123],v15 offset:22528\n"
"MF v[128:131],v[96:99],a[80:83],v[128:131]\n"
"WT lgkmcnt(10)\n"
"TR v[124:125],v15 offset:61440\n"
"MF v[128:131],v[100:103],a[88:91],v[128:131]\n"
"TR v[126:127],v15 offset:63488\n"
"MF v[144:147],v[96:99],a[84:87],v[144:147]\n"
"LD a[64:67],v11,s[40:43],s60 offen offset:0 sc1\n"
"MF v[144:147],v[100:103],a[92:95],v[144:147]\n"
"WT lgkmcnt(10)\n"
"TR v[96:97],v12 offset:24576\n"
"TR v[98:99],v12 offset:26624\n"
"MF v[132:135],v[104:107],a[80:83],v[132:135]\n"
"WT lgkmcnt(10)\n"
"ST v[44:47],v1,s[40:43],s64 offen offset:3072 sc1\n"
"MF v[132:135],v[108:111],a[88:91],v[132:135]\n"
"TR v[100:101],v24 offset:0\n"
"MF v[148:151],v[104:107],a[84:87],v[148:151]\n"
"LD a[68:71],v11,s[40:43],s61 offen offset:0 sc1\n"
"MF v[148:151],v[108:111],a[92:95],v[148:151]\n"
"WT lgkmcnt(9)\n"
"TR v[102:103],v24 offset:2048\n"
"TR v[104:105],v13 offset:24576\n"
"MF v[136:139],v[112:115],a[80:83],v[136:139]\n"
"WT lgkmcnt(9)\n"
"TR v[106:107],v13 offset:26624\n"
"TR v[108:109],v25 offset:0\n"
"MF v[136:139],v[116:119],a[88:91],v[136:139]\n"
"TR v[110:111],v25 offset:2048\n"
"MF v[152:155],v[112:115],a[84:87],v[152:155]\n"
"LD a[72:75],v11,s[40:43],s62 offen offset:0 sc1\n"
"MF v[152:155],v[116:119],a[92:95],v[152:155]\n"
"WT lgkmcnt(10)\n"
"TR v[112:113],v14 offset:24576\n"
"TR v[114:115],v14 offset:26624\n"
"MF v[140:143],v[120:123],a[80:83],v[140:143]\n"
"WT lgkmcnt(10)\n"
"TR v[116:117],v26 offset:0\n"
"MF v[140:143],v[124:127],a[88:91],v[140:143]\n"
"TR v[118:119],v26 offset:2048\n"
"MF v[156:159],v[120:123],a[84:87],v[156:159]\n"
"LD a[76:79],v11,s[40:43],s63 offen offset:0 sc1\n"
"MF v[156:159],v[124:127],a[92:95],v[156:159]\n"
"WT lgkmcnt(10)\n"
"TR v[120:121],v15 offset:24576\n"
"TR v[122:123],v15 offset:26624\n"
"MF v[128:131],v[96:99],a[96:99],v[128:131]\n"
"WT lgkmcnt(10)\n"
"TR v[124:125],v27 offset:0\n"
"MF v[128:131],v[100:103],a[104:107],v[128:131]\n"
"TR v[126:127],v27 offset:2048\n"
"MF v[144:147],v[96:99],a[100:103],v[144:147]\n"
"LD a[80:83],v11,s[40:43],s60 offen offset:1024 sc1\n"
"MF v[144:147],v[100:103],a[108:111],v[144:147]\n"
"WT lgkmcnt(10)\n"
"TR v[96:97],v12 offset:28672\n"
"TR v[98:99],v12 offset:30720\n"
"MF v[132:135],v[104:107],a[96:99],v[132:135]\n"
"WT lgkmcnt(10)\n"
"TR v[100:101],v24 offset:4096\n"
"MF v[132:135],v[108:111],a[104:107],v[132:135]\n"
"TR v[102:103],v24 offset:6144\n"
"MF v[148:151],v[104:107],a[100:103],v[148:151]\n"
"LD a[84:87],v11,s[40:43],s61 offen offset:1024 sc1\n"
"MF v[148:151],v[108:111],a[108:111],v[148:151]\n"
"WT lgkmcnt(10)\n"
"TR v[104:105],v13 offset:28672\n"
"TR v[106:107],v13 offset:30720\n"
"MF v[136:139],v[112:115],a[96:99],v[136:139]\n"
"WT lgkmcnt(10)\n"
"TR v[108:109],v25 offset:4096\n"
"MF v[136:139],v[116:119],a[104:107],v[136:139]\n"
"TR v[110:111],v25 offset:6144\n"
"MF v[152:155],v[112:115],a[100:103],v[152:155]\n"
"LD a[88:91],v11,s[40:43],s62 offen offset:1024 sc1\n"
"MF v[152:155],v[116:119],a[108:111],v[152:155]\n"
"WT lgkmcnt(10)\n"
"TR v[112:113],v14 offset:28672\n"
"TR v[114:115],v14 offset:30720\n"
"MF v[140:143],v[120:123],a[96:99],v[140:143]\n"
"WT lgkmcnt(10)\n"
"TR v[116:117],v26 offset:4096\n"
"MF v[140:143],v[124:127],a[104:107],v[140:143]\n"
"TR v[118:119],v26 offset:6144\n"
"MF v[156:159],v[120:123],a[100:103],v[156:159]\n"
"LD a[92:95],v11,s[40:43],s63 offen offset:1024 sc1\n"
"MF v[156:159],v[124:127],a[108:111],v[156:159]\n"
"WT vmcnt(53) lgkmcnt(10)\n"
"TR v[120:121],v15 offset:28672\n"
"TR v[122:123],v15 offset:30720\n"
"MF v[128:131],v[96:99],a[112:115],v[128:131]\n"
"WT vmcnt(50) lgkmcnt(10)\n"
"TR v[124:125],v27 offset:4096\n"
"MF v[128:131],v[100:103],a[120:123],v[128:131]\n"
"TR v[126:127],v27 offset:6144\n"
"MF v[144:147],v[96:99],a[116:119],v[144:147]\n"
"WT vmcnt(49)\n"
"LD a[96:99],v11,s[40:43],s60 offen offset:2048 sc1\n"
"MF v[144:147],v[100:103],a[124:127],v[144:147]\n"
"WT lgkmcnt(10)\n"
"TR v[96:97],v12 offset:32768\n"
"TR v[98:99],v12 offset:34816\n"
"MF v[132:135],v[104:107],a[112:115],v[132:135]\n"
"WT lgkmcnt(10)\n"
"TR v[100:101],v24 offset:8192\n"
"MF v[132:135],v[108:111],a[120:123],v[132:135]\n"
"TR v[102:103],v24 offset:10240\n"
"MF v[148:151],v[104:107],a[116:119],v[148:151]\n"
"LD a[100:103],v11,s[40:43],s61 offen offset:2048 sc1\n"
"MF v[148:151],v[108:111],a[124:127],v[148:151]\n"
"WT lgkmcnt(10)\n"
"TR v[104:105],v13 offset:32768\n"
"TR v[106:107],v13 offset:34816\n"
"MF v[136:139],v[112:115],a[112:115],v[136:139]\n"
"WT lgkmcnt(10)\n"
"TR v[108:109],v25 offset:8192\n"
"MF v[136:139],v[116:119],a[120:123],v[136:139]\n"
"TR v[110:111],v25 offset:10240\n"
"MF v[152:155],v[112:115],a[116:119],v[152:155]\n"
"LD a[104:107],v11,s[40:43],s62 offen offset:2048 sc1\n"
"MF v[152:155],v[116:119],a[124:127],v[152:155]\n"
"WT lgkmcnt(10)\n"
"TR v[112:113],v14 offset:32768\n"
"TR v[114:115],v14 offset:34816\n"
"MF v[140:143],v[120:123],a[112:115],v[140:143]\n"
"WT lgkmcnt(10)\n"
"TR v[116:117],v26 offset:8192\n"
"MF v[140:143],v[124:127],a[120:123],v[140:143]\n"
"TR v[118:119],v26 offset:10240\n"
"MF v[156:159],v[120:123],a[116:119],v[156:159]\n"
"LD a[108:111],v11,s[40:43],s63 offen offset:2048 sc1\n"
"MF v[156:159],v[124:127],a[124:127],v[156:159]\n"
"WT vmcnt(52) lgkmcnt(10)\n"
"TR v[120:121],v15 offset:32768\n"
"TR v[122:123],v15 offset:34816\n"
"MF v[128:131],v[96:99],a[128:131],v[128:131]\n"
"WT vmcnt(50) lgkmcnt(10)\n"
"TR v[124:125],v27 offset:8192\n"
"MF v[128:131],v[100:103],a[136:139],v[128:131]\n"
"TR v[126:127],v27 offset:10240\n"
"MF v[144:147],v[96:99],a[132:135],v[144:147]\n"
"WT vmcnt(49)\n"
"LD a[112:115],v11,s[40:43],s60 offen offset:3072 sc1\n"
"MF v[144:147],v[100:103],a[140:143],v[144:147]\n"
"WT lgkmcnt(10)\n"
"TR v[96:97],v12 offset:36864\n"
"TR v[98:99],v12 offset:38912\n"
"MF v[132:135],v[104:107],a[128:131],v[132:135]\n"
"WT lgkmcnt(10)\n"
"TR v[100:101],v24 offset:12288\n"
"MF v[132:135],v[108:111],a[136:139],v[132:135]\n"
"TR v[102:103],v24 offset:14336\n"
"MF v[148:151],v[104:107],a[132:135],v[148:151]\n"
"LD a[116:119],v11,s[40:43],s61 offen offset:3072 sc1\n"
"MF v[148:151],v[108:111],a[140:143],v[148:151]\n"
"WT lgkmcnt(10)\n"
"TR v[104:105],v13 offset:36864\n"
"TR v[106:107],v13 offset:38912\n"
"MF v[136:139],v[112:115],a[128:131],v[136:139]\n"
"WT lgkmcnt(10)\n"
"TR v[108:109],v25 offset:12288\n"
"MF v[136:139],v[116:119],a[136:139],v[136:139]\n"
"TR v[110:111],v25 offset:14336\n"
"MF v[152:155],v[112:115],a[132:135],v[152:155]\n"
"LD a[120:123],v11,s[40:43],s62 offen offset:3072 sc1\n"
"MF v[152:155],v[116:119],a[140:143],v[152:155]\n"
"WT lgkmcnt(10)\n"
"TR v[112:113],v14 offset:36864\n"
"TR v[114:115],v14 offset:38912\n"
"MF v[140:143],v[120:123],a[128:131],v[140:143]\n"
"WT lgkmcnt(10)\n"
"TR v[116:117],v26 offset:12288\n"
"MF v[140:143],v[124:127],a[136:139],v[140:143]\n"
"TR v[118:119],v26 offset:14336\n"
"MF v[156:159],v[120:123],a[132:135],v[156:159]\n"
"LD a[124:127],v11,s[40:43],s63 offen offset:3072 sc1\n"
"MF v[156:159],v[124:127],a[140:143],v[156:159]\n"
"WT vmcnt(47) lgkmcnt(10)\n"
"TR v[120:121],v15 offset:36864\n"
"TR v[122:123],v15 offset:38912\n"
"MF v[128:131],v[96:99],a[144:147],v[128:131]\n"
"WT vmcnt(45) lgkmcnt(10)\n"
"TR v[124:125],v27 offset:12288\n"
"MF v[128:131],v[100:103],a[152:155],v[128:131]\n"
"TR v[126:127],v27 offset:14336\n"
"v_add_u32 v10, 8192, v10\n"
"MF v[144:147],v[96:99],a[148:151],v[144:147]\n"
"WT vmcnt(44)\n"
"LD a[128:131],v10,s[40:43],s60 offen offset:0 sc1\n"
"MF v[144:147],v[100:103],a[156:159],v[144:147]\n"
"WT lgkmcnt(10)\n"
"MF v[132:135],v[104:107],a[144:147],v[132:135]\n"
"WT lgkmcnt(8)\n"
"MF v[132:135],v[108:111],a[152:155],v[132:135]\n"
"MF v[148:151],v[104:107],a[148:151],v[148:151]\n"
"LD a[132:135],v10,s[40:43],s61 offen offset:0 sc1\n"
"MF v[148:151],v[108:111],a[156:159],v[148:151]\n"
"WT lgkmcnt(6)\n"
"MF v[136:139],v[112:115],a[144:147],v[136:139]\n"
"WT lgkmcnt(4)\n"
"MF v[136:139],v[116:119],a[152:155],v[136:139]\n"
"MF v[152:155],v[112:115],a[148:151],v[152:155]\n"
"LD a[136:139],v10,s[40:43],s62 offen offset:0 sc1\n"
"MF v[152:155],v[116:119],a[156:159],v[152:155]\n"
"WT lgkmcnt(2)\n"
"MF v[140:143],v[120:123],a[144:147],v[140:143]\n"
"WT lgkmcnt(0)\n"
"MF v[140:143],v[124:127],a[152:155],v[140:143]\n"
"MF v[156:159],v[120:123],a[148:151],v[156:159]\n"
"LD a[140:143],v10,s[40:43],s63 offen offset:0 sc1\n"
"MF v[156:159],v[124:127],a[156:159],v[156:159]\n"
"s_barrier\n"
"TR v[96:97],v24 offset:32768\n"
"TR v[98:99],v24 offset:34816\n"
"TR v[100:101],v24 offset:40960\n"
"TR v[102:103],v24 offset:43008\n"
"TR v[104:105],v24 offset:49152\n"
"TR v[106:107],v24 offset:51200\n"
"TR v[108:109],v24 offset:57344\n"
"TR v[110:111],v24 offset:59392\n"
"TR v[112:113],v25 offset:32768\n"
"TR v[114:115],v25 offset:34816\n"
"TR v[116:117],v25 offset:40960\n"
"TR v[118:119],v25 offset:43008\n"
"TR v[120:121],v25 offset:49152\n"
"TR v[122:123],v25 offset:51200\n"
"TR v[124:125],v25 offset:57344\n"
"TR v[126:127],v25 offset:59392\n"
"v_pk_mul_f32 v[160:161], v[128:129], v[128:129]\n"
"v_pk_mul_f32 v[162:163], v[130:131], v[130:131]\n"
"v_pk_mul_f32 v[164:165], v[132:133], v[132:133]\n"
"v_pk_mul_f32 v[166:167], v[134:135], v[134:135]\n"
"v_pk_mul_f32 v[168:169], v[136:137], v[136:137]\n"
"v_pk_mul_f32 v[170:171], v[138:139], v[138:139]\n"
"v_pk_mul_f32 v[172:173], v[140:141], v[140:141]\n"
"v_pk_mul_f32 v[174:175], v[142:143], v[142:143]\n"
"v_pk_mul_f32 v[176:177], v[144:145], v[144:145]\n"
"v_pk_mul_f32 v[178:179], v[146:147], v[146:147]\n"
"v_pk_mul_f32 v[180:181], v[148:149], v[148:149]\n"
"v_pk_mul_f32 v[182:183], v[150:151], v[150:151]\n"
"v_pk_mul_f32 v[184:185], v[152:153], v[152:153]\n"
"v_pk_mul_f32 v[186:187], v[154:155], v[154:155]\n"
"v_pk_mul_f32 v[188:189], v[156:157], v[156:157]\n"
"v_pk_mul_f32 v[190:191], v[158:159], v[158:159]\n"
"v_pk_fma_f32 v[160:161], v[160:161], s[74:75], s[74:75] op_sel:[0,0,1] op_sel_hi:[1,0,1]\n"
"v_pk_fma_f32 v[162:163], v[162:163], s[74:75], s[74:75] op_sel:[0,0,1] op_sel_hi:[1,0,1]\n"
"v_pk_fma_f32 v[164:165], v[164:165], s[74:75], s[74:75] op_sel:[0,0,1] op_sel_hi:[1,0,1]\n"
"v_pk_fma_f32 v[166:167], v[166:167], s[74:75], s[74:75] op_sel:[0,0,1] op_sel_hi:[1,0,1]\n"
"LD a[144:147],v10,s[40:43],s60 offen offset:1024 sc1\n"
"v_pk_fma_f32 v[168:169], v[168:169], s[74:75], s[74:75] op_sel:[0,0,1] op_sel_hi:[1,0,1]\n"
"v_pk_fma_f32 v[170:171], v[170:171], s[74:75], s[74:75] op_sel:[0,0,1] op_sel_hi:[1,0,1]\n"
"v_pk_fma_f32 v[172:173], v[172:173], s[74:75], s[74:75] op_sel:[0,0,1] op_sel_hi:[1,0,1]\n"
"v_pk_fma_f32 v[174:175], v[174:175], s[74:75], s[74:75] op_sel:[0,0,1] op_sel_hi:[1,0,1]\n"
"v_pk_fma_f32 v[176:177], v[176:177], s[74:75], s[74:75] op_sel:[0,0,1] op_sel_hi:[1,0,1]\n"
"v_pk_fma_f32 v[178:179], v[178:179], s[74:75], s[74:75] op_sel:[0,0,1] op_sel_hi:[1,0,1]\n"
"v_pk_fma_f32 v[180:181], v[180:181], s[74:75], s[74:75] op_sel:[0,0,1] op_sel_hi:[1,0,1]\n"
"v_pk_fma_f32 v[182:183], v[182:183], s[74:75], s[74:75] op_sel:[0,0,1] op_sel_hi:[1,0,1]\n"
"v_pk_fma_f32 v[184:185], v[184:185], s[74:75], s[74:75] op_sel:[0,0,1] op_sel_hi:[1,0,1]\n"
"v_pk_fma_f32 v[186:187], v[186:187], s[74:75], s[74:75] op_sel:[0,0,1] op_sel_hi:[1,0,1]\n"
"v_pk_fma_f32 v[188:189], v[188:189], s[74:75], s[74:75] op_sel:[0,0,1] op_sel_hi:[1,0,1]\n"
"v_pk_fma_f32 v[190:191], v[190:191], s[74:75], s[74:75] op_sel:[0,0,1] op_sel_hi:[1,0,1]\n"
"v_pk_mul_f32 v[160:161], v[160:161], v[128:129]\n"
"v_pk_mul_f32 v[162:163], v[162:163], v[130:131]\n"
"v_pk_mul_f32 v[164:165], v[164:165], v[132:133]\n"
"v_pk_mul_f32 v[166:167], v[166:167], v[134:135]\n"
"v_pk_mul_f32 v[168:169], v[168:169], v[136:137]\n"
"v_pk_mul_f32 v[170:171], v[170:171], v[138:139]\n"
"v_pk_mul_f32 v[172:173], v[172:173], v[140:141]\n"
"v_pk_mul_f32 v[174:175], v[174:175], v[142:143]\n"
"LD a[148:151],v10,s[40:43],s61 offen offset:1024 sc1\n"
"v_pk_mul_f32 v[176:177], v[176:177], v[144:145]\n"
"v_pk_mul_f32 v[178:179], v[178:179], v[146:147]\n"
"v_pk_mul_f32 v[180:181], v[180:181], v[148:149]\n"
"v_pk_mul_f32 v[182:183], v[182:183], v[150:151]\n"
"v_pk_mul_f32 v[184:185], v[184:185], v[152:153]\n"
"v_pk_mul_f32 v[186:187], v[186:187], v[154:155]\n"
"v_pk_mul_f32 v[188:189], v[188:189], v[156:157]\n"
"v_pk_mul_f32 v[190:191], v[190:191], v[158:159]\n"
"EX v160,v160\n"
"EX v161,v161\n"
"EX v162,v162\n"
"EX v163,v163\n"
"EX v164,v164\n"
"EX v165,v165\n"
"EX v166,v166\n"
"EX v167,v167\n"
"EX v168,v168\n"
"EX v169,v169\n"
"EX v170,v170\n"
"EX v171,v171\n"
"LD a[152:155],v10,s[40:43],s62 offen offset:1024 sc1\n"
"EX v172,v172\n"
"EX v173,v173\n"
"EX v174,v174\n"
"EX v175,v175\n"
"EX v176,v176\n"
"EX v177,v177\n"
"EX v178,v178\n"
"EX v179,v179\n"
"EX v180,v180\n"
"EX v181,v181\n"
"EX v182,v182\n"
"EX v183,v183\n"
"EX v184,v184\n"
"EX v185,v185\n"
"EX v186,v186\n"
"EX v187,v187\n"
"EX v188,v188\n"
"EX v189,v189\n"
"EX v190,v190\n"
"EX v191,v191\n"
"LD a[156:159],v10,s[40:43],s63 offen offset:1024 sc1\n"
"v_pk_add_f32 v[160:161], v[160:161], 1.0 op_sel_hi:[1,0]\n"
"v_pk_add_f32 v[162:163], v[162:163], 1.0 op_sel_hi:[1,0]\n"
"v_pk_add_f32 v[164:165], v[164:165], 1.0 op_sel_hi:[1,0]\n"
"v_pk_add_f32 v[166:167], v[166:167], 1.0 op_sel_hi:[1,0]\n"
"v_pk_add_f32 v[168:169], v[168:169], 1.0 op_sel_hi:[1,0]\n"
"v_pk_add_f32 v[170:171], v[170:171], 1.0 op_sel_hi:[1,0]\n"
"v_pk_add_f32 v[172:173], v[172:173], 1.0 op_sel_hi:[1,0]\n"
"v_pk_add_f32 v[174:175], v[174:175], 1.0 op_sel_hi:[1,0]\n"
"v_pk_add_f32 v[176:177], v[176:177], 1.0 op_sel_hi:[1,0]\n"
"v_pk_add_f32 v[178:179], v[178:179], 1.0 op_sel_hi:[1,0]\n"
"v_pk_add_f32 v[180:181], v[180:181], 1.0 op_sel_hi:[1,0]\n"
"v_pk_add_f32 v[182:183], v[182:183], 1.0 op_sel_hi:[1,0]\n"
"v_pk_add_f32 v[184:185], v[184:185], 1.0 op_sel_hi:[1,0]\n"
"v_pk_add_f32 v[186:187], v[186:187], 1.0 op_sel_hi:[1,0]\n"
"v_pk_add_f32 v[188:189], v[188:189], 1.0 op_sel_hi:[1,0]\n"
"v_pk_add_f32 v[190:191], v[190:191], 1.0 op_sel_hi:[1,0]\n"
"v_rcp_f32 v160, v160\n"
"v_rcp_f32 v161, v161\n"
"v_rcp_f32 v162, v162\n"
"s_add_u32 s64, s80, 12288\n"
"v_rcp_f32 v163, v163\n"
"ST v[48:51],v1,s[40:43],s64 offen offset:0 sc1\n"
"v_rcp_f32 v164, v164\n"
"v_rcp_f32 v165, v165\n"
"v_rcp_f32 v166, v166\n"
"v_rcp_f32 v167, v167\n"
"v_rcp_f32 v168, v168\n"
"v_rcp_f32 v169, v169\n"
"v_rcp_f32 v170, v170\n"
"v_rcp_f32 v171, v171\n"
"v_rcp_f32 v172, v172\n"
"v_rcp_f32 v173, v173\n"
"v_rcp_f32 v174, v174\n"
"v_rcp_f32 v175, v175\n"
"v_rcp_f32 v176, v176\n"
"v_rcp_f32 v177, v177\n"
"v_rcp_f32 v178, v178\n"
"v_rcp_f32 v179, v179\n"
"v_rcp_f32 v180, v180\n"
"v_rcp_f32 v181, v181\n"
"v_rcp_f32 v182, v182\n"
"v_rcp_f32 v183, v183\n"
"ST v[52:55],v1,s[40:43],s64 offen offset:1024 sc1\n"
"v_rcp_f32 v184, v184\n"
"v_rcp_f32 v185, v185\n"
"v_rcp_f32 v186, v186\n"
"v_rcp_f32 v187, v187\n"
"v_rcp_f32 v188, v188\n"
"v_rcp_f32 v189, v189\n"
"v_rcp_f32 v190, v190\n"
"v_rcp_f32 v191, v191\n"
"v_pk_mul_f32 v[128:129], v[128:129], v[160:161]\n"
"v_pk_mul_f32 v[130:131], v[130:131], v[162:163]\n"
"v_pk_mul_f32 v[132:133], v[132:133], v[164:165]\n"
"v_pk_mul_f32 v[134:135], v[134:135], v[166:167]\n"
"v_pk_mul_f32 v[136:137], v[136:137], v[168:169]\n"
"v_pk_mul_f32 v[138:139], v[138:139], v[170:171]\n"
"v_pk_mul_f32 v[140:141], v[140:141], v[172:173]\n"
"v_pk_mul_f32 v[142:143], v[142:143], v[174:175]\n"
"v_pk_mul_f32 v[144:145], v[144:145], v[176:177]\n"
"v_pk_mul_f32 v[146:147], v[146:147], v[178:179]\n"
"v_pk_mul_f32 v[148:149], v[148:149], v[180:181]\n"
"v_pk_mul_f32 v[150:151], v[150:151], v[182:183]\n"
"v_pk_mul_f32 v[152:153], v[152:153], v[184:185]\n"
"v_pk_mul_f32 v[154:155], v[154:155], v[186:187]\n"
"v_pk_mul_f32 v[156:157], v[156:157], v[188:189]\n"
"v_pk_mul_f32 v[158:159], v[158:159], v[190:191]\n"
"v_and_b32 v21, 15, v0\n"
"s_lshl_b32 s56, s70, 7\n"
"s_lshl_b32 s57, s34, 5\n"
"s_add_u32 s56, s56, s57\n"
"v_add_u32 v21, s56, v21\n"
"v_and_b32 v23, 48, v0\n"
"v_lshlrev_b32 v23, 4, v23\n"
"s_movk_i32 s68, 2048\n"
"s_movk_i32 s67, 20480\n"
"s_lshl_b32 s57, s71, 1\n"
"s_add_u32 s57, s57, 0\n"
"s_mul_i32 s58, s57, 52\n"
"s_lshr_b32 s58, s58, 8\n"
"s_lshr_b32 s59, s58, 1\n"
"s_add_u32 s59, s59, 1\n"
"s_sub_u32 s73, 0, s59\n"
"s_bitcmp1_b32 s58, 0\n"
"s_cselect_b32 s59, s73, s59\n"
"s_lshl_b32 s57, s57, 10\n"
"s_add_u32 s57, s57, 0x700000\n"
"CV v128,v128,v129\n"
"CV v129,v130,v131\n"
"CV v130,v132,v133\n"
"CV v131,v134,v135\n"
"s_add_u32 s58, s59, 0\n"
"v_add_u32 v22, s58, v21\n"
"v_cmp_gt_u32 vcc, s68, v22\n"
"s_nop 1\n"
"v_cndmask_b32 v128, 0, v128, vcc\n"
"v_cndmask_b32 v129, 0, v129, vcc\n"
"v_cndmask_b32 v130, 0, v130, vcc\n"
"v_cndmask_b32 v131, 0, v131, vcc\n"
"v_bfe_u32 v132, v22, 4, 7\n"
"v_and_b32 v22, 15, v22\n"
"v_lshl_add_u32 v22, v22, 4, v23\n"
"v_mad_u32_u24 v132, v132, s67, v22\n"
"ST v[128:131],v132,s[40:43],s57 offen sc1\n"
"CV v144,v144,v145\n"
"CV v145,v146,v147\n"
"CV v146,v148,v149\n"
"CV v147,v150,v151\n"
"s_add_u32 s58, s59, 16\n"
"v_add_u32 v22, s58, v21\n"
"v_cmp_gt_u32 vcc, s68, v22\n"
"s_nop 1\n"
"v_cndmask_b32 v144, 0, v144, vcc\n"
"v_cndmask_b32 v145, 0, v145, vcc\n"
"v_cndmask_b32 v146, 0, v146, vcc\n"
"v_cndmask_b32 v147, 0, v147, vcc\n"
"v_bfe_u32 v148, v22, 4, 7\n"
"v_and_b32 v22, 15, v22\n"
"v_lshl_add_u32 v22, v22, 4, v23\n"
"v_mad_u32_u24 v148, v148, s67, v22\n"
"ST v[144:147],v148,s[40:43],s57 offen sc1\n"
"s_lshl_b32 s57, s71, 1\n"
"s_add_u32 s57, s57, 1\n"
"s_mul_i32 s58, s57, 52\n"
"s_lshr_b32 s58, s58, 8\n"
"s_lshr_b32 s59, s58, 1\n"
"s_add_u32 s59, s59, 1\n"
"s_sub_u32 s73, 0, s59\n"
"s_bitcmp1_b32 s58, 0\n"
"s_cselect_b32 s59, s73, s59\n"
"s_lshl_b32 s57, s57, 10\n"
"s_add_u32 s57, s57, 0x700000\n"
"CV v136,v136,v137\n"
"CV v137,v138,v139\n"
"CV v138,v140,v141\n"
"CV v139,v142,v143\n"
"s_add_u32 s58, s59, 0\n"
"v_add_u32 v22, s58, v21\n"
"v_cmp_gt_u32 vcc, s68, v22\n"
"s_nop 1\n"
"v_cndmask_b32 v136, 0, v136, vcc\n"
"v_cndmask_b32 v137, 0, v137, vcc\n"
"v_cndmask_b32 v138, 0, v138, vcc\n"
"v_cndmask_b32 v139, 0, v139, vcc\n"
"v_bfe_u32 v140, v22, 4, 7\n"
"v_and_b32 v22, 15, v22\n"
"v_lshl_add_u32 v22, v22, 4, v23\n"
"v_mad_u32_u24 v140, v140, s67, v22\n"
"ST v[136:139],v140,s[40:43],s57 offen sc1\n"
"CV v152,v152,v153\n"
"CV v153,v154,v155\n"
"CV v154,v156,v157\n"
"CV v155,v158,v159\n"
"s_add_u32 s58, s59, 16\n"
"v_add_u32 v22, s58, v21\n"
"v_cmp_gt_u32 vcc, s68, v22\n"
"s_nop 1\n"
"v_cndmask_b32 v152, 0, v152, vcc\n"
"v_cndmask_b32 v153, 0, v153, vcc\n"
"v_cndmask_b32 v154, 0, v154, vcc\n"
"v_cndmask_b32 v155, 0, v155, vcc\n"
"v_bfe_u32 v156, v22, 4, 7\n"
"v_and_b32 v22, 15, v22\n"
"v_lshl_add_u32 v22, v22, 4, v23\n"
"v_mad_u32_u24 v156, v156, s67, v22\n"
"ST v[152:155],v156,s[40:43],s57 offen sc1\n"
"L_p2join_12:\n"
"WT vmcnt(43) lgkmcnt(14)\n"
"MF v[128:131],v[96:99],a[0:3],0\n"
"WT vmcnt(39) lgkmcnt(12)\n"
"MF v[128:131],v[100:103],a[16:19],v[128:131]\n"
"WT vmcnt(34) lgkmcnt(10)\n"
"s_add_u32 s65, s65, 524288\n"
"s_add_u32 m0, s66, 0\n"
"MF v[128:131],v[104:107],a[32:35],v[128:131]\n"
"WT vmcnt(30) lgkmcnt(8)\n"
"LD v6,s[48:51],s65 offen offset:0 lds\n"
"MF v[128:131],v[108:111],a[48:51],v[128:131]\n"
"MF v[160:163],v[96:99],a[4:7],0\n"
"MF v[160:163],v[100:103],a[20:23],v[160:163]\n"
"MF v[160:163],v[104:107],a[36:39],v[160:163]\n"
"WT vmcnt(30)\n"
"MF v[160:163],v[108:111],a[52:55],v[160:163]\n"
"MF v[192:195],v[96:99],a[8:11],0\n"
"MF v[192:195],v[100:103],a[24:27],v[192:195]\n"
"MF v[192:195],v[104:107],a[40:43],v[192:195]\n"
"WT vmcnt(29)\n"
"LD v7,s[48:51],s65 offen offset:1024 lds\n"
"MF v[192:195],v[108:111],a[56:59],v[192:195]\n"
"MF v[224:227],v[96:99],a[12:15],0\n"
"MF v[224:227],v[100:103],a[28:31],v[224:227]\n"
"TR v[96:97],v26 offset:32768\n"
"MF v[224:227],v[104:107],a[44:47],v[224:227]\n"
"WT vmcnt(29)\n"
"ST v[56:59],v1,s[40:43],s64 offen offset:2048 sc1\n"
"MF v[224:227],v[108:111],a[60:63],v[224:227]\n"
"WT lgkmcnt(7)\n"
"TR v[98:99],v26 offset:34816\n"
"MF v[132:135],v[112:115],a[0:3],0\n"
"WT lgkmcnt(6)\n"
"MF v[132:135],v[116:119],a[16:19],v[132:135]\n"
"WT lgkmcnt(4)\n"
"TR v[100:101],v26 offset:40960\n"
"MF v[132:135],v[120:123],a[32:35],v[132:135]\n"
"WT lgkmcnt(3)\n"
"LD v8,s[48:51],s65 offen offset:2048 lds\n"
"MF v[132:135],v[124:127],a[48:51],v[132:135]\n"
"TR v[102:103],v26 offset:43008\n"
"MF v[164:167],v[112:115],a[4:7],0\n"
"MF v[164:167],v[116:119],a[20:23],v[164:167]\n"
"TR v[104:105],v26 offset:49152\n"
"MF v[164:167],v[120:123],a[36:39],v[164:167]\n"
"MF v[164:167],v[124:127],a[52:55],v[164:167]\n"
"TR v[106:107],v26 offset:51200\n"
"MF v[196:199],v[112:115],a[8:11],0\n"
"MF v[196:199],v[116:119],a[24:27],v[196:199]\n"
"TR v[108:109],v26 offset:57344\n"
"MF v[196:199],v[120:123],a[40:43],v[196:199]\n"
"LD v9,s[48:51],s65 offen offset:3072 lds\n"
"MF v[196:199],v[124:127],a[56:59],v[196:199]\n"
"TR v[110:111],v26 offset:59392\n"
"MF v[228:231],v[112:115],a[12:15],0\n"
"MF v[228:231],v[116:119],a[28:31],v[228:231]\n"
"TR v[112:113],v27 offset:32768\n"
"MF v[228:231],v[120:123],a[44:47],v[228:231]\n"
"MF v[228:231],v[124:127],a[60:63],v[228:231]\n"
"WT lgkmcnt(7)\n"
"TR v[114:115],v27 offset:34816\n"
"MF v[136:139],v[96:99],a[0:3],0\n"
"WT lgkmcnt(6)\n"
"MF v[136:139],v[100:103],a[16:19],v[136:139]\n"
"WT lgkmcnt(4)\n"
"s_add_u32 s65, s65, 524288\n"
"s_add_u32 m0, s66, 16384\n"
"TR v[116:117],v27 offset:40960\n"
"MF v[136:139],v[104:107],a[32:35],v[136:139]\n"
"WT lgkmcnt(3)\n"
"LD v6,s[48:51],s65 offen offset:0 lds\n"
"MF v[136:139],v[108:111],a[48:51],v[136:139]\n"
"TR v[118:119],v27 offset:43008\n"
"MF v[168:171],v[96:99],a[4:7],0\n"
"MF v[168:171],v[100:103],a[20:23],v[168:171]\n"
"TR v[120:121],v27 offset:49152\n"
"MF v[168:171],v[104:107],a[36:39],v[168:171]\n"
"MF v[168:171],v[108:111],a[52:55],v[168:171]\n"
"TR v[122:123],v27 offset:51200\n"
"MF v[200:203],v[96:99],a[8:11],0\n"
"MF v[200:203],v[100:103],a[24:27],v[200:203]\n"
"TR v[124:125],v27 offset:57344\n"
"MF v[200:203],v[104:107],a[40:43],v[200:203]\n"
"LD v7,s[48:51],s65 offen offset:1024 lds\n"
"MF v[200:203],v[108:111],a[56:59],v[200:203]\n"
"TR v[126:127],v27 offset:59392\n"
"MF v[232:235],v[96:99],a[12:15],0\n"
"MF v[232:235],v[100:103],a[28:31],v[232:235]\n"
"TR v[96:97],v24 offset:36864\n"
"MF v[232:235],v[104:107],a[44:47],v[232:235]\n"
"ST v[60:63],v1,s[40:43],s64 offen offset:3072 sc1\n"
"MF v[232:235],v[108:111],a[60:63],v[232:235]\n"
"WT lgkmcnt(7)\n"
"TR v[98:99],v24 offset:38912\n"
"MF v[140:143],v[112:115],a[0:3],0\n"
"WT lgkmcnt(6)\n"
"MF v[140:143],v[116:119],a[16:19],v[140:143]\n"
"WT lgkmcnt(4)\n"
"TR v[100:101],v24 offset:45056\n"
"MF v[140:143],v[120:123],a[32:35],v[140:143]\n"
"WT lgkmcnt(3)\n"
"LD v8,s[48:51],s65 offen offset:2048 lds\n"
"MF v[140:143],v[124:127],a[48:51],v[140:143]\n"
"TR v[102:103],v24 offset:47104\n"
"MF v[172:175],v[112:115],a[4:7],0\n"
"MF v[172:175],v[116:119],a[20:23],v[172:175]\n"
"TR v[104:105],v24 offset:53248\n"
"MF v[172:175],v[120:123],a[36:39],v[172:175]\n"
"MF v[172:175],v[124:127],a[52:55],v[172:175]\n"
"TR v[106:107],v24 offset:55296\n"
"MF v[204:207],v[112:115],a[8:11],0\n"
"MF v[204:207],v[116:119],a[24:27],v[204:207]\n"
"TR v[108:109],v24 offset:61440\n"
"MF v[204:207],v[120:123],a[40:43],v[204:207]\n"
"LD v9,s[48:51],s65 offen offset:3072 lds\n"
"MF v[204:207],v[124:127],a[56:59],v[204:207]\n"
"TR v[110:111],v24 offset:63488\n"
"MF v[236:239],v[112:115],a[12:15],0\n"
"MF v[236:239],v[116:119],a[28:31],v[236:239]\n"
"TR v[112:113],v25 offset:36864\n"
"MF v[236:239],v[120:123],a[44:47],v[236:239]\n"
"MF v[236:239],v[124:127],a[60:63],v[236:239]\n"
"WT lgkmcnt(7)\n"
"TR v[114:115],v25 offset:38912\n"
"MF v[144:147],v[96:99],a[0:3],0\n"
"WT lgkmcnt(6)\n"
"MF v[144:147],v[100:103],a[16:19],v[144:147]\n"
"WT lgkmcnt(4)\n"
"s_add_u32 s65, s65, 524288\n"
"s_add_u32 m0, s66, 32768\n"
"TR v[116:117],v25 offset:45056\n"
"MF v[144:147],v[104:107],a[32:35],v[144:147]\n"
"WT lgkmcnt(3)\n"
"LD v6,s[48:51],s65 offen offset:0 lds\n"
"MF v[144:147],v[108:111],a[48:51],v[144:147]\n"
"TR v[118:119],v25 offset:47104\n"
"MF v[176:179],v[96:99],a[4:7],0\n"
"MF v[176:179],v[100:103],a[20:23],v[176:179]\n"
"TR v[120:121],v25 offset:53248\n"
"MF v[176:179],v[104:107],a[36:39],v[176:179]\n"
"MF v[176:179],v[108:111],a[52:55],v[176:179]\n"
"TR v[122:123],v25 offset:55296\n"
"MF v[208:211],v[96:99],a[8:11],0\n"
"MF v[208:211],v[100:103],a[24:27],v[208:211]\n"
"TR v[124:125],v25 offset:61440\n"
"MF v[208:211],v[104:107],a[40:43],v[208:211]\n"
"LD v7,s[48:51],s65 offen offset:1024 lds\n"
"MF v[208:211],v[108:111],a[56:59],v[208:211]\n"
"TR v[126:127],v25 offset:63488\n"
"MF v[240:243],v[96:99],a[12:15],0\n"
"MF v[240:243],v[100:103],a[28:31],v[240:243]\n"
"s_add_u32 s64, s80, 24576\n"
"TR v[96:97],v26 offset:36864\n"
"MF v[240:243],v[104:107],a[44:47],v[240:243]\n"
"ST v[64:67],v1,s[40:43],s64 offen offset:0 sc1\n"
"MF v[240:243],v[108:111],a[60:63],v[240:243]\n"
"WT lgkmcnt(7)\n"
"TR v[98:99],v26 offset:38912\n"
"MF v[148:151],v[112:115],a[0:3],0\n"
"WT lgkmcnt(6)\n"
"MF v[148:151],v[116:119],a[16:19],v[148:151]\n"
"WT lgkmcnt(4)\n"
"TR v[100:101],v26 offset:45056\n"
"MF v[148:151],v[120:123],a[32:35],v[148:151]\n"
"WT lgkmcnt(3)\n"
"LD v8,s[48:51],s65 offen offset:2048 lds\n"
"MF v[148:151],v[124:127],a[48:51],v[148:151]\n"
"TR v[102:103],v26 offset:47104\n"
"MF v[180:183],v[112:115],a[4:7],0\n"
"MF v[180:183],v[116:119],a[20:23],v[180:183]\n"
"TR v[104:105],v26 offset:53248\n"
"MF v[180:183],v[120:123],a[36:39],v[180:183]\n"
"MF v[180:183],v[124:127],a[52:55],v[180:183]\n"
"TR v[106:107],v26 offset:55296\n"
"MF v[212:215],v[112:115],a[8:11],0\n"
"MF v[212:215],v[116:119],a[24:27],v[212:215]\n"
"TR v[108:109],v26 offset:61440\n"
"MF v[212:215],v[120:123],a[40:43],v[212:215]\n"
"LD v9,s[48:51],s65 offen offset:3072 lds\n"
"MF v[212:215],v[124:127],a[56:59],v[212:215]\n"
"TR v[110:111],v26 offset:63488\n"
"MF v[244:247],v[112:115],a[12:15],0\n"
"MF v[244:247],v[116:119],a[28:31],v[244:247]\n"
"TR v[112:113],v27 offset:36864\n"
"MF v[244:247],v[120:123],a[44:47],v[244:247]\n"
"MF v[244:247],v[124:127],a[60:63],v[244:247]\n"
"WT lgkmcnt(7)\n"
"TR v[114:115],v27 offset:38912\n"
"MF v[152:155],v[96:99],a[0:3],0\n"
"WT lgkmcnt(6)\n"
"MF v[152:155],v[100:103],a[16:19],v[152:155]\n"
"WT lgkmcnt(4)\n"
"s_add_u32 s65, s65, 524288\n"
"s_add_u32 m0, s66, 49152\n"
"WT vmcnt(14)\n"
"s_barrier\n"
"s_cmp_lg_u32 s34, 0\n"
"s_cbranch_scc1 L_pub_13\n"
"s_mov_b64 exec, 1\n"
"s_lshl_b32 s56, s33, 7\n"
"v_lshl_add_u32 v21, v0, 3, s56\n"
"s_mov_b32 s57, 0x50000\n"
"buffer_store_dwordx2 v[16:17], v21, s[40:43], s57 offen sc1\n"
"s_mov_b64 exec, -1\n"
"L_pub_13:\n"
"TR v[116:117],v27 offset:45056\n"
"MF v[152:155],v[104:107],a[32:35],v[152:155]\n"
"WT lgkmcnt(3)\n"
"LD v6,s[48:51],s65 offen offset:0 lds\n"
"MF v[152:155],v[108:111],a[48:51],v[152:155]\n"
"TR v[118:119],v27 offset:47104\n"
"MF v[184:187],v[96:99],a[4:7],0\n"
"MF v[184:187],v[100:103],a[20:23],v[184:187]\n"
"TR v[120:121],v27 offset:53248\n"
"MF v[184:187],v[104:107],a[36:39],v[184:187]\n"
"MF v[184:187],v[108:111],a[52:55],v[184:187]\n"
"TR v[122:123],v27 offset:55296\n"
"MF v[216:219],v[96:99],a[8:11],0\n"
"MF v[216:219],v[100:103],a[24:27],v[216:219]\n"
"TR v[124:125],v27 offset:61440\n"
"MF v[216:219],v[104:107],a[40:43],v[216:219]\n"
"LD v7,s[48:51],s65 offen offset:1024 lds\n"
"MF v[216:219],v[108:111],a[56:59],v[216:219]\n"
"TR v[126:127],v27 offset:63488\n"
"MF v[248:251],v[96:99],a[12:15],0\n"
"MF v[248:251],v[100:103],a[28:31],v[248:251]\n"
"TR v[96:97],v28 offset:0\n"
"MF v[248:251],v[104:107],a[44:47],v[248:251]\n"
"ST v[68:71],v1,s[40:43],s64 offen offset:1024 sc1\n"
"MF v[248:251],v[108:111],a[60:63],v[248:251]\n"
"WT lgkmcnt(7)\n"
"TR v[98:99],v28 offset:2048\n"
"MF v[156:159],v[112:115],a[0:3],0\n"
"WT lgkmcnt(6)\n"
"MF v[156:159],v[116:119],a[16:19],v[156:159]\n"
"WT lgkmcnt(4)\n"
"TR v[100:101],v28 offset:8192\n"
"MF v[156:159],v[120:123],a[32:35],v[156:159]\n"
"WT lgkmcnt(3)\n"
"LD v8,s[48:51],s65 offen offset:2048 lds\n"
"MF v[156:159],v[124:127],a[48:51],v[156:159]\n"
"TR v[102:103],v28 offset:10240\n"
"MF v[188:191],v[112:115],a[4:7],0\n"
"MF v[188:191],v[116:119],a[20:23],v[188:191]\n"
"TR v[104:105],v28 offset:16384\n"
"MF v[188:191],v[120:123],a[36:39],v[188:191]\n"
"MF v[188:191],v[124:127],a[52:55],v[188:191]\n"
"TR v[106:107],v28 offset:18432\n"
"MF v[220:223],v[112:115],a[8:11],0\n"
"MF v[220:223],v[116:119],a[24:27],v[220:223]\n"
"TR v[108:109],v28 offset:24576\n"
"MF v[220:223],v[120:123],a[40:43],v[220:223]\n"
"LD v9,s[48:51],s65 offen offset:3072 lds\n"
"MF v[220:223],v[124:127],a[56:59],v[220:223]\n"
"TR v[110:111],v28 offset:26624\n"
"MF v[252:255],v[112:115],a[12:15],0\n"
"MF v[252:255],v[116:119],a[28:31],v[252:255]\n"
"TR v[112:113],v29 offset:0\n"
"MF v[252:255],v[120:123],a[44:47],v[252:255]\n"
"MF v[252:255],v[124:127],a[60:63],v[252:255]\n"
"WT lgkmcnt(7)\n"
"TR v[114:115],v29 offset:2048\n"
"MF v[128:131],v[96:99],a[64:67],v[128:131]\n"
"WT lgkmcnt(6)\n"
"MF v[128:131],v[100:103],a[80:83],v[128:131]\n"
"WT lgkmcnt(4)\n"
"s_add_u32 s65, s65, 524288\n"
"s_add_u32 m0, s66, 65536\n"
"TR v[116:117],v29 offset:8192\n"
"MF v[128:131],v[104:107],a[96:99],v[128:131]\n"
"WT lgkmcnt(3)\n"
"LD v6,s[48:51],s65 offen offset:0 lds\n"
"MF v[128:131],v[108:111],a[112:115],v[128:131]\n"
"TR v[118:119],v29 offset:10240\n"
"MF v[160:163],v[96:99],a[68:71],v[160:163]\n"
"MF v[160:163],v[100:103],a[84:87],v[160:163]\n"
"TR v[120:121],v29 offset:16384\n"
"MF v[160:163],v[104:107],a[100:103],v[160:163]\n"
"MF v[160:163],v[108:111],a[116:119],v[160:163]\n"
"TR v[122:123],v29 offset:18432\n"
"MF v[192:195],v[96:99],a[72:75],v[192:195]\n"
"MF v[192:195],v[100:103],a[88:91],v[192:195]\n"
"TR v[124:125],v29 offset:24576\n"
"MF v[192:195],v[104:107],a[104:107],v[192:195]\n"
"LD v7,s[48:51],s65 offen offset:1024 lds\n"
"MF v[192:195],v[108:111],a[120:123],v[192:195]\n"
"TR v[126:127],v29 offset:26624\n"
"MF v[224:227],v[96:99],a[76:79],v[224:227]\n"
"MF v[224:227],v[100:103],a[92:95],v[224:227]\n"
"TR v[96:97],v30 offset:0\n"
"MF v[224:227],v[104:107],a[108:111],v[224:227]\n"
"ST v[72:75],v1,s[40:43],s64 offen offset:2048 sc1\n"
"MF v[224:227],v[108:111],a[124:127],v[224:227]\n"
"WT lgkmcnt(7)\n"
"TR v[98:99],v30 offset:2048\n"
"MF v[132:135],v[112:115],a[64:67],v[132:135]\n"
"WT lgkmcnt(6)\n"
"MF v[132:135],v[116:119],a[80:83],v[132:135]\n"
"WT lgkmcnt(4)\n"
"TR v[100:101],v30 offset:8192\n"
"MF v[132:135],v[120:123],a[96:99],v[132:135]\n"
"WT lgkmcnt(3)\n"
"LD v8,s[48:51],s65 offen offset:2048 lds\n"
"MF v[132:135],v[124:127],a[112:115],v[132:135]\n"
"TR v[102:103],v30 offset:10240\n"
"MF v[164:167],v[112:115],a[68:71],v[164:167]\n"
"MF v[164:167],v[116:119],a[84:87],v[164:167]\n"
"TR v[104:105],v30 offset:16384\n"
"MF v[164:167],v[120:123],a[100:103],v[164:167]\n"
"MF v[164:167],v[124:127],a[116:119],v[164:167]\n"
"TR v[106:107],v30 offset:18432\n"
"MF v[196:199],v[112:115],a[72:75],v[196:199]\n"
"MF v[196:199],v[116:119],a[88:91],v[196:199]\n"
"TR v[108:109],v30 offset:24576\n"
"MF v[196:199],v[120:123],a[104:107],v[196:199]\n"
"LD v9,s[48:51],s65 offen offset:3072 lds\n"
"MF v[196:199],v[124:127],a[120:123],v[196:199]\n"
"TR v[110:111],v30 offset:26624\n"
"MF v[228:231],v[112:115],a[76:79],v[228:231]\n"
"MF v[228:231],v[116:119],a[92:95],v[228:231]\n"
"TR v[112:113],v31 offset:0\n"
"MF v[228:231],v[120:123],a[108:111],v[228:231]\n"
"MF v[228:231],v[124:127],a[124:127],v[228:231]\n"
"WT lgkmcnt(7)\n"
"TR v[114:115],v31 offset:2048\n"
"MF v[136:139],v[96:99],a[64:67],v[136:139]\n"
"WT lgkmcnt(6)\n"
"MF v[136:139],v[100:103],a[80:83],v[136:139]\n"
"WT lgkmcnt(4)\n"
"TR v[116:117],v31 offset:8192\n"
"MF v[136:139],v[104:107],a[96:99],v[136:139]\n"
"WT lgkmcnt(3)\n"
"LD a[0:3],v10,s[40:43],s60 offen offset:2048 sc1\n"
"MF v[136:139],v[108:111],a[112:115],v[136:139]\n"
"TR v[118:119],v31 offset:10240\n"
"MF v[168:171],v[96:99],a[68:71],v[168:171]\n"
"v_min_u32 v22, 29, v0\n"
"v_mul_u32_u24 v21, 26, v22\n"
"MF v[168:171],v[100:103],a[84:87],v[168:171]\n"
"TR v[120:121],v31 offset:16384\n"
"MF v[168:171],v[104:107],a[100:103],v[168:171]\n"
"v_lshrrev_b32 v21, 8, v21\n"
"v_mul_u32_u24 v23, 10, v21\n"
"MF v[168:171],v[108:111],a[116:119],v[168:171]\n"
"TR v[122:123],v31 offset:18432\n"
"MF v[200:203],v[96:99],a[72:75],v[200:203]\n"
"MF v[200:203],v[100:103],a[88:91],v[200:203]\n"
"TR v[124:125],v31 offset:24576\n"
"MF v[200:203],v[104:107],a[104:107],v[200:203]\n"
"LD a[4:7],v10,s[40:43],s61 offen offset:2048 sc1\n"
"MF v[200:203],v[108:111],a[120:123],v[200:203]\n"
"TR v[126:127],v31 offset:26624\n"
"MF v[232:235],v[96:99],a[76:79],v[232:235]\n"
"v_sub_u32 v22, v22, v23\n"
"s_add_u32 s56, s70, 15\n"
"MF v[232:235],v[100:103],a[92:95],v[232:235]\n"
"TR v[96:97],v28 offset:4096\n"
"MF v[232:235],v[104:107],a[108:111],v[232:235]\n"
"ST v[76:79],v1,s[40:43],s64 offen offset:3072 sc1\n"
"MF v[232:235],v[108:111],a[124:127],v[232:235]\n"
"WT lgkmcnt(7)\n"
"TR v[98:99],v28 offset:6144\n"
"MF v[140:143],v[112:115],a[64:67],v[140:143]\n"
"WT lgkmcnt(6)\n"
"MF v[140:143],v[116:119],a[80:83],v[140:143]\n"
"WT lgkmcnt(4)\n"
"TR v[100:101],v28 offset:12288\n"
"MF v[140:143],v[120:123],a[96:99],v[140:143]\n"
"WT lgkmcnt(3)\n"
"LD a[8:11],v10,s[40:43],s62 offen offset:2048 sc1\n"
"MF v[140:143],v[124:127],a[112:115],v[140:143]\n"
"TR v[102:103],v28 offset:14336\n"
"MF v[172:175],v[112:115],a[68:71],v[172:175]\n"
"v_add_u32 v21, s56, v21\n"
"v_and_b32 v21, 15, v21\n"
"MF v[172:175],v[116:119],a[84:87],v[172:175]\n"
"TR v[104:105],v28 offset:20480\n"
"MF v[172:175],v[120:123],a[100:103],v[172:175]\n"
"v_and_b32 v23, 1, v21\n"
"v_mad_u32_u24 v22, v23, 10, v22\n"
"MF v[172:175],v[124:127],a[116:119],v[172:175]\n"
"TR v[106:107],v28 offset:22528\n"
"MF v[204:207],v[112:115],a[72:75],v[204:207]\n"
"MF v[204:207],v[116:119],a[88:91],v[204:207]\n"
"TR v[108:109],v28 offset:28672\n"
"MF v[204:207],v[120:123],a[104:107],v[204:207]\n"
"LD a[12:15],v10,s[40:43],s63 offen offset:2048 sc1\n"
"MF v[204:207],v[124:127],a[120:123],v[204:207]\n"
"TR v[110:111],v28 offset:30720\n"
"MF v[236:239],v[112:115],a[76:79],v[236:239]\n"
"MF v[236:239],v[116:119],a[92:95],v[236:239]\n"
"TR v[112:113],v29 offset:4096\n"
"MF v[236:239],v[120:123],a[108:111],v[236:239]\n"
"v_add_u32 v22, 12, v22\n"
"v_lshrrev_b32 v21, 1, v21\n"
"MF v[236:239],v[124:127],a[124:127],v[236:239]\n"
"WT lgkmcnt(7)\n"
"TR v[114:115],v29 offset:6144\n"
"MF v[144:147],v[96:99],a[64:67],v[144:147]\n"
"WT lgkmcnt(6)\n"
"MF v[144:147],v[100:103],a[80:83],v[144:147]\n"
"WT lgkmcnt(4)\n"
"TR v[116:117],v29 offset:12288\n"
"MF v[144:147],v[104:107],a[96:99],v[144:147]\n"
"WT lgkmcnt(3)\n"
"LD a[16:19],v10,s[40:43],s60 offen offset:3072 sc1\n"
"MF v[144:147],v[108:111],a[112:115],v[144:147]\n"
"TR v[118:119],v29 offset:14336\n"
"MF v[176:179],v[96:99],a[68:71],v[176:179]\n"
"v_lshl_add_u32 v21, v22, 3, v21\n"
"v_lshlrev_b32 v21, 7, v21\n"
"MF v[176:179],v[100:103],a[84:87],v[176:179]\n"
"TR v[120:121],v29 offset:20480\n"
"MF v[176:179],v[104:107],a[100:103],v[176:179]\n"
"v_add_u32 v20, 0x50000, v21\n"
"s_mov_b32 s48, s22\n"
"MF v[176:179],v[108:111],a[116:119],v[176:179]\n"
"TR v[122:123],v29 offset:22528\n"
"MF v[208:211],v[96:99],a[72:75],v[208:211]\n"
"MF v[208:211],v[100:103],a[88:91],v[208:211]\n"
"TR v[124:125],v29 offset:28672\n"
"MF v[208:211],v[104:107],a[104:107],v[208:211]\n"
"LD a[20:23],v10,s[40:43],s61 offen offset:3072 sc1\n"
"MF v[208:211],v[108:111],a[120:123],v[208:211]\n"
"TR v[126:127],v29 offset:30720\n"
"MF v[240:243],v[96:99],a[76:79],v[240:243]\n"
"s_and_b32 s49, s23, 0xffff\n"
"s_mov_b32 s50, s69\n"
"MF v[240:243],v[100:103],a[92:95],v[240:243]\n"
"s_add_u32 s64, s80, 28672\n"
"TR v[96:97],v30 offset:4096\n"
"MF v[240:243],v[104:107],a[108:111],v[240:243]\n"
"ST v[80:83],v1,s[40:43],s64 offen offset:0 sc1\n"
"MF v[240:243],v[108:111],a[124:127],v[240:243]\n"
"WT lgkmcnt(7)\n"
"TR v[98:99],v30 offset:6144\n"
"MF v[148:151],v[112:115],a[64:67],v[148:151]\n"
"WT lgkmcnt(6)\n"
"MF v[148:151],v[116:119],a[80:83],v[148:151]\n"
"WT lgkmcnt(4)\n"
"TR v[100:101],v30 offset:12288\n"
"MF v[148:151],v[120:123],a[96:99],v[148:151]\n"
"WT lgkmcnt(3)\n"
"LD a[24:27],v10,s[40:43],s62 offen offset:3072 sc1\n"
"MF v[148:151],v[124:127],a[112:115],v[148:151]\n"
"TR v[102:103],v30 offset:14336\n"
"MF v[180:183],v[112:115],a[68:71],v[180:183]\n"
"s_mov_b32 s51, 0x20000\n"
"s_mul_i32 s65, s34, 40960\n"
"MF v[180:183],v[116:119],a[84:87],v[180:183]\n"
"TR v[104:105],v30 offset:20480\n"
"MF v[180:183],v[120:123],a[100:103],v[180:183]\n"
"s_mul_i32 s56, s71, 128\n"
"s_add_u32 s65, s65, s56\n"
"MF v[180:183],v[124:127],a[116:119],v[180:183]\n"
"TR v[106:107],v30 offset:22528\n"
"MF v[212:215],v[112:115],a[72:75],v[212:215]\n"
"MF v[212:215],v[116:119],a[88:91],v[212:215]\n"
"TR v[108:109],v30 offset:28672\n"
"MF v[212:215],v[120:123],a[104:107],v[212:215]\n"
"LD a[28:31],v10,s[40:43],s63 offen offset:3072 sc1\n"
"MF v[212:215],v[124:127],a[120:123],v[212:215]\n"
"TR v[110:111],v30 offset:30720\n"
"MF v[244:247],v[112:115],a[76:79],v[244:247]\n"
"MF v[244:247],v[116:119],a[92:95],v[244:247]\n"
"TR v[112:113],v31 offset:4096\n"
"MF v[244:247],v[120:123],a[108:111],v[244:247]\n"
"s_lshl_b32 s66, s34, 12\n"
"s_add_u32 s66, s66, 81920\n"
"MF v[244:247],v[124:127],a[124:127],v[244:247]\n"
"WT lgkmcnt(7)\n"
"TR v[114:115],v31 offset:6144\n"
"MF v[152:155],v[96:99],a[64:67],v[152:155]\n"
"WT lgkmcnt(6)\n"
"MF v[152:155],v[100:103],a[80:83],v[152:155]\n"
"WT lgkmcnt(4)\n"
"TR v[116:117],v31 offset:12288\n"
"v_add_u32 v11, 8192, v11\n"
"MF v[152:155],v[104:107],a[96:99],v[152:155]\n"
"WT lgkmcnt(3)\n"
"LD a[32:35],v11,s[40:43],s60 offen offset:0 sc1\n"
"MF v[152:155],v[108:111],a[112:115],v[152:155]\n"
"TR v[118:119],v31 offset:14336\n"
"MF v[184:187],v[96:99],a[68:71],v[184:187]\n"
"s_lshl_b32 s67, s70, 2\n"
"s_add_u32 s67, s67, s34\n"
"MF v[184:187],v[100:103],a[84:87],v[184:187]\n"
"TR v[120:121],v31 offset:20480\n"
"MF v[184:187],v[104:107],a[100:103],v[184:187]\n"
"s_mul_i32 s67, s67, 40960\n"
"s_add_u32 s67, s67, 0x700000\n"
"MF v[184:187],v[108:111],a[116:119],v[184:187]\n"
"TR v[122:123],v31 offset:22528\n"
"MF v[216:219],v[96:99],a[72:75],v[216:219]\n"
"MF v[216:219],v[100:103],a[88:91],v[216:219]\n"
"TR v[124:125],v31 offset:28672\n"
"MF v[216:219],v[104:107],a[104:107],v[216:219]\n"
"LD a[36:39],v11,s[40:43],s61 offen offset:0 sc1\n"
"MF v[216:219],v[108:111],a[120:123],v[216:219]\n"
"TR v[126:127],v31 offset:30720\n"
"MF v[248:251],v[96:99],a[76:79],v[248:251]\n"
"s_lshl_b32 s81, s71, 11\n"
"s_add_u32 s81, s81, s67\n"
"MF v[248:251],v[100:103],a[92:95],v[248:251]\n"
"TR v[96:97],v24 offset:16384\n"
"MF v[248:251],v[104:107],a[108:111],v[248:251]\n"
"ST v[84:87],v1,s[40:43],s64 offen offset:1024 sc1\n"
"MF v[248:251],v[108:111],a[124:127],v[248:251]\n"
"WT lgkmcnt(7)\n"
"TR v[98:99],v24 offset:18432\n"
"MF v[156:159],v[112:115],a[64:67],v[156:159]\n"
"WT lgkmcnt(6)\n"
"MF v[156:159],v[116:119],a[80:83],v[156:159]\n"
"WT lgkmcnt(4)\n"
"TR v[100:101],v24 offset:24576\n"
"MF v[156:159],v[120:123],a[96:99],v[156:159]\n"
"WT lgkmcnt(3)\n"
"LD a[40:43],v11,s[40:43],s62 offen offset:0 sc1\n"
"MF v[156:159],v[124:127],a[112:115],v[156:159]\n"
"TR v[102:103],v24 offset:26624\n"
"MF v[188:191],v[112:115],a[68:71],v[188:191]\n"
"s_add_u32 s81, s81, 0xffa00000\n"
"s_add_u32 s12, s81, 20480\n"
"MF v[188:191],v[116:119],a[84:87],v[188:191]\n"
"TR v[104:105],v25 offset:16384\n"
"MF v[188:191],v[120:123],a[100:103],v[188:191]\n"
"MF v[188:191],v[124:127],a[116:119],v[188:191]\n"
"TR v[106:107],v25 offset:18432\n"
"MF v[220:223],v[112:115],a[72:75],v[220:223]\n"
"MF v[220:223],v[116:119],a[88:91],v[220:223]\n"
"TR v[108:109],v25 offset:24576\n"
"MF v[220:223],v[120:123],a[104:107],v[220:223]\n"
"LD a[44:47],v11,s[40:43],s63 offen offset:0 sc1\n"
"MF v[220:223],v[124:127],a[120:123],v[220:223]\n"
"TR v[110:111],v25 offset:26624\n"
"MF v[252:255],v[112:115],a[76:79],v[252:255]\n"
"MF v[252:255],v[116:119],a[92:95],v[252:255]\n"
"TR v[112:113],v26 offset:16384\n"
"MF v[252:255],v[120:123],a[108:111],v[252:255]\n"
"MF v[252:255],v[124:127],a[124:127],v[252:255]\n"
"WT lgkmcnt(7)\n"
"TR v[114:115],v26 offset:18432\n"
"MF v[128:131],v[96:99],a[128:131],v[128:131]\n"
"WT lgkmcnt(6)\n"
"MF v[128:131],v[100:103],a[144:147],v[128:131]\n"
"TR v[116:117],v26 offset:24576\n"
"MF v[160:163],v[96:99],a[132:135],v[160:163]\n"
"LD a[48:51],v11,s[40:43],s60 offen offset:1024 sc1\n"
"MF v[160:163],v[100:103],a[148:151],v[160:163]\n"
"TR v[118:119],v26 offset:26624\n"
"MF v[192:195],v[96:99],a[136:139],v[192:195]\n"
"MF v[192:195],v[100:103],a[152:155],v[192:195]\n"
"TR v[120:121],v27 offset:16384\n"
"MF v[224:227],v[96:99],a[140:143],v[224:227]\n"
"MF v[224:227],v[100:103],a[156:159],v[224:227]\n"
"WT lgkmcnt(7)\n"
"TR v[122:123],v27 offset:18432\n"
"MF v[132:135],v[104:107],a[128:131],v[132:135]\n"
"WT lgkmcnt(6)\n"
"MF v[132:135],v[108:111],a[144:147],v[132:135]\n"
"TR v[124:125],v27 offset:24576\n"
"MF v[164:167],v[104:107],a[132:135],v[164:167]\n"
"LD a[52:55],v11,s[40:43],s61 offen offset:1024 sc1\n"
"MF v[164:167],v[108:111],a[148:151],v[164:167]\n"
"TR v[126:127],v27 offset:26624\n"
"MF v[196:199],v[104:107],a[136:139],v[196:199]\n"
"MF v[196:199],v[108:111],a[152:155],v[196:199]\n"
"TR v[96:97],v24 offset:20480\n"
"MF v[228:231],v[104:107],a[140:143],v[228:231]\n"
"ST v[88:91],v1,s[40:43],s64 offen offset:2048 sc1\n"
"MF v[228:231],v[108:111],a[156:159],v[228:231]\n"
"WT lgkmcnt(7)\n"
"TR v[98:99],v24 offset:22528\n"
"MF v[136:139],v[112:115],a[128:131],v[136:139]\n"
"WT lgkmcnt(6)\n"
"MF v[136:139],v[116:119],a[144:147],v[136:139]\n"
"TR v[100:101],v24 offset:28672\n"
"MF v[168:171],v[112:115],a[132:135],v[168:171]\n"
"LD a[56:59],v11,s[40:43],s62 offen offset:1024 sc1\n"
"MF v[168:171],v[116:119],a[148:151],v[168:171]\n"
"TR v[102:103],v24 offset:30720\n"
"MF v[200:203],v[112:115],a[136:139],v[200:203]\n"
"MF v[200:203],v[116:119],a[152:155],v[200:203]\n"
"TR v[104:105],v25 offset:20480\n"
"MF v[232:235],v[112:115],a[140:143],v[232:235]\n"
"MF v[232:235],v[116:119],a[156:159],v[232:235]\n"
"WT lgkmcnt(7)\n"
"TR v[106:107],v25 offset:22528\n"
"MF v[140:143],v[120:123],a[128:131],v[140:143]\n"
"WT lgkmcnt(6)\n"
"MF v[140:143],v[124:127],a[144:147],v[140:143]\n"
"TR v[108:109],v25 offset:28672\n"
"MF v[172:175],v[120:123],a[132:135],v[172:175]\n"
"LD a[60:63],v11,s[40:43],s63 offen offset:1024 sc1\n"
"MF v[172:175],v[124:127],a[148:151],v[172:175]\n"
"TR v[110:111],v25 offset:30720\n"
"MF v[204:207],v[120:123],a[136:139],v[204:207]\n"
"MF v[204:207],v[124:127],a[152:155],v[204:207]\n"
"TR v[112:113],v26 offset:20480\n"
"MF v[236:239],v[120:123],a[140:143],v[236:239]\n"
"MF v[236:239],v[124:127],a[156:159],v[236:239]\n"
"WT lgkmcnt(7)\n"
"TR v[114:115],v26 offset:22528\n"
"MF v[144:147],v[96:99],a[128:131],v[144:147]\n"
"WT lgkmcnt(6)\n"
"MF v[144:147],v[100:103],a[144:147],v[144:147]\n"
"WT vmcnt(20)\n"
"s_barrier\n"
"TR v[116:117],v26 offset:28672\n"
"MF v[176:179],v[96:99],a[132:135],v[176:179]\n"
"LD a[64:67],v11,s[40:43],s60 offen offset:2048 sc1\n"
"MF v[176:179],v[100:103],a[148:151],v[176:179]\n"
"TR v[118:119],v26 offset:30720\n"
"MF v[208:211],v[96:99],a[136:139],v[208:211]\n"
"MF v[208:211],v[100:103],a[152:155],v[208:211]\n"
"TR v[120:121],v27 offset:20480\n"
"MF v[240:243],v[96:99],a[140:143],v[240:243]\n"
"MF v[240:243],v[100:103],a[156:159],v[240:243]\n"
"WT lgkmcnt(7)\n"
"TR v[122:123],v27 offset:22528\n"
"MF v[148:151],v[104:107],a[128:131],v[148:151]\n"
"WT lgkmcnt(6)\n"
"MF v[148:151],v[108:111],a[144:147],v[148:151]\n"
"TR v[124:125],v27 offset:28672\n"
"MF v[180:183],v[104:107],a[132:135],v[180:183]\n"
"LD a[68:71],v11,s[40:43],s61 offen offset:2048 sc1\n"
"MF v[180:183],v[108:111],a[148:151],v[180:183]\n"
"TR v[126:127],v27 offset:30720\n"
"MF v[212:215],v[104:107],a[136:139],v[212:215]\n"
"MF v[212:215],v[108:111],a[152:155],v[212:215]\n"
"TR v[96:97],v12 offset:0\n"
"MF v[244:247],v[104:107],a[140:143],v[244:247]\n"
"ST v[92:95],v1,s[40:43],s64 offen offset:3072 sc1\n"
"MF v[244:247],v[108:111],a[156:159],v[244:247]\n"
"WT lgkmcnt(7)\n"
"TR v[98:99],v12 offset:2048\n"
"MF v[152:155],v[112:115],a[128:131],v[152:155]\n"
"WT lgkmcnt(6)\n"
"MF v[152:155],v[116:119],a[144:147],v[152:155]\n"
"TR v[100:101],v12 offset:8192\n"
"MF v[184:187],v[112:115],a[132:135],v[184:187]\n"
"LD a[72:75],v11,s[40:43],s62 offen offset:2048 sc1\n"
"MF v[184:187],v[116:119],a[148:151],v[184:187]\n"
"TR v[102:103],v12 offset:10240\n"
"MF v[216:219],v[112:115],a[136:139],v[216:219]\n"
"MF v[216:219],v[116:119],a[152:155],v[216:219]\n"
"TR v[104:105],v13 offset:0\n"
"MF v[248:251],v[112:115],a[140:143],v[248:251]\n"
"MF v[248:251],v[116:119],a[156:159],v[248:251]\n"
"WT lgkmcnt(7)\n"
"TR v[106:107],v13 offset:2048\n"
"MF v[156:159],v[120:123],a[128:131],v[156:159]\n"
"WT lgkmcnt(6)\n"
"MF v[156:159],v[124:127],a[144:147],v[156:159]\n"
"TR v[108:109],v13 offset:8192\n"
"MF v[188:191],v[120:123],a[132:135],v[188:191]\n"
"LD a[76:79],v11,s[40:43],s63 offen offset:2048 sc1\n"
"MF v[188:191],v[124:127],a[148:151],v[188:191]\n"
"TR v[110:111],v13 offset:10240\n"
"MF v[220:223],v[120:123],a[136:139],v[220:223]\n"
"MF v[220:223],v[124:127],a[152:155],v[220:223]\n"
"TR v[112:113],v14 offset:0\n"
"MF v[252:255],v[120:123],a[140:143],v[252:255]\n"
"MF v[252:255],v[124:127],a[156:159],v[252:255]\n"
"WT vmcnt(24) lgkmcnt(7)\n"
"TR v[114:115],v14 offset:2048\n"
"MF v[128:131],v[96:99],a[0:3],v[128:131]\n"
"WT vmcnt(19) lgkmcnt(6)\n"
"MF v[128:131],v[100:103],a[16:19],v[128:131]\n"
"TR v[116:117],v14 offset:8192\n"
"MF v[160:163],v[96:99],a[4:7],v[160:163]\n"
"WT vmcnt(18)\n"
"LD a[80:83],v11,s[40:43],s60 offen offset:3072 sc1\n"
"MF v[160:163],v[100:103],a[20:23],v[160:163]\n"
"TR v[118:119],v14 offset:10240\n"
"MF v[192:195],v[96:99],a[8:11],v[192:195]\n"
"WT vmcnt(17)\n"
"MF v[192:195],v[100:103],a[24:27],v[192:195]\n"
"TR v[120:121],v15 offset:0\n"
"MF v[224:227],v[96:99],a[12:15],v[224:227]\n"
"WT vmcnt(16)\n"
"MF v[224:227],v[100:103],a[28:31],v[224:227]\n"
"WT lgkmcnt(7)\n"
"TR v[122:123],v15 offset:2048\n"
"MF v[132:135],v[104:107],a[0:3],v[132:135]\n"
"WT lgkmcnt(6)\n"
"MF v[132:135],v[108:111],a[16:19],v[132:135]\n"
"TR v[124:125],v15 offset:8192\n"
"MF v[164:167],v[104:107],a[4:7],v[164:167]\n"
"LD a[84:87],v11,s[40:43],s61 offen offset:3072 sc1\n"
"MF v[164:167],v[108:111],a[20:23],v[164:167]\n"
"TR v[126:127],v15 offset:10240\n"
"MF v[196:199],v[104:107],a[8:11],v[196:199]\n"
"MF v[196:199],v[108:111],a[24:27],v[196:199]\n"
"TR v[96:97],v12 offset:4096\n"
"MF v[228:231],v[104:107],a[12:15],v[228:231]\n"
"MF v[228:231],v[108:111],a[28:31],v[228:231]\n"
"WT lgkmcnt(7)\n"
"TR v[98:99],v12 offset:6144\n"
"MF v[136:139],v[112:115],a[0:3],v[136:139]\n"
"WT lgkmcnt(6)\n"
"MF v[136:139],v[116:119],a[16:19],v[136:139]\n"
"TR v[100:101],v12 offset:12288\n"
"MF v[168:171],v[112:115],a[4:7],v[168:171]\n"
"LD a[88:91],v11,s[40:43],s62 offen offset:3072 sc1\n"
"MF v[168:171],v[116:119],a[20:23],v[168:171]\n"
"TR v[102:103],v12 offset:14336\n"
"MF v[200:203],v[112:115],a[8:11],v[200:203]\n"
"MF v[200:203],v[116:119],a[24:27],v[200:203]\n"
"TR v[104:105],v13 offset:4096\n"
"MF v[232:235],v[112:115],a[12:15],v[232:235]\n"
"buffer_load_dwordx2 v[18:19], v20, s[40:43], 0 offen sc1\n"
"MF v[232:235],v[116:119],a[28:31],v[232:235]\n"
"WT lgkmcnt(7)\n"
"TR v[106:107],v13 offset:6144\n"
"MF v[140:143],v[120:123],a[0:3],v[140:143]\n"
"WT lgkmcnt(6)\n"
"MF v[140:143],v[124:127],a[16:19],v[140:143]\n"
"TR v[108:109],v13 offset:12288\n"
"MF v[172:175],v[120:123],a[4:7],v[172:175]\n"
"LD a[92:95],v11,s[40:43],s63 offen offset:3072 sc1\n"
"MF v[172:175],v[124:127],a[20:23],v[172:175]\n"
"TR v[110:111],v13 offset:14336\n"
"MF v[204:207],v[120:123],a[8:11],v[204:207]\n"
"MF v[204:207],v[124:127],a[24:27],v[204:207]\n"
"TR v[112:113],v14 offset:4096\n"
"MF v[236:239],v[120:123],a[12:15],v[236:239]\n"
"MF v[236:239],v[124:127],a[28:31],v[236:239]\n"
"WT lgkmcnt(7)\n"
"TR v[114:115],v14 offset:6144\n"
"MF v[144:147],v[96:99],a[0:3],v[144:147]\n"
"WT lgkmcnt(6)\n"
"s_barrier\n"
"MF v[144:147],v[100:103],a[16:19],v[144:147]\n"
"TR v[116:117],v14 offset:12288\n"
"v_add_u32 v10, 8192, v10\n"
"MF v[176:179],v[96:99],a[4:7],v[176:179]\n"
"LD a[96:99],v10,s[40:43],s60 offen offset:0 sc1\n"
"MF v[176:179],v[100:103],a[20:23],v[176:179]\n"
"TR v[118:119],v14 offset:14336\n"
"MF v[208:211],v[96:99],a[8:11],v[208:211]\n"
"MF v[208:211],v[100:103],a[24:27],v[208:211]\n"
"s_add_u32 m0, s66, 0\n"
"TR v[120:121],v15 offset:4096\n"
"MF v[240:243],v[96:99],a[12:15],v[240:243]\n"
"LD v2,s[48:51],s65 offen offset:0 lds\n"
"MF v[240:243],v[100:103],a[28:31],v[240:243]\n"
"WT lgkmcnt(7)\n"
"TR v[122:123],v15 offset:6144\n"
"MF v[148:151],v[104:107],a[0:3],v[148:151]\n"
"WT lgkmcnt(6)\n"
"MF v[148:151],v[108:111],a[16:19],v[148:151]\n"
"TR v[124:125],v15 offset:12288\n"
"MF v[180:183],v[104:107],a[4:7],v[180:183]\n"
"LD a[100:103],v10,s[40:43],s61 offen offset:0 sc1\n"
"MF v[180:183],v[108:111],a[20:23],v[180:183]\n"
"TR v[126:127],v15 offset:14336\n"
"MF v[212:215],v[104:107],a[8:11],v[212:215]\n"
"MF v[212:215],v[108:111],a[24:27],v[212:215]\n"
"TR v[96:97],v12 offset:16384\n"
"MF v[244:247],v[104:107],a[12:15],v[244:247]\n"
"LD v3,s[48:51],s65 offen offset:1024 lds\n"
"MF v[244:247],v[108:111],a[28:31],v[244:247]\n"
"WT lgkmcnt(7)\n"
"TR v[98:99],v12 offset:18432\n"
"MF v[152:155],v[112:115],a[0:3],v[152:155]\n"
"WT lgkmcnt(6)\n"
"MF v[152:155],v[116:119],a[16:19],v[152:155]\n"
"TR v[100:101],v12 offset:24576\n"
"MF v[184:187],v[112:115],a[4:7],v[184:187]\n"
"LD a[104:107],v10,s[40:43],s62 offen offset:0 sc1\n"
"MF v[184:187],v[116:119],a[20:23],v[184:187]\n"
"TR v[102:103],v12 offset:26624\n"
"MF v[216:219],v[112:115],a[8:11],v[216:219]\n"
"MF v[216:219],v[116:119],a[24:27],v[216:219]\n"
"TR v[104:105],v12 offset:32768\n"
"MF v[248:251],v[112:115],a[12:15],v[248:251]\n"
"LD v4,s[48:51],s65 offen offset:2048 lds\n"
"MF v[248:251],v[116:119],a[28:31],v[248:251]\n"
"WT lgkmcnt(7)\n"
"TR v[106:107],v12 offset:34816\n"
"MF v[156:159],v[120:123],a[0:3],v[156:159]\n"
"WT lgkmcnt(6)\n"
"MF v[156:159],v[124:127],a[16:19],v[156:159]\n"
"TR v[108:109],v12 offset:40960\n"
"MF v[188:191],v[120:123],a[4:7],v[188:191]\n"
"LD a[108:111],v10,s[40:43],s63 offen offset:0 sc1\n"
"MF v[188:191],v[124:127],a[20:23],v[188:191]\n"
"TR v[110:111],v12 offset:43008\n"
"MF v[220:223],v[120:123],a[8:11],v[220:223]\n"
"MF v[220:223],v[124:127],a[24:27],v[220:223]\n"
"TR v[112:113],v13 offset:16384\n"
"MF v[252:255],v[120:123],a[12:15],v[252:255]\n"
"MF v[252:255],v[124:127],a[28:31],v[252:255]\n"
"WT vmcnt(26) lgkmcnt(7)\n"
"TR v[114:115],v13 offset:18432\n"
"MF v[128:131],v[96:99],a[32:35],v[128:131]\n"
"WT vmcnt(21) lgkmcnt(6)\n"
"MF v[128:131],v[100:103],a[48:51],v[128:131]\n"
"WT vmcnt(16) lgkmcnt(4)\n"
"TR v[116:117],v13 offset:24576\n"
"MF v[128:131],v[104:107],a[64:67],v[128:131]\n"
"WT vmcnt(11) lgkmcnt(3)\n"
"LD a[112:115],v10,s[40:43],s60 offen offset:1024 sc1\n"
"MF v[128:131],v[108:111],a[80:83],v[128:131]\n"
"TR v[118:119],v13 offset:26624\n"
"MF v[160:163],v[96:99],a[36:39],v[160:163]\n"
"AW a0,0\n"
"AW a1,0\n"
"MF v[160:163],v[100:103],a[52:55],v[160:163]\n"
"TR v[120:121],v13 offset:32768\n"
"MF v[160:163],v[104:107],a[68:71],v[160:163]\n"
"WT vmcnt(11)\n"
"LD v5,s[48:51],s65 offen offset:3072 lds\n"
"MF v[160:163],v[108:111],a[84:87],v[160:163]\n"
"TR v[122:123],v13 offset:34816\n"
"MF v[192:195],v[96:99],a[40:43],v[192:195]\n"
"AW a2,0\n"
"AW a3,0\n"
"MF v[192:195],v[100:103],a[56:59],v[192:195]\n"
"TR v[124:125],v13 offset:40960\n"
"MF v[192:195],v[104:107],a[72:75],v[192:195]\n"
"WT vmcnt(11)\n"
"LD a[116:119],v10,s[40:43],s61 offen offset:1024 sc1\n"
"MF v[192:195],v[108:111],a[88:91],v[192:195]\n"
"TR v[126:127],v13 offset:43008\n"
"MF v[224:227],v[96:99],a[44:47],v[224:227]\n"
"AW a4,0\n"
"AW a5,0\n"
"MF v[224:227],v[100:103],a[60:63],v[224:227]\n"
"s_add_u32 s65, s65, 163840\n"
"s_add_u32 m0, s66, 16384\n"
"TR v[96:97],v14 offset:16384\n"
"MF v[224:227],v[104:107],a[76:79],v[224:227]\n"
"WT vmcnt(10)\n"
"LD v2,s[48:51],s65 offen offset:0 lds\n"
"MF v[224:227],v[108:111],a[92:95],v[224:227]\n"
"WT lgkmcnt(7)\n"
"TR v[98:99],v14 offset:18432\n"
"MF v[132:135],v[112:115],a[32:35],v[132:135]\n"
"WT lgkmcnt(6)\n"
"AW a6,0\n"
"AW a7,0\n"
"MF v[132:135],v[116:119],a[48:51],v[132:135]\n"
"WT lgkmcnt(4)\n"
"TR v[100:101],v14 offset:24576\n"
"MF v[132:135],v[120:123],a[64:67],v[132:135]\n"
"WT lgkmcnt(3)\n"
"LD a[120:123],v10,s[40:43],s62 offen offset:1024 sc1\n"
"MF v[132:135],v[124:127],a[80:83],v[132:135]\n"
"TR v[102:103],v14 offset:26624\n"
"MF v[164:167],v[112:115],a[36:39],v[164:167]\n"
"AW a8,0\n"
"AW a9,0\n"
"MF v[164:167],v[116:119],a[52:55],v[164:167]\n"
"TR v[104:105],v14 offset:32768\n"
"MF v[164:167],v[120:123],a[68:71],v[164:167]\n"
"LD v3,s[48:51],s65 offen offset:1024 lds\n"
"MF v[164:167],v[124:127],a[84:87],v[164:167]\n"
"TR v[106:107],v14 offset:34816\n"
"MF v[196:199],v[112:115],a[40:43],v[196:199]\n"
"AW a10,0\n"
"AW a11,0\n"
"MF v[196:199],v[116:119],a[56:59],v[196:199]\n"
"TR v[108:109],v14 offset:40960\n"
"MF v[196:199],v[120:123],a[72:75],v[196:199]\n"
"LD a[124:127],v10,s[40:43],s63 offen offset:1024 sc1\n"
"MF v[196:199],v[124:127],a[88:91],v[196:199]\n"
"TR v[110:111],v14 offset:43008\n"
"MF v[228:231],v[112:115],a[44:47],v[228:231]\n"
"AW a12,0\n"
"AW a13,0\n"
"MF v[228:231],v[116:119],a[60:63],v[228:231]\n"
"TR v[112:113],v15 offset:16384\n"
"MF v[228:231],v[120:123],a[76:79],v[228:231]\n"
"AW a14,0\n"
"AW a15,0\n"
"MF v[228:231],v[124:127],a[92:95],v[228:231]\n"
"WT lgkmcnt(7)\n"
"TR v[114:115],v15 offset:18432\n"
"MF v[136:139],v[96:99],a[32:35],v[136:139]\n"
"WT lgkmcnt(6)\n"
"MF v[136:139],v[100:103],a[48:51],v[136:139]\n"
"WT lgkmcnt(4)\n"
"s_barrier\n"
"s_cmp_lg_u32 s34, 0\n"
"s_cbranch_scc1 L_pub_14\n"
"s_mov_b64 exec, 1\n"
"s_lshl_b32 s56, s33, 7\n"
"v_lshl_add_u32 v21, v0, 3, s56\n"
"s_mov_b32 s57, 0x48000\n"
"buffer_store_dwordx2 v[16:17], v21, s[40:43], s57 offen sc1\n"
"s_mov_b64 exec, -1\n"
"L_pub_14:\n"
"v_cmp_ne_u64 vcc, s[38:39], v[18:19]\n"
"s_nop 1\n"
"s_cmp_eq_u64 vcc, 0\n"
"s_cbranch_scc0 L_psp_16\n"
"L_pok_15:\n"
"TR v[116:117],v15 offset:24576\n"
"MF v[136:139],v[104:107],a[64:67],v[136:139]\n"
"WT lgkmcnt(3)\n"
"LD a[128:131],v10,s[40:43],s60 offen offset:2048 sc1\n"
"MF v[136:139],v[108:111],a[80:83],v[136:139]\n"
"TR v[118:119],v15 offset:26624\n"
"MF v[168:171],v[96:99],a[36:39],v[168:171]\n"
"AW a16,0\n"
"AW a17,0\n"
"MF v[168:171],v[100:103],a[52:55],v[168:171]\n"
"TR v[120:121],v15 offset:32768\n"
"MF v[168:171],v[104:107],a[68:71],v[168:171]\n"
"LD v4,s[48:51],s65 offen offset:2048 lds\n"
"MF v[168:171],v[108:111],a[84:87],v[168:171]\n"
"TR v[122:123],v15 offset:34816\n"
"MF v[200:203],v[96:99],a[40:43],v[200:203]\n"
"AW a18,0\n"
"AW a19,0\n"
"MF v[200:203],v[100:103],a[56:59],v[200:203]\n"
"TR v[124:125],v15 offset:40960\n"
"MF v[200:203],v[104:107],a[72:75],v[200:203]\n"
"LD a[132:135],v10,s[40:43],s61 offen offset:2048 sc1\n"
"MF v[200:203],v[108:111],a[88:91],v[200:203]\n"
"TR v[126:127],v15 offset:43008\n"
"MF v[232:235],v[96:99],a[44:47],v[232:235]\n"
"AW a20,0\n"
"AW a21,0\n"
"MF v[232:235],v[100:103],a[60:63],v[232:235]\n"
"TR v[96:97],v12 offset:20480\n"
"MF v[232:235],v[104:107],a[76:79],v[232:235]\n"
"LD v5,s[48:51],s65 offen offset:3072 lds\n"
"MF v[232:235],v[108:111],a[92:95],v[232:235]\n"
"WT lgkmcnt(7)\n"
"TR v[98:99],v12 offset:22528\n"
"MF v[140:143],v[112:115],a[32:35],v[140:143]\n"
"WT lgkmcnt(6)\n"
"AW a22,0\n"
"AW a23,0\n"
"MF v[140:143],v[116:119],a[48:51],v[140:143]\n"
"WT lgkmcnt(4)\n"
"TR v[100:101],v12 offset:28672\n"
"MF v[140:143],v[120:123],a[64:67],v[140:143]\n"
"WT lgkmcnt(3)\n"
"LD a[136:139],v10,s[40:43],s62 offen offset:2048 sc1\n"
"MF v[140:143],v[124:127],a[80:83],v[140:143]\n"
"TR v[102:103],v12 offset:30720\n"
"MF v[172:175],v[112:115],a[36:39],v[172:175]\n"
"AW a24,0\n"
"AW a25,0\n"
"MF v[172:175],v[116:119],a[52:55],v[172:175]\n"
"s_add_u32 s65, s65, 163840\n"
"s_add_u32 m0, s66, 32768\n"
"TR v[104:105],v12 offset:36864\n"
"MF v[172:175],v[120:123],a[68:71],v[172:175]\n"
"LD v2,s[48:51],s65 offen offset:0 lds\n"
"MF v[172:175],v[124:127],a[84:87],v[172:175]\n"
"TR v[106:107],v12 offset:38912\n"
"MF v[204:207],v[112:115],a[40:43],v[204:207]\n"
"AW a26,0\n"
"AW a27,0\n"
"MF v[204:207],v[116:119],a[56:59],v[204:207]\n"
"TR v[108:109],v12 offset:45056\n"
"MF v[204:207],v[120:123],a[72:75],v[204:207]\n"
"LD a[140:143],v10,s[40:43],s63 offen offset:2048 sc1\n"
"MF v[204:207],v[124:127],a[88:91],v[204:207]\n"
"TR v[110:111],v12 offset:47104\n"
"MF v[236:239],v[112:115],a[44:47],v[236:239]\n"
"AW a28,0\n"
"AW a29,0\n"
"MF v[236:239],v[116:119],a[60:63],v[236:239]\n"
"TR v[112:113],v13 offset:20480\n"
"MF v[236:239],v[120:123],a[76:79],v[236:239]\n"
"AW a30,0\n"
"AW a31,0\n"
"MF v[236:239],v[124:127],a[92:95],v[236:239]\n"
"WT lgkmcnt(7)\n"
"TR v[114:115],v13 offset:22528\n"
"MF v[144:147],v[96:99],a[32:35],v[144:147]\n"
"WT lgkmcnt(6)\n"
"MF v[144:147],v[100:103],a[48:51],v[144:147]\n"
"WT lgkmcnt(4)\n"
"TR v[116:117],v13 offset:28672\n"
"MF v[144:147],v[104:107],a[64:67],v[144:147]\n"
"WT lgkmcnt(3)\n"
"LD a[144:147],v10,s[40:43],s60 offen offset:3072 sc1\n"
"MF v[144:147],v[108:111],a[80:83],v[144:147]\n"
"TR v[118:119],v13 offset:30720\n"
"MF v[176:179],v[96:99],a[36:39],v[176:179]\n"
"v_and_b32 v21, 7, v0\n"
"v_lshlrev_b32 v21, 5, v21\n"
"MF v[176:179],v[100:103],a[52:55],v[176:179]\n"
"TR v[120:121],v13 offset:36864\n"
"MF v[176:179],v[104:107],a[68:71],v[176:179]\n"
"LD v3,s[48:51],s65 offen offset:1024 lds\n"
"MF v[176:179],v[108:111],a[84:87],v[176:179]\n"
"TR v[122:123],v13 offset:38912\n"
"MF v[208:211],v[96:99],a[40:43],v[208:211]\n"
"s_and_b32 s56, s33, 31\n"
"v_add_u32 v21, s56, v21\n"
"MF v[208:211],v[100:103],a[56:59],v[208:211]\n"
"TR v[124:125],v13 offset:45056\n"
"MF v[208:211],v[104:107],a[72:75],v[208:211]\n"
"LD a[148:151],v10,s[40:43],s61 offen offset:3072 sc1\n"
"MF v[208:211],v[108:111],a[88:91],v[208:211]\n"
"TR v[126:127],v13 offset:47104\n"
"MF v[240:243],v[96:99],a[44:47],v[240:243]\n"
"MF v[240:243],v[100:103],a[60:63],v[240:243]\n"
"TR v[96:97],v14 offset:20480\n"
"MF v[240:243],v[104:107],a[76:79],v[240:243]\n"
"LD v4,s[48:51],s65 offen offset:2048 lds\n"
"MF v[240:243],v[108:111],a[92:95],v[240:243]\n"
"WT lgkmcnt(7)\n"
"TR v[98:99],v14 offset:22528\n"
"MF v[148:151],v[112:115],a[32:35],v[148:151]\n"
"WT lgkmcnt(6)\n"
"v_lshlrev_b32 v21, 7, v21\n"
"v_add_u32 v20, 0x48000, v21\n"
"MF v[148:151],v[116:119],a[48:51],v[148:151]\n"
"WT lgkmcnt(4)\n"
"TR v[100:101],v14 offset:28672\n"
"MF v[148:151],v[120:123],a[64:67],v[148:151]\n"
"WT lgkmcnt(3)\n"
"LD a[152:155],v10,s[40:43],s62 offen offset:3072 sc1\n"
"MF v[148:151],v[124:127],a[80:83],v[148:151]\n"
"TR v[102:103],v14 offset:30720\n"
"MF v[180:183],v[112:115],a[36:39],v[180:183]\n"
"s_mul_i32 s56, s35, 1092352\n"
"s_add_u32 s56, s56, 0x1c00000\n"
"MF v[180:183],v[116:119],a[52:55],v[180:183]\n"
"TR v[104:105],v14 offset:36864\n"
"MF v[180:183],v[120:123],a[68:71],v[180:183]\n"
"LD v5,s[48:51],s65 offen offset:3072 lds\n"
"MF v[180:183],v[124:127],a[84:87],v[180:183]\n"
"TR v[106:107],v14 offset:38912\n"
"MF v[212:215],v[112:115],a[40:43],v[212:215]\n"
"MF v[212:215],v[116:119],a[56:59],v[212:215]\n"
"TR v[108:109],v14 offset:45056\n"
"MF v[212:215],v[120:123],a[72:75],v[212:215]\n"
"LD a[156:159],v10,s[40:43],s63 offen offset:3072 sc1\n"
"MF v[212:215],v[124:127],a[88:91],v[212:215]\n"
"TR v[110:111],v14 offset:47104\n"
"MF v[244:247],v[112:115],a[44:47],v[244:247]\n"
"s_sub_u32 s57, 16, s36\n"
"s_mul_i32 s57, s57, 68096\n"
"MF v[244:247],v[116:119],a[60:63],v[244:247]\n"
"TR v[112:113],v15 offset:20480\n"
"MF v[244:247],v[120:123],a[76:79],v[244:247]\n"
"MF v[244:247],v[124:127],a[92:95],v[244:247]\n"
"WT lgkmcnt(7)\n"
"TR v[114:115],v15 offset:22528\n"
"MF v[152:155],v[96:99],a[32:35],v[152:155]\n"
"WT lgkmcnt(6)\n"
"MF v[152:155],v[100:103],a[48:51],v[152:155]\n"
"WT lgkmcnt(4)\n"
"s_add_u32 s65, s65, 163840\n"
"s_add_u32 m0, s66, 49152\n"
"TR v[116:117],v15 offset:28672\n"
"MF v[152:155],v[104:107],a[64:67],v[152:155]\n"
"WT lgkmcnt(3)\n"
"LD v2,s[48:51],s65 offen offset:0 lds\n"
"MF v[152:155],v[108:111],a[80:83],v[152:155]\n"
"TR v[118:119],v15 offset:30720\n"
"MF v[184:187],v[96:99],a[36:39],v[184:187]\n"
"s_add_u32 s44, s30, s56\n"
"s_addc_u32 s45, s31, 0\n"
"MF v[184:187],v[100:103],a[52:55],v[184:187]\n"
"TR v[120:121],v15 offset:36864\n"
"MF v[184:187],v[104:107],a[68:71],v[184:187]\n"
"LD v3,s[48:51],s65 offen offset:1024 lds\n"
"MF v[184:187],v[108:111],a[84:87],v[184:187]\n"
"TR v[122:123],v15 offset:38912\n"
"MF v[216:219],v[96:99],a[40:43],v[216:219]\n"
"s_and_b32 s45, s45, 0xffff\n"
"s_mov_b32 s46, s57\n"
"MF v[216:219],v[100:103],a[56:59],v[216:219]\n"
"TR v[124:125],v15 offset:45056\n"
"MF v[216:219],v[104:107],a[72:75],v[216:219]\n"
"LD v4,s[48:51],s65 offen offset:2048 lds\n"
"MF v[216:219],v[108:111],a[88:91],v[216:219]\n"
"TR v[126:127],v15 offset:47104\n"
"MF v[248:251],v[96:99],a[44:47],v[248:251]\n"
"MF v[248:251],v[100:103],a[60:63],v[248:251]\n"
"TR v[96:97],v12 offset:49152\n"
"MF v[248:251],v[104:107],a[76:79],v[248:251]\n"
"LD v5,s[48:51],s65 offen offset:3072 lds\n"
"MF v[248:251],v[108:111],a[92:95],v[248:251]\n"
"WT lgkmcnt(7)\n"
"TR v[98:99],v12 offset:51200\n"
"MF v[156:159],v[112:115],a[32:35],v[156:159]\n"
"WT lgkmcnt(6)\n"
"s_mov_b32 s47, 0x20000\n"
"s_and_b32 s56, s34, 2\n"
"MF v[156:159],v[116:119],a[48:51],v[156:159]\n"
"WT lgkmcnt(4)\n"
"TR v[100:101],v12 offset:57344\n"
"MF v[156:159],v[120:123],a[64:67],v[156:159]\n"
"WT lgkmcnt(3)\n"
"MF v[156:159],v[124:127],a[80:83],v[156:159]\n"
"TR v[102:103],v12 offset:59392\n"
"MF v[188:191],v[112:115],a[36:39],v[188:191]\n"
"s_add_u32 s56, s56, s34\n"
"s_lshl_b32 s17, s56, 12\n"
"MF v[188:191],v[116:119],a[52:55],v[188:191]\n"
"s_add_u32 s65, s65, 163840\n"
"s_add_u32 m0, s66, 65536\n"
"TR v[104:105],v24 offset:0\n"
"MF v[188:191],v[120:123],a[68:71],v[188:191]\n"
"LD v2,s[48:51],s65 offen offset:0 lds\n"
"MF v[188:191],v[124:127],a[84:87],v[188:191]\n"
"TR v[106:107],v24 offset:2048\n"
"MF v[220:223],v[112:115],a[40:43],v[220:223]\n"
"MF v[220:223],v[116:119],a[56:59],v[220:223]\n"
"TR v[108:109],v24 offset:8192\n"
"MF v[220:223],v[120:123],a[72:75],v[220:223]\n"
"LD v3,s[48:51],s65 offen offset:1024 lds\n"
"MF v[220:223],v[124:127],a[88:91],v[220:223]\n"
"TR v[110:111],v24 offset:10240\n"
"MF v[252:255],v[112:115],a[44:47],v[252:255]\n"
"s_sub_u32 s16, s17, 25856\n"
"s_add_u32 s17, s17, 98304\n"
"MF v[252:255],v[116:119],a[60:63],v[252:255]\n"
"TR v[112:113],v13 offset:49152\n"
"MF v[252:255],v[120:123],a[76:79],v[252:255]\n"
"LD v4,s[48:51],s65 offen offset:2048 lds\n"
"MF v[252:255],v[124:127],a[92:95],v[252:255]\n"
"WT vmcnt(34) lgkmcnt(7)\n"
"TR v[114:115],v13 offset:51200\n"
"MF v[128:131],v[96:99],a[96:99],v[128:131]\n"
"WT vmcnt(27) lgkmcnt(6)\n"
"MF v[128:131],v[100:103],a[112:115],v[128:131]\n"
"WT vmcnt(20) lgkmcnt(4)\n"
"TR v[116:117],v13 offset:57344\n"
"MF v[128:131],v[104:107],a[128:131],v[128:131]\n"
"WT vmcnt(13) lgkmcnt(3)\n"
"LD v5,s[48:51],s65 offen offset:3072 lds\n"
"MF v[128:131],v[108:111],a[144:147],v[128:131]\n"
"TR v[118:119],v13 offset:59392\n"
"MF v[160:163],v[96:99],a[100:103],v[160:163]\n"
"MF v[160:163],v[100:103],a[116:119],v[160:163]\n"
"s_add_u32 s68, s67, 0\n"
"TR v[120:121],v25 offset:0\n"
"MF v[160:163],v[104:107],a[132:135],v[160:163]\n"
"WT vmcnt(12)\n"
"LD a[160:163],v1,s[76:79],s68 offen offset:0 sc1\n"
"MF v[160:163],v[108:111],a[148:151],v[160:163]\n"
"TR v[122:123],v25 offset:2048\n"
"MF v[192:195],v[96:99],a[104:107],v[192:195]\n"
"MF v[192:195],v[100:103],a[120:123],v[192:195]\n"
"s_add_u32 s73, s67, 20480\n"
"TR v[124:125],v25 offset:8192\n"
"MF v[192:195],v[104:107],a[136:139],v[192:195]\n"
"WT vmcnt(11)\n"
"LD a[164:167],v1,s[76:79],s73 offen offset:0 sc1\n"
"MF v[192:195],v[108:111],a[152:155],v[192:195]\n"
"TR v[126:127],v25 offset:10240\n"
"MF v[224:227],v[96:99],a[108:111],v[224:227]\n"
"MF v[224:227],v[100:103],a[124:127],v[224:227]\n"
"TR v[96:97],v14 offset:49152\n"
"MF v[224:227],v[104:107],a[140:143],v[224:227]\n"
"WT vmcnt(10)\n"
"LD a[168:171],v1,s[76:79],s68 offen offset:1024 sc1\n"
"MF v[224:227],v[108:111],a[156:159],v[224:227]\n"
"WT lgkmcnt(7)\n"
"TR v[98:99],v14 offset:51200\n"
"MF v[132:135],v[112:115],a[96:99],v[132:135]\n"
"WT lgkmcnt(6)\n"
"MF v[132:135],v[116:119],a[112:115],v[132:135]\n"
"WT lgkmcnt(4)\n"
"TR v[100:101],v14 offset:57344\n"
"MF v[132:135],v[120:123],a[128:131],v[132:135]\n"
"WT lgkmcnt(3)\n"
"MF v[132:135],v[124:127],a[144:147],v[132:135]\n"
"TR v[102:103],v14 offset:59392\n"
"MF v[164:167],v[112:115],a[100:103],v[164:167]\n"
"MF v[164:167],v[116:119],a[116:119],v[164:167]\n"
"TR v[104:105],v26 offset:0\n"
"MF v[164:167],v[120:123],a[132:135],v[164:167]\n"
"LD a[172:175],v1,s[76:79],s73 offen offset:1024 sc1\n"
"MF v[164:167],v[124:127],a[148:151],v[164:167]\n"
"TR v[106:107],v26 offset:2048\n"
"MF v[196:199],v[112:115],a[104:107],v[196:199]\n"
"v_mul_f32 v128, 0x3e0293ee, v128\n"
"v_mul_f32 v129, 0x3e0293ee, v129\n"
"MF v[196:199],v[116:119],a[120:123],v[196:199]\n"
"TR v[108:109],v26 offset:8192\n"
"v_mul_f32 v130, 0x3e0293ee, v130\n"
"MF v[196:199],v[120:123],a[136:139],v[196:199]\n"
"LD a[176:179],v1,s[76:79],s68 offen offset:2048 sc1\n"
"MF v[196:199],v[124:127],a[152:155],v[196:199]\n"
"TR v[110:111],v26 offset:10240\n"
"v_mul_f32 v131, 0x3e0293ee, v131\n"
"MF v[228:231],v[112:115],a[108:111],v[228:231]\n"
"v_mul_f32 v132, 0x3e0293ee, v132\n"
"v_mul_f32 v133, 0x3e0293ee, v133\n"
"MF v[228:231],v[116:119],a[124:127],v[228:231]\n"
"TR v[112:113],v15 offset:49152\n"
"v_mul_f32 v134, 0x3e0293ee, v134\n"
"MF v[228:231],v[120:123],a[140:143],v[228:231]\n"
"LD a[180:183],v1,s[76:79],s73 offen offset:2048 sc1\n"
"MF v[228:231],v[124:127],a[156:159],v[228:231]\n"
"WT lgkmcnt(7)\n"
"TR v[114:115],v15 offset:51200\n"
"v_mul_f32 v135, 0x3e0293ee, v135\n"
"MF v[136:139],v[96:99],a[96:99],v[136:139]\n"
"WT lgkmcnt(6)\n"
"CV v32,v128,v129\n"
"CV v33,v130,v131\n"
"MF v[136:139],v[100:103],a[112:115],v[136:139]\n"
"WT lgkmcnt(4)\n"
"TR v[116:117],v15 offset:57344\n"
"CV v34,v132,v133\n"
"MF v[136:139],v[104:107],a[128:131],v[136:139]\n"
"WT lgkmcnt(3)\n"
"LD a[184:187],v1,s[76:79],s68 offen offset:3072 sc1\n"
"MF v[136:139],v[108:111],a[144:147],v[136:139]\n"
"TR v[118:119],v15 offset:59392\n"
"CV v35,v134,v135\n"
"MF v[168:171],v[96:99],a[100:103],v[168:171]\n"
"v_mul_f32 v160, 0x3e0293ee, v160\n"
"v_mul_f32 v161, 0x3e0293ee, v161\n"
"MF v[168:171],v[100:103],a[116:119],v[168:171]\n"
"TR v[120:121],v27 offset:0\n"
"v_mul_f32 v162, 0x3e0293ee, v162\n"
"MF v[168:171],v[104:107],a[132:135],v[168:171]\n"
"LD a[188:191],v1,s[76:79],s73 offen offset:3072 sc1\n"
"MF v[168:171],v[108:111],a[148:151],v[168:171]\n"
"TR v[122:123],v27 offset:2048\n"
"v_mul_f32 v163, 0x3e0293ee, v163\n"
"MF v[200:203],v[96:99],a[104:107],v[200:203]\n"
"v_mul_f32 v164, 0x3e0293ee, v164\n"
"v_mul_f32 v165, 0x3e0293ee, v165\n"
"MF v[200:203],v[100:103],a[120:123],v[200:203]\n"
"s_add_u32 s68, s67, 4096\n"
"TR v[124:125],v27 offset:8192\n"
"v_mul_f32 v166, 0x3e0293ee, v166\n"
"MF v[200:203],v[104:107],a[136:139],v[200:203]\n"
"LD a[192:195],v1,s[76:79],s68 offen offset:0 sc1\n"
"MF v[200:203],v[108:111],a[152:155],v[200:203]\n"
"TR v[126:127],v27 offset:10240\n"
"v_mul_f32 v167, 0x3e0293ee, v167\n"
"MF v[232:235],v[96:99],a[108:111],v[232:235]\n"
"CV v48,v160,v161\n"
"CV v49,v162,v163\n"
"MF v[232:235],v[100:103],a[124:127],v[232:235]\n"
"s_add_u32 s73, s67, 24576\n"
"TR v[96:97],v12 offset:53248\n"
"CV v50,v164,v165\n"
"MF v[232:235],v[104:107],a[140:143],v[232:235]\n"
"LD a[196:199],v1,s[76:79],s73 offen offset:0 sc1\n"
"MF v[232:235],v[108:111],a[156:159],v[232:235]\n"
"WT lgkmcnt(7)\n"
"TR v[98:99],v12 offset:55296\n"
"CV v51,v166,v167\n"
"MF v[140:143],v[112:115],a[96:99],v[140:143]\n"
"WT lgkmcnt(6)\n"
"v_mul_f32 v192, 0x3e0293ee, v192\n"
"v_mul_f32 v193, 0x3e0293ee, v193\n"
"MF v[140:143],v[116:119],a[112:115],v[140:143]\n"
"WT lgkmcnt(4)\n"
"TR v[100:101],v12 offset:61440\n"
"v_mul_f32 v194, 0x3e0293ee, v194\n"
"MF v[140:143],v[120:123],a[128:131],v[140:143]\n"
"WT lgkmcnt(3)\n"
"v_mul_f32 v195, 0x3e0293ee, v195\n"
"v_mul_f32 v196, 0x3e0293ee, v196\n"
"MF v[140:143],v[124:127],a[144:147],v[140:143]\n"
"TR v[102:103],v12 offset:63488\n"
"v_mul_f32 v197, 0x3e0293ee, v197\n"
"MF v[172:175],v[112:115],a[100:103],v[172:175]\n"
"v_mul_f32 v198, 0x3e0293ee, v198\n"
"v_mul_f32 v199, 0x3e0293ee, v199\n"
"MF v[172:175],v[116:119],a[116:119],v[172:175]\n"
"TR v[104:105],v24 offset:4096\n"
"CV v64,v192,v193\n"
"MF v[172:175],v[120:123],a[132:135],v[172:175]\n"
"LD a[200:203],v1,s[76:79],s68 offen offset:1024 sc1\n"
"MF v[172:175],v[124:127],a[148:151],v[172:175]\n"
"TR v[106:107],v24 offset:6144\n"
"CV v65,v194,v195\n"
"MF v[204:207],v[112:115],a[104:107],v[204:207]\n"
"CV v66,v196,v197\n"
"CV v67,v198,v199\n"
"MF v[204:207],v[116:119],a[120:123],v[204:207]\n"
"TR v[108:109],v24 offset:12288\n"
"v_mul_f32 v224, 0x3e0293ee, v224\n"
"MF v[204:207],v[120:123],a[136:139],v[204:207]\n"
"LD a[204:207],v1,s[76:79],s73 offen offset:1024 sc1\n"
"MF v[204:207],v[124:127],a[152:155],v[204:207]\n"
"TR v[110:111],v24 offset:14336\n"
"v_mul_f32 v225, 0x3e0293ee, v225\n"
"MF v[236:239],v[112:115],a[108:111],v[236:239]\n"
"v_mul_f32 v226, 0x3e0293ee, v226\n"
"v_mul_f32 v227, 0x3e0293ee, v227\n"
"MF v[236:239],v[116:119],a[124:127],v[236:239]\n"
"TR v[112:113],v13 offset:53248\n"
"v_mul_f32 v228, 0x3e0293ee, v228\n"
"MF v[236:239],v[120:123],a[140:143],v[236:239]\n"
"LD a[208:211],v1,s[76:79],s68 offen offset:2048 sc1\n"
"MF v[236:239],v[124:127],a[156:159],v[236:239]\n"
"WT lgkmcnt(7)\n"
"TR v[114:115],v13 offset:55296\n"
"v_mul_f32 v229, 0x3e0293ee, v229\n"
"MF v[144:147],v[96:99],a[96:99],v[144:147]\n"
"WT lgkmcnt(6)\n"
"v_mul_f32 v230, 0x3e0293ee, v230\n"
"v_mul_f32 v231, 0x3e0293ee, v231\n"
"MF v[144:147],v[100:103],a[112:115],v[144:147]\n"
"WT lgkmcnt(4)\n"
"TR v[116:117],v13 offset:61440\n"
"CV v80,v224,v225\n"
"MF v[144:147],v[104:107],a[128:131],v[144:147]\n"
"WT lgkmcnt(3)\n"
"LD a[212:215],v1,s[76:79],s73 offen offset:2048 sc1\n"
"MF v[144:147],v[108:111],a[144:147],v[144:147]\n"
"TR v[118:119],v13 offset:63488\n"
"CV v81,v226,v227\n"
"MF v[176:179],v[96:99],a[100:103],v[176:179]\n"
"CV v82,v228,v229\n"
"CV v83,v230,v231\n"
"MF v[176:179],v[100:103],a[116:119],v[176:179]\n"
"TR v[120:121],v25 offset:4096\n"
"v_mul_f32 v136, 0x3e0293ee, v136\n"
"MF v[176:179],v[104:107],a[132:135],v[176:179]\n"
"LD a[216:219],v1,s[76:79],s68 offen offset:3072 sc1\n"
"MF v[176:179],v[108:111],a[148:151],v[176:179]\n"
"TR v[122:123],v25 offset:6144\n"
"v_mul_f32 v137, 0x3e0293ee, v137\n"
"MF v[208:211],v[96:99],a[104:107],v[208:211]\n"
"v_mul_f32 v138, 0x3e0293ee, v138\n"
"v_mul_f32 v139, 0x3e0293ee, v139\n"
"MF v[208:211],v[100:103],a[120:123],v[208:211]\n"
"TR v[124:125],v25 offset:12288\n"
"v_mul_f32 v140, 0x3e0293ee, v140\n"
"MF v[208:211],v[104:107],a[136:139],v[208:211]\n"
"LD a[220:223],v1,s[76:79],s73 offen offset:3072 sc1\n"
"MF v[208:211],v[108:111],a[152:155],v[208:211]\n"
"TR v[126:127],v25 offset:14336\n"
"v_mul_f32 v141, 0x3e0293ee, v141\n"
"MF v[240:243],v[96:99],a[108:111],v[240:243]\n"
"v_mul_f32 v142, 0x3e0293ee, v142\n"
"v_mul_f32 v143, 0x3e0293ee, v143\n"
"MF v[240:243],v[100:103],a[124:127],v[240:243]\n"
"s_add_u32 s68, s67, 8192\n"
"TR v[96:97],v14 offset:53248\n"
"CV v36,v136,v137\n"
"MF v[240:243],v[104:107],a[140:143],v[240:243]\n"
"LD a[224:227],v1,s[76:79],s68 offen offset:0 sc1\n"
"MF v[240:243],v[108:111],a[156:159],v[240:243]\n"
"WT lgkmcnt(7)\n"
"TR v[98:99],v14 offset:55296\n"
"CV v37,v138,v139\n"
"MF v[148:151],v[112:115],a[96:99],v[148:151]\n"
"WT lgkmcnt(6)\n"
"CV v38,v140,v141\n"
"CV v39,v142,v143\n"
"MF v[148:151],v[116:119],a[112:115],v[148:151]\n"
"WT lgkmcnt(4)\n"
"TR v[100:101],v14 offset:61440\n"
"v_mul_f32 v168, 0x3e0293ee, v168\n"
"MF v[148:151],v[120:123],a[128:131],v[148:151]\n"
"WT lgkmcnt(3)\n"
"v_mul_f32 v169, 0x3e0293ee, v169\n"
"v_mul_f32 v170, 0x3e0293ee, v170\n"
"MF v[148:151],v[124:127],a[144:147],v[148:151]\n"
"TR v[102:103],v14 offset:63488\n"
"v_mul_f32 v171, 0x3e0293ee, v171\n"
"MF v[180:183],v[112:115],a[100:103],v[180:183]\n"
"v_mul_f32 v172, 0x3e0293ee, v172\n"
"v_mul_f32 v173, 0x3e0293ee, v173\n"
"MF v[180:183],v[116:119],a[116:119],v[180:183]\n"
"s_add_u32 s73, s67, 28672\n"
"TR v[104:105],v26 offset:4096\n"
"v_mul_f32 v174, 0x3e0293ee, v174\n"
"MF v[180:183],v[120:123],a[132:135],v[180:183]\n"
"LD a[228:231],v1,s[76:79],s73 offen offset:0 sc1\n"
"MF v[180:183],v[124:127],a[148:151],v[180:183]\n"
"TR v[106:107],v26 offset:6144\n"
"v_mul_f32 v175, 0x3e0293ee, v175\n"
"MF v[212:215],v[112:115],a[104:107],v[212:215]\n"
"CV v52,v168,v169\n"
"CV v53,v170,v171\n"
"MF v[212:215],v[116:119],a[120:123],v[212:215]\n"
"TR v[108:109],v26 offset:12288\n"
"CV v54,v172,v173\n"
"MF v[212:215],v[120:123],a[136:139],v[212:215]\n"
"LD a[232:235],v1,s[76:79],s68 offen offset:1024 sc1\n"
"MF v[212:215],v[124:127],a[152:155],v[212:215]\n"
"TR v[110:111],v26 offset:14336\n"
"CV v55,v174,v175\n"
"MF v[244:247],v[112:115],a[108:111],v[244:247]\n"
"v_mul_f32 v200, 0x3e0293ee, v200\n"
"v_mul_f32 v201, 0x3e0293ee, v201\n"
"MF v[244:247],v[116:119],a[124:127],v[244:247]\n"
"TR v[112:113],v15 offset:53248\n"
"v_mul_f32 v202, 0x3e0293ee, v202\n"
"MF v[244:247],v[120:123],a[140:143],v[244:247]\n"
"LD a[236:239],v1,s[76:79],s73 offen offset:1024 sc1\n"
"MF v[244:247],v[124:127],a[156:159],v[244:247]\n"
"WT lgkmcnt(7)\n"
"TR v[114:115],v15 offset:55296\n"
"v_mul_f32 v203, 0x3e0293ee, v203\n"
"MF v[152:155],v[96:99],a[96:99],v[152:155]\n"
"WT lgkmcnt(6)\n"
"v_mul_f32 v204, 0x3e0293ee, v204\n"
"v_mul_f32 v205, 0x3e0293ee, v205\n"
"MF v[152:155],v[100:103],a[112:115],v[152:155]\n"
"WT lgkmcnt(4)\n"
"TR v[116:117],v15 offset:61440\n"
"v_mul_f32 v206, 0x3e0293ee, v206\n"
"MF v[152:155],v[104:107],a[128:131],v[152:155]\n"
"WT lgkmcnt(3)\n"
"LD a[240:243],v1,s[76:79],s68 offen offset:2048 sc1\n"
"MF v[152:155],v[108:111],a[144:147],v[152:155]\n"
"TR v[118:119],v15 offset:63488\n"
"v_mul_f32 v207, 0x3e0293ee, v207\n"
"MF v[184:187],v[96:99],a[100:103],v[184:187]\n"
"CV v68,v200,v201\n"
"CV v69,v202,v203\n"
"MF v[184:187],v[100:103],a[116:119],v[184:187]\n"
"TR v[120:121],v27 offset:4096\n"
"CV v70,v204,v205\n"
"MF v[184:187],v[104:107],a[132:135],v[184:187]\n"
"LD a[244:247],v1,s[76:79],s73 offen offset:2048 sc1\n"
"MF v[184:187],v[108:111],a[148:151],v[184:187]\n"
"TR v[122:123],v27 offset:6144\n"
"CV v71,v206,v207\n"
"MF v[216:219],v[96:99],a[104:107],v[216:219]\n"
"v_mul_f32 v232, 0x3e0293ee, v232\n"
"v_mul_f32 v233, 0x3e0293ee, v233\n"
"MF v[216:219],v[100:103],a[120:123],v[216:219]\n"
"TR v[124:125],v27 offset:12288\n"
"v_mul_f32 v234, 0x3e0293ee, v234\n"
"MF v[216:219],v[104:107],a[136:139],v[216:219]\n"
"LD a[248:251],v1,s[76:79],s68 offen offset:3072 sc1\n"
"MF v[216:219],v[108:111],a[152:155],v[216:219]\n"
"TR v[126:127],v27 offset:14336\n"
"v_mul_f32 v235, 0x3e0293ee, v235\n"
"MF v[248:251],v[96:99],a[108:111],v[248:251]\n"
"v_mul_f32 v236, 0x3e0293ee, v236\n"
"v_mul_f32 v237, 0x3e0293ee, v237\n"
"MF v[248:251],v[100:103],a[124:127],v[248:251]\n"
"v_mul_f32 v238, 0x3e0293ee, v238\n"
"v_mul_f32 v239, 0x3e0293ee, v239\n"
"MF v[248:251],v[104:107],a[140:143],v[248:251]\n"
"LD a[252:255],v1,s[76:79],s73 offen offset:3072 sc1\n"
"MF v[248:251],v[108:111],a[156:159],v[248:251]\n"
"WT lgkmcnt(6)\n"
"CV v84,v232,v233\n"
"CV v85,v234,v235\n"
"MF v[156:159],v[112:115],a[96:99],v[156:159]\n"
"WT lgkmcnt(4)\n"
"CV v86,v236,v237\n"
"CV v87,v238,v239\n"
"MF v[156:159],v[116:119],a[112:115],v[156:159]\n"
"WT lgkmcnt(2)\n"
"v_mul_f32 v144, 0x3e0293ee, v144\n"
"v_mul_f32 v145, 0x3e0293ee, v145\n"
"MF v[156:159],v[120:123],a[128:131],v[156:159]\n"
"WT lgkmcnt(0)\n"
"v_mul_f32 v146, 0x3e0293ee, v146\n"
"v_mul_f32 v147, 0x3e0293ee, v147\n"
"MF v[156:159],v[124:127],a[144:147],v[156:159]\n"
"v_mul_f32 v148, 0x3e0293ee, v148\n"
"v_mul_f32 v149, 0x3e0293ee, v149\n"
"MF v[188:191],v[112:115],a[100:103],v[188:191]\n"
"v_mul_f32 v150, 0x3e0293ee, v150\n"
"v_mul_f32 v151, 0x3e0293ee, v151\n"
"MF v[188:191],v[116:119],a[116:119],v[188:191]\n"
"CV v40,v144,v145\n"
"CV v41,v146,v147\n"
"MF v[188:191],v[120:123],a[132:135],v[188:191]\n"
"CV v42,v148,v149\n"
"CV v43,v150,v151\n"
"MF v[188:191],v[124:127],a[148:151],v[188:191]\n"
"v_mul_f32 v176, 0x3e0293ee, v176\n"
"v_mul_f32 v177, 0x3e0293ee, v177\n"
"MF v[220:223],v[112:115],a[104:107],v[220:223]\n"
"v_mul_f32 v178, 0x3e0293ee, v178\n"
"v_mul_f32 v179, 0x3e0293ee, v179\n"
"MF v[220:223],v[116:119],a[120:123],v[220:223]\n"
"v_mul_f32 v180, 0x3e0293ee, v180\n"
"v_mul_f32 v181, 0x3e0293ee, v181\n"
"MF v[220:223],v[120:123],a[136:139],v[220:223]\n"
"v_mul_f32 v182, 0x3e0293ee, v182\n"
"v_mul_f32 v183, 0x3e0293ee, v183\n"
"MF v[220:223],v[124:127],a[152:155],v[220:223]\n"
"CV v56,v176,v177\n"
"CV v57,v178,v179\n"
"MF v[252:255],v[112:115],a[108:111],v[252:255]\n"
"CV v58,v180,v181\n"
"CV v59,v182,v183\n"
"MF v[252:255],v[116:119],a[124:127],v[252:255]\n"
"v_mul_f32 v208, 0x3e0293ee, v208\n"
"v_mul_f32 v209, 0x3e0293ee, v209\n"
"MF v[252:255],v[120:123],a[140:143],v[252:255]\n"
"v_mul_f32 v210, 0x3e0293ee, v210\n"
"v_mul_f32 v211, 0x3e0293ee, v211\n"
"MF v[252:255],v[124:127],a[156:159],v[252:255]\n"
"v_mul_f32 v212, 0x3e0293ee, v212\n"
"v_mul_f32 v213, 0x3e0293ee, v213\n"
"WT vmcnt(24)\n"
"s_barrier\n"
"TR v[96:97],v24 offset:16384\n"
"TR v[98:99],v24 offset:18432\n"
"TR v[100:101],v25 offset:16384\n"
"TR v[102:103],v25 offset:18432\n"
"TR v[104:105],v26 offset:16384\n"
"TR v[106:107],v26 offset:18432\n"
"TR v[108:109],v27 offset:16384\n"
"TR v[110:111],v27 offset:18432\n"
"TR v[112:113],v24 offset:20480\n"
"TR v[114:115],v24 offset:22528\n"
"TR v[116:117],v25 offset:20480\n"
"TR v[118:119],v25 offset:22528\n"
"TR v[120:121],v26 offset:20480\n"
"TR v[122:123],v26 offset:22528\n"
"TR v[124:125],v27 offset:20480\n"
"TR v[126:127],v27 offset:22528\n"
"s_mov_b32 s56, 0x3e0293ee\n"
"v_mul_f32 v214, 0x3e0293ee, v214\n"
"v_mul_f32 v215, 0x3e0293ee, v215\n"
"CV v72,v208,v209\n"
"CV v73,v210,v211\n"
"CV v74,v212,v213\n"
"CV v75,v214,v215\n"
"v_mul_f32 v240, 0x3e0293ee, v240\n"
"s_add_u32 s68, s67, 12288\n"
"v_mul_f32 v241, 0x3e0293ee, v241\n"
"LD a[128:131],v1,s[76:79],s68 offen offset:0 sc1\n"
"v_mul_f32 v242, 0x3e0293ee, v242\n"
"v_mul_f32 v243, 0x3e0293ee, v243\n"
"v_mul_f32 v244, 0x3e0293ee, v244\n"
"v_mul_f32 v245, 0x3e0293ee, v245\n"
"v_mul_f32 v246, 0x3e0293ee, v246\n"
"v_mul_f32 v247, 0x3e0293ee, v247\n"
"CV v88,v240,v241\n"
"s_add_u32 s73, s67, 32768\n"
"CV v89,v242,v243\n"
"LD a[132:135],v1,s[76:79],s73 offen offset:0 sc1\n"
"CV v90,v244,v245\n"
"CV v91,v246,v247\n"
"v_pk_mul_f32 v[152:153], v[152:153], s[56:57] op_sel_hi:[1,0]\n"
"v_pk_mul_f32 v[154:155], v[154:155], s[56:57] op_sel_hi:[1,0]\n"
"v_pk_mul_f32 v[156:157], v[156:157], s[56:57] op_sel_hi:[1,0]\n"
"v_pk_mul_f32 v[158:159], v[158:159], s[56:57] op_sel_hi:[1,0]\n"
"CV v44,v152,v153\n"
"CV v45,v154,v155\n"
"LD a[136:139],v1,s[76:79],s68 offen offset:1024 sc1\n"
"CV v46,v156,v157\n"
"CV v47,v158,v159\n"
"v_pk_mul_f32 v[184:185], v[184:185], s[56:57] op_sel_hi:[1,0]\n"
"v_pk_mul_f32 v[186:187], v[186:187], s[56:57] op_sel_hi:[1,0]\n"
"v_pk_mul_f32 v[188:189], v[188:189], s[56:57] op_sel_hi:[1,0]\n"
"v_pk_mul_f32 v[190:191], v[190:191], s[56:57] op_sel_hi:[1,0]\n"
"CV v60,v184,v185\n"
"CV v61,v186,v187\n"
"LD a[140:143],v1,s[76:79],s73 offen offset:1024 sc1\n"
"CV v62,v188,v189\n"
"CV v63,v190,v191\n"
"v_pk_mul_f32 v[216:217], v[216:217], s[56:57] op_sel_hi:[1,0]\n"
"v_pk_mul_f32 v[218:219], v[218:219], s[56:57] op_sel_hi:[1,0]\n"
"v_pk_mul_f32 v[220:221], v[220:221], s[56:57] op_sel_hi:[1,0]\n"
"v_pk_mul_f32 v[222:223], v[222:223], s[56:57] op_sel_hi:[1,0]\n"
"CV v76,v216,v217\n"
"CV v77,v218,v219\n"
"LD a[144:147],v1,s[76:79],s68 offen offset:2048 sc1\n"
"CV v78,v220,v221\n"
"CV v79,v222,v223\n"
"v_pk_mul_f32 v[248:249], v[248:249], s[56:57] op_sel_hi:[1,0]\n"
"v_pk_mul_f32 v[250:251], v[250:251], s[56:57] op_sel_hi:[1,0]\n"
"v_pk_mul_f32 v[252:253], v[252:253], s[56:57] op_sel_hi:[1,0]\n"
"v_pk_mul_f32 v[254:255], v[254:255], s[56:57] op_sel_hi:[1,0]\n"
"CV v92,v248,v249\n"
"CV v93,v250,v251\n"
"LD a[148:151],v1,s[76:79],s73 offen offset:2048 sc1\n"
"CV v94,v252,v253\n"
"CV v95,v254,v255\n"
"LD a[152:155],v1,s[76:79],s68 offen offset:3072 sc1\n"
"LD a[156:159],v1,s[76:79],s73 offen offset:3072 sc1\n"
"s_cmp_lt_u32 s33, 96\n"
"s_cbranch_scc1 L_p4free_17\n"
"WT vmcnt(31) lgkmcnt(14)\n"
"MF v[128:131],v[96:99],a[160:163],0\n"
"WT vmcnt(30)\n"
"AW a32,0\n"
"MF v[144:147],v[96:99],a[164:167],0\n"
"WT lgkmcnt(12)\n"
"s_add_u32 s68, s67, 16384\n"
"TR v[96:97],v24 offset:24576\n"
"AW a33,0\n"
"MF v[132:135],v[100:103],a[160:163],0\n"
"LD v[160:163],v1,s[76:79],s68 offen offset:0 sc1\n"
"AW a34,0\n"
"MF v[148:151],v[100:103],a[164:167],0\n"
"WT lgkmcnt(11)\n"
"TR v[98:99],v24 offset:26624\n"
"AW a35,0\n"
"MF v[136:139],v[104:107],a[160:163],0\n"
"TR v[100:101],v25 offset:24576\n"
"AW a36,0\n"
"MF v[152:155],v[104:107],a[164:167],0\n"
"WT lgkmcnt(11)\n"
"s_add_u32 s73, s67, 36864\n"
"TR v[102:103],v25 offset:26624\n"
"AW a37,0\n"
"MF v[140:143],v[108:111],a[160:163],0\n"
"LD v[164:167],v1,s[76:79],s73 offen offset:0 sc1\n"
"AW a38,0\n"
"MF v[156:159],v[108:111],a[164:167],0\n"
"WT vmcnt(31) lgkmcnt(10)\n"
"TR v[104:105],v26 offset:24576\n"
"AW a39,0\n"
"MF v[128:131],v[112:115],a[168:171],v[128:131]\n"
"WT vmcnt(30)\n"
"TR v[106:107],v26 offset:26624\n"
"AW a40,0\n"
"MF v[144:147],v[112:115],a[172:175],v[144:147]\n"
"WT lgkmcnt(10)\n"
"TR v[108:109],v27 offset:24576\n"
"AW a41,0\n"
"MF v[132:135],v[116:119],a[168:171],v[132:135]\n"
"LD v[168:171],v1,s[76:79],s68 offen offset:1024 sc1\n"
"AW a42,0\n"
"MF v[148:151],v[116:119],a[172:175],v[148:151]\n"
"WT lgkmcnt(9)\n"
"TR v[110:111],v27 offset:26624\n"
"AW a43,0\n"
"MF v[136:139],v[120:123],a[168:171],v[136:139]\n"
"TR v[112:113],v24 offset:28672\n"
"AW a44,0\n"
"MF v[152:155],v[120:123],a[172:175],v[152:155]\n"
"WT lgkmcnt(9)\n"
"TR v[114:115],v24 offset:30720\n"
"AW a45,0\n"
"MF v[140:143],v[124:127],a[168:171],v[140:143]\n"
"LD v[172:175],v1,s[76:79],s73 offen offset:1024 sc1\n"
"AW a46,0\n"
"MF v[156:159],v[124:127],a[172:175],v[156:159]\n"
"WT vmcnt(31) lgkmcnt(8)\n"
"TR v[116:117],v25 offset:28672\n"
"AW a47,0\n"
"MF v[128:131],v[96:99],a[176:179],v[128:131]\n"
"WT vmcnt(30)\n"
"TR v[118:119],v25 offset:30720\n"
"AW a48,0\n"
"MF v[144:147],v[96:99],a[180:183],v[144:147]\n"
"WT lgkmcnt(8)\n"
"TR v[120:121],v26 offset:28672\n"
"AW a49,0\n"
"MF v[132:135],v[100:103],a[176:179],v[132:135]\n"
"TR v[122:123],v26 offset:30720\n"
"LD v[176:179],v1,s[76:79],s68 offen offset:2048 sc1\n"
"AW a50,0\n"
"MF v[148:151],v[100:103],a[180:183],v[148:151]\n"
"WT lgkmcnt(8)\n"
"TR v[124:125],v27 offset:28672\n"
"AW a51,0\n"
"MF v[136:139],v[104:107],a[176:179],v[136:139]\n"
"TR v[126:127],v27 offset:30720\n"
"AW a52,0\n"
"MF v[152:155],v[104:107],a[180:183],v[152:155]\n"
"WT lgkmcnt(8)\n"
"TR v[96:97],v24 offset:32768\n"
"AW a53,0\n"
"MF v[140:143],v[108:111],a[176:179],v[140:143]\n"
"TR v[98:99],v24 offset:34816\n"
"LD v[180:183],v1,s[76:79],s73 offen offset:2048 sc1\n"
"AW a54,0\n"
"MF v[156:159],v[108:111],a[180:183],v[156:159]\n"
"WT vmcnt(31) lgkmcnt(8)\n"
"TR v[100:101],v25 offset:32768\n"
"AW a55,0\n"
"MF v[128:131],v[112:115],a[184:187],v[128:131]\n"
"WT vmcnt(30)\n"
"TR v[102:103],v25 offset:34816\n"
"AW a56,0\n"
"MF v[144:147],v[112:115],a[188:191],v[144:147]\n"
"WT lgkmcnt(8)\n"
"TR v[104:105],v26 offset:32768\n"
"AW a57,0\n"
"MF v[132:135],v[116:119],a[184:187],v[132:135]\n"
"TR v[106:107],v26 offset:34816\n"
"LD v[184:187],v1,s[76:79],s68 offen offset:3072 sc1\n"
"AW a58,0\n"
"MF v[148:151],v[116:119],a[188:191],v[148:151]\n"
"WT lgkmcnt(8)\n"
"TR v[108:109],v27 offset:32768\n"
"AW a59,0\n"
"MF v[136:139],v[120:123],a[184:187],v[136:139]\n"
"TR v[110:111],v27 offset:34816\n"
"AW a60,0\n"
"MF v[152:155],v[120:123],a[188:191],v[152:155]\n"
"WT lgkmcnt(8)\n"
"TR v[112:113],v24 offset:36864\n"
"AW a61,0\n"
"MF v[140:143],v[124:127],a[184:187],v[140:143]\n"
"TR v[114:115],v24 offset:38912\n"
"LD v[188:191],v1,s[76:79],s73 offen offset:3072 sc1\n"
"AW a62,0\n"
"MF v[156:159],v[124:127],a[188:191],v[156:159]\n"
"WT vmcnt(31) lgkmcnt(8)\n"
"TR v[116:117],v25 offset:36864\n"
"AW a63,0\n"
"MF v[128:131],v[96:99],a[192:195],v[128:131]\n"
"WT vmcnt(30)\n"
"TR v[118:119],v25 offset:38912\n"
"AW a64,0\n"
"MF v[144:147],v[96:99],a[196:199],v[144:147]\n"
"WT lgkmcnt(8)\n"
"TR v[120:121],v26 offset:36864\n"
"AW a65,0\n"
"MF v[132:135],v[100:103],a[192:195],v[132:135]\n"
"TR v[122:123],v26 offset:38912\n"
"LD v[224:227],v1,s[76:79],s81 offen offset:0 sc1\n"
"AW a66,0\n"
"MF v[148:151],v[100:103],a[196:199],v[148:151]\n"
"WT lgkmcnt(8)\n"
"TR v[124:125],v27 offset:36864\n"
"AW a67,0\n"
"MF v[136:139],v[104:107],a[192:195],v[136:139]\n"
"TR v[126:127],v27 offset:38912\n"
"AW a68,0\n"
"MF v[152:155],v[104:107],a[196:199],v[152:155]\n"
"WT lgkmcnt(8)\n"
"TR v[96:97],v24 offset:40960\n"
"AW a69,0\n"
"MF v[140:143],v[108:111],a[192:195],v[140:143]\n"
"TR v[98:99],v24 offset:43008\n"
"LD v[228:231],v1,s[76:79],s81 offen offset:1024 sc1\n"
"AW a70,0\n"
"MF v[156:159],v[108:111],a[196:199],v[156:159]\n"
"WT vmcnt(31) lgkmcnt(8)\n"
"TR v[100:101],v25 offset:40960\n"
"AW a71,0\n"
"MF v[128:131],v[112:115],a[200:203],v[128:131]\n"
"WT vmcnt(30)\n"
"TR v[102:103],v25 offset:43008\n"
"AW a72,0\n"
"MF v[144:147],v[112:115],a[204:207],v[144:147]\n"
"WT lgkmcnt(8)\n"
"TR v[104:105],v26 offset:40960\n"
"AW a73,0\n"
"MF v[132:135],v[116:119],a[200:203],v[132:135]\n"
"TR v[106:107],v26 offset:43008\n"
"LD v[232:235],v1,s[76:79],s12 offen offset:0 sc1\n"
"AW a74,0\n"
"MF v[148:151],v[116:119],a[204:207],v[148:151]\n"
"WT lgkmcnt(8)\n"
"TR v[108:109],v27 offset:40960\n"
"AW a75,0\n"
"MF v[136:139],v[120:123],a[200:203],v[136:139]\n"
"TR v[110:111],v27 offset:43008\n"
"AW a76,0\n"
"MF v[152:155],v[120:123],a[204:207],v[152:155]\n"
"WT lgkmcnt(8)\n"
"TR v[112:113],v24 offset:45056\n"
"AW a77,0\n"
"MF v[140:143],v[124:127],a[200:203],v[140:143]\n"
"TR v[114:115],v24 offset:47104\n"
"LD v[236:239],v1,s[76:79],s12 offen offset:1024 sc1\n"
"AW a78,0\n"
"MF v[156:159],v[124:127],a[204:207],v[156:159]\n"
"WT vmcnt(31) lgkmcnt(8)\n"
"TR v[116:117],v25 offset:45056\n"
"AW a79,0\n"
"MF v[128:131],v[96:99],a[208:211],v[128:131]\n"
"WT vmcnt(30)\n"
"TR v[118:119],v25 offset:47104\n"
"AW a80,0\n"
"MF v[144:147],v[96:99],a[212:215],v[144:147]\n"
"WT lgkmcnt(8)\n"
"TR v[120:121],v26 offset:45056\n"
"AW a81,0\n"
"MF v[132:135],v[100:103],a[208:211],v[132:135]\n"
"buffer_load_dwordx2 v[18:19], v20, s[40:43], 0 offen sc1\n"
"TR v[122:123],v26 offset:47104\n"
"AW a82,0\n"
"MF v[148:151],v[100:103],a[212:215],v[148:151]\n"
"WT lgkmcnt(8)\n"
"TR v[124:125],v27 offset:45056\n"
"AW a83,0\n"
"MF v[136:139],v[104:107],a[208:211],v[136:139]\n"
"TR v[126:127],v27 offset:47104\n"
"AW a84,0\n"
"MF v[152:155],v[104:107],a[212:215],v[152:155]\n"
"WT lgkmcnt(8)\n"
"TR v[96:97],v24 offset:49152\n"
"AW a85,0\n"
"MF v[140:143],v[108:111],a[208:211],v[140:143]\n"
"TR v[98:99],v24 offset:51200\n"
"AW a86,0\n"
"MF v[156:159],v[108:111],a[212:215],v[156:159]\n"
"WT vmcnt(30) lgkmcnt(8)\n"
"TR v[100:101],v25 offset:49152\n"
"AW a87,0\n"
"MF v[128:131],v[112:115],a[216:219],v[128:131]\n"
"WT vmcnt(29)\n"
"TR v[102:103],v25 offset:51200\n"
"AW a88,0\n"
"MF v[144:147],v[112:115],a[220:223],v[144:147]\n"
"WT lgkmcnt(8)\n"
"TR v[104:105],v26 offset:49152\n"
"AW a89,0\n"
"MF v[132:135],v[116:119],a[216:219],v[132:135]\n"
"TR v[106:107],v26 offset:51200\n"
"AW a90,0\n"
"MF v[148:151],v[116:119],a[220:223],v[148:151]\n"
"WT lgkmcnt(8)\n"
"TR v[108:109],v27 offset:49152\n"
"AW a91,0\n"
"MF v[136:139],v[120:123],a[216:219],v[136:139]\n"
"TR v[110:111],v27 offset:51200\n"
"AW a92,0\n"
"MF v[152:155],v[120:123],a[220:223],v[152:155]\n"
"WT lgkmcnt(8)\n"
"TR v[112:113],v24 offset:53248\n"
"AW a93,0\n"
"MF v[140:143],v[124:127],a[216:219],v[140:143]\n"
"TR v[114:115],v24 offset:55296\n"
"AW a94,0\n"
"MF v[156:159],v[124:127],a[220:223],v[156:159]\n"
"WT vmcnt(28) lgkmcnt(8)\n"
"TR v[116:117],v25 offset:53248\n"
"AW a95,0\n"
"MF v[128:131],v[96:99],a[224:227],v[128:131]\n"
"WT vmcnt(27)\n"
"TR v[118:119],v25 offset:55296\n"
"AW a96,0\n"
"MF v[144:147],v[96:99],a[228:231],v[144:147]\n"
"WT lgkmcnt(8)\n"
"TR v[120:121],v26 offset:53248\n"
"AW a97,0\n"
"MF v[132:135],v[100:103],a[224:227],v[132:135]\n"
"TR v[122:123],v26 offset:55296\n"
"AW a98,0\n"
"MF v[148:151],v[100:103],a[228:231],v[148:151]\n"
"WT lgkmcnt(8)\n"
"TR v[124:125],v27 offset:53248\n"
"AW a99,0\n"
"MF v[136:139],v[104:107],a[224:227],v[136:139]\n"
"TR v[126:127],v27 offset:55296\n"
"AW a100,0\n"
"MF v[152:155],v[104:107],a[228:231],v[152:155]\n"
"WT lgkmcnt(8)\n"
"TR v[96:97],v24 offset:57344\n"
"AW a101,0\n"
"MF v[140:143],v[108:111],a[224:227],v[140:143]\n"
"TR v[98:99],v24 offset:59392\n"
"AW a102,0\n"
"MF v[156:159],v[108:111],a[228:231],v[156:159]\n"
"WT vmcnt(26) lgkmcnt(8)\n"
"TR v[100:101],v25 offset:57344\n"
"AW a103,0\n"
"MF v[128:131],v[112:115],a[232:235],v[128:131]\n"
"WT vmcnt(25)\n"
"TR v[102:103],v25 offset:59392\n"
"AW a104,0\n"
"MF v[144:147],v[112:115],a[236:239],v[144:147]\n"
"WT lgkmcnt(8)\n"
"TR v[104:105],v26 offset:57344\n"
"AW a105,0\n"
"MF v[132:135],v[116:119],a[232:235],v[132:135]\n"
"TR v[106:107],v26 offset:59392\n"
"AW a106,0\n"
"MF v[148:151],v[116:119],a[236:239],v[148:151]\n"
"WT lgkmcnt(8)\n"
"TR v[108:109],v27 offset:57344\n"
"AW a107,0\n"
"MF v[136:139],v[120:123],a[232:235],v[136:139]\n"
"TR v[110:111],v27 offset:59392\n"
"AW a108,0\n"
"MF v[152:155],v[120:123],a[236:239],v[152:155]\n"
"WT lgkmcnt(8)\n"
"TR v[112:113],v24 offset:61440\n"
"AW a109,0\n"
"MF v[140:143],v[124:127],a[232:235],v[140:143]\n"
"TR v[114:115],v24 offset:63488\n"
"AW a110,0\n"
"MF v[156:159],v[124:127],a[236:239],v[156:159]\n"
"WT vmcnt(24) lgkmcnt(8)\n"
"TR v[116:117],v25 offset:61440\n"
"AW a111,0\n"
"MF v[128:131],v[96:99],a[240:243],v[128:131]\n"
"WT vmcnt(23)\n"
"TR v[118:119],v25 offset:63488\n"
"AW a112,0\n"
"MF v[144:147],v[96:99],a[244:247],v[144:147]\n"
"WT lgkmcnt(8)\n"
"TR v[120:121],v26 offset:61440\n"
"AW a113,0\n"
"MF v[132:135],v[100:103],a[240:243],v[132:135]\n"
"TR v[122:123],v26 offset:63488\n"
"AW a114,0\n"
"MF v[148:151],v[100:103],a[244:247],v[148:151]\n"
"WT lgkmcnt(8)\n"
"TR v[124:125],v27 offset:61440\n"
"AW a115,0\n"
"MF v[136:139],v[104:107],a[240:243],v[136:139]\n"
"TR v[126:127],v27 offset:63488\n"
"AW a116,0\n"
"MF v[152:155],v[104:107],a[244:247],v[152:155]\n"
"WT lgkmcnt(8)\n"
"TR v[96:97],v28 offset:0\n"
"AW a117,0\n"
"MF v[140:143],v[108:111],a[240:243],v[140:143]\n"
"TR v[98:99],v28 offset:2048\n"
"AW a118,0\n"
"MF v[156:159],v[108:111],a[244:247],v[156:159]\n"
"WT vmcnt(22) lgkmcnt(8)\n"
"TR v[100:101],v29 offset:0\n"
"AW a119,0\n"
"MF v[128:131],v[112:115],a[248:251],v[128:131]\n"
"WT vmcnt(21)\n"
"WT vmcnt(0)\n"
"v_cmp_ne_u64 vcc, s[38:39], v[18:19]\n"
"s_nop 1\n"
"s_cmp_eq_u64 vcc, 0\n"
"s_cbranch_scc0 L_psp_20\n"
"L_pok_19:\n"
"TR v[102:103],v29 offset:2048\n"
"AW a120,0\n"
"MF v[144:147],v[112:115],a[252:255],v[144:147]\n"
"WT lgkmcnt(8)\n"
"TR v[104:105],v30 offset:0\n"
"AW a121,0\n"
"MF v[132:135],v[116:119],a[248:251],v[132:135]\n"
"TR v[106:107],v30 offset:2048\n"
"AW a122,0\n"
"MF v[148:151],v[116:119],a[252:255],v[148:151]\n"
"WT lgkmcnt(8)\n"
"TR v[108:109],v31 offset:0\n"
"AW a123,0\n"
"MF v[136:139],v[120:123],a[248:251],v[136:139]\n"
"TR v[110:111],v31 offset:2048\n"
"AW a124,0\n"
"MF v[152:155],v[120:123],a[252:255],v[152:155]\n"
"WT lgkmcnt(8)\n"
"TR v[112:113],v28 offset:4096\n"
"AW a125,0\n"
"MF v[140:143],v[124:127],a[248:251],v[140:143]\n"
"TR v[114:115],v28 offset:6144\n"
"AW a126,0\n"
"MF v[156:159],v[124:127],a[252:255],v[156:159]\n"
"WT lgkmcnt(8)\n"
"TR v[116:117],v29 offset:4096\n"
"AW a127,0\n"
"MF v[128:131],v[96:99],a[128:131],v[128:131]\n"
"TR v[118:119],v29 offset:6144\n"
"AW a192,0\n"
"MF v[144:147],v[96:99],a[132:135],v[144:147]\n"
"WT lgkmcnt(8)\n"
"s_add_u32 s16, s16, 25856\n"
"s_add_u32 s17, s17, 32768\n"
"s_and_b32 s17, s17, 0x1ffff\n"
"s_mov_b32 m0, s17\n"
"TR v[120:121],v30 offset:4096\n"
"AW a193,0\n"
"MF v[132:135],v[100:103],a[128:131],v[132:135]\n"
"TR v[122:123],v30 offset:6144\n"
"LD v1,s[44:47],s16 offen offset:0 sc1 lds\n"
"AW a194,0\n"
"MF v[148:151],v[100:103],a[132:135],v[148:151]\n"
"WT lgkmcnt(8)\n"
"TR v[124:125],v31 offset:4096\n"
"AW a195,0\n"
"MF v[136:139],v[104:107],a[128:131],v[136:139]\n"
"TR v[126:127],v31 offset:6144\n"
"AW a196,0\n"
"MF v[152:155],v[104:107],a[132:135],v[152:155]\n"
"WT lgkmcnt(8)\n"
"TR v[96:97],v28 offset:8192\n"
"AW a197,0\n"
"MF v[140:143],v[108:111],a[128:131],v[140:143]\n"
"TR v[98:99],v28 offset:10240\n"
"LD v1,s[44:47],s16 offen offset:1024 sc1 lds\n"
"AW a198,0\n"
"MF v[156:159],v[108:111],a[132:135],v[156:159]\n"
"WT lgkmcnt(8)\n"
"TR v[100:101],v29 offset:8192\n"
"AW a199,0\n"
"MF v[128:131],v[112:115],a[136:139],v[128:131]\n"
"TR v[102:103],v29 offset:10240\n"
"AW a200,0\n"
"MF v[144:147],v[112:115],a[140:143],v[144:147]\n"
"WT lgkmcnt(8)\n"
"TR v[104:105],v30 offset:8192\n"
"AW a201,0\n"
"MF v[132:135],v[116:119],a[136:139],v[132:135]\n"
"TR v[106:107],v30 offset:10240\n"
"LD v1,s[44:47],s16 offen offset:2048 sc1 lds\n"
"AW a202,0\n"
"MF v[148:151],v[116:119],a[140:143],v[148:151]\n"
"WT lgkmcnt(8)\n"
"TR v[108:109],v31 offset:8192\n"
"AW a203,0\n"
"MF v[136:139],v[120:123],a[136:139],v[136:139]\n"
"TR v[110:111],v31 offset:10240\n"
"AW a204,0\n"
"MF v[152:155],v[120:123],a[140:143],v[152:155]\n"
"WT lgkmcnt(8)\n"
"TR v[112:113],v28 offset:12288\n"
"AW a205,0\n"
"MF v[140:143],v[124:127],a[136:139],v[140:143]\n"
"TR v[114:115],v28 offset:14336\n"
"LD v1,s[44:47],s16 offen offset:3072 sc1 lds\n"
"AW a206,0\n"
"MF v[156:159],v[124:127],a[140:143],v[156:159]\n"
"WT lgkmcnt(8)\n"
"TR v[116:117],v29 offset:12288\n"
"AW a207,0\n"
"MF v[128:131],v[96:99],a[144:147],v[128:131]\n"
"TR v[118:119],v29 offset:14336\n"
"AW a208,0\n"
"MF v[144:147],v[96:99],a[148:151],v[144:147]\n"
"WT lgkmcnt(8)\n"
"s_add_u32 s16, s16, 8192\n"
"s_add_u32 m0, s17, 8192\n"
"TR v[120:121],v30 offset:12288\n"
"AW a209,0\n"
"MF v[132:135],v[100:103],a[144:147],v[132:135]\n"
"TR v[122:123],v30 offset:14336\n"
"LD v1,s[44:47],s16 offen offset:0 sc1 lds\n"
"AW a210,0\n"
"MF v[148:151],v[100:103],a[148:151],v[148:151]\n"
"WT lgkmcnt(8)\n"
"TR v[124:125],v31 offset:12288\n"
"AW a211,0\n"
"MF v[136:139],v[104:107],a[144:147],v[136:139]\n"
"TR v[126:127],v31 offset:14336\n"
"AW a212,0\n"
"MF v[152:155],v[104:107],a[148:151],v[152:155]\n"
"WT lgkmcnt(8)\n"
"TR v[96:97],v28 offset:16384\n"
"AW a213,0\n"
"MF v[140:143],v[108:111],a[144:147],v[140:143]\n"
"TR v[98:99],v28 offset:18432\n"
"LD v1,s[44:47],s16 offen offset:1024 sc1 lds\n"
"AW a214,0\n"
"MF v[156:159],v[108:111],a[148:151],v[156:159]\n"
"WT lgkmcnt(8)\n"
"TR v[100:101],v29 offset:16384\n"
"AW a215,0\n"
"MF v[128:131],v[112:115],a[152:155],v[128:131]\n"
"TR v[102:103],v29 offset:18432\n"
"AW a216,0\n"
"MF v[144:147],v[112:115],a[156:159],v[144:147]\n"
"WT lgkmcnt(8)\n"
"TR v[104:105],v30 offset:16384\n"
"AW a217,0\n"
"MF v[132:135],v[116:119],a[152:155],v[132:135]\n"
"TR v[106:107],v30 offset:18432\n"
"LD v1,s[44:47],s16 offen offset:2048 sc1 lds\n"
"AW a218,0\n"
"MF v[148:151],v[116:119],a[156:159],v[148:151]\n"
"WT lgkmcnt(8)\n"
"TR v[108:109],v31 offset:16384\n"
"AW a219,0\n"
"MF v[136:139],v[120:123],a[152:155],v[136:139]\n"
"TR v[110:111],v31 offset:18432\n"
"AW a220,0\n"
"MF v[152:155],v[120:123],a[156:159],v[152:155]\n"
"WT lgkmcnt(8)\n"
"TR v[112:113],v28 offset:20480\n"
"AW a221,0\n"
"MF v[140:143],v[124:127],a[152:155],v[140:143]\n"
"TR v[114:115],v28 offset:22528\n"
"LD v1,s[44:47],s16 offen offset:3072 sc1 lds\n"
"AW a222,0\n"
"MF v[156:159],v[124:127],a[156:159],v[156:159]\n"
"WT lgkmcnt(8)\n"
"TR v[116:117],v29 offset:20480\n"
"AW a223,0\n"
"MF v[128:131],v[96:99],v[160:163],v[128:131]\n"
"TR v[118:119],v29 offset:22528\n"
"AW a224,0\n"
"MF v[144:147],v[96:99],v[164:167],v[144:147]\n"
"WT lgkmcnt(8)\n"
"s_add_u32 s16, s16, 25856\n"
"s_add_u32 s17, s17, 32768\n"
"s_and_b32 s17, s17, 0x1ffff\n"
"s_mov_b32 m0, s17\n"
"TR v[120:121],v30 offset:20480\n"
"AW a225,0\n"
"MF v[132:135],v[100:103],v[160:163],v[132:135]\n"
"TR v[122:123],v30 offset:22528\n"
"LD v1,s[44:47],s16 offen offset:0 sc1 lds\n"
"AW a226,0\n"
"MF v[148:151],v[100:103],v[164:167],v[148:151]\n"
"WT lgkmcnt(8)\n"
"TR v[124:125],v31 offset:20480\n"
"AW a227,0\n"
"MF v[136:139],v[104:107],v[160:163],v[136:139]\n"
"TR v[126:127],v31 offset:22528\n"
"AW a228,0\n"
"MF v[152:155],v[104:107],v[164:167],v[152:155]\n"
"WT lgkmcnt(8)\n"
"TR v[96:97],v28 offset:24576\n"
"AW a229,0\n"
"MF v[140:143],v[108:111],v[160:163],v[140:143]\n"
"TR v[98:99],v28 offset:26624\n"
"LD v1,s[44:47],s16 offen offset:1024 sc1 lds\n"
"AW a230,0\n"
"MF v[156:159],v[108:111],v[164:167],v[156:159]\n"
"WT lgkmcnt(8)\n"
"TR v[100:101],v29 offset:24576\n"
"AW a231,0\n"
"MF v[128:131],v[112:115],v[168:171],v[128:131]\n"
"TR v[102:103],v29 offset:26624\n"
"AW a232,0\n"
"MF v[144:147],v[112:115],v[172:175],v[144:147]\n"
"WT lgkmcnt(8)\n"
"TR v[104:105],v30 offset:24576\n"
"AW a233,0\n"
"MF v[132:135],v[116:119],v[168:171],v[132:135]\n"
"TR v[106:107],v30 offset:26624\n"
"LD v1,s[44:47],s16 offen offset:2048 sc1 lds\n"
"AW a234,0\n"
"MF v[148:151],v[116:119],v[172:175],v[148:151]\n"
"WT lgkmcnt(8)\n"
"TR v[108:109],v31 offset:24576\n"
"AW a235,0\n"
"MF v[136:139],v[120:123],v[168:171],v[136:139]\n"
"TR v[110:111],v31 offset:26624\n"
"AW a236,0\n"
"MF v[152:155],v[120:123],v[172:175],v[152:155]\n"
"WT lgkmcnt(8)\n"
"TR v[112:113],v28 offset:28672\n"
"AW a237,0\n"
"MF v[140:143],v[124:127],v[168:171],v[140:143]\n"
"TR v[114:115],v28 offset:30720\n"
"LD v1,s[44:47],s16 offen offset:3072 sc1 lds\n"
"AW a238,0\n"
"MF v[156:159],v[124:127],v[172:175],v[156:159]\n"
"WT lgkmcnt(8)\n"
"TR v[116:117],v29 offset:28672\n"
"AW a239,0\n"
"MF v[128:131],v[96:99],v[176:179],v[128:131]\n"
"TR v[118:119],v29 offset:30720\n"
"AW a240,0\n"
"MF v[144:147],v[96:99],v[180:183],v[144:147]\n"
"WT lgkmcnt(8)\n"
"s_add_u32 s16, s16, 8192\n"
"s_add_u32 m0, s17, 8192\n"
"TR v[120:121],v30 offset:28672\n"
"AW a241,0\n"
"MF v[132:135],v[100:103],v[176:179],v[132:135]\n"
"LD v1,s[44:47],s16 offen offset:0 sc1 lds\n"
"AW a242,0\n"
"MF v[148:151],v[100:103],v[180:183],v[148:151]\n"
"WT lgkmcnt(7)\n"
"TR v[122:123],v30 offset:30720\n"
"AW a243,0\n"
"MF v[136:139],v[104:107],v[176:179],v[136:139]\n"
"TR v[124:125],v31 offset:28672\n"
"AW a244,0\n"
"MF v[152:155],v[104:107],v[180:183],v[152:155]\n"
"WT lgkmcnt(7)\n"
"TR v[126:127],v31 offset:30720\n"
"AW a245,0\n"
"MF v[140:143],v[108:111],v[176:179],v[140:143]\n"
"LD v1,s[44:47],s16 offen offset:1024 sc1 lds\n"
"AW a246,0\n"
"MF v[156:159],v[108:111],v[180:183],v[156:159]\n"
"WT lgkmcnt(6)\n"
"AW a247,0\n"
"MF v[128:131],v[112:115],v[184:187],v[128:131]\n"
"AW a248,0\n"
"MF v[144:147],v[112:115],v[188:191],v[144:147]\n"
"WT lgkmcnt(4)\n"
"AW a249,0\n"
"MF v[132:135],v[116:119],v[184:187],v[132:135]\n"
"LD v1,s[44:47],s16 offen offset:2048 sc1 lds\n"
"AW a250,0\n"
"MF v[148:151],v[116:119],v[188:191],v[148:151]\n"
"WT lgkmcnt(2)\n"
"AW a251,0\n"
"MF v[136:139],v[120:123],v[184:187],v[136:139]\n"
"AW a252,0\n"
"MF v[152:155],v[120:123],v[188:191],v[152:155]\n"
"WT lgkmcnt(0)\n"
"AW a253,0\n"
"MF v[140:143],v[124:127],v[184:187],v[140:143]\n"
"LD v1,s[44:47],s16 offen offset:3072 sc1 lds\n"
"AW a254,0\n"
"MF v[156:159],v[124:127],v[188:191],v[156:159]\n"
"AW a255,0\n"
"s_barrier\n"
"s_add_u32 s57, s81, 0x900000\n"
"s_add_u32 s58, s57, 20480\n"
"v_mov_b32 v200, 0x3f803f80\n"
"v_lshlrev_b32 v192, 16, v224\n"
"v_and_b32 v193, 0xffff0000, v224\n"
"v_lshlrev_b32 v194, 16, v225\n"
"v_and_b32 v195, 0xffff0000, v225\n"
"v_lshlrev_b32 v196, 16, v226\n"
"v_and_b32 v197, 0xffff0000, v226\n"
"v_lshlrev_b32 v198, 16, v227\n"
"s_add_u32 s16, s16, 25856\n"
"s_add_u32 s17, s17, 32768\n"
"s_and_b32 s17, s17, 0x1ffff\n"
"s_mov_b32 m0, s17\n"
"v_and_b32 v199, 0xffff0000, v227\n"
"LD v1,s[44:47],s16 offen offset:0 sc1 lds\n"
"v_pk_add_f32 v[128:129], v[128:129], v[192:193]\n"
"v_pk_add_f32 v[130:131], v[130:131], v[194:195]\n"
"v_pk_add_f32 v[132:133], v[132:133], v[196:197]\n"
"v_pk_add_f32 v[134:135], v[134:135], v[198:199]\n"
"CV v128,v128,v129\n"
"CV v129,v130,v131\n"
"CV v130,v132,v133\n"
"CV v131,v134,v135\n"
"LD v1,s[44:47],s16 offen offset:1024 sc1 lds\n"
"ST v[128:131],v1,s[40:43],s57 offen offset:0 sc1\n"
"v_dot2_f32_bf16 v208, v128, v200, 0\n"
"v_dot2_f32_bf16 v209, v128, v128, 0\n"
"v_dot2_f32_bf16 v208, v129, v200, v208\n"
"v_dot2_f32_bf16 v209, v129, v129, v209\n"
"v_dot2_f32_bf16 v208, v130, v200, v208\n"
"v_dot2_f32_bf16 v209, v130, v130, v209\n"
"v_dot2_f32_bf16 v208, v131, v200, v208\n"
"v_dot2_f32_bf16 v209, v131, v131, v209\n"
"LD v1,s[44:47],s16 offen offset:2048 sc1 lds\n"
"v_lshlrev_b32 v192, 16, v228\n"
"v_and_b32 v193, 0xffff0000, v228\n"
"v_lshlrev_b32 v194, 16, v229\n"
"v_and_b32 v195, 0xffff0000, v229\n"
"v_lshlrev_b32 v196, 16, v230\n"
"v_and_b32 v197, 0xffff0000, v230\n"
"v_lshlrev_b32 v198, 16, v231\n"
"v_and_b32 v199, 0xffff0000, v231\n"
"LD v1,s[44:47],s16 offen offset:3072 sc1 lds\n"
"v_pk_add_f32 v[136:137], v[136:137], v[192:193]\n"
"v_pk_add_f32 v[138:139], v[138:139], v[194:195]\n"
"v_pk_add_f32 v[140:141], v[140:141], v[196:197]\n"
"v_pk_add_f32 v[142:143], v[142:143], v[198:199]\n"
"CV v136,v136,v137\n"
"CV v137,v138,v139\n"
"CV v138,v140,v141\n"
"s_add_u32 s16, s16, 8192\n"
"s_add_u32 m0, s17, 8192\n"
"CV v139,v142,v143\n"
"LD v1,s[44:47],s16 offen offset:0 sc1 lds\n"
"ST v[136:139],v1,s[40:43],s57 offen offset:1024 sc1\n"
"v_dot2_f32_bf16 v208, v136, v200, v208\n"
"v_dot2_f32_bf16 v209, v136, v136, v209\n"
"v_dot2_f32_bf16 v208, v137, v200, v208\n"
"v_dot2_f32_bf16 v209, v137, v137, v209\n"
"v_dot2_f32_bf16 v208, v138, v200, v208\n"
"v_dot2_f32_bf16 v209, v138, v138, v209\n"
"v_dot2_f32_bf16 v208, v139, v200, v208\n"
"v_dot2_f32_bf16 v209, v139, v139, v209\n"
"LD v1,s[44:47],s16 offen offset:1024 sc1 lds\n"
"v_lshlrev_b32 v192, 16, v232\n"
"v_and_b32 v193, 0xffff0000, v232\n"
"v_lshlrev_b32 v194, 16, v233\n"
"v_and_b32 v195, 0xffff0000, v233\n"
"v_lshlrev_b32 v196, 16, v234\n"
"v_and_b32 v197, 0xffff0000, v234\n"
"v_lshlrev_b32 v198, 16, v235\n"
"v_and_b32 v199, 0xffff0000, v235\n"
"LD v1,s[44:47],s16 offen offset:2048 sc1 lds\n"
"v_pk_add_f32 v[144:145], v[144:145], v[192:193]\n"
"v_pk_add_f32 v[146:147], v[146:147], v[194:195]\n"
"v_pk_add_f32 v[148:149], v[148:149], v[196:197]\n"
"v_pk_add_f32 v[150:151], v[150:151], v[198:199]\n"
"CV v144,v144,v145\n"
"CV v145,v146,v147\n"
"CV v146,v148,v149\n"
"CV v147,v150,v151\n"
"LD v1,s[44:47],s16 offen offset:3072 sc1 lds\n"
"ST v[144:147],v1,s[40:43],s58 offen offset:0 sc1\n"
"v_dot2_f32_bf16 v210, v144, v200, 0\n"
"v_dot2_f32_bf16 v211, v144, v144, 0\n"
"v_dot2_f32_bf16 v210, v145, v200, v210\n"
"v_dot2_f32_bf16 v211, v145, v145, v211\n"
"v_dot2_f32_bf16 v210, v146, v200, v210\n"
"v_dot2_f32_bf16 v211, v146, v146, v211\n"
"v_dot2_f32_bf16 v210, v147, v200, v210\n"
"s_add_u32 s16, s16, 25856\n"
"s_add_u32 s17, s17, 32768\n"
"s_and_b32 s17, s17, 0x1ffff\n"
"s_mov_b32 m0, s17\n"
"v_dot2_f32_bf16 v211, v147, v147, v211\n"
"LD v1,s[44:47],s16 offen offset:0 sc1 lds\n"
"v_lshlrev_b32 v192, 16, v236\n"
"v_and_b32 v193, 0xffff0000, v236\n"
"v_lshlrev_b32 v194, 16, v237\n"
"v_and_b32 v195, 0xffff0000, v237\n"
"v_lshlrev_b32 v196, 16, v238\n"
"v_and_b32 v197, 0xffff0000, v238\n"
"v_lshlrev_b32 v198, 16, v239\n"
"v_and_b32 v199, 0xffff0000, v239\n"
"LD v1,s[44:47],s16 offen offset:1024 sc1 lds\n"
"v_pk_add_f32 v[152:153], v[152:153], v[192:193]\n"
"v_pk_add_f32 v[154:155], v[154:155], v[194:195]\n"
"v_pk_add_f32 v[156:157], v[156:157], v[196:197]\n"
"v_pk_add_f32 v[158:159], v[158:159], v[198:199]\n"
"CV v152,v152,v153\n"
"CV v153,v154,v155\n"
"CV v154,v156,v157\n"
"CV v155,v158,v159\n"
"LD v1,s[44:47],s16 offen offset:2048 sc1 lds\n"
"ST v[152:155],v1,s[40:43],s58 offen offset:1024 sc1\n"
"v_dot2_f32_bf16 v210, v152, v200, v210\n"
"v_dot2_f32_bf16 v211, v152, v152, v211\n"
"v_dot2_f32_bf16 v210, v153, v200, v210\n"
"v_dot2_f32_bf16 v211, v153, v153, v211\n"
"v_dot2_f32_bf16 v210, v154, v200, v210\n"
"v_dot2_f32_bf16 v211, v154, v154, v211\n"
"v_dot2_f32_bf16 v210, v155, v200, v210\n"
"v_dot2_f32_bf16 v211, v155, v155, v211\n"
"LD v1,s[44:47],s16 offen offset:3072 sc1 lds\n"
"s_nop 2\n"
"v_mov_b32 v212, v208\n"
"v_mov_b32 v213, v209\n"
"v_mov_b32 v214, v210\n"
"v_mov_b32 v215, v211\n"
"v_permlane32_swap_b32 v208, v212\n"
"v_permlane32_swap_b32 v209, v213\n"
"v_permlane32_swap_b32 v210, v214\n"
"v_permlane32_swap_b32 v211, v215\n"
"v_add_f32 v208, v208, v212\n"
"v_add_f32 v209, v209, v213\n"
"v_add_f32 v210, v210, v214\n"
"v_add_f32 v211, v211, v215\n"
"v_mov_b32 v212, v208\n"
"v_mov_b32 v213, v209\n"
"v_mov_b32 v214, v210\n"
"v_mov_b32 v215, v211\n"
"v_permlane16_swap_b32 v208, v212\n"
"v_permlane16_swap_b32 v209, v213\n"
"v_permlane16_swap_b32 v210, v214\n"
"v_permlane16_swap_b32 v211, v215\n"
"v_add_f32 v208, v208, v212\n"
"v_add_f32 v209, v209, v213\n"
"v_add_f32 v210, v210, v214\n"
"v_add_f32 v211, v211, v215\n"
"s_lshl_b32 s56, s70, 10\n"
"s_lshl_b32 s57, s34, 8\n"
"s_add_u32 s56, s56, s57\n"
"s_lshl_b32 s57, s71, 14\n"
"s_add_u32 s56, s56, s57\n"
"s_add_u32 s56, s56, 0xd00000\n"
"v_cmp_gt_u32 vcc, 16, v0\n"
"v_lshlrev_b32 v21, 3, v0\n"
"s_nop 0\n"
"v_cndmask_b32 v208, v210, v208, vcc\n"
"v_cndmask_b32 v209, v211, v209, vcc\n"
"s_mov_b64 exec, 0xffffffff\n"
"buffer_store_dwordx2 v[208:209], v21, s[40:43], s56 offen sc1\n"
"s_mov_b64 exec, -1\n"
"L_p4join_18:\n"
"v_mov_b32 v248, v1\n"
"v_mov_b32 v249, v1\n"
"WT vmcnt(20)\n"
"s_barrier\n"
"RD a[128:131],v248 offset:0\n"
"RD a[132:135],v248 offset:1024\n"
"RD a[136:139],v248 offset:2048\n"
"RD a[140:143],v248 offset:3072\n"
"RD a[144:147],v248 offset:4096\n"
"RD a[148:151],v248 offset:5120\n"
"RD a[152:155],v248 offset:6144\n"
"RD a[156:159],v248 offset:7168\n"
"RD a[160:163],v248 offset:16384\n"
"RD a[164:167],v248 offset:17408\n"
"RD a[168:171],v248 offset:18432\n"
"RD a[172:175],v248 offset:19456\n"
"RD a[176:179],v248 offset:20480\n"
"RD a[180:183],v248 offset:21504\n"
"RD a[184:187],v248 offset:22528\n"
"RD a[188:191],v248 offset:23552\n"
"v_add_u32 v248, 32768, v1\n"
"v_and_b32 v21, 15, v0\n"
"v_lshrrev_b32 v22, 4, v0\n"
"v_lshlrev_b32 v22, 2, v22\n"
"v_sub_u32 v21, v21, v22\n"
"s_lshl_b32 s56, s34, 4\n"
"v_add_u32 v21, s56, v21\n"
"v_mov_b32 v22, 0xff800000\n"
"v_cmp_le_i32 s[64:65], 0, v21\n"
"v_cmp_le_i32 s[66:67], 1, v21\n"
"WT lgkmcnt(15)\n"
"MF v[236:239],a[128:131],v[80:83],0\n"
"v_cmp_le_i32 s[68:69], 2, v21\n"
"v_cmp_le_i32 s[56:57], 3, v21\n"
"WT lgkmcnt(14)\n"
"MF v[236:239],a[132:135],v[84:87],v[236:239]\n"
"v_cndmask_b32 v180, v22, 0, s[64:65]\n"
"v_cndmask_b32 v181, v22, 0, s[66:67]\n"
"WT lgkmcnt(13)\n"
"MF v[236:239],a[136:139],v[88:91],v[236:239]\n"
"v_cndmask_b32 v182, v22, 0, s[68:69]\n"
"v_cndmask_b32 v183, v22, 0, s[56:57]\n"
"WT lgkmcnt(12)\n"
"MF v[236:239],a[140:143],v[92:95],v[236:239]\n"
"v_cmp_le_i32 s[64:65], 16, v21\n"
"v_cmp_le_i32 s[66:67], 17, v21\n"
"WT lgkmcnt(11)\n"
"MF v[240:243],a[144:147],v[80:83],0\n"
"v_cmp_le_i32 s[68:69], 18, v21\n"
"v_cmp_le_i32 s[56:57], 19, v21\n"
"WT lgkmcnt(10)\n"
"MF v[240:243],a[148:151],v[84:87],v[240:243]\n"
"v_cndmask_b32 v184, v22, 0, s[64:65]\n"
"v_cndmask_b32 v185, v22, 0, s[66:67]\n"
"WT lgkmcnt(9)\n"
"MF v[240:243],a[152:155],v[88:91],v[240:243]\n"
"v_cndmask_b32 v186, v22, 0, s[68:69]\n"
"v_cndmask_b32 v187, v22, 0, s[56:57]\n"
"WT lgkmcnt(8)\n"
"MF v[240:243],a[156:159],v[92:95],v[240:243]\n"
"v_cmp_le_i32 s[64:65], 32, v21\n"
"v_cmp_le_i32 s[66:67], 33, v21\n"
"MF v[228:231],a[128:131],v[64:67],0\n"
"v_cmp_le_i32 s[68:69], 34, v21\n"
"v_cmp_le_i32 s[56:57], 35, v21\n"
"MF v[228:231],a[132:135],v[68:71],v[228:231]\n"
"v_cndmask_b32 v188, v22, 0, s[64:65]\n"
"v_cndmask_b32 v189, v22, 0, s[66:67]\n"
"MF v[228:231],a[136:139],v[72:75],v[228:231]\n"
"v_cndmask_b32 v190, v22, 0, s[68:69]\n"
"v_cndmask_b32 v191, v22, 0, s[56:57]\n"
"MF v[228:231],a[140:143],v[76:79],v[228:231]\n"
"v_cmp_le_i32 s[64:65], 48, v21\n"
"v_cmp_le_i32 s[66:67], 49, v21\n"
"MF v[232:235],a[144:147],v[64:67],0\n"
"v_cmp_le_i32 s[68:69], 50, v21\n"
"v_cmp_le_i32 s[56:57], 51, v21\n"
"MF v[232:235],a[148:151],v[68:71],v[232:235]\n"
"v_cndmask_b32 v192, v22, 0, s[64:65]\n"
"v_cndmask_b32 v193, v22, 0, s[66:67]\n"
"MF v[232:235],a[152:155],v[72:75],v[232:235]\n"
"v_cndmask_b32 v194, v22, 0, s[68:69]\n"
"v_cndmask_b32 v195, v22, 0, s[56:57]\n"
"MF v[232:235],a[156:159],v[76:79],v[232:235]\n"
"s_cmp_eq_u32 s36, 0\n"
"s_cselect_b64 s[56:57], -1, 0\n"
"MF v[220:223],a[128:131],v[48:51],0\n"
"v_cndmask_b32 v196, 0, v180, s[56:57]\n"
"v_cndmask_b32 v197, 0, v181, s[56:57]\n"
"MF v[220:223],a[132:135],v[52:55],v[220:223]\n"
"v_cndmask_b32 v198, 0, v182, s[56:57]\n"
"v_cndmask_b32 v199, 0, v183, s[56:57]\n"
"MF v[220:223],a[136:139],v[56:59],v[220:223]\n"
"v_cndmask_b32 v200, 0, v184, s[56:57]\n"
"v_cndmask_b32 v201, 0, v185, s[56:57]\n"
"MF v[220:223],a[140:143],v[60:63],v[220:223]\n"
"v_cndmask_b32 v202, 0, v186, s[56:57]\n"
"v_cndmask_b32 v203, 0, v187, s[56:57]\n"
"MF v[224:227],a[144:147],v[48:51],0\n"
"s_lshl_b32 s59, s35, 19\n"
"s_add_u32 s59, s59, 0x4000000\n"
"MF v[224:227],a[148:151],v[52:55],v[224:227]\n"
"s_lshl_b32 s56, s36, 3\n"
"s_add_u32 s57, s56, s34\n"
"MF v[224:227],a[152:155],v[56:59],v[224:227]\n"
"v_max3_f32 v236, v236, v237, v238\n"
"v_max3_f32 v239, v239, v240, v241\n"
"MF v[224:227],a[156:159],v[60:63],v[224:227]\n"
"v_max3_f32 v236, v236, v242, v243\n"
"v_max_f32 v247, v236, v239\n"
"MF v[212:215],a[128:131],v[32:35],v[196:199]\n"
"v_max3_f32 v228, v228, v229, v230\n"
"v_max3_f32 v231, v231, v232, v233\n"
"MF v[212:215],a[132:135],v[36:39],v[212:215]\n"
"v_max3_f32 v228, v228, v234, v235\n"
"v_max_f32 v246, v228, v231\n"
"MF v[212:215],a[136:139],v[40:43],v[212:215]\n"
"s_sub_u32 s58, s34, s56\n"
"s_add_u32 s58, s58, 120\n"
"MF v[212:215],a[140:143],v[44:47],v[212:215]\n"
"s_mul_i32 s60, s57, 4096\n"
"s_add_u32 s60, s60, s59\n"
"MF v[216:219],a[144:147],v[32:35],v[200:203]\n"
"s_add_u32 s61, s60, 16384\n"
"s_mul_i32 s62, s58, 4096\n"
"MF v[216:219],a[148:151],v[36:39],v[216:219]\n"
"s_add_u32 s62, s62, s59\n"
"s_add_u32 s63, s62, 16384\n"
"MF v[216:219],a[152:155],v[40:43],v[216:219]\n"
"MF v[216:219],a[156:159],v[44:47],v[216:219]\n"
"v_max3_f32 v220, v220, v221, v222\n"
"v_max3_f32 v223, v223, v224, v225\n"
"v_max3_f32 v220, v220, v226, v227\n"
"v_max_f32 v245, v220, v223\n"
"s_mov_b32 s20, 32768\n"
"s_mov_b64 s[10:11], 0\n"
"v_mov_b32 v160, 0\n"
"v_mov_b32 v161, 0\n"
"v_mov_b32 v162, 0\n"
"v_mov_b32 v163, 0\n"
"v_mov_b32 v164, 0\n"
"v_mov_b32 v165, 0\n"
"v_max3_f32 v212, v212, v213, v214\n"
"v_max3_f32 v215, v215, v216, v217\n"
"v_max3_f32 v212, v212, v218, v219\n"
"v_max_f32 v244, v212, v215\n"
"v_mov_b32 v212, v244\n"
"v_mov_b32 v213, v245\n"
"v_mov_b32 v214, v246\n"
"v_mov_b32 v215, v247\n"
"v_permlane32_swap_b32 v244, v212\n"
"v_permlane32_swap_b32 v245, v213\n"
"v_permlane32_swap_b32 v246, v214\n"
"v_permlane32_swap_b32 v247, v215\n"
"v_max_f32 v244, v244, v212\n"
"v_max_f32 v245, v245, v213\n"
"v_max_f32 v246, v246, v214\n"
"v_max_f32 v247, v247, v215\n"
"v_mov_b32 v212, v244\n"
"v_mov_b32 v213, v245\n"
"v_mov_b32 v214, v246\n"
"v_mov_b32 v215, v247\n"
"v_permlane16_swap_b32 v244, v212\n"
"v_permlane16_swap_b32 v245, v213\n"
"v_permlane16_swap_b32 v246, v214\n"
"v_permlane16_swap_b32 v247, v215\n"
"v_max_f32 v244, v244, v212\n"
"v_max_f32 v245, v245, v213\n"
"v_max_f32 v246, v246, v214\n"
"v_max_f32 v247, v247, v215\n"
"v_sub_f32 v156, 0, v247\n"
"v_sub_f32 v157, 0, v247\n"
"v_sub_f32 v158, 0, v247\n"
"v_sub_f32 v159, 0, v247\n"
"v_sub_f32 v152, 0, v246\n"
"v_sub_f32 v153, 0, v246\n"
"MF v[96:99],a[128:131],v[80:83],v[156:159]\n"
"v_sub_f32 v154, 0, v246\n"
"v_sub_f32 v155, 0, v246\n"
"MF v[96:99],a[132:135],v[84:87],v[96:99]\n"
"v_sub_f32 v148, 0, v245\n"
"v_sub_f32 v149, 0, v245\n"
"MF v[96:99],a[136:139],v[88:91],v[96:99]\n"
"v_sub_f32 v150, 0, v245\n"
"v_sub_f32 v151, 0, v245\n"
"MF v[96:99],a[140:143],v[92:95],v[96:99]\n"
"v_sub_f32 v144, 0, v244\n"
"v_sub_f32 v145, 0, v244\n"
"MF v[100:103],a[144:147],v[80:83],v[156:159]\n"
"v_sub_f32 v146, 0, v244\n"
"v_sub_f32 v147, 0, v244\n"
"MF v[100:103],a[148:151],v[84:87],v[100:103]\n"
"v_mov_b32 v166, 0\n"
"v_mov_b32 v167, 0\n"
"MF v[100:103],a[152:155],v[88:91],v[100:103]\n"
"v_mov_b32 v168, 0\n"
"v_mov_b32 v169, 0\n"
"MF v[100:103],a[156:159],v[92:95],v[100:103]\n"
"v_mov_b32 v170, 0\n"
"v_mov_b32 v171, 0\n"
"WT lgkmcnt(7)\n"
"MF v[104:107],a[160:163],v[80:83],v[156:159]\n"
"v_mov_b32 v172, 0\n"
"v_mov_b32 v173, 0\n"
"WT lgkmcnt(6)\n"
"MF v[104:107],a[164:167],v[84:87],v[104:107]\n"
"v_mov_b32 v174, 0\n"
"v_mov_b32 v175, 0\n"
"WT lgkmcnt(5)\n"
"MF v[104:107],a[168:171],v[88:91],v[104:107]\n"
"v_mov_b32 v136, 0\n"
"v_mov_b32 v140, 0\n"
"WT lgkmcnt(4)\n"
"MF v[104:107],a[172:175],v[92:95],v[104:107]\n"
"v_mov_b32 v176, 0x3f803f80\n"
"v_mov_b32 v137, 0\n"
"WT lgkmcnt(3)\n"
"MF v[108:111],a[176:179],v[80:83],v[156:159]\n"
"v_mov_b32 v141, 0\n"
"v_mov_b32 v177, 0x3f803f80\n"
"WT lgkmcnt(2)\n"
"MF v[108:111],a[180:183],v[84:87],v[108:111]\n"
"v_mov_b32 v138, 0\n"
"v_mov_b32 v142, 0\n"
"WT lgkmcnt(1)\n"
"MF v[108:111],a[184:187],v[88:91],v[108:111]\n"
"v_mov_b32 v178, 0x3f803f80\n"
"v_mov_b32 v139, 0\n"
"WT lgkmcnt(0)\n"
"MF v[108:111],a[188:191],v[92:95],v[108:111]\n"
"v_mov_b32 v143, 0\n"
"v_mov_b32 v179, 0x3f803f80\n"
"v_mov_b32 v250, 0x5f800000\n"
"WT lgkmcnt(0)\n"
"s_lshl_b32 s64, s33, 2\n"
"s_add_u32 s64, s64, s34\n"
"s_mov_b32 s56, s18\n"
"s_and_b32 s57, s19, 0xffff\n"
"s_mov_b32 s58, 0x500000\n"
"s_mov_b32 s59, 0x20000\n"
"v_min_u32 v21, 39, v0\n"
"v_lshlrev_b32 v21, 7, v21\n"
"s_mul_i32 s65, s64, 5120\n"
"buffer_load_dword v251, v21, s[56:59], s65 offen\n"
"s_mov_b32 s56, s24\n"
"s_and_b32 s57, s25, 0xffff\n"
"s_mov_b32 s58, 0x21c000\n"
"s_mov_b32 s59, 0x20000\n"
"v_min_u32 v21, 16, v0\n"
"v_lshlrev_b32 v21, 7, v21\n"
"s_mul_i32 s65, s64, 2176\n"
"buffer_load_dword v251, v21, s[56:59], s65 offen\n"
"s_mov_b32 s56, s26\n"
"s_and_b32 s57, s27, 0xffff\n"
"s_mov_b32 s58, 0x21c000\n"
"s_mov_b32 s59, 0x20000\n"
"v_min_u32 v21, 16, v0\n"
"v_lshlrev_b32 v21, 7, v21\n"
"s_mul_i32 s65, s64, 2176\n"
"buffer_load_dword v251, v21, s[56:59], s65 offen\n"
"s_lshl_b32 s14, s36, 1\n"
"s_sub_u32 s15, s14, 2\n"
"s_cmp_eq_u32 s14, 0\n"
"s_cbranch_scc1 L_skip_21\n"
"L_a4:\n"
"MF v[112:115],a[128:131],v[32:35],v[144:147]\n"
"EX v96,v96\n"
"MF v[112:115],a[132:135],v[36:39],v[112:115]\n"
"MF v[112:115],a[136:139],v[40:43],v[112:115]\n"
"EX v97,v97\n"
"MF v[112:115],a[140:143],v[44:47],v[112:115]\n"
"CV v128,v96,v97\n"
"MF v[116:119],a[144:147],v[32:35],v[144:147]\n"
"EX v98,v98\n"
"MF v[116:119],a[148:151],v[36:39],v[116:119]\n"
"MF v[116:119],a[152:155],v[40:43],v[116:119]\n"
"EX v99,v99\n"
"MF v[116:119],a[156:159],v[44:47],v[116:119]\n"
"CV v129,v98,v99\n"
"MF v[120:123],a[160:163],v[32:35],v[144:147]\n"
"EX v100,v100\n"
"MF v[120:123],a[164:167],v[36:39],v[120:123]\n"
"MF v[120:123],a[168:171],v[40:43],v[120:123]\n"
"EX v101,v101\n"
"MF v[120:123],a[172:175],v[44:47],v[120:123]\n"
"CV v130,v100,v101\n"
"MF v[124:127],a[176:179],v[32:35],v[144:147]\n"
"EX v102,v102\n"
"MF v[124:127],a[180:183],v[36:39],v[124:127]\n"
"MF v[124:127],a[184:187],v[40:43],v[124:127]\n"
"EX v103,v103\n"
"MF v[124:127],a[188:191],v[44:47],v[124:127]\n"
"CV v131,v102,v103\n"
"MF a[64:67],a[192:195],v[136:139],a[64:67]\n"
"EX v104,v104\n"
"MF a[64:67],a[224:227],v[140:143],a[64:67]\n"
"RD a[192:195],v249 offset:8192\n"
"RD a[224:227],v249 offset:24576\n"
"MF a[68:71],a[196:199],v[136:139],a[68:71]\n"
"EX v105,v105\n"
"MF a[68:71],a[228:231],v[140:143],a[68:71]\n"
"RD a[196:199],v249 offset:9216\n"
"CV v132,v104,v105\n"
"MF a[72:75],a[200:203],v[136:139],a[72:75]\n"
"EX v106,v106\n"
"MF a[72:75],a[232:235],v[140:143],a[72:75]\n"
"RD a[228:231],v249 offset:25600\n"
"RD a[200:203],v249 offset:10240\n"
"MF a[76:79],a[204:207],v[136:139],a[76:79]\n"
"EX v107,v107\n"
"MF a[76:79],a[236:239],v[140:143],a[76:79]\n"
"RD a[232:235],v249 offset:26624\n"
"CV v133,v106,v107\n"
"MF a[80:83],a[208:211],v[136:139],a[80:83]\n"
"EX v108,v108\n"
"MF a[80:83],a[240:243],v[140:143],a[80:83]\n"
"RD a[204:207],v249 offset:11264\n"
"RD a[236:239],v249 offset:27648\n"
"MF a[84:87],a[212:215],v[136:139],a[84:87]\n"
"EX v109,v109\n"
"MF a[84:87],a[244:247],v[140:143],a[84:87]\n"
"RD a[208:211],v249 offset:12288\n"
"CV v134,v108,v109\n"
"MF a[88:91],a[216:219],v[136:139],a[88:91]\n"
"EX v110,v110\n"
"MF a[88:91],a[248:251],v[140:143],a[88:91]\n"
"RD a[240:243],v249 offset:28672\n"
"RD a[212:215],v249 offset:13312\n"
"MF a[92:95],a[220:223],v[136:139],a[92:95]\n"
"EX v111,v111\n"
"MF a[92:95],a[252:255],v[140:143],a[92:95]\n"
"RD a[244:247],v249 offset:29696\n"
"CV v135,v110,v111\n"
"MF v[168:171],v[176:179],v[136:139],v[168:171]\n"
"RD a[216:219],v249 offset:14336\n"
"RD a[248:251],v249 offset:30720\n"
"MF v[168:171],v[176:179],v[140:143],v[168:171]\n"
"RD a[220:223],v249 offset:15360\n"
"RD a[252:255],v249 offset:31744\n"
"MF v[96:99],a[128:131],v[48:51],v[148:151]\n"
"EX v112,v112\n"
"MF v[96:99],a[132:135],v[52:55],v[96:99]\n"
"MF v[96:99],a[136:139],v[56:59],v[96:99]\n"
"EX v113,v113\n"
"MF v[96:99],a[140:143],v[60:63],v[96:99]\n"
"CV v136,v112,v113\n"
"MF v[100:103],a[144:147],v[48:51],v[148:151]\n"
"EX v114,v114\n"
"MF v[100:103],a[148:151],v[52:55],v[100:103]\n"
"MF v[100:103],a[152:155],v[56:59],v[100:103]\n"
"EX v115,v115\n"
"MF v[100:103],a[156:159],v[60:63],v[100:103]\n"
"CV v137,v114,v115\n"
"MF v[104:107],a[160:163],v[48:51],v[148:151]\n"
"EX v116,v116\n"
"MF v[104:107],a[164:167],v[52:55],v[104:107]\n"
"MF v[104:107],a[168:171],v[56:59],v[104:107]\n"
"EX v117,v117\n"
"MF v[104:107],a[172:175],v[60:63],v[104:107]\n"
"CV v138,v116,v117\n"
"MF v[108:111],a[176:179],v[48:51],v[148:151]\n"
"EX v118,v118\n"
"MF v[108:111],a[180:183],v[52:55],v[108:111]\n"
"MF v[108:111],a[184:187],v[56:59],v[108:111]\n"
"EX v119,v119\n"
"MF v[108:111],a[188:191],v[60:63],v[108:111]\n"
"WT lgkmcnt(14)\n"
"CV v139,v118,v119\n"
"MF a[96:99],a[192:195],v[128:131],a[96:99]\n"
"EX v120,v120\n"
"MF a[96:99],a[224:227],v[132:135],a[96:99]\n"
"WT lgkmcnt(12)\n"
"MF a[100:103],a[196:199],v[128:131],a[100:103]\n"
"EX v121,v121\n"
"MF a[100:103],a[228:231],v[132:135],a[100:103]\n"
"WT lgkmcnt(10)\n"
"CV v140,v120,v121\n"
"MF a[104:107],a[200:203],v[128:131],a[104:107]\n"
"EX v122,v122\n"
"MF a[104:107],a[232:235],v[132:135],a[104:107]\n"
"WT lgkmcnt(8)\n"
"MF a[108:111],a[204:207],v[128:131],a[108:111]\n"
"EX v123,v123\n"
"MF a[108:111],a[236:239],v[132:135],a[108:111]\n"
"WT lgkmcnt(6)\n"
"CV v141,v122,v123\n"
"MF a[112:115],a[208:211],v[128:131],a[112:115]\n"
"EX v124,v124\n"
"MF a[112:115],a[240:243],v[132:135],a[112:115]\n"
"WT lgkmcnt(4)\n"
"MF a[116:119],a[212:215],v[128:131],a[116:119]\n"
"EX v125,v125\n"
"MF a[116:119],a[244:247],v[132:135],a[116:119]\n"
"WT lgkmcnt(2)\n"
"CV v142,v124,v125\n"
"MF a[120:123],a[216:219],v[128:131],a[120:123]\n"
"EX v126,v126\n"
"MF a[120:123],a[248:251],v[132:135],a[120:123]\n"
"WT lgkmcnt(0)\n"
"MF a[124:127],a[220:223],v[128:131],a[124:127]\n"
"EX v127,v127\n"
"MF a[124:127],a[252:255],v[132:135],a[124:127]\n"
"CV v143,v126,v127\n"
"MF v[172:175],v[176:179],v[128:131],v[172:175]\n"
"WT vmcnt(12)\n"
"MF v[172:175],v[176:179],v[132:135],v[172:175]\n"
"s_barrier\n"
"MF v[112:115],a[128:131],v[64:67],v[152:155]\n"
"EX v96,v96\n"
"MF v[112:115],a[132:135],v[68:71],v[112:115]\n"
"RD a[128:131],v248 offset:0\n"
"RD a[132:135],v248 offset:1024\n"
"MF v[112:115],a[136:139],v[72:75],v[112:115]\n"
"EX v97,v97\n"
"MF v[112:115],a[140:143],v[76:79],v[112:115]\n"
"RD a[136:139],v248 offset:2048\n"
"RD a[140:143],v248 offset:3072\n"
"MF v[116:119],a[144:147],v[64:67],v[152:155]\n"
"EX v98,v98\n"
"MF v[116:119],a[148:151],v[68:71],v[116:119]\n"
"RD a[144:147],v248 offset:4096\n"
"RD a[148:151],v248 offset:5120\n"
"MF v[116:119],a[152:155],v[72:75],v[116:119]\n"
"EX v99,v99\n"
"MF v[116:119],a[156:159],v[76:79],v[116:119]\n"
"s_add_u32 s16, s16, 8192\n"
"RD a[152:155],v248 offset:6144\n"
"CV v128,v96,v97\n"
"MF v[120:123],a[160:163],v[64:67],v[152:155]\n"
"s_add_u32 m0, s17, 8192\n"
"EX v100,v100\n"
"MF v[120:123],a[164:167],v[68:71],v[120:123]\n"
"LD v1,s[44:47],s16 offen offset:0 sc1 lds\n"
"MF v[120:123],a[168:171],v[72:75],v[120:123]\n"
"EX v101,v101\n"
"MF v[120:123],a[172:175],v[76:79],v[120:123]\n"
"RD a[156:159],v248 offset:7168\n"
"CV v129,v98,v99\n"
"MF v[124:127],a[176:179],v[64:67],v[152:155]\n"
"EX v102,v102\n"
"MF v[124:127],a[180:183],v[68:71],v[124:127]\n"
"RD a[160:163],v248 offset:16384\n"
"RD a[164:167],v248 offset:17408\n"
"MF v[124:127],a[184:187],v[72:75],v[124:127]\n"
"EX v103,v103\n"
"MF v[124:127],a[188:191],v[76:79],v[124:127]\n"
"RD a[168:171],v248 offset:18432\n"
"CV v130,v100,v101\n"
"MF a[0:3],a[192:195],v[136:139],a[0:3]\n"
"EX v104,v104\n"
"MF a[0:3],a[224:227],v[140:143],a[0:3]\n"
"LD v1,s[44:47],s16 offen offset:1024 sc1 lds\n"
"MF a[4:7],a[196:199],v[136:139],a[4:7]\n"
"EX v105,v105\n"
"MF a[4:7],a[228:231],v[140:143],a[4:7]\n"
"RD a[172:175],v248 offset:19456\n"
"CV v131,v102,v103\n"
"MF a[8:11],a[200:203],v[136:139],a[8:11]\n"
"EX v106,v106\n"
"MF a[8:11],a[232:235],v[140:143],a[8:11]\n"
"RD a[176:179],v248 offset:20480\n"
"RD a[180:183],v248 offset:21504\n"
"MF a[12:15],a[204:207],v[136:139],a[12:15]\n"
"EX v107,v107\n"
"MF a[12:15],a[236:239],v[140:143],a[12:15]\n"
"RD a[184:187],v248 offset:22528\n"
"CV v132,v104,v105\n"
"MF a[16:19],a[208:211],v[136:139],a[16:19]\n"
"EX v108,v108\n"
"MF a[16:19],a[240:243],v[140:143],a[16:19]\n"
"LD v1,s[44:47],s16 offen offset:2048 sc1 lds\n"
"MF a[20:23],a[212:215],v[136:139],a[20:23]\n"
"EX v109,v109\n"
"MF a[20:23],a[244:247],v[140:143],a[20:23]\n"
"RD a[188:191],v248 offset:23552\n"
"CV v133,v106,v107\n"
"MF a[24:27],a[216:219],v[136:139],a[24:27]\n"
"EX v110,v110\n"
"MF a[24:27],a[248:251],v[140:143],a[24:27]\n"
"CV v134,v108,v109\n"
"MF a[28:31],a[220:223],v[136:139],a[28:31]\n"
"EX v111,v111\n"
"MF a[28:31],a[252:255],v[140:143],a[28:31]\n"
"CV v135,v110,v111\n"
"MF v[160:163],v[176:179],v[136:139],v[160:163]\n"
"LD v1,s[44:47],s16 offen offset:3072 sc1 lds\n"
"MF v[160:163],v[176:179],v[140:143],v[160:163]\n"
"WT lgkmcnt(14)\n"
"MF v[96:99],a[128:131],v[80:83],v[156:159]\n"
"EX v112,v112\n"
"MF v[96:99],a[132:135],v[84:87],v[96:99]\n"
"WT lgkmcnt(12)\n"
"s_add_u32 s16, s16, 25856\n"
"MF v[96:99],a[136:139],v[88:91],v[96:99]\n"
"s_add_u32 s17, s17, 32768\n"
"EX v113,v113\n"
"MF v[96:99],a[140:143],v[92:95],v[96:99]\n"
"WT lgkmcnt(10)\n"
"s_and_b32 s17, s17, 0x1ffff\n"
"CV v136,v112,v113\n"
"MF v[100:103],a[144:147],v[80:83],v[156:159]\n"
"s_mov_b32 m0, s17\n"
"EX v114,v114\n"
"MF v[100:103],a[148:151],v[84:87],v[100:103]\n"
"WT lgkmcnt(8)\n"
"LD v1,s[44:47],s16 offen offset:0 sc1 lds\n"
"MF v[100:103],a[152:155],v[88:91],v[100:103]\n"
"EX v115,v115\n"
"MF v[100:103],a[156:159],v[92:95],v[100:103]\n"
"WT lgkmcnt(6)\n"
"CV v137,v114,v115\n"
"MF v[104:107],a[160:163],v[80:83],v[156:159]\n"
"EX v116,v116\n"
"MF v[104:107],a[164:167],v[84:87],v[104:107]\n"
"WT lgkmcnt(4)\n"
"MF v[104:107],a[168:171],v[88:91],v[104:107]\n"
"EX v117,v117\n"
"MF v[104:107],a[172:175],v[92:95],v[104:107]\n"
"WT lgkmcnt(2)\n"
"CV v138,v116,v117\n"
"MF v[108:111],a[176:179],v[80:83],v[156:159]\n"
"EX v118,v118\n"
"MF v[108:111],a[180:183],v[84:87],v[108:111]\n"
"WT lgkmcnt(0)\n"
"LD v1,s[44:47],s16 offen offset:1024 sc1 lds\n"
"MF v[108:111],a[184:187],v[88:91],v[108:111]\n"
"EX v119,v119\n"
"MF v[108:111],a[188:191],v[92:95],v[108:111]\n"
"CV v139,v118,v119\n"
"MF a[32:35],a[192:195],v[128:131],a[32:35]\n"
"EX v120,v120\n"
"MF a[32:35],a[224:227],v[132:135],a[32:35]\n"
"MF a[36:39],a[196:199],v[128:131],a[36:39]\n"
"EX v121,v121\n"
"MF a[36:39],a[228:231],v[132:135],a[36:39]\n"
"CV v140,v120,v121\n"
"MF a[40:43],a[200:203],v[128:131],a[40:43]\n"
"s_add_u32 s20, s20, 32768\n"
"EX v122,v122\n"
"MF a[40:43],a[232:235],v[132:135],a[40:43]\n"
"LD v1,s[44:47],s16 offen offset:2048 sc1 lds\n"
"MF a[44:47],a[204:207],v[128:131],a[44:47]\n"
"s_and_b32 s20, s20, 0x1ffff\n"
"EX v123,v123\n"
"MF a[44:47],a[236:239],v[132:135],a[44:47]\n"
"CV v141,v122,v123\n"
"MF a[48:51],a[208:211],v[128:131],a[48:51]\n"
"EX v124,v124\n"
"MF a[48:51],a[240:243],v[132:135],a[48:51]\n"
"s_cmp_eq_u32 s14, s15\n"
"s_cbranch_scc1 L_hook_22\n"
"L_back_23:\n"
"MF a[52:55],a[212:215],v[128:131],a[52:55]\n"
"EX v125,v125\n"
"MF a[52:55],a[244:247],v[132:135],a[52:55]\n"
"CV v142,v124,v125\n"
"MF a[56:59],a[216:219],v[128:131],a[56:59]\n"
"s_sub_u32 s14, s14, 1\n"
"EX v126,v126\n"
"MF a[56:59],a[248:251],v[132:135],a[56:59]\n"
"LD v1,s[44:47],s16 offen offset:3072 sc1 lds\n"
"MF a[60:63],a[220:223],v[128:131],a[60:63]\n"
"s_cmp_lg_u32 s14, 0\n"
"EX v127,v127\n"
"MF a[60:63],a[252:255],v[132:135],a[60:63]\n"
"v_mov_b32 v249, v248\n"
"v_add_u32 v248, s20, v1\n"
"MF v[164:167],v[176:179],v[128:131],v[164:167]\n"
"WT lgkmcnt(0)\n"
"CV v143,v126,v127\n"
"MF v[164:167],v[176:179],v[132:135],v[164:167]\n"
"s_cbranch_scc1 L_a4\n"
"L_skip_21:\n"
"s_mov_b32 s56, 0x5000000\n"
"s_add_u32 s52, s30, s56\n"
"s_addc_u32 s53, s31, 0\n"
"s_and_b32 s53, s53, 0xffff\n"
"s_mov_b32 s54, 0xa00000\n"
"s_mov_b32 s55, 0x20000\n"
"s_lshl_b32 s21, s33, 2\n"
"s_add_u32 s21, s21, s34\n"
"s_mul_i32 s21, s21, 16384\n"
"v_mov_b64 v[252:253], -1\n"
"v_mov_b64 v[254:255], -1\n"
"v_add_f32 v196, v180, v144\n"
"v_add_f32 v197, v181, v144\n"
"v_add_f32 v198, v182, v144\n"
"v_add_f32 v199, v183, v144\n"
"v_add_f32 v200, v184, v144\n"
"v_add_f32 v201, v185, v144\n"
"v_add_f32 v202, v186, v144\n"
"v_add_f32 v203, v187, v144\n"
"v_add_f32 v204, v188, v144\n"
"v_add_f32 v205, v189, v144\n"
"v_add_f32 v206, v190, v144\n"
"v_add_f32 v207, v191, v144\n"
"v_add_f32 v208, v192, v144\n"
"v_add_f32 v209, v193, v144\n"
"v_add_f32 v210, v194, v144\n"
"v_add_f32 v211, v195, v144\n"
"MF v[112:115],a[128:131],v[32:35],v[196:199]\n"
"EX v96,v96\n"
"MF v[112:115],a[132:135],v[36:39],v[112:115]\n"
"MF v[112:115],a[136:139],v[40:43],v[112:115]\n"
"EX v97,v97\n"
"MF v[112:115],a[140:143],v[44:47],v[112:115]\n"
"CV v128,v96,v97\n"
"MF v[116:119],a[144:147],v[32:35],v[200:203]\n"
"EX v98,v98\n"
"MF v[116:119],a[148:151],v[36:39],v[116:119]\n"
"MF v[116:119],a[152:155],v[40:43],v[116:119]\n"
"EX v99,v99\n"
"MF v[116:119],a[156:159],v[44:47],v[116:119]\n"
"ST v[252:255],v1,s[52:55],s21 offen offset:0 sc1\n"
"MF v[120:123],a[160:163],v[32:35],v[204:207]\n"
"EX v100,v100\n"
"MF v[120:123],a[164:167],v[36:39],v[120:123]\n"
"CV v129,v98,v99\n"
"MF v[120:123],a[168:171],v[40:43],v[120:123]\n"
"EX v101,v101\n"
"MF v[120:123],a[172:175],v[44:47],v[120:123]\n"
"CV v130,v100,v101\n"
"MF v[124:127],a[176:179],v[32:35],v[208:211]\n"
"EX v102,v102\n"
"MF v[124:127],a[180:183],v[36:39],v[124:127]\n"
"MF v[124:127],a[184:187],v[40:43],v[124:127]\n"
"EX v103,v103\n"
"MF v[124:127],a[188:191],v[44:47],v[124:127]\n"
"CV v131,v102,v103\n"
"MF a[64:67],a[192:195],v[136:139],a[64:67]\n"
"EX v104,v104\n"
"MF a[64:67],a[224:227],v[140:143],a[64:67]\n"
"RD a[192:195],v249 offset:8192\n"
"RD a[224:227],v249 offset:24576\n"
"MF a[68:71],a[196:199],v[136:139],a[68:71]\n"
"EX v105,v105\n"
"MF a[68:71],a[228:231],v[140:143],a[68:71]\n"
"ST v[252:255],v1,s[52:55],s21 offen offset:1024 sc1\n"
"MF a[72:75],a[200:203],v[136:139],a[72:75]\n"
"EX v106,v106\n"
"MF a[72:75],a[232:235],v[140:143],a[72:75]\n"
"RD a[196:199],v249 offset:9216\n"
"CV v132,v104,v105\n"
"MF a[76:79],a[204:207],v[136:139],a[76:79]\n"
"EX v107,v107\n"
"MF a[76:79],a[236:239],v[140:143],a[76:79]\n"
"RD a[228:231],v249 offset:25600\n"
"CV v133,v106,v107\n"
"MF a[80:83],a[208:211],v[136:139],a[80:83]\n"
"EX v108,v108\n"
"MF a[80:83],a[240:243],v[140:143],a[80:83]\n"
"RD a[200:203],v249 offset:10240\n"
"RD a[232:235],v249 offset:26624\n"
"MF a[84:87],a[212:215],v[136:139],a[84:87]\n"
"EX v109,v109\n"
"MF a[84:87],a[244:247],v[140:143],a[84:87]\n"
"RD a[204:207],v249 offset:11264\n"
"CV v134,v108,v109\n"
"MF a[88:91],a[216:219],v[136:139],a[88:91]\n"
"EX v110,v110\n"
"MF a[88:91],a[248:251],v[140:143],a[88:91]\n"
"RD a[236:239],v249 offset:27648\n"
"RD a[208:211],v249 offset:12288\n"
"MF a[92:95],a[220:223],v[136:139],a[92:95]\n"
"EX v111,v111\n"
"MF a[92:95],a[252:255],v[140:143],a[92:95]\n"
"ST v[252:255],v1,s[52:55],s21 offen offset:2048 sc1\n"
"MF v[168:171],v[176:179],v[136:139],v[168:171]\n"
"RD a[240:243],v249 offset:28672\n"
"CV v135,v110,v111\n"
"MF v[168:171],v[176:179],v[140:143],v[168:171]\n"
"RD a[212:215],v249 offset:13312\n"
"RD a[244:247],v249 offset:29696\n"
"MF v[96:99],a[128:131],v[48:51],v[148:151]\n"
"EX v112,v112\n"
"MF v[96:99],a[132:135],v[52:55],v[96:99]\n"
"RD a[216:219],v249 offset:14336\n"
"RD a[248:251],v249 offset:30720\n"
"MF v[96:99],a[136:139],v[56:59],v[96:99]\n"
"EX v113,v113\n"
"MF v[96:99],a[140:143],v[60:63],v[96:99]\n"
"RD a[220:223],v249 offset:15360\n"
"RD a[252:255],v249 offset:31744\n"
"MF v[100:103],a[144:147],v[48:51],v[148:151]\n"
"EX v114,v114\n"
"MF v[100:103],a[148:151],v[52:55],v[100:103]\n"
"CV v136,v112,v113\n"
"MF v[100:103],a[152:155],v[56:59],v[100:103]\n"
"EX v115,v115\n"
"MF v[100:103],a[156:159],v[60:63],v[100:103]\n"
"ST v[252:255],v1,s[52:55],s21 offen offset:3072 sc1\n"
"MF v[104:107],a[160:163],v[48:51],v[148:151]\n"
"EX v116,v116\n"
"MF v[104:107],a[164:167],v[52:55],v[104:107]\n"
"CV v137,v114,v115\n"
"MF v[104:107],a[168:171],v[56:59],v[104:107]\n"
"EX v117,v117\n"
"MF v[104:107],a[172:175],v[60:63],v[104:107]\n"
"CV v138,v116,v117\n"
"MF v[108:111],a[176:179],v[48:51],v[148:151]\n"
"EX v118,v118\n"
"MF v[108:111],a[180:183],v[52:55],v[108:111]\n"
"MF v[108:111],a[184:187],v[56:59],v[108:111]\n"
"EX v119,v119\n"
"MF v[108:111],a[188:191],v[60:63],v[108:111]\n"
"WT lgkmcnt(14)\n"
"CV v139,v118,v119\n"
"MF a[96:99],a[192:195],v[128:131],a[96:99]\n"
"EX v120,v120\n"
"MF a[96:99],a[224:227],v[132:135],a[96:99]\n"
"WT lgkmcnt(12)\n"
"MF a[100:103],a[196:199],v[128:131],a[100:103]\n"
"s_add_u32 s21, s21, 4096\n"
"EX v121,v121\n"
"MF a[100:103],a[228:231],v[132:135],a[100:103]\n"
"WT lgkmcnt(10)\n"
"ST v[252:255],v1,s[52:55],s21 offen offset:0 sc1\n"
"MF a[104:107],a[200:203],v[128:131],a[104:107]\n"
"EX v122,v122\n"
"MF a[104:107],a[232:235],v[132:135],a[104:107]\n"
"WT lgkmcnt(8)\n"
"CV v140,v120,v121\n"
"MF a[108:111],a[204:207],v[128:131],a[108:111]\n"
"EX v123,v123\n"
"MF a[108:111],a[236:239],v[132:135],a[108:111]\n"
"WT lgkmcnt(6)\n"
"CV v141,v122,v123\n"
"MF a[112:115],a[208:211],v[128:131],a[112:115]\n"
"EX v124,v124\n"
"MF a[112:115],a[240:243],v[132:135],a[112:115]\n"
"WT lgkmcnt(4)\n"
"MF a[116:119],a[212:215],v[128:131],a[116:119]\n"
"EX v125,v125\n"
"MF a[116:119],a[244:247],v[132:135],a[116:119]\n"
"WT lgkmcnt(2)\n"
"CV v142,v124,v125\n"
"MF a[120:123],a[216:219],v[128:131],a[120:123]\n"
"EX v126,v126\n"
"MF a[120:123],a[248:251],v[132:135],a[120:123]\n"
"WT lgkmcnt(0)\n"
"MF a[124:127],a[220:223],v[128:131],a[124:127]\n"
"EX v127,v127\n"
"MF a[124:127],a[252:255],v[132:135],a[124:127]\n"
"CV v143,v126,v127\n"
"MF v[172:175],v[176:179],v[128:131],v[172:175]\n"
"WT vmcnt(12)\n"
"ST v[252:255],v1,s[52:55],s21 offen offset:1024 sc1\n"
"MF v[172:175],v[176:179],v[132:135],v[172:175]\n"
"s_barrier\n"
"MF v[112:115],a[128:131],v[64:67],v[152:155]\n"
"EX v96,v96\n"
"MF v[112:115],a[132:135],v[68:71],v[112:115]\n"
"RD a[128:131],v248 offset:0\n"
"RD a[132:135],v248 offset:1024\n"
"MF v[112:115],a[136:139],v[72:75],v[112:115]\n"
"EX v97,v97\n"
"MF v[112:115],a[140:143],v[76:79],v[112:115]\n"
"RD a[136:139],v248 offset:2048\n"
"RD a[140:143],v248 offset:3072\n"
"MF v[116:119],a[144:147],v[64:67],v[152:155]\n"
"EX v98,v98\n"
"MF v[116:119],a[148:151],v[68:71],v[116:119]\n"
"RD a[144:147],v248 offset:4096\n"
"RD a[148:151],v248 offset:5120\n"
"MF v[116:119],a[152:155],v[72:75],v[116:119]\n"
"EX v99,v99\n"
"MF v[116:119],a[156:159],v[76:79],v[116:119]\n"
"s_add_u32 s16, s16, 8192\n"
"RD a[152:155],v248 offset:6144\n"
"CV v128,v96,v97\n"
"MF v[120:123],a[160:163],v[64:67],v[152:155]\n"
"s_add_u32 m0, s17, 8192\n"
"EX v100,v100\n"
"MF v[120:123],a[164:167],v[68:71],v[120:123]\n"
"LD v1,s[44:47],s16 offen offset:0 sc1 lds\n"
"MF v[120:123],a[168:171],v[72:75],v[120:123]\n"
"EX v101,v101\n"
"MF v[120:123],a[172:175],v[76:79],v[120:123]\n"
"ST v[252:255],v1,s[52:55],s21 offen offset:2048 sc1\n"
"MF v[124:127],a[176:179],v[64:67],v[152:155]\n"
"EX v102,v102\n"
"MF v[124:127],a[180:183],v[68:71],v[124:127]\n"
"RD a[156:159],v248 offset:7168\n"
"CV v129,v98,v99\n"
"MF v[124:127],a[184:187],v[72:75],v[124:127]\n"
"EX v103,v103\n"
"MF v[124:127],a[188:191],v[76:79],v[124:127]\n"
"RD a[160:163],v248 offset:16384\n"
"RD a[164:167],v248 offset:17408\n"
"MF a[0:3],a[192:195],v[136:139],a[0:3]\n"
"EX v104,v104\n"
"MF a[0:3],a[224:227],v[140:143],a[0:3]\n"
"LD v1,s[44:47],s16 offen offset:1024 sc1 lds\n"
"MF a[4:7],a[196:199],v[136:139],a[4:7]\n"
"EX v105,v105\n"
"MF a[4:7],a[228:231],v[140:143],a[4:7]\n"
"RD a[168:171],v248 offset:18432\n"
"CV v130,v100,v101\n"
"MF a[8:11],a[200:203],v[136:139],a[8:11]\n"
"EX v106,v106\n"
"MF a[8:11],a[232:235],v[140:143],a[8:11]\n"
"RD a[172:175],v248 offset:19456\n"
"CV v131,v102,v103\n"
"MF a[12:15],a[204:207],v[136:139],a[12:15]\n"
"EX v107,v107\n"
"MF a[12:15],a[236:239],v[140:143],a[12:15]\n"
"RD a[176:179],v248 offset:20480\n"
"RD a[180:183],v248 offset:21504\n"
"MF a[16:19],a[208:211],v[136:139],a[16:19]\n"
"EX v108,v108\n"
"MF a[16:19],a[240:243],v[140:143],a[16:19]\n"
"LD v1,s[44:47],s16 offen offset:2048 sc1 lds\n"
"MF a[20:23],a[212:215],v[136:139],a[20:23]\n"
"EX v109,v109\n"
"MF a[20:23],a[244:247],v[140:143],a[20:23]\n"
"ST v[252:255],v1,s[52:55],s21 offen offset:3072 sc1\n"
"MF a[24:27],a[216:219],v[136:139],a[24:27]\n"
"EX v110,v110\n"
"MF a[24:27],a[248:251],v[140:143],a[24:27]\n"
"RD a[184:187],v248 offset:22528\n"
"CV v132,v104,v105\n"
"MF a[28:31],a[220:223],v[136:139],a[28:31]\n"
"EX v111,v111\n"
"MF a[28:31],a[252:255],v[140:143],a[28:31]\n"
"RD a[188:191],v248 offset:23552\n"
"CV v133,v106,v107\n"
"MF v[160:163],v[176:179],v[136:139],v[160:163]\n"
"LD v1,s[44:47],s16 offen offset:3072 sc1 lds\n"
"MF v[160:163],v[176:179],v[140:143],v[160:163]\n"
"WT lgkmcnt(14)\n"
"CV v134,v108,v109\n"
"CV v135,v110,v111\n"
"MF v[96:99],a[128:131],v[80:83],v[156:159]\n"
"EX v112,v112\n"
"MF v[96:99],a[132:135],v[84:87],v[96:99]\n"
"WT lgkmcnt(12)\n"
"s_add_u32 s16, s16, 25856\n"
"MF v[96:99],a[136:139],v[88:91],v[96:99]\n"
"s_add_u32 s17, s17, 32768\n"
"EX v113,v113\n"
"MF v[96:99],a[140:143],v[92:95],v[96:99]\n"
"WT lgkmcnt(10)\n"
"s_and_b32 s17, s17, 0x1ffff\n"
"CV v136,v112,v113\n"
"MF v[100:103],a[144:147],v[80:83],v[156:159]\n"
"s_mov_b32 m0, s17\n"
"EX v114,v114\n"
"MF v[100:103],a[148:151],v[84:87],v[100:103]\n"
"WT lgkmcnt(8)\n"
"LD v1,s[44:47],s16 offen offset:0 sc1 lds\n"
"MF v[100:103],a[152:155],v[88:91],v[100:103]\n"
"s_add_u32 s21, s21, 4096\n"
"EX v115,v115\n"
"MF v[100:103],a[156:159],v[92:95],v[100:103]\n"
"WT lgkmcnt(6)\n"
"ST v[252:255],v1,s[52:55],s21 offen offset:0 sc1\n"
"MF v[104:107],a[160:163],v[80:83],v[156:159]\n"
"EX v116,v116\n"
"MF v[104:107],a[164:167],v[84:87],v[104:107]\n"
"WT lgkmcnt(4)\n"
"CV v137,v114,v115\n"
"MF v[104:107],a[168:171],v[88:91],v[104:107]\n"
"EX v117,v117\n"
"MF v[104:107],a[172:175],v[92:95],v[104:107]\n"
"WT lgkmcnt(2)\n"
"CV v138,v116,v117\n"
"MF v[108:111],a[176:179],v[80:83],v[156:159]\n"
"EX v118,v118\n"
"MF v[108:111],a[180:183],v[84:87],v[108:111]\n"
"WT lgkmcnt(0)\n"
"LD v1,s[44:47],s16 offen offset:1024 sc1 lds\n"
"MF v[108:111],a[184:187],v[88:91],v[108:111]\n"
"EX v119,v119\n"
"MF v[108:111],a[188:191],v[92:95],v[108:111]\n"
"CV v139,v118,v119\n"
"MF a[32:35],a[192:195],v[128:131],a[32:35]\n"
"EX v120,v120\n"
"MF a[32:35],a[224:227],v[132:135],a[32:35]\n"
"MF a[36:39],a[196:199],v[128:131],a[36:39]\n"
"EX v121,v121\n"
"MF a[36:39],a[228:231],v[132:135],a[36:39]\n"
"CV v140,v120,v121\n"
"MF a[40:43],a[200:203],v[128:131],a[40:43]\n"
"s_add_u32 s20, s20, 32768\n"
"EX v122,v122\n"
"MF a[40:43],a[232:235],v[132:135],a[40:43]\n"
"LD v1,s[44:47],s16 offen offset:2048 sc1 lds\n"
"MF a[44:47],a[204:207],v[128:131],a[44:47]\n"
"s_and_b32 s20, s20, 0x1ffff\n"
"EX v123,v123\n"
"MF a[44:47],a[236:239],v[132:135],a[44:47]\n"
"ST v[252:255],v1,s[52:55],s21 offen offset:1024 sc1\n"
"MF a[48:51],a[208:211],v[128:131],a[48:51]\n"
"EX v124,v124\n"
"MF a[48:51],a[240:243],v[132:135],a[48:51]\n"
"CV v141,v122,v123\n"
"MF a[52:55],a[212:215],v[128:131],a[52:55]\n"
"EX v125,v125\n"
"MF a[52:55],a[244:247],v[132:135],a[52:55]\n"
"CV v142,v124,v125\n"
"MF a[56:59],a[216:219],v[128:131],a[56:59]\n"
"EX v126,v126\n"
"MF a[56:59],a[248:251],v[132:135],a[56:59]\n"
"LD v1,s[44:47],s16 offen offset:3072 sc1 lds\n"
"MF a[60:63],a[220:223],v[128:131],a[60:63]\n"
"EX v127,v127\n"
"MF a[60:63],a[252:255],v[132:135],a[60:63]\n"
"v_mov_b32 v249, v248\n"
"v_add_u32 v248, s20, v1\n"
"MF v[164:167],v[176:179],v[128:131],v[164:167]\n"
"WT lgkmcnt(0)\n"
"CV v143,v126,v127\n"
"MF v[164:167],v[176:179],v[132:135],v[164:167]\n"
"s_min_u32 s59, s37, 27\n"
"s_cmp_ge_u32 s59, 14\n"
"s_cselect_b32 s57, 1, 0\n"
"s_cselect_b32 s58, 14, 0\n"
"s_sub_u32 s71, s59, s58\n"
"s_lshl_b32 s70, s72, 1\n"
"s_add_u32 s70, s70, s57\n"
"s_cmp_lt_u32 s37, 28\n"
"s_cselect_b32 s69, 0x21c000, 0\n"
"s_cselect_b32 s78, 0x5c00000, 0\n"
"v_min_u32 v21, 19, v0\n"
"v_add_u32 v21, 12, v21\n"
"v_lshlrev_b32 v21, 3, v21\n"
"v_add_u32 v21, s72, v21\n"
"v_lshlrev_b32 v21, 7, v21\n"
"v_add_u32 v20, 0x58000, v21\n"
"s_lshl_b32 s56, s71, 1\n"
"v_and_b32 v21, 1, v0\n"
"v_add_u32 v21, s56, v21\n"
"v_min_u32 v21, 26, v21\n"
"v_lshlrev_b32 v21, 7, v21\n"
"v_add_u32 v21, 0x41000, v21\n"
"v_cmp_gt_u32 vcc, 20, v0\n"
"s_nop 1\n"
"v_cndmask_b32 v20, v21, v20, vcc\n"
"v_lshrrev_b32 v21, 3, v0\n"
"v_and_b32 v22, 7, v0\n"
"v_and_b32 v23, 6, v21\n"
"v_xor_b32 v22, v22, v23\n"
"v_lshlrev_b32 v22, 4, v22\n"
"s_mov_b32 s56, 3456\n"
"v_mad_u32_u24 v6, v21, s56, v22\n"
"v_add_u32 v7, 26624, v6\n"
"v_add_u32 v8, 53248, v6\n"
"v_add_u32 v9, 79872, v6\n"
"s_mov_b32 s48, s24\n"
"s_and_b32 s49, s25, 0xffff\n"
"s_mov_b32 s50, s69\n"
"s_mov_b32 s51, 0x20000\n"
"s_lshr_b32 s56, s34, 1\n"
"s_and_b32 s57, s34, 1\n"
"s_mul_i32 s65, s56, 110592\n"
"s_lshl_b32 s58, s57, 7\n"
"s_add_u32 s65, s65, s58\n"
"s_lshl_b32 s58, s71, 8\n"
"s_add_u32 s65, s65, s58\n"
"s_lshl_b32 s66, s34, 12\n"
"s_lshl_b32 s67, s70, 2\n"
"s_add_u32 s67, s67, s34\n"
"s_mul_i32 s67, s67, 40960\n"
"s_add_u32 s67, s67, 0xa00000\n"
"AR v212,a0\n"
"AR v213,a1\n"
"AR v214,a2\n"
"AR v215,a3\n"
"AR v216,a4\n"
"AR v217,a5\n"
"AR v218,a6\n"
"AR v219,a7\n"
"AR v220,a8\n"
"AR v221,a9\n"
"AR v222,a10\n"
"s_add_u32 m0, s66, 131072\n"
"AR v223,a11\n"
"LD v6,s[48:51],s65 offen offset:0 lds\n"
"AR v224,a12\n"
"AR v225,a13\n"
"AR v226,a14\n"
"AR v227,a15\n"
"AR v228,a16\n"
"AR v229,a17\n"
"AR v230,a18\n"
"AR v231,a19\n"
"AR v232,a20\n"
"AR v233,a21\n"
"AR v234,a22\n"
"AR v235,a23\n"
"LD v7,s[48:51],s65 offen offset:1024 lds\n"
"AR v236,a24\n"
"AR v237,a25\n"
"AR v238,a26\n"
"AR v239,a27\n"
"AR v240,a28\n"
"AR v241,a29\n"
"AR v242,a30\n"
"AR v243,a31\n"
"v_rcp_f32 v244, v160\n"
"v_cmp_nlt_f32 vcc, v160, v250\n"
"s_nop 1\n"
"s_or_b64 s[10:11], s[10:11], vcc\n"
"v_pk_mul_f32 v[212:213], v[212:213], v[244:245] op_sel_hi:[1,0]\n"
"v_pk_mul_f32 v[214:215], v[214:215], v[244:245] op_sel_hi:[1,0]\n"
"LD v8,s[48:51],s65 offen offset:2048 lds\n"
"v_pk_mul_f32 v[216:217], v[216:217], v[244:245] op_sel_hi:[1,0]\n"
"v_pk_mul_f32 v[218:219], v[218:219], v[244:245] op_sel_hi:[1,0]\n"
"CV v212,v212,v213\n"
"CV v213,v214,v215\n"
"CV v214,v216,v217\n"
"CV v215,v218,v219\n"
"ST v[212:215],v1,s[40:43],s60 offen offset:0 sc1\n"
"v_pk_mul_f32 v[220:221], v[220:221], v[244:245] op_sel_hi:[1,0]\n"
"v_pk_mul_f32 v[222:223], v[222:223], v[244:245] op_sel_hi:[1,0]\n"
"v_pk_mul_f32 v[224:225], v[224:225], v[244:245] op_sel_hi:[1,0]\n"
"v_pk_mul_f32 v[226:227], v[226:227], v[244:245] op_sel_hi:[1,0]\n"
"CV v220,v220,v221\n"
"CV v221,v222,v223\n"
"LD v9,s[48:51],s65 offen offset:3072 lds\n"
"CV v222,v224,v225\n"
"CV v223,v226,v227\n"
"ST v[220:223],v1,s[40:43],s60 offen offset:1024 sc1\n"
"v_pk_mul_f32 v[228:229], v[228:229], v[244:245] op_sel_hi:[1,0]\n"
"v_pk_mul_f32 v[230:231], v[230:231], v[244:245] op_sel_hi:[1,0]\n"
"v_pk_mul_f32 v[232:233], v[232:233], v[244:245] op_sel_hi:[1,0]\n"
"v_pk_mul_f32 v[234:235], v[234:235], v[244:245] op_sel_hi:[1,0]\n"
"CV v228,v228,v229\n"
"CV v229,v230,v231\n"
"CV v230,v232,v233\n"
"CV v231,v234,v235\n"
"ST v[228:231],v1,s[40:43],s60 offen offset:2048 sc1\n"
"v_pk_mul_f32 v[236:237], v[236:237], v[244:245] op_sel_hi:[1,0]\n"
"v_pk_mul_f32 v[238:239], v[238:239], v[244:245] op_sel_hi:[1,0]\n"
"v_pk_mul_f32 v[240:241], v[240:241], v[244:245] op_sel_hi:[1,0]\n"
"v_pk_mul_f32 v[242:243], v[242:243], v[244:245] op_sel_hi:[1,0]\n"
"CV v236,v236,v237\n"
"CV v237,v238,v239\n"
"CV v238,v240,v241\n"
"CV v239,v242,v243\n"
"ST v[236:239],v1,s[40:43],s60 offen offset:3072 sc1\n"
"buffer_load_dwordx2 v[18:19], v20, s[40:43], 0 offen sc1\n"
"v_add_f32 v196, v180, v148\n"
"v_add_f32 v197, v181, v148\n"
"v_add_f32 v198, v182, v148\n"
"v_add_f32 v199, v183, v148\n"
"v_add_f32 v200, v184, v148\n"
"v_add_f32 v201, v185, v148\n"
"v_add_f32 v202, v186, v148\n"
"v_add_f32 v203, v187, v148\n"
"v_add_f32 v204, v188, v148\n"
"v_add_f32 v205, v189, v148\n"
"v_add_f32 v206, v190, v148\n"
"v_add_f32 v207, v191, v148\n"
"v_add_f32 v208, v192, v148\n"
"v_add_f32 v209, v193, v148\n"
"v_add_f32 v210, v194, v148\n"
"v_add_f32 v211, v195, v148\n"
"MF v[112:115],a[128:131],v[48:51],v[196:199]\n"
"EX v96,v96\n"
"MF v[112:115],a[132:135],v[52:55],v[112:115]\n"
"MF v[112:115],a[136:139],v[56:59],v[112:115]\n"
"EX v97,v97\n"
"MF v[112:115],a[140:143],v[60:63],v[112:115]\n"
"CV v128,v96,v97\n"
"MF v[116:119],a[144:147],v[48:51],v[200:203]\n"
"EX v98,v98\n"
"MF v[116:119],a[148:151],v[52:55],v[116:119]\n"
"MF v[116:119],a[152:155],v[56:59],v[116:119]\n"
"EX v99,v99\n"
"MF v[116:119],a[156:159],v[60:63],v[116:119]\n"
"ST v[252:255],v1,s[52:55],s21 offen offset:2048 sc1\n"
"MF v[120:123],a[160:163],v[48:51],v[204:207]\n"
"EX v100,v100\n"
"MF v[120:123],a[164:167],v[52:55],v[120:123]\n"
"CV v129,v98,v99\n"
"MF v[120:123],a[168:171],v[56:59],v[120:123]\n"
"EX v101,v101\n"
"MF v[120:123],a[172:175],v[60:63],v[120:123]\n"
"CV v130,v100,v101\n"
"MF v[124:127],a[176:179],v[48:51],v[208:211]\n"
"EX v102,v102\n"
"MF v[124:127],a[180:183],v[52:55],v[124:127]\n"
"MF v[124:127],a[184:187],v[56:59],v[124:127]\n"
"EX v103,v103\n"
"MF v[124:127],a[188:191],v[60:63],v[124:127]\n"
"CV v131,v102,v103\n"
"MF a[64:67],a[192:195],v[136:139],a[64:67]\n"
"EX v104,v104\n"
"MF a[64:67],a[224:227],v[140:143],a[64:67]\n"
"RD a[192:195],v249 offset:8192\n"
"RD a[224:227],v249 offset:24576\n"
"MF a[68:71],a[196:199],v[136:139],a[68:71]\n"
"EX v105,v105\n"
"MF a[68:71],a[228:231],v[140:143],a[68:71]\n"
"RD a[196:199],v249 offset:9216\n"
"CV v132,v104,v105\n"
"MF a[72:75],a[200:203],v[136:139],a[72:75]\n"
"EX v106,v106\n"
"MF a[72:75],a[232:235],v[140:143],a[72:75]\n"
"RD a[228:231],v249 offset:25600\n"
"RD a[200:203],v249 offset:10240\n"
"MF a[76:79],a[204:207],v[136:139],a[76:79]\n"
"EX v107,v107\n"
"MF a[76:79],a[236:239],v[140:143],a[76:79]\n"
"ST v[252:255],v1,s[52:55],s21 offen offset:3072 sc1\n"
"MF a[80:83],a[208:211],v[136:139],a[80:83]\n"
"EX v108,v108\n"
"MF a[80:83],a[240:243],v[140:143],a[80:83]\n"
"RD a[232:235],v249 offset:26624\n"
"CV v133,v106,v107\n"
"MF a[84:87],a[212:215],v[136:139],a[84:87]\n"
"EX v109,v109\n"
"MF a[84:87],a[244:247],v[140:143],a[84:87]\n"
"RD a[204:207],v249 offset:11264\n"
"CV v134,v108,v109\n"
"MF a[88:91],a[216:219],v[136:139],a[88:91]\n"
"EX v110,v110\n"
"MF a[88:91],a[248:251],v[140:143],a[88:91]\n"
"RD a[236:239],v249 offset:27648\n"
"RD a[208:211],v249 offset:12288\n"
"MF a[92:95],a[220:223],v[136:139],a[92:95]\n"
"EX v111,v111\n"
"MF a[92:95],a[252:255],v[140:143],a[92:95]\n"
"RD a[240:243],v249 offset:28672\n"
"CV v135,v110,v111\n"
"MF v[168:171],v[176:179],v[136:139],v[168:171]\n"
"WT vmcnt(12)\n"
"RD a[212:215],v249 offset:13312\n"
"RD a[244:247],v249 offset:29696\n"
"MF v[168:171],v[176:179],v[140:143],v[168:171]\n"
"s_barrier\n"
"RD a[216:219],v249 offset:14336\n"
"RD a[248:251],v249 offset:30720\n"
"MF v[96:99],a[128:131],v[64:67],v[152:155]\n"
"s_add_u32 s21, s21, 4096\n"
"EX v112,v112\n"
"MF v[96:99],a[132:135],v[68:71],v[96:99]\n"
"ST v[252:255],v1,s[52:55],s21 offen offset:0 sc1\n"
"MF v[96:99],a[136:139],v[72:75],v[96:99]\n"
"EX v113,v113\n"
"MF v[96:99],a[140:143],v[76:79],v[96:99]\n"
"RD a[220:223],v249 offset:15360\n"
"RD a[252:255],v249 offset:31744\n"
"MF v[100:103],a[144:147],v[64:67],v[152:155]\n"
"EX v114,v114\n"
"MF v[100:103],a[148:151],v[68:71],v[100:103]\n"
"RD a[128:131],v248 offset:0\n"
"RD a[132:135],v248 offset:1024\n"
"MF v[100:103],a[152:155],v[72:75],v[100:103]\n"
"EX v115,v115\n"
"MF v[100:103],a[156:159],v[76:79],v[100:103]\n"
"s_add_u32 s16, s16, 8192\n"
"RD a[136:139],v248 offset:2048\n"
"RD a[140:143],v248 offset:3072\n"
"MF v[104:107],a[160:163],v[64:67],v[152:155]\n"
"s_add_u32 m0, s17, 8192\n"
"EX v116,v116\n"
"MF v[104:107],a[164:167],v[68:71],v[104:107]\n"
"LD v1,s[44:47],s16 offen offset:0 sc1 lds\n"
"MF v[104:107],a[168:171],v[72:75],v[104:107]\n"
"EX v117,v117\n"
"MF v[104:107],a[172:175],v[76:79],v[104:107]\n"
"RD a[144:147],v248 offset:4096\n"
"RD a[148:151],v248 offset:5120\n"
"MF v[108:111],a[176:179],v[64:67],v[152:155]\n"
"EX v118,v118\n"
"MF v[108:111],a[180:183],v[68:71],v[108:111]\n"
"RD a[152:155],v248 offset:6144\n"
"CV v136,v112,v113\n"
"MF v[108:111],a[184:187],v[72:75],v[108:111]\n"
"EX v119,v119\n"
"MF v[108:111],a[188:191],v[76:79],v[108:111]\n"
"WT lgkmcnt(15)\n"
"RD a[156:159],v248 offset:7168\n"
"CV v137,v114,v115\n"
"MF a[96:99],a[192:195],v[128:131],a[96:99]\n"
"EX v120,v120\n"
"MF a[96:99],a[224:227],v[132:135],a[96:99]\n"
"LD v1,s[44:47],s16 offen offset:1024 sc1 lds\n"
"MF a[100:103],a[196:199],v[128:131],a[100:103]\n"
"EX v121,v121\n"
"MF a[100:103],a[228:231],v[132:135],a[100:103]\n"
"ST v[252:255],v1,s[52:55],s21 offen offset:1024 sc1\n"
"MF a[104:107],a[200:203],v[128:131],a[104:107]\n"
"EX v122,v122\n"
"MF a[104:107],a[232:235],v[132:135],a[104:107]\n"
"RD a[160:163],v248 offset:16384\n"
"RD a[164:167],v248 offset:17408\n"
"MF a[108:111],a[204:207],v[128:131],a[108:111]\n"
"EX v123,v123\n"
"MF a[108:111],a[236:239],v[132:135],a[108:111]\n"
"WT lgkmcnt(15)\n"
"RD a[168:171],v248 offset:18432\n"
"CV v138,v116,v117\n"
"MF a[112:115],a[208:211],v[128:131],a[112:115]\n"
"EX v124,v124\n"
"MF a[112:115],a[240:243],v[132:135],a[112:115]\n"
"WT lgkmcnt(15)\n"
"LD v1,s[44:47],s16 offen offset:2048 sc1 lds\n"
"MF a[116:119],a[212:215],v[128:131],a[116:119]\n"
"EX v125,v125\n"
"MF a[116:119],a[244:247],v[132:135],a[116:119]\n"
"WT lgkmcnt(13)\n"
"RD a[172:175],v248 offset:19456\n"
"CV v139,v118,v119\n"
"MF a[120:123],a[216:219],v[128:131],a[120:123]\n"
"EX v126,v126\n"
"MF a[120:123],a[248:251],v[132:135],a[120:123]\n"
"WT lgkmcnt(12)\n"
"RD a[176:179],v248 offset:20480\n"
"RD a[180:183],v248 offset:21504\n"
"MF a[124:127],a[220:223],v[128:131],a[124:127]\n"
"EX v127,v127\n"
"MF a[124:127],a[252:255],v[132:135],a[124:127]\n"
"RD a[184:187],v248 offset:22528\n"
"CV v140,v120,v121\n"
"MF v[172:175],v[176:179],v[128:131],v[172:175]\n"
"LD v1,s[44:47],s16 offen offset:3072 sc1 lds\n"
"MF v[172:175],v[176:179],v[132:135],v[172:175]\n"
"WT lgkmcnt(13)\n"
"RD a[188:191],v248 offset:23552\n"
"CV v141,v122,v123\n"
"MF v[112:115],a[128:131],v[80:83],v[156:159]\n"
"EX v96,v96\n"
"MF v[112:115],a[132:135],v[84:87],v[112:115]\n"
"WT lgkmcnt(12)\n"
"s_add_u32 s16, s16, 25856\n"
"CV v142,v124,v125\n"
"CV v143,v126,v127\n"
"MF v[112:115],a[136:139],v[88:91],v[112:115]\n"
"s_add_u32 s17, s17, 32768\n"
"EX v97,v97\n"
"MF v[112:115],a[140:143],v[92:95],v[112:115]\n"
"WT lgkmcnt(10)\n"
"s_and_b32 s17, s17, 0x1ffff\n"
"ST v[252:255],v1,s[52:55],s21 offen offset:2048 sc1\n"
"MF v[116:119],a[144:147],v[80:83],v[156:159]\n"
"s_mov_b32 m0, s17\n"
"EX v98,v98\n"
"MF v[116:119],a[148:151],v[84:87],v[116:119]\n"
"WT lgkmcnt(8)\n"
"LD v1,s[44:47],s16 offen offset:0 sc1 lds\n"
"MF v[116:119],a[152:155],v[88:91],v[116:119]\n"
"EX v99,v99\n"
"MF v[116:119],a[156:159],v[92:95],v[116:119]\n"
"WT lgkmcnt(6)\n"
"CV v128,v96,v97\n"
"CV v129,v98,v99\n"
"MF v[120:123],a[160:163],v[80:83],v[156:159]\n"
"EX v100,v100\n"
"MF v[120:123],a[164:167],v[84:87],v[120:123]\n"
"WT lgkmcnt(4)\n"
"MF v[120:123],a[168:171],v[88:91],v[120:123]\n"
"EX v101,v101\n"
"MF v[120:123],a[172:175],v[92:95],v[120:123]\n"
"WT lgkmcnt(2)\n"
"CV v130,v100,v101\n"
"MF v[124:127],a[176:179],v[80:83],v[156:159]\n"
"EX v102,v102\n"
"MF v[124:127],a[180:183],v[84:87],v[124:127]\n"
"WT lgkmcnt(0)\n"
"LD v1,s[44:47],s16 offen offset:1024 sc1 lds\n"
"MF v[124:127],a[184:187],v[88:91],v[124:127]\n"
"EX v103,v103\n"
"MF v[124:127],a[188:191],v[92:95],v[124:127]\n"
"CV v131,v102,v103\n"
"MF a[32:35],a[192:195],v[136:139],a[32:35]\n"
"EX v104,v104\n"
"MF a[32:35],a[224:227],v[140:143],a[32:35]\n"
"MF a[36:39],a[196:199],v[136:139],a[36:39]\n"
"EX v105,v105\n"
"MF a[36:39],a[228:231],v[140:143],a[36:39]\n"
"CV v132,v104,v105\n"
"MF a[40:43],a[200:203],v[136:139],a[40:43]\n"
"s_add_u32 s20, s20, 32768\n"
"EX v106,v106\n"
"MF a[40:43],a[232:235],v[140:143],a[40:43]\n"
"LD v1,s[44:47],s16 offen offset:2048 sc1 lds\n"
"MF a[44:47],a[204:207],v[136:139],a[44:47]\n"
"s_and_b32 s20, s20, 0x1ffff\n"
"EX v107,v107\n"
"MF a[44:47],a[236:239],v[140:143],a[44:47]\n"
"ST v[252:255],v1,s[52:55],s21 offen offset:3072 sc1\n"
"MF a[48:51],a[208:211],v[136:139],a[48:51]\n"
"EX v108,v108\n"
"MF a[48:51],a[240:243],v[140:143],a[48:51]\n"
"CV v133,v106,v107\n"
"MF a[52:55],a[212:215],v[136:139],a[52:55]\n"
"EX v109,v109\n"
"MF a[52:55],a[244:247],v[140:143],a[52:55]\n"
"CV v134,v108,v109\n"
"MF a[56:59],a[216:219],v[136:139],a[56:59]\n"
"EX v110,v110\n"
"MF a[56:59],a[248:251],v[140:143],a[56:59]\n"
"LD v1,s[44:47],s16 offen offset:3072 sc1 lds\n"
"MF a[60:63],a[220:223],v[136:139],a[60:63]\n"
"EX v111,v111\n"
"MF a[60:63],a[252:255],v[140:143],a[60:63]\n"
"v_mov_b32 v249, v248\n"
"v_add_u32 v248, s20, v1\n"
"MF v[164:167],v[176:179],v[136:139],v[164:167]\n"
"WT vmcnt(16) lgkmcnt(0)\n"
"CV v135,v110,v111\n"
"MF v[164:167],v[176:179],v[140:143],v[164:167]\n"
"s_barrier\n"
"AR v212,a32\n"
"AR v213,a33\n"
"AR v214,a34\n"
"AR v215,a35\n"
"AR v216,a36\n"
"AR v217,a37\n"
"AR v218,a38\n"
"AR v219,a39\n"
"AR v220,a40\n"
"AR v221,a41\n"
"AR v222,a42\n"
"s_add_u32 s65, s65, 221184\n"
"s_add_u32 m0, s66, 147456\n"
"AR v223,a43\n"
"LD v6,s[48:51],s65 offen offset:0 lds\n"
"AR v224,a44\n"
"AR v225,a45\n"
"AR v226,a46\n"
"AR v227,a47\n"
"AR v228,a48\n"
"AR v229,a49\n"
"AR v230,a50\n"
"AR v231,a51\n"
"AR v232,a52\n"
"AR v233,a53\n"
"AR v234,a54\n"
"AR v235,a55\n"
"LD v7,s[48:51],s65 offen offset:1024 lds\n"
"AR v236,a56\n"
"AR v237,a57\n"
"AR v238,a58\n"
"AR v239,a59\n"
"AR v240,a60\n"
"AR v241,a61\n"
"AR v242,a62\n"
"AR v243,a63\n"
"v_rcp_f32 v244, v164\n"
"v_cmp_nlt_f32 vcc, v164, v250\n"
"s_nop 1\n"
"s_or_b64 s[10:11], s[10:11], vcc\n"
"v_pk_mul_f32 v[212:213], v[212:213], v[244:245] op_sel_hi:[1,0]\n"
"v_pk_mul_f32 v[214:215], v[214:215], v[244:245] op_sel_hi:[1,0]\n"
"LD v8,s[48:51],s65 offen offset:2048 lds\n"
"v_pk_mul_f32 v[216:217], v[216:217], v[244:245] op_sel_hi:[1,0]\n"
"v_pk_mul_f32 v[218:219], v[218:219], v[244:245] op_sel_hi:[1,0]\n"
"CV v212,v212,v213\n"
"CV v213,v214,v215\n"
"CV v214,v216,v217\n"
"CV v215,v218,v219\n"
"ST v[212:215],v1,s[40:43],s61 offen offset:0 sc1\n"
"v_pk_mul_f32 v[220:221], v[220:221], v[244:245] op_sel_hi:[1,0]\n"
"v_pk_mul_f32 v[222:223], v[222:223], v[244:245] op_sel_hi:[1,0]\n"
"v_pk_mul_f32 v[224:225], v[224:225], v[244:245] op_sel_hi:[1,0]\n"
"v_pk_mul_f32 v[226:227], v[226:227], v[244:245] op_sel_hi:[1,0]\n"
"CV v220,v220,v221\n"
"CV v221,v222,v223\n"
"LD v9,s[48:51],s65 offen offset:3072 lds\n"
"CV v222,v224,v225\n"
"CV v223,v226,v227\n"
"ST v[220:223],v1,s[40:43],s61 offen offset:1024 sc1\n"
"v_pk_mul_f32 v[228:229], v[228:229], v[244:245] op_sel_hi:[1,0]\n"
"v_pk_mul_f32 v[230:231], v[230:231], v[244:245] op_sel_hi:[1,0]\n"
"v_pk_mul_f32 v[232:233], v[232:233], v[244:245] op_sel_hi:[1,0]\n"
"v_pk_mul_f32 v[234:235], v[234:235], v[244:245] op_sel_hi:[1,0]\n"
"CV v228,v228,v229\n"
"CV v229,v230,v231\n"
"CV v230,v232,v233\n"
"CV v231,v234,v235\n"
"ST v[228:231],v1,s[40:43],s61 offen offset:2048 sc1\n"
"v_pk_mul_f32 v[236:237], v[236:237], v[244:245] op_sel_hi:[1,0]\n"
"v_pk_mul_f32 v[238:239], v[238:239], v[244:245] op_sel_hi:[1,0]\n"
"v_pk_mul_f32 v[240:241], v[240:241], v[244:245] op_sel_hi:[1,0]\n"
"v_pk_mul_f32 v[242:243], v[242:243], v[244:245] op_sel_hi:[1,0]\n"
"CV v236,v236,v237\n"
"CV v237,v238,v239\n"
"CV v238,v240,v241\n"
"CV v239,v242,v243\n"
"ST v[236:239],v1,s[40:43],s61 offen offset:3072 sc1\n"
"s_lshl_b32 s14, s36, 1\n"
"s_sub_u32 s14, 14, s14\n"
"s_cmp_eq_u32 s14, 0\n"
"s_cbranch_scc1 L_skip_24\n"
"L_a2:\n"
"MF v[96:99],a[128:131],v[64:67],v[152:155]\n"
"EX v112,v112\n"
"MF v[96:99],a[132:135],v[68:71],v[96:99]\n"
"RD a[128:131],v248 offset:0\n"
"RD a[132:135],v248 offset:1024\n"
"MF v[96:99],a[136:139],v[72:75],v[96:99]\n"
"EX v113,v113\n"
"MF v[96:99],a[140:143],v[76:79],v[96:99]\n"
"RD a[136:139],v248 offset:2048\n"
"RD a[140:143],v248 offset:3072\n"
"MF v[100:103],a[144:147],v[64:67],v[152:155]\n"
"EX v114,v114\n"
"MF v[100:103],a[148:151],v[68:71],v[100:103]\n"
"RD a[144:147],v248 offset:4096\n"
"RD a[148:151],v248 offset:5120\n"
"MF v[100:103],a[152:155],v[72:75],v[100:103]\n"
"EX v115,v115\n"
"MF v[100:103],a[156:159],v[76:79],v[100:103]\n"
"s_add_u32 s16, s16, 8192\n"
"RD a[152:155],v248 offset:6144\n"
"CV v136,v112,v113\n"
"MF v[104:107],a[160:163],v[64:67],v[152:155]\n"
"s_add_u32 m0, s17, 8192\n"
"EX v116,v116\n"
"MF v[104:107],a[164:167],v[68:71],v[104:107]\n"
"LD v1,s[44:47],s16 offen offset:0 sc1 lds\n"
"MF v[104:107],a[168:171],v[72:75],v[104:107]\n"
"EX v117,v117\n"
"MF v[104:107],a[172:175],v[76:79],v[104:107]\n"
"RD a[156:159],v248 offset:7168\n"
"CV v137,v114,v115\n"
"MF v[108:111],a[176:179],v[64:67],v[152:155]\n"
"EX v118,v118\n"
"MF v[108:111],a[180:183],v[68:71],v[108:111]\n"
"RD a[160:163],v248 offset:16384\n"
"RD a[164:167],v248 offset:17408\n"
"MF v[108:111],a[184:187],v[72:75],v[108:111]\n"
"EX v119,v119\n"
"MF v[108:111],a[188:191],v[76:79],v[108:111]\n"
"RD a[168:171],v248 offset:18432\n"
"CV v138,v116,v117\n"
"MF a[64:67],a[192:195],v[128:131],a[64:67]\n"
"EX v120,v120\n"
"MF a[64:67],a[224:227],v[132:135],a[64:67]\n"
"LD v1,s[44:47],s16 offen offset:1024 sc1 lds\n"
"MF a[68:71],a[196:199],v[128:131],a[68:71]\n"
"EX v121,v121\n"
"MF a[68:71],a[228:231],v[132:135],a[68:71]\n"
"RD a[172:175],v248 offset:19456\n"
"CV v139,v118,v119\n"
"MF a[72:75],a[200:203],v[128:131],a[72:75]\n"
"EX v122,v122\n"
"MF a[72:75],a[232:235],v[132:135],a[72:75]\n"
"RD a[176:179],v248 offset:20480\n"
"RD a[180:183],v248 offset:21504\n"
"MF a[76:79],a[204:207],v[128:131],a[76:79]\n"
"EX v123,v123\n"
"MF a[76:79],a[236:239],v[132:135],a[76:79]\n"
"RD a[184:187],v248 offset:22528\n"
"CV v140,v120,v121\n"
"MF a[80:83],a[208:211],v[128:131],a[80:83]\n"
"EX v124,v124\n"
"MF a[80:83],a[240:243],v[132:135],a[80:83]\n"
"LD v1,s[44:47],s16 offen offset:2048 sc1 lds\n"
"MF a[84:87],a[212:215],v[128:131],a[84:87]\n"
"EX v125,v125\n"
"MF a[84:87],a[244:247],v[132:135],a[84:87]\n"
"RD a[188:191],v248 offset:23552\n"
"CV v141,v122,v123\n"
"MF a[88:91],a[216:219],v[128:131],a[88:91]\n"
"EX v126,v126\n"
"MF a[88:91],a[248:251],v[132:135],a[88:91]\n"
"RD a[192:195],v249 offset:8192\n"
"RD a[224:227],v249 offset:24576\n"
"MF a[92:95],a[220:223],v[128:131],a[92:95]\n"
"EX v127,v127\n"
"MF a[92:95],a[252:255],v[132:135],a[92:95]\n"
"RD a[196:199],v249 offset:9216\n"
"CV v142,v124,v125\n"
"MF v[168:171],v[176:179],v[128:131],v[168:171]\n"
"LD v1,s[44:47],s16 offen offset:3072 sc1 lds\n"
"MF v[168:171],v[176:179],v[132:135],v[168:171]\n"
"WT lgkmcnt(15)\n"
"RD a[228:231],v249 offset:25600\n"
"CV v143,v126,v127\n"
"MF v[112:115],a[128:131],v[80:83],v[156:159]\n"
"EX v96,v96\n"
"MF v[112:115],a[132:135],v[84:87],v[112:115]\n"
"s_add_u32 s16, s16, 25856\n"
"RD a[200:203],v249 offset:10240\n"
"RD a[232:235],v249 offset:26624\n"
"MF v[112:115],a[136:139],v[88:91],v[112:115]\n"
"s_add_u32 s17, s17, 32768\n"
"EX v97,v97\n"
"MF v[112:115],a[140:143],v[92:95],v[112:115]\n"
"WT lgkmcnt(15)\n"
"s_and_b32 s17, s17, 0x1ffff\n"
"RD a[204:207],v249 offset:11264\n"
"RD a[236:239],v249 offset:27648\n"
"MF v[116:119],a[144:147],v[80:83],v[156:159]\n"
"s_mov_b32 m0, s17\n"
"EX v98,v98\n"
"MF v[116:119],a[148:151],v[84:87],v[116:119]\n"
"WT lgkmcnt(15)\n"
"LD v1,s[44:47],s16 offen offset:0 sc1 lds\n"
"MF v[116:119],a[152:155],v[88:91],v[116:119]\n"
"EX v99,v99\n"
"MF v[116:119],a[156:159],v[92:95],v[116:119]\n"
"WT lgkmcnt(14)\n"
"RD a[208:211],v249 offset:12288\n"
"RD a[240:243],v249 offset:28672\n"
"MF v[120:123],a[160:163],v[80:83],v[156:159]\n"
"EX v100,v100\n"
"MF v[120:123],a[164:167],v[84:87],v[120:123]\n"
"WT lgkmcnt(14)\n"
"RD a[212:215],v249 offset:13312\n"
"RD a[244:247],v249 offset:29696\n"
"MF v[120:123],a[168:171],v[88:91],v[120:123]\n"
"EX v101,v101\n"
"MF v[120:123],a[172:175],v[92:95],v[120:123]\n"
"WT lgkmcnt(14)\n"
"RD a[216:219],v249 offset:14336\n"
"RD a[248:251],v249 offset:30720\n"
"MF v[124:127],a[176:179],v[80:83],v[156:159]\n"
"EX v102,v102\n"
"MF v[124:127],a[180:183],v[84:87],v[124:127]\n"
"WT lgkmcnt(14)\n"
"LD v1,s[44:47],s16 offen offset:1024 sc1 lds\n"
"MF v[124:127],a[184:187],v[88:91],v[124:127]\n"
"EX v103,v103\n"
"MF v[124:127],a[188:191],v[92:95],v[124:127]\n"
"WT lgkmcnt(12)\n"
"RD a[220:223],v249 offset:15360\n"
"RD a[252:255],v249 offset:31744\n"
"MF a[96:99],a[192:195],v[136:139],a[96:99]\n"
"EX v104,v104\n"
"MF a[96:99],a[224:227],v[140:143],a[96:99]\n"
"WT lgkmcnt(12)\n"
"CV v128,v96,v97\n"
"CV v129,v98,v99\n"
"MF a[100:103],a[196:199],v[136:139],a[100:103]\n"
"EX v105,v105\n"
"MF a[100:103],a[228:231],v[140:143],a[100:103]\n"
"WT lgkmcnt(10)\n"
"CV v130,v100,v101\n"
"CV v131,v102,v103\n"
"MF a[104:107],a[200:203],v[136:139],a[104:107]\n"
"s_add_u32 s20, s20, 32768\n"
"EX v106,v106\n"
"MF a[104:107],a[232:235],v[140:143],a[104:107]\n"
"WT lgkmcnt(8)\n"
"LD v1,s[44:47],s16 offen offset:2048 sc1 lds\n"
"MF a[108:111],a[204:207],v[136:139],a[108:111]\n"
"s_and_b32 s20, s20, 0x1ffff\n"
"EX v107,v107\n"
"MF a[108:111],a[236:239],v[140:143],a[108:111]\n"
"WT lgkmcnt(6)\n"
"CV v132,v104,v105\n"
"CV v133,v106,v107\n"
"MF a[112:115],a[208:211],v[136:139],a[112:115]\n"
"EX v108,v108\n"
"MF a[112:115],a[240:243],v[140:143],a[112:115]\n"
"WT lgkmcnt(4)\n"
"MF a[116:119],a[212:215],v[136:139],a[116:119]\n"
"EX v109,v109\n"
"MF a[116:119],a[244:247],v[140:143],a[116:119]\n"
"WT lgkmcnt(2)\n"
"CV v134,v108,v109\n"
"MF a[120:123],a[216:219],v[136:139],a[120:123]\n"
"EX v110,v110\n"
"MF a[120:123],a[248:251],v[140:143],a[120:123]\n"
"WT lgkmcnt(0)\n"
"LD v1,s[44:47],s16 offen offset:3072 sc1 lds\n"
"MF a[124:127],a[220:223],v[136:139],a[124:127]\n"
"EX v111,v111\n"
"MF a[124:127],a[252:255],v[140:143],a[124:127]\n"
"v_mov_b32 v249, v248\n"
"v_add_u32 v248, s20, v1\n"
"MF v[172:175],v[176:179],v[136:139],v[172:175]\n"
"WT vmcnt(16) lgkmcnt(0)\n"
"CV v135,v110,v111\n"
"MF v[172:175],v[176:179],v[140:143],v[172:175]\n"
"s_barrier\n"
"MF v[96:99],a[128:131],v[64:67],v[152:155]\n"
"EX v112,v112\n"
"MF v[96:99],a[132:135],v[68:71],v[96:99]\n"
"RD a[128:131],v248 offset:0\n"
"RD a[132:135],v248 offset:1024\n"
"MF v[96:99],a[136:139],v[72:75],v[96:99]\n"
"EX v113,v113\n"
"MF v[96:99],a[140:143],v[76:79],v[96:99]\n"
"RD a[136:139],v248 offset:2048\n"
"RD a[140:143],v248 offset:3072\n"
"MF v[100:103],a[144:147],v[64:67],v[152:155]\n"
"EX v114,v114\n"
"MF v[100:103],a[148:151],v[68:71],v[100:103]\n"
"RD a[144:147],v248 offset:4096\n"
"RD a[148:151],v248 offset:5120\n"
"MF v[100:103],a[152:155],v[72:75],v[100:103]\n"
"EX v115,v115\n"
"MF v[100:103],a[156:159],v[76:79],v[100:103]\n"
"s_add_u32 s16, s16, 8192\n"
"RD a[152:155],v248 offset:6144\n"
"CV v136,v112,v113\n"
"MF v[104:107],a[160:163],v[64:67],v[152:155]\n"
"s_add_u32 m0, s17, 8192\n"
"EX v116,v116\n"
"MF v[104:107],a[164:167],v[68:71],v[104:107]\n"
"LD v1,s[44:47],s16 offen offset:0 sc1 lds\n"
"MF v[104:107],a[168:171],v[72:75],v[104:107]\n"
"EX v117,v117\n"
"MF v[104:107],a[172:175],v[76:79],v[104:107]\n"
"RD a[156:159],v248 offset:7168\n"
"CV v137,v114,v115\n"
"MF v[108:111],a[176:179],v[64:67],v[152:155]\n"
"EX v118,v118\n"
"MF v[108:111],a[180:183],v[68:71],v[108:111]\n"
"RD a[160:163],v248 offset:16384\n"
"RD a[164:167],v248 offset:17408\n"
"MF v[108:111],a[184:187],v[72:75],v[108:111]\n"
"EX v119,v119\n"
"MF v[108:111],a[188:191],v[76:79],v[108:111]\n"
"RD a[168:171],v248 offset:18432\n"
"CV v138,v116,v117\n"
"MF a[64:67],a[192:195],v[128:131],a[64:67]\n"
"EX v120,v120\n"
"MF a[64:67],a[224:227],v[132:135],a[64:67]\n"
"LD v1,s[44:47],s16 offen offset:1024 sc1 lds\n"
"MF a[68:71],a[196:199],v[128:131],a[68:71]\n"
"EX v121,v121\n"
"MF a[68:71],a[228:231],v[132:135],a[68:71]\n"
"RD a[172:175],v248 offset:19456\n"
"CV v139,v118,v119\n"
"MF a[72:75],a[200:203],v[128:131],a[72:75]\n"
"EX v122,v122\n"
"MF a[72:75],a[232:235],v[132:135],a[72:75]\n"
"RD a[176:179],v248 offset:20480\n"
"RD a[180:183],v248 offset:21504\n"
"MF a[76:79],a[204:207],v[128:131],a[76:79]\n"
"EX v123,v123\n"
"MF a[76:79],a[236:239],v[132:135],a[76:79]\n"
"RD a[184:187],v248 offset:22528\n"
"CV v140,v120,v121\n"
"MF a[80:83],a[208:211],v[128:131],a[80:83]\n"
"EX v124,v124\n"
"MF a[80:83],a[240:243],v[132:135],a[80:83]\n"
"LD v1,s[44:47],s16 offen offset:2048 sc1 lds\n"
"MF a[84:87],a[212:215],v[128:131],a[84:87]\n"
"EX v125,v125\n"
"MF a[84:87],a[244:247],v[132:135],a[84:87]\n"
"RD a[188:191],v248 offset:23552\n"
"CV v141,v122,v123\n"
"MF a[88:91],a[216:219],v[128:131],a[88:91]\n"
"EX v126,v126\n"
"MF a[88:91],a[248:251],v[132:135],a[88:91]\n"
"RD a[192:195],v249 offset:8192\n"
"RD a[224:227],v249 offset:24576\n"
"MF a[92:95],a[220:223],v[128:131],a[92:95]\n"
"EX v127,v127\n"
"MF a[92:95],a[252:255],v[132:135],a[92:95]\n"
"RD a[196:199],v249 offset:9216\n"
"CV v142,v124,v125\n"
"MF v[168:171],v[176:179],v[128:131],v[168:171]\n"
"LD v1,s[44:47],s16 offen offset:3072 sc1 lds\n"
"MF v[168:171],v[176:179],v[132:135],v[168:171]\n"
"WT lgkmcnt(15)\n"
"RD a[228:231],v249 offset:25600\n"
"CV v143,v126,v127\n"
"MF v[112:115],a[128:131],v[80:83],v[156:159]\n"
"EX v96,v96\n"
"MF v[112:115],a[132:135],v[84:87],v[112:115]\n"
"s_add_u32 s16, s16, 25856\n"
"RD a[200:203],v249 offset:10240\n"
"RD a[232:235],v249 offset:26624\n"
"MF v[112:115],a[136:139],v[88:91],v[112:115]\n"
"s_add_u32 s17, s17, 32768\n"
"EX v97,v97\n"
"MF v[112:115],a[140:143],v[92:95],v[112:115]\n"
"WT lgkmcnt(15)\n"
"s_and_b32 s17, s17, 0x1ffff\n"
"RD a[204:207],v249 offset:11264\n"
"RD a[236:239],v249 offset:27648\n"
"MF v[116:119],a[144:147],v[80:83],v[156:159]\n"
"s_mov_b32 m0, s17\n"
"EX v98,v98\n"
"MF v[116:119],a[148:151],v[84:87],v[116:119]\n"
"WT lgkmcnt(15)\n"
"LD v1,s[44:47],s16 offen offset:0 sc1 lds\n"
"MF v[116:119],a[152:155],v[88:91],v[116:119]\n"
"EX v99,v99\n"
"MF v[116:119],a[156:159],v[92:95],v[116:119]\n"
"WT lgkmcnt(14)\n"
"RD a[208:211],v249 offset:12288\n"
"RD a[240:243],v249 offset:28672\n"
"MF v[120:123],a[160:163],v[80:83],v[156:159]\n"
"EX v100,v100\n"
"MF v[120:123],a[164:167],v[84:87],v[120:123]\n"
"WT lgkmcnt(14)\n"
"RD a[212:215],v249 offset:13312\n"
"RD a[244:247],v249 offset:29696\n"
"MF v[120:123],a[168:171],v[88:91],v[120:123]\n"
"EX v101,v101\n"
"MF v[120:123],a[172:175],v[92:95],v[120:123]\n"
"WT lgkmcnt(14)\n"
"RD a[216:219],v249 offset:14336\n"
"RD a[248:251],v249 offset:30720\n"
"MF v[124:127],a[176:179],v[80:83],v[156:159]\n"
"EX v102,v102\n"
"MF v[124:127],a[180:183],v[84:87],v[124:127]\n"
"WT lgkmcnt(14)\n"
"LD v1,s[44:47],s16 offen offset:1024 sc1 lds\n"
"MF v[124:127],a[184:187],v[88:91],v[124:127]\n"
"EX v103,v103\n"
"MF v[124:127],a[188:191],v[92:95],v[124:127]\n"
"WT lgkmcnt(12)\n"
"RD a[220:223],v249 offset:15360\n"
"RD a[252:255],v249 offset:31744\n"
"MF a[96:99],a[192:195],v[136:139],a[96:99]\n"
"EX v104,v104\n"
"MF a[96:99],a[224:227],v[140:143],a[96:99]\n"
"WT lgkmcnt(12)\n"
"CV v128,v96,v97\n"
"CV v129,v98,v99\n"
"MF a[100:103],a[196:199],v[136:139],a[100:103]\n"
"EX v105,v105\n"
"MF a[100:103],a[228:231],v[140:143],a[100:103]\n"
"WT lgkmcnt(10)\n"
"CV v130,v100,v101\n"
"CV v131,v102,v103\n"
"MF a[104:107],a[200:203],v[136:139],a[104:107]\n"
"s_add_u32 s20, s20, 32768\n"
"EX v106,v106\n"
"MF a[104:107],a[232:235],v[140:143],a[104:107]\n"
"WT lgkmcnt(8)\n"
"LD v1,s[44:47],s16 offen offset:2048 sc1 lds\n"
"MF a[108:111],a[204:207],v[136:139],a[108:111]\n"
"s_and_b32 s20, s20, 0x1ffff\n"
"EX v107,v107\n"
"MF a[108:111],a[236:239],v[140:143],a[108:111]\n"
"WT lgkmcnt(6)\n"
"CV v132,v104,v105\n"
"CV v133,v106,v107\n"
"MF a[112:115],a[208:211],v[136:139],a[112:115]\n"
"EX v108,v108\n"
"MF a[112:115],a[240:243],v[140:143],a[112:115]\n"
"WT lgkmcnt(4)\n"
"s_cmp_eq_u32 s14, 2\n"
"s_cbranch_scc1 L_hook_25\n"
"L_back_26:\n"
"MF a[116:119],a[212:215],v[136:139],a[116:119]\n"
"EX v109,v109\n"
"MF a[116:119],a[244:247],v[140:143],a[116:119]\n"
"WT lgkmcnt(2)\n"
"CV v134,v108,v109\n"
"MF a[120:123],a[216:219],v[136:139],a[120:123]\n"
"s_sub_u32 s14, s14, 1\n"
"EX v110,v110\n"
"MF a[120:123],a[248:251],v[140:143],a[120:123]\n"
"WT lgkmcnt(0)\n"
"LD v1,s[44:47],s16 offen offset:3072 sc1 lds\n"
"MF a[124:127],a[220:223],v[136:139],a[124:127]\n"
"s_cmp_lg_u32 s14, 0\n"
"EX v111,v111\n"
"MF a[124:127],a[252:255],v[140:143],a[124:127]\n"
"v_mov_b32 v249, v248\n"
"v_add_u32 v248, s20, v1\n"
"MF v[172:175],v[176:179],v[136:139],v[172:175]\n"
"WT vmcnt(16) lgkmcnt(0)\n"
"CV v135,v110,v111\n"
"MF v[172:175],v[176:179],v[140:143],v[172:175]\n"
"s_barrier\n"
"s_cbranch_scc1 L_a2\n"
"L_skip_24:\n"
"v_add_f32 v196, v180, v152\n"
"v_add_f32 v197, v181, v152\n"
"v_add_f32 v198, v182, v152\n"
"v_add_f32 v199, v183, v152\n"
"v_add_f32 v200, v184, v152\n"
"v_add_f32 v201, v185, v152\n"
"v_add_f32 v202, v186, v152\n"
"v_add_f32 v203, v187, v152\n"
"v_add_f32 v204, v188, v152\n"
"v_add_f32 v205, v189, v152\n"
"v_add_f32 v206, v190, v152\n"
"v_add_f32 v207, v191, v152\n"
"v_add_f32 v208, v192, v152\n"
"v_add_f32 v209, v193, v152\n"
"v_add_f32 v210, v194, v152\n"
"v_add_f32 v211, v195, v152\n"
"WT vmcnt(8)\n"
"v_cmp_ne_u64 vcc, s[38:39], v[18:19]\n"
"s_nop 1\n"
"s_cmp_eq_u64 vcc, 0\n"
"s_cbranch_scc0 L_psp_28\n"
"L_pok_27:\n"
"MF v[96:99],a[128:131],v[64:67],v[196:199]\n"
"EX v112,v112\n"
"MF v[96:99],a[132:135],v[68:71],v[96:99]\n"
"RD a[128:131],v248 offset:0\n"
"RD a[132:135],v248 offset:1024\n"
"MF v[96:99],a[136:139],v[72:75],v[96:99]\n"
"EX v113,v113\n"
"MF v[96:99],a[140:143],v[76:79],v[96:99]\n"
"RD a[136:139],v248 offset:2048\n"
"RD a[140:143],v248 offset:3072\n"
"MF v[100:103],a[144:147],v[64:67],v[200:203]\n"
"EX v114,v114\n"
"MF v[100:103],a[148:151],v[68:71],v[100:103]\n"
"RD a[144:147],v248 offset:4096\n"
"RD a[148:151],v248 offset:5120\n"
"MF v[100:103],a[152:155],v[72:75],v[100:103]\n"
"s_add_u32 s68, s67, 0\n"
"EX v115,v115\n"
"MF v[100:103],a[156:159],v[76:79],v[100:103]\n"
"LD a[0:3],v1,s[76:79],s68 offen offset:0 sc1\n"
"MF v[104:107],a[160:163],v[64:67],v[204:207]\n"
"EX v116,v116\n"
"MF v[104:107],a[164:167],v[68:71],v[104:107]\n"
"RD a[152:155],v248 offset:6144\n"
"CV v136,v112,v113\n"
"MF v[104:107],a[168:171],v[72:75],v[104:107]\n"
"EX v117,v117\n"
"MF v[104:107],a[172:175],v[76:79],v[104:107]\n"
"RD a[156:159],v248 offset:7168\n"
"CV v137,v114,v115\n"
"MF v[108:111],a[176:179],v[64:67],v[208:211]\n"
"s_add_u32 s73, s67, 20480\n"
"EX v118,v118\n"
"MF v[108:111],a[180:183],v[68:71],v[108:111]\n"
"LD a[4:7],v1,s[76:79],s73 offen offset:0 sc1\n"
"MF v[108:111],a[184:187],v[72:75],v[108:111]\n"
"EX v119,v119\n"
"MF v[108:111],a[188:191],v[76:79],v[108:111]\n"
"RD a[160:163],v248 offset:16384\n"
"RD a[164:167],v248 offset:17408\n"
"MF a[64:67],a[192:195],v[128:131],a[64:67]\n"
"EX v120,v120\n"
"MF a[64:67],a[224:227],v[132:135],a[64:67]\n"
"v_add_f32 v196, v180, v156\n"
"v_add_f32 v197, v181, v156\n"
"MF a[68:71],a[196:199],v[128:131],a[68:71]\n"
"EX v121,v121\n"
"MF a[68:71],a[228:231],v[132:135],a[68:71]\n"
"v_add_f32 v198, v182, v156\n"
"v_add_f32 v199, v183, v156\n"
"MF a[72:75],a[200:203],v[128:131],a[72:75]\n"
"EX v122,v122\n"
"MF a[72:75],a[232:235],v[132:135],a[72:75]\n"
"LD a[8:11],v1,s[76:79],s68 offen offset:1024 sc1\n"
"MF a[76:79],a[204:207],v[128:131],a[76:79]\n"
"EX v123,v123\n"
"MF a[76:79],a[236:239],v[132:135],a[76:79]\n"
"v_add_f32 v200, v184, v156\n"
"v_add_f32 v201, v185, v156\n"
"MF a[80:83],a[208:211],v[128:131],a[80:83]\n"
"EX v124,v124\n"
"MF a[80:83],a[240:243],v[132:135],a[80:83]\n"
"v_add_f32 v202, v186, v156\n"
"v_add_f32 v203, v187, v156\n"
"MF a[84:87],a[212:215],v[128:131],a[84:87]\n"
"EX v125,v125\n"
"MF a[84:87],a[244:247],v[132:135],a[84:87]\n"
"LD a[12:15],v1,s[76:79],s73 offen offset:1024 sc1\n"
"MF a[88:91],a[216:219],v[128:131],a[88:91]\n"
"EX v126,v126\n"
"MF a[88:91],a[248:251],v[132:135],a[88:91]\n"
"v_add_f32 v204, v188, v156\n"
"v_add_f32 v205, v189, v156\n"
"MF a[92:95],a[220:223],v[128:131],a[92:95]\n"
"EX v127,v127\n"
"MF a[92:95],a[252:255],v[132:135],a[92:95]\n"
"v_add_f32 v206, v190, v156\n"
"v_add_f32 v207, v191, v156\n"
"v_add_f32 v208, v192, v156\n"
"v_add_f32 v209, v193, v156\n"
"v_add_f32 v210, v194, v156\n"
"v_add_f32 v211, v195, v156\n"
"MF v[168:171],v[176:179],v[128:131],v[168:171]\n"
"WT vmcnt(4)\n"
"LD a[16:19],v1,s[76:79],s68 offen offset:2048 sc1\n"
"MF v[168:171],v[176:179],v[132:135],v[168:171]\n"
"WT lgkmcnt(8)\n"
"s_barrier\n"
"RD a[168:171],v248 offset:18432\n"
"CV v138,v116,v117\n"
"MF v[112:115],a[128:131],v[80:83],v[196:199]\n"
"EX v96,v96\n"
"MF v[112:115],a[132:135],v[84:87],v[112:115]\n"
"WT lgkmcnt(7)\n"
"RD a[172:175],v248 offset:19456\n"
"CV v139,v118,v119\n"
"MF v[112:115],a[136:139],v[88:91],v[112:115]\n"
"EX v97,v97\n"
"MF v[112:115],a[140:143],v[92:95],v[112:115]\n"
"WT lgkmcnt(6)\n"
"LD a[20:23],v1,s[76:79],s73 offen offset:2048 sc1\n"
"MF v[116:119],a[144:147],v[80:83],v[200:203]\n"
"RD a[176:179],v248 offset:20480\n"
"EX v98,v98\n"
"MF v[116:119],a[148:151],v[84:87],v[116:119]\n"
"WT lgkmcnt(5)\n"
"RD a[180:183],v248 offset:21504\n"
"RD a[184:187],v248 offset:22528\n"
"MF v[116:119],a[152:155],v[88:91],v[116:119]\n"
"EX v99,v99\n"
"MF v[116:119],a[156:159],v[92:95],v[116:119]\n"
"WT lgkmcnt(5)\n"
"RD a[188:191],v248 offset:23552\n"
"CV v140,v120,v121\n"
"CV v141,v122,v123\n"
"MF v[120:123],a[160:163],v[80:83],v[204:207]\n"
"RD a[192:195],v249 offset:8192\n"
"EX v100,v100\n"
"MF v[120:123],a[164:167],v[84:87],v[120:123]\n"
"WT lgkmcnt(5)\n"
"RD a[224:227],v249 offset:24576\n"
"LD a[24:27],v1,s[76:79],s68 offen offset:3072 sc1\n"
"MF v[120:123],a[168:171],v[88:91],v[120:123]\n"
"RD a[196:199],v249 offset:9216\n"
"EX v101,v101\n"
"MF v[120:123],a[172:175],v[92:95],v[120:123]\n"
"WT lgkmcnt(5)\n"
"RD a[228:231],v249 offset:25600\n"
"CV v142,v124,v125\n"
"CV v143,v126,v127\n"
"MF v[124:127],a[176:179],v[80:83],v[208:211]\n"
"RD a[200:203],v249 offset:10240\n"
"EX v102,v102\n"
"MF v[124:127],a[180:183],v[84:87],v[124:127]\n"
"WT lgkmcnt(5)\n"
"RD a[232:235],v249 offset:26624\n"
"RD a[204:207],v249 offset:11264\n"
"MF v[124:127],a[184:187],v[88:91],v[124:127]\n"
"EX v103,v103\n"
"MF v[124:127],a[188:191],v[92:95],v[124:127]\n"
"WT lgkmcnt(5)\n"
"RD a[236:239],v249 offset:27648\n"
"RD a[208:211],v249 offset:12288\n"
"MF a[96:99],a[192:195],v[136:139],a[96:99]\n"
"EX v104,v104\n"
"MF a[96:99],a[224:227],v[140:143],a[96:99]\n"
"WT lgkmcnt(5)\n"
"RD a[240:243],v249 offset:28672\n"
"LD a[28:31],v1,s[76:79],s73 offen offset:3072 sc1\n"
"MF a[100:103],a[196:199],v[136:139],a[100:103]\n"
"RD a[212:215],v249 offset:13312\n"
"EX v105,v105\n"
"MF a[100:103],a[228:231],v[140:143],a[100:103]\n"
"WT lgkmcnt(5)\n"
"RD a[244:247],v249 offset:29696\n"
"RD a[216:219],v249 offset:14336\n"
"MF a[104:107],a[200:203],v[136:139],a[104:107]\n"
"s_add_u32 s20, s20, 32768\n"
"EX v106,v106\n"
"MF a[104:107],a[232:235],v[140:143],a[104:107]\n"
"WT lgkmcnt(5)\n"
"RD a[248:251],v249 offset:30720\n"
"RD a[220:223],v249 offset:15360\n"
"MF a[108:111],a[204:207],v[136:139],a[108:111]\n"
"s_and_b32 s20, s20, 0x1ffff\n"
"s_add_u32 s68, s67, 4096\n"
"EX v107,v107\n"
"MF a[108:111],a[236:239],v[140:143],a[108:111]\n"
"WT lgkmcnt(5)\n"
"RD a[252:255],v249 offset:31744\n"
"LD a[32:35],v1,s[76:79],s68 offen offset:0 sc1\n"
"MF a[112:115],a[208:211],v[136:139],a[112:115]\n"
"EX v108,v108\n"
"MF a[112:115],a[240:243],v[140:143],a[112:115]\n"
"WT lgkmcnt(4)\n"
"CV v128,v96,v97\n"
"CV v129,v98,v99\n"
"MF a[116:119],a[212:215],v[136:139],a[116:119]\n"
"EX v109,v109\n"
"MF a[116:119],a[244:247],v[140:143],a[116:119]\n"
"WT lgkmcnt(2)\n"
"CV v130,v100,v101\n"
"CV v131,v102,v103\n"
"MF a[120:123],a[216:219],v[136:139],a[120:123]\n"
"s_add_u32 s73, s67, 24576\n"
"EX v110,v110\n"
"MF a[120:123],a[248:251],v[140:143],a[120:123]\n"
"WT lgkmcnt(0)\n"
"LD a[36:39],v1,s[76:79],s73 offen offset:0 sc1\n"
"MF a[124:127],a[220:223],v[136:139],a[124:127]\n"
"EX v111,v111\n"
"MF a[124:127],a[252:255],v[140:143],a[124:127]\n"
"v_mov_b32 v249, v248\n"
"v_add_u32 v248, s20, v1\n"
"MF v[172:175],v[176:179],v[136:139],v[172:175]\n"
"WT lgkmcnt(0)\n"
"CV v132,v104,v105\n"
"CV v133,v106,v107\n"
"MF v[172:175],v[176:179],v[140:143],v[172:175]\n"
"CV v134,v108,v109\n"
"CV v135,v110,v111\n"
"s_getpc_b64 s[56:57]\n"
"L_pc_29:\n"
"s_and_b32 s57, s57, 0xffff\n"
"s_mov_b32 s58, L_codeend-L_pc_29\n"
"s_mov_b32 s59, 0x20000\n"
"v_and_b32 v23, 7, v0\n"
"v_lshlrev_b32 v23, 7, v23\n"
"s_sub_u32 s64, s37, 0\n"
"s_lshl_b32 s64, s64, 2\n"
"s_add_u32 s64, s64, s34\n"
"s_lshl_b32 s64, s64, 10\n"
"buffer_load_dword v251, v23, s[56:59], s64 offen\n"
"MF a[64:67],a[192:195],v[128:131],a[64:67]\n"
"EX v112,v112\n"
"MF a[64:67],a[224:227],v[132:135],a[64:67]\n"
"RD a[192:195],v249 offset:8192\n"
"EX v113,v113\n"
"MF a[68:71],a[196:199],v[128:131],a[68:71]\n"
"RD a[224:227],v249 offset:24576\n"
"EX v114,v114\n"
"MF a[68:71],a[228:231],v[132:135],a[68:71]\n"
"RD a[196:199],v249 offset:9216\n"
"EX v115,v115\n"
"MF a[72:75],a[200:203],v[128:131],a[72:75]\n"
"RD a[228:231],v249 offset:25600\n"
"EX v116,v116\n"
"MF a[72:75],a[232:235],v[132:135],a[72:75]\n"
"RD a[200:203],v249 offset:10240\n"
"EX v117,v117\n"
"MF a[76:79],a[204:207],v[128:131],a[76:79]\n"
"RD a[232:235],v249 offset:26624\n"
"EX v118,v118\n"
"MF a[76:79],a[236:239],v[132:135],a[76:79]\n"
"RD a[204:207],v249 offset:11264\n"
"EX v119,v119\n"
"MF a[80:83],a[208:211],v[128:131],a[80:83]\n"
"RD a[236:239],v249 offset:27648\n"
"EX v120,v120\n"
"MF a[80:83],a[240:243],v[132:135],a[80:83]\n"
"RD a[208:211],v249 offset:12288\n"
"EX v121,v121\n"
"MF a[84:87],a[212:215],v[128:131],a[84:87]\n"
"RD a[240:243],v249 offset:28672\n"
"EX v122,v122\n"
"MF a[84:87],a[244:247],v[132:135],a[84:87]\n"
"RD a[212:215],v249 offset:13312\n"
"EX v123,v123\n"
"MF a[88:91],a[216:219],v[128:131],a[88:91]\n"
"RD a[244:247],v249 offset:29696\n"
"EX v124,v124\n"
"MF a[88:91],a[248:251],v[132:135],a[88:91]\n"
"RD a[216:219],v249 offset:14336\n"
"EX v125,v125\n"
"MF a[92:95],a[220:223],v[128:131],a[92:95]\n"
"RD a[248:251],v249 offset:30720\n"
"EX v126,v126\n"
"MF a[92:95],a[252:255],v[132:135],a[92:95]\n"
"RD a[220:223],v249 offset:15360\n"
"EX v127,v127\n"
"MF v[168:171],v[176:179],v[128:131],v[168:171]\n"
"RD a[252:255],v249 offset:31744\n"
"MF v[168:171],v[176:179],v[132:135],v[168:171]\n"
"CV v136,v112,v113\n"
"CV v137,v114,v115\n"
"CV v138,v116,v117\n"
"CV v139,v118,v119\n"
"CV v140,v120,v121\n"
"CV v141,v122,v123\n"
"CV v142,v124,v125\n"
"CV v143,v126,v127\n"
"AR v212,a64\n"
"AR v213,a65\n"
"AR v214,a66\n"
"AR v215,a67\n"
"AR v216,a68\n"
"AR v217,a69\n"
"AR v218,a70\n"
"AR v219,a71\n"
"AR v220,a72\n"
"AR v221,a73\n"
"LD a[40:43],v1,s[76:79],s68 offen offset:1024 sc1\n"
"AR v222,a74\n"
"AR v223,a75\n"
"AR v224,a76\n"
"AR v225,a77\n"
"AR v226,a78\n"
"AR v227,a79\n"
"AR v228,a80\n"
"AR v229,a81\n"
"AR v230,a82\n"
"AR v231,a83\n"
"LD a[44:47],v1,s[76:79],s73 offen offset:1024 sc1\n"
"AR v232,a84\n"
"AR v233,a85\n"
"AR v234,a86\n"
"AR v235,a87\n"
"AR v236,a88\n"
"AR v237,a89\n"
"AR v238,a90\n"
"AR v239,a91\n"
"AR v240,a92\n"
"AR v241,a93\n"
"LD a[48:51],v1,s[76:79],s68 offen offset:2048 sc1\n"
"AR v242,a94\n"
"AR v243,a95\n"
"v_rcp_f32 v244, v168\n"
"v_cmp_nlt_f32 vcc, v168, v250\n"
"s_nop 1\n"
"s_or_b64 s[10:11], s[10:11], vcc\n"
"v_pk_mul_f32 v[212:213], v[212:213], v[244:245] op_sel_hi:[1,0]\n"
"v_pk_mul_f32 v[214:215], v[214:215], v[244:245] op_sel_hi:[1,0]\n"
"v_pk_mul_f32 v[216:217], v[216:217], v[244:245] op_sel_hi:[1,0]\n"
"v_pk_mul_f32 v[218:219], v[218:219], v[244:245] op_sel_hi:[1,0]\n"
"CV v212,v212,v213\n"
"CV v213,v214,v215\n"
"LD a[52:55],v1,s[76:79],s73 offen offset:2048 sc1\n"
"CV v214,v216,v217\n"
"CV v215,v218,v219\n"
"ST v[212:215],v1,s[40:43],s62 offen offset:0 sc1\n"
"v_pk_mul_f32 v[220:221], v[220:221], v[244:245] op_sel_hi:[1,0]\n"
"v_pk_mul_f32 v[222:223], v[222:223], v[244:245] op_sel_hi:[1,0]\n"
"v_pk_mul_f32 v[224:225], v[224:225], v[244:245] op_sel_hi:[1,0]\n"
"v_pk_mul_f32 v[226:227], v[226:227], v[244:245] op_sel_hi:[1,0]\n"
"CV v220,v220,v221\n"
"CV v221,v222,v223\n"
"CV v222,v224,v225\n"
"CV v223,v226,v227\n"
"LD a[56:59],v1,s[76:79],s68 offen offset:3072 sc1\n"
"ST v[220:223],v1,s[40:43],s62 offen offset:1024 sc1\n"
"v_pk_mul_f32 v[228:229], v[228:229], v[244:245] op_sel_hi:[1,0]\n"
"v_pk_mul_f32 v[230:231], v[230:231], v[244:245] op_sel_hi:[1,0]\n"
"v_pk_mul_f32 v[232:233], v[232:233], v[244:245] op_sel_hi:[1,0]\n"
"v_pk_mul_f32 v[234:235], v[234:235], v[244:245] op_sel_hi:[1,0]\n"
"CV v228,v228,v229\n"
"CV v229,v230,v231\n"
"CV v230,v232,v233\n"
"CV v231,v234,v235\n"
"ST v[228:231],v1,s[40:43],s62 offen offset:2048 sc1\n"
"v_pk_mul_f32 v[236:237], v[236:237], v[244:245] op_sel_hi:[1,0]\n"
"v_pk_mul_f32 v[238:239], v[238:239], v[244:245] op_sel_hi:[1,0]\n"
"LD a[60:63],v1,s[76:79],s73 offen offset:3072 sc1\n"
"v_pk_mul_f32 v[240:241], v[240:241], v[244:245] op_sel_hi:[1,0]\n"
"v_pk_mul_f32 v[242:243], v[242:243], v[244:245] op_sel_hi:[1,0]\n"
"CV v236,v236,v237\n"
"CV v237,v238,v239\n"
"CV v238,v240,v241\n"
"CV v239,v242,v243\n"
"ST v[236:239],v1,s[40:43],s62 offen offset:3072 sc1\n"
"WT lgkmcnt(0)\n"
"s_barrier\n"
"MF a[96:99],a[192:195],v[136:139],a[96:99]\n"
"s_add_u32 s65, s65, 221184\n"
"s_add_u32 m0, s66, 98304\n"
"MF a[96:99],a[224:227],v[140:143],a[96:99]\n"
"LD v6,s[48:51],s65 offen offset:0 lds\n"
"MF a[100:103],a[196:199],v[136:139],a[100:103]\n"
"MF a[100:103],a[228:231],v[140:143],a[100:103]\n"
"MF a[104:107],a[200:203],v[136:139],a[104:107]\n"
"MF a[104:107],a[232:235],v[140:143],a[104:107]\n"
"LD v7,s[48:51],s65 offen offset:1024 lds\n"
"MF a[108:111],a[204:207],v[136:139],a[108:111]\n"
"MF a[108:111],a[236:239],v[140:143],a[108:111]\n"
"MF a[112:115],a[208:211],v[136:139],a[112:115]\n"
"MF a[112:115],a[240:243],v[140:143],a[112:115]\n"
"LD v8,s[48:51],s65 offen offset:2048 lds\n"
"MF a[116:119],a[212:215],v[136:139],a[116:119]\n"
"MF a[116:119],a[244:247],v[140:143],a[116:119]\n"
"MF a[120:123],a[216:219],v[136:139],a[120:123]\n"
"MF a[120:123],a[248:251],v[140:143],a[120:123]\n"
"LD v9,s[48:51],s65 offen offset:3072 lds\n"
"MF a[124:127],a[220:223],v[136:139],a[124:127]\n"
"MF a[124:127],a[252:255],v[140:143],a[124:127]\n"
"MF v[172:175],v[176:179],v[136:139],v[172:175]\n"
"MF v[172:175],v[176:179],v[140:143],v[172:175]\n"
"AR v212,a96\n"
"AR v213,a97\n"
"AR v214,a98\n"
"AR v215,a99\n"
"AR v216,a100\n"
"AR v217,a101\n"
"AR v218,a102\n"
"s_add_u32 s65, s65, 221184\n"
"s_add_u32 m0, s66, 114688\n"
"AR v219,a103\n"
"LD v6,s[48:51],s65 offen offset:0 lds\n"
"AR v220,a104\n"
"AR v221,a105\n"
"AR v222,a106\n"
"AR v223,a107\n"
"AR v224,a108\n"
"AR v225,a109\n"
"AR v226,a110\n"
"AR v227,a111\n"
"LD v7,s[48:51],s65 offen offset:1024 lds\n"
"AR v228,a112\n"
"AR v229,a113\n"
"AR v230,a114\n"
"AR v231,a115\n"
"AR v232,a116\n"
"AR v233,a117\n"
"AR v234,a118\n"
"AR v235,a119\n"
"LD v8,s[48:51],s65 offen offset:2048 lds\n"
"AR v236,a120\n"
"AR v237,a121\n"
"AR v238,a122\n"
"AR v239,a123\n"
"AR v240,a124\n"
"AR v241,a125\n"
"AR v242,a126\n"
"AR v243,a127\n"
"LD v9,s[48:51],s65 offen offset:3072 lds\n"
"v_rcp_f32 v244, v172\n"
"v_cmp_nlt_f32 vcc, v172, v250\n"
"s_nop 1\n"
"s_or_b64 s[10:11], s[10:11], vcc\n"
"v_pk_mul_f32 v[212:213], v[212:213], v[244:245] op_sel_hi:[1,0]\n"
"v_pk_mul_f32 v[214:215], v[214:215], v[244:245] op_sel_hi:[1,0]\n"
"v_pk_mul_f32 v[216:217], v[216:217], v[244:245] op_sel_hi:[1,0]\n"
"v_pk_mul_f32 v[218:219], v[218:219], v[244:245] op_sel_hi:[1,0]\n"
"CV v212,v212,v213\n"
"CV v213,v214,v215\n"
"CV v214,v216,v217\n"
"CV v215,v218,v219\n"
"ST v[212:215],v1,s[40:43],s63 offen offset:0 sc1\n"
"v_pk_mul_f32 v[220:221], v[220:221], v[244:245] op_sel_hi:[1,0]\n"
"v_pk_mul_f32 v[222:223], v[222:223], v[244:245] op_sel_hi:[1,0]\n"
"v_pk_mul_f32 v[224:225], v[224:225], v[244:245] op_sel_hi:[1,0]\n"
"v_pk_mul_f32 v[226:227], v[226:227], v[244:245] op_sel_hi:[1,0]\n"
"CV v220,v220,v221\n"
"CV v221,v222,v223\n"
"CV v222,v224,v225\n"
"CV v223,v226,v227\n"
"ST v[220:223],v1,s[40:43],s63 offen offset:1024 sc1\n"
"v_pk_mul_f32 v[228:229], v[228:229], v[244:245] op_sel_hi:[1,0]\n"
"v_pk_mul_f32 v[230:231], v[230:231], v[244:245] op_sel_hi:[1,0]\n"
"v_pk_mul_f32 v[232:233], v[232:233], v[244:245] op_sel_hi:[1,0]\n"
"v_pk_mul_f32 v[234:235], v[234:235], v[244:245] op_sel_hi:[1,0]\n"
"CV v228,v228,v229\n"
"CV v229,v230,v231\n"
"CV v230,v232,v233\n"
"CV v231,v234,v235\n"
"ST v[228:231],v1,s[40:43],s63 offen offset:2048 sc1\n"
"v_pk_mul_f32 v[236:237], v[236:237], v[244:245] op_sel_hi:[1,0]\n"
"v_pk_mul_f32 v[238:239], v[238:239], v[244:245] op_sel_hi:[1,0]\n"
"v_pk_mul_f32 v[240:241], v[240:241], v[244:245] op_sel_hi:[1,0]\n"
"v_pk_mul_f32 v[242:243], v[242:243], v[244:245] op_sel_hi:[1,0]\n"
"CV v236,v236,v237\n"
"CV v237,v238,v239\n"
"CV v238,v240,v241\n"
"CV v239,v242,v243\n"
"ST v[236:239],v1,s[40:43],s63 offen offset:3072 sc1\n"
"s_cmp_lg_u64 s[10:11], 0\n"
"s_cbranch_scc1 L_slow_30\n"
"L_slowback_31:\n"
"s_cmp_ge_u32 s37, 28\n"
"s_cbranch_scc1 L_no6_32\n"
"TR a[192:193],v28 offset:0\n"
"TR a[194:195],v28 offset:2048\n"
"TR a[196:197],v29 offset:0\n"
"TR a[198:199],v29 offset:2048\n"
"TR a[200:201],v30 offset:0\n"
"TR a[202:203],v30 offset:2048\n"
"TR a[204:205],v31 offset:0\n"
"TR a[206:207],v31 offset:2048\n"
"TR a[208:209],v28 offset:4096\n"
"TR a[210:211],v28 offset:6144\n"
"TR a[212:213],v29 offset:4096\n"
"TR a[214:215],v29 offset:6144\n"
"TR a[216:217],v30 offset:4096\n"
"TR a[218:219],v30 offset:6144\n"
"TR a[220:221],v31 offset:4096\n"
"TR a[222:223],v31 offset:6144\n"
"WT vmcnt(27) lgkmcnt(14)\n"
"MF v[32:35],a[192:195],a[0:3],0\n"
"WT vmcnt(26)\n"
"MF v[64:67],a[192:195],a[4:7],0\n"
"WT lgkmcnt(12)\n"
"s_add_u32 s65, s65, 221184\n"
"s_add_u32 m0, s66, 0\n"
"TR a[192:193],v28 offset:8192\n"
"MF v[36:39],a[196:199],a[0:3],0\n"
"LD v6,s[48:51],s65 offen offset:0 lds\n"
"MF v[68:71],a[196:199],a[4:7],0\n"
"WT lgkmcnt(11)\n"
"TR a[194:195],v28 offset:10240\n"
"TR a[196:197],v29 offset:8192\n"
"MF v[40:43],a[200:203],a[0:3],0\n"
"TR a[198:199],v29 offset:10240\n"
"MF v[72:75],a[200:203],a[4:7],0\n"
"WT lgkmcnt(12)\n"
"TR a[200:201],v30 offset:8192\n"
"MF v[44:47],a[204:207],a[0:3],0\n"
"LD v7,s[48:51],s65 offen offset:1024 lds\n"
"MF v[76:79],a[204:207],a[4:7],0\n"
"WT lgkmcnt(11)\n"
"TR a[202:203],v30 offset:10240\n"
"TR a[204:205],v31 offset:8192\n"
"MF v[48:51],a[208:211],a[0:3],0\n"
"TR a[206:207],v31 offset:10240\n"
"MF v[80:83],a[208:211],a[4:7],0\n"
"WT lgkmcnt(12)\n"
"s_add_u32 s68, s67, 8192\n"
"TR a[208:209],v28 offset:12288\n"
"MF v[52:55],a[212:215],a[0:3],0\n"
"LD a[64:67],v1,s[76:79],s68 offen offset:0 sc1\n"
"MF v[84:87],a[212:215],a[4:7],0\n"
"WT lgkmcnt(11)\n"
"TR a[210:211],v28 offset:14336\n"
"TR a[212:213],v29 offset:12288\n"
"MF v[56:59],a[216:219],a[0:3],0\n"
"TR a[214:215],v29 offset:14336\n"
"MF v[88:91],a[216:219],a[4:7],0\n"
"WT lgkmcnt(12)\n"
"TR a[216:217],v30 offset:12288\n"
"MF v[60:63],a[220:223],a[0:3],0\n"
"LD v8,s[48:51],s65 offen offset:2048 lds\n"
"MF v[92:95],a[220:223],a[4:7],0\n"
"WT vmcnt(29) lgkmcnt(11)\n"
"TR a[218:219],v30 offset:14336\n"
"TR a[220:221],v31 offset:12288\n"
"MF v[32:35],a[192:195],a[8:11],v[32:35]\n"
"WT vmcnt(28)\n"
"TR a[222:223],v31 offset:14336\n"
"MF v[64:67],a[192:195],a[12:15],v[64:67]\n"
"WT lgkmcnt(12)\n"
"TR a[192:193],v28 offset:16384\n"
"MF v[36:39],a[196:199],a[8:11],v[36:39]\n"
"LD v9,s[48:51],s65 offen offset:3072 lds\n"
"MF v[68:71],a[196:199],a[12:15],v[68:71]\n"
"WT lgkmcnt(11)\n"
"TR a[194:195],v28 offset:18432\n"
"TR a[196:197],v29 offset:16384\n"
"MF v[40:43],a[200:203],a[8:11],v[40:43]\n"
"TR a[198:199],v29 offset:18432\n"
"MF v[72:75],a[200:203],a[12:15],v[72:75]\n"
"WT lgkmcnt(12)\n"
"s_add_u32 s65, s65, 221184\n"
"s_add_u32 m0, s66, 16384\n"
"TR a[200:201],v30 offset:16384\n"
"MF v[44:47],a[204:207],a[8:11],v[44:47]\n"
"LD v6,s[48:51],s65 offen offset:0 lds\n"
"MF v[76:79],a[204:207],a[12:15],v[76:79]\n"
"WT lgkmcnt(11)\n"
"TR a[202:203],v30 offset:18432\n"
"TR a[204:205],v31 offset:16384\n"
"MF v[48:51],a[208:211],a[8:11],v[48:51]\n"
"TR a[206:207],v31 offset:18432\n"
"MF v[80:83],a[208:211],a[12:15],v[80:83]\n"
"WT lgkmcnt(12)\n"
"s_add_u32 s73, s67, 28672\n"
"TR a[208:209],v28 offset:20480\n"
"MF v[52:55],a[212:215],a[8:11],v[52:55]\n"
"LD a[68:71],v1,s[76:79],s73 offen offset:0 sc1\n"
"MF v[84:87],a[212:215],a[12:15],v[84:87]\n"
"WT lgkmcnt(11)\n"
"TR a[210:211],v28 offset:22528\n"
"TR a[212:213],v29 offset:20480\n"
"MF v[56:59],a[216:219],a[8:11],v[56:59]\n"
"TR a[214:215],v29 offset:22528\n"
"MF v[88:91],a[216:219],a[12:15],v[88:91]\n"
"WT lgkmcnt(12)\n"
"TR a[216:217],v30 offset:20480\n"
"MF v[60:63],a[220:223],a[8:11],v[60:63]\n"
"LD v7,s[48:51],s65 offen offset:1024 lds\n"
"MF v[92:95],a[220:223],a[12:15],v[92:95]\n"
"WT vmcnt(31) lgkmcnt(11)\n"
"TR a[218:219],v30 offset:22528\n"
"TR a[220:221],v31 offset:20480\n"
"MF v[32:35],a[192:195],a[16:19],v[32:35]\n"
"WT vmcnt(30)\n"
"TR a[222:223],v31 offset:22528\n"
"MF v[64:67],a[192:195],a[20:23],v[64:67]\n"
"WT lgkmcnt(12)\n"
"TR a[192:193],v28 offset:24576\n"
"MF v[36:39],a[196:199],a[16:19],v[36:39]\n"
"LD v8,s[48:51],s65 offen offset:2048 lds\n"
"MF v[68:71],a[196:199],a[20:23],v[68:71]\n"
"WT lgkmcnt(11)\n"
"TR a[194:195],v28 offset:26624\n"
"TR a[196:197],v29 offset:24576\n"
"MF v[40:43],a[200:203],a[16:19],v[40:43]\n"
"TR a[198:199],v29 offset:26624\n"
"MF v[72:75],a[200:203],a[20:23],v[72:75]\n"
"WT lgkmcnt(12)\n"
"TR a[200:201],v30 offset:24576\n"
"MF v[44:47],a[204:207],a[16:19],v[44:47]\n"
"LD v9,s[48:51],s65 offen offset:3072 lds\n"
"MF v[76:79],a[204:207],a[20:23],v[76:79]\n"
"WT lgkmcnt(11)\n"
"TR a[202:203],v30 offset:26624\n"
"TR a[204:205],v31 offset:24576\n"
"MF v[48:51],a[208:211],a[16:19],v[48:51]\n"
"TR a[206:207],v31 offset:26624\n"
"MF v[80:83],a[208:211],a[20:23],v[80:83]\n"
"WT lgkmcnt(12)\n"
"TR a[208:209],v28 offset:28672\n"
"MF v[52:55],a[212:215],a[16:19],v[52:55]\n"
"LD a[72:75],v1,s[76:79],s68 offen offset:1024 sc1\n"
"MF v[84:87],a[212:215],a[20:23],v[84:87]\n"
"WT lgkmcnt(11)\n"
"TR a[210:211],v28 offset:30720\n"
"TR a[212:213],v29 offset:28672\n"
"MF v[56:59],a[216:219],a[16:19],v[56:59]\n"
"TR a[214:215],v29 offset:30720\n"
"MF v[88:91],a[216:219],a[20:23],v[88:91]\n"
"WT lgkmcnt(12)\n"
"s_add_u32 s65, s65, 221184\n"
"s_add_u32 m0, s66, 32768\n"
"TR a[216:217],v30 offset:28672\n"
"MF v[60:63],a[220:223],a[16:19],v[60:63]\n"
"LD v6,s[48:51],s65 offen offset:0 lds\n"
"MF v[92:95],a[220:223],a[20:23],v[92:95]\n"
"WT vmcnt(33) lgkmcnt(11)\n"
"TR a[218:219],v30 offset:30720\n"
"TR a[220:221],v31 offset:28672\n"
"MF v[32:35],a[192:195],a[24:27],v[32:35]\n"
"WT vmcnt(32)\n"
"WT vmcnt(16)\n"
"s_barrier\n"
"TR a[222:223],v31 offset:30720\n"
"MF v[64:67],a[192:195],a[28:31],v[64:67]\n"
"WT lgkmcnt(12)\n"
"TR a[192:193],v24 offset:32768\n"
"MF v[36:39],a[196:199],a[24:27],v[36:39]\n"
"LD v7,s[48:51],s65 offen offset:1024 lds\n"
"MF v[68:71],a[196:199],a[28:31],v[68:71]\n"
"WT lgkmcnt(11)\n"
"TR a[194:195],v24 offset:34816\n"
"TR a[196:197],v25 offset:32768\n"
"MF v[40:43],a[200:203],a[24:27],v[40:43]\n"
"TR a[198:199],v25 offset:34816\n"
"MF v[72:75],a[200:203],a[28:31],v[72:75]\n"
"WT lgkmcnt(12)\n"
"TR a[200:201],v26 offset:32768\n"
"MF v[44:47],a[204:207],a[24:27],v[44:47]\n"
"LD v8,s[48:51],s65 offen offset:2048 lds\n"
"MF v[76:79],a[204:207],a[28:31],v[76:79]\n"
"WT lgkmcnt(11)\n"
"TR a[202:203],v26 offset:34816\n"
"TR a[204:205],v27 offset:32768\n"
"MF v[48:51],a[208:211],a[24:27],v[48:51]\n"
"TR a[206:207],v27 offset:34816\n"
"MF v[80:83],a[208:211],a[28:31],v[80:83]\n"
"WT lgkmcnt(12)\n"
"TR a[208:209],v24 offset:36864\n"
"MF v[52:55],a[212:215],a[24:27],v[52:55]\n"
"LD a[76:79],v1,s[76:79],s73 offen offset:1024 sc1\n"
"MF v[84:87],a[212:215],a[28:31],v[84:87]\n"
"WT lgkmcnt(11)\n"
"TR a[210:211],v24 offset:38912\n"
"TR a[212:213],v25 offset:36864\n"
"MF v[56:59],a[216:219],a[24:27],v[56:59]\n"
"TR a[214:215],v25 offset:38912\n"
"MF v[88:91],a[216:219],a[28:31],v[88:91]\n"
"WT lgkmcnt(12)\n"
"TR a[216:217],v26 offset:36864\n"
"MF v[60:63],a[220:223],a[24:27],v[60:63]\n"
"LD v9,s[48:51],s65 offen offset:3072 lds\n"
"MF v[92:95],a[220:223],a[28:31],v[92:95]\n"
"WT lgkmcnt(11)\n"
"TR a[218:219],v26 offset:38912\n"
"TR a[220:221],v27 offset:36864\n"
"MF v[32:35],a[192:195],a[32:35],v[32:35]\n"
"s_lshl_b32 s60, s71, 9\n"
"s_add_u32 s60, s60, 0x20000\n"
"s_lshl_b32 s62, s34, 10\n"
"s_add_u32 s62, s62, 147456\n"
"v_and_b32 v10, 48, v0\n"
"v_add_u32 v10, s62, v10\n"
"v_and_b32 v11, 31, v0\n"
"v_lshlrev_b32 v11, 3, v11\n"
"v_lshrrev_b32 v21, 5, v0\n"
"v_lshl_add_u32 v11, v21, 14, v11\n"
"s_lshl_b32 s61, s70, 10\n"
"s_lshl_b32 s57, s34, 8\n"
"s_add_u32 s61, s61, s57\n"
"s_add_u32 s61, s61, 0xd00000\n"
"TR a[222:223],v27 offset:38912\n"
"MF v[64:67],a[192:195],a[36:39],v[64:67]\n"
"WT lgkmcnt(12)\n"
"TR a[192:193],v24 offset:40960\n"
"MF v[36:39],a[196:199],a[32:35],v[36:39]\n"
"LD a[80:83],v1,s[76:79],s68 offen offset:2048 sc1\n"
"MF v[68:71],a[196:199],a[36:39],v[68:71]\n"
"WT lgkmcnt(11)\n"
"TR a[194:195],v24 offset:43008\n"
"TR a[196:197],v25 offset:40960\n"
"MF v[40:43],a[200:203],a[32:35],v[40:43]\n"
"TR a[198:199],v25 offset:43008\n"
"MF v[72:75],a[200:203],a[36:39],v[72:75]\n"
"WT lgkmcnt(12)\n"
"s_add_u32 s65, s65, 221184\n"
"s_add_u32 m0, s66, 49152\n"
"TR a[200:201],v26 offset:40960\n"
"MF v[44:47],a[204:207],a[32:35],v[44:47]\n"
"LD v6,s[48:51],s65 offen offset:0 lds\n"
"MF v[76:79],a[204:207],a[36:39],v[76:79]\n"
"WT lgkmcnt(11)\n"
"TR a[202:203],v26 offset:43008\n"
"TR a[204:205],v27 offset:40960\n"
"MF v[48:51],a[208:211],a[32:35],v[48:51]\n"
"TR a[206:207],v27 offset:43008\n"
"MF v[80:83],a[208:211],a[36:39],v[80:83]\n"
"WT lgkmcnt(12)\n"
"TR a[208:209],v24 offset:45056\n"
"MF v[52:55],a[212:215],a[32:35],v[52:55]\n"
"LD a[84:87],v1,s[76:79],s73 offen offset:2048 sc1\n"
"MF v[84:87],a[212:215],a[36:39],v[84:87]\n"
"WT lgkmcnt(11)\n"
"TR a[210:211],v24 offset:47104\n"
"TR a[212:213],v25 offset:45056\n"
"MF v[56:59],a[216:219],a[32:35],v[56:59]\n"
"TR a[214:215],v25 offset:47104\n"
"MF v[88:91],a[216:219],a[36:39],v[88:91]\n"
"WT lgkmcnt(12)\n"
"TR a[216:217],v26 offset:45056\n"
"MF v[60:63],a[220:223],a[32:35],v[60:63]\n"
"LD v7,s[48:51],s65 offen offset:1024 lds\n"
"MF v[92:95],a[220:223],a[36:39],v[92:95]\n"
"WT lgkmcnt(11)\n"
"TR a[218:219],v26 offset:47104\n"
"TR a[220:221],v27 offset:45056\n"
"MF v[32:35],a[192:195],a[40:43],v[32:35]\n"
"TR a[222:223],v27 offset:47104\n"
"MF v[64:67],a[192:195],a[44:47],v[64:67]\n"
"WT lgkmcnt(12)\n"
"TR a[192:193],v24 offset:49152\n"
"MF v[36:39],a[196:199],a[40:43],v[36:39]\n"
"LD a[88:91],v1,s[76:79],s68 offen offset:3072 sc1\n"
"MF v[68:71],a[196:199],a[44:47],v[68:71]\n"
"WT lgkmcnt(11)\n"
"TR a[194:195],v24 offset:51200\n"
"TR a[196:197],v25 offset:49152\n"
"MF v[40:43],a[200:203],a[40:43],v[40:43]\n"
"TR a[198:199],v25 offset:51200\n"
"MF v[72:75],a[200:203],a[44:47],v[72:75]\n"
"WT lgkmcnt(12)\n"
"TR a[200:201],v26 offset:49152\n"
"MF v[44:47],a[204:207],a[40:43],v[44:47]\n"
"LD v8,s[48:51],s65 offen offset:2048 lds\n"
"MF v[76:79],a[204:207],a[44:47],v[76:79]\n"
"WT lgkmcnt(11)\n"
"TR a[202:203],v26 offset:51200\n"
"TR a[204:205],v27 offset:49152\n"
"MF v[48:51],a[208:211],a[40:43],v[48:51]\n"
"TR a[206:207],v27 offset:51200\n"
"MF v[80:83],a[208:211],a[44:47],v[80:83]\n"
"WT lgkmcnt(12)\n"
"TR a[208:209],v24 offset:53248\n"
"MF v[52:55],a[212:215],a[40:43],v[52:55]\n"
"LD a[92:95],v1,s[76:79],s73 offen offset:3072 sc1\n"
"MF v[84:87],a[212:215],a[44:47],v[84:87]\n"
"WT lgkmcnt(11)\n"
"TR a[210:211],v24 offset:55296\n"
"TR a[212:213],v25 offset:53248\n"
"MF v[56:59],a[216:219],a[40:43],v[56:59]\n"
"buffer_load_dwordx2 v[128:129], v11, s[40:43], s61 offen sc1\n"
"MF v[88:91],a[216:219],a[44:47],v[88:91]\n"
"WT lgkmcnt(11)\n"
"TR a[214:215],v25 offset:55296\n"
"TR a[216:217],v26 offset:53248\n"
"MF v[60:63],a[220:223],a[40:43],v[60:63]\n"
"LD v9,s[48:51],s65 offen offset:3072 lds\n"
"MF v[92:95],a[220:223],a[44:47],v[92:95]\n"
"WT lgkmcnt(11)\n"
"TR a[218:219],v26 offset:55296\n"
"TR a[220:221],v27 offset:53248\n"
"MF v[32:35],a[192:195],a[48:51],v[32:35]\n"
"TR a[222:223],v27 offset:55296\n"
"MF v[64:67],a[192:195],a[52:55],v[64:67]\n"
"WT lgkmcnt(12)\n"
"s_add_u32 s68, s67, 12288\n"
"TR a[192:193],v24 offset:57344\n"
"MF v[36:39],a[196:199],a[48:51],v[36:39]\n"
"LD a[128:131],v1,s[76:79],s68 offen offset:0 sc1\n"
"MF v[68:71],a[196:199],a[52:55],v[68:71]\n"
"WT lgkmcnt(11)\n"
"TR a[194:195],v24 offset:59392\n"
"TR a[196:197],v25 offset:57344\n"
"MF v[40:43],a[200:203],a[48:51],v[40:43]\n"
"TR a[198:199],v25 offset:59392\n"
"MF v[72:75],a[200:203],a[52:55],v[72:75]\n"
"WT lgkmcnt(12)\n"
"s_add_u32 s65, s65, 221184\n"
"s_add_u32 m0, s66, 65536\n"
"TR a[200:201],v26 offset:57344\n"
"MF v[44:47],a[204:207],a[48:51],v[44:47]\n"
"LD v6,s[48:51],s65 offen offset:0 lds\n"
"MF v[76:79],a[204:207],a[52:55],v[76:79]\n"
"WT lgkmcnt(11)\n"
"TR a[202:203],v26 offset:59392\n"
"TR a[204:205],v27 offset:57344\n"
"MF v[48:51],a[208:211],a[48:51],v[48:51]\n"
"TR a[206:207],v27 offset:59392\n"
"MF v[80:83],a[208:211],a[52:55],v[80:83]\n"
"WT lgkmcnt(12)\n"
"s_add_u32 s73, s67, 32768\n"
"TR a[208:209],v24 offset:61440\n"
"MF v[52:55],a[212:215],a[48:51],v[52:55]\n"
"LD a[132:135],v1,s[76:79],s73 offen offset:0 sc1\n"
"MF v[84:87],a[212:215],a[52:55],v[84:87]\n"
"WT lgkmcnt(11)\n"
"s_add_u32 s61, s61, 32768\n"
"TR a[210:211],v24 offset:63488\n"
"TR a[212:213],v25 offset:61440\n"
"MF v[56:59],a[216:219],a[48:51],v[56:59]\n"
"buffer_load_dwordx2 v[130:131], v11, s[40:43], s61 offen sc1\n"
"MF v[88:91],a[216:219],a[52:55],v[88:91]\n"
"WT lgkmcnt(11)\n"
"TR a[214:215],v25 offset:63488\n"
"TR a[216:217],v26 offset:61440\n"
"MF v[60:63],a[220:223],a[48:51],v[60:63]\n"
"LD v7,s[48:51],s65 offen offset:1024 lds\n"
"MF v[92:95],a[220:223],a[52:55],v[92:95]\n"
"WT lgkmcnt(11)\n"
"TR a[218:219],v26 offset:63488\n"
"TR a[220:221],v27 offset:61440\n"
"MF v[32:35],a[192:195],a[56:59],v[32:35]\n"
"WT vmcnt(20)\n"
"s_barrier\n"
"s_cmp_lg_u32 s34, 0\n"
"s_cbranch_scc1 L_pub_39\n"
"s_mov_b64 exec, 1\n"
"s_lshl_b32 s56, s33, 7\n"
"v_lshl_add_u32 v21, v0, 3, s56\n"
"s_mov_b32 s57, 0x60000\n"
"buffer_store_dwordx2 v[16:17], v21, s[40:43], s57 offen sc1\n"
"s_mov_b64 exec, -1\n"
"L_pub_39:\n"
"TR a[222:223],v27 offset:63488\n"
"MF v[64:67],a[192:195],a[60:63],v[64:67]\n"
"WT lgkmcnt(12)\n"
"TR a[192:193],v12 offset:0\n"
"MF v[36:39],a[196:199],a[56:59],v[36:39]\n"
"LD a[136:139],v1,s[76:79],s68 offen offset:1024 sc1\n"
"MF v[68:71],a[196:199],a[60:63],v[68:71]\n"
"WT lgkmcnt(11)\n"
"TR a[194:195],v12 offset:2048\n"
"TR a[196:197],v13 offset:0\n"
"MF v[40:43],a[200:203],a[56:59],v[40:43]\n"
"TR a[198:199],v13 offset:2048\n"
"MF v[72:75],a[200:203],a[60:63],v[72:75]\n"
"WT lgkmcnt(12)\n"
"TR a[200:201],v14 offset:0\n"
"MF v[44:47],a[204:207],a[56:59],v[44:47]\n"
"LD v8,s[48:51],s65 offen offset:2048 lds\n"
"MF v[76:79],a[204:207],a[60:63],v[76:79]\n"
"WT lgkmcnt(11)\n"
"TR a[202:203],v14 offset:2048\n"
"TR a[204:205],v15 offset:0\n"
"MF v[48:51],a[208:211],a[56:59],v[48:51]\n"
"TR a[206:207],v15 offset:2048\n"
"MF v[80:83],a[208:211],a[60:63],v[80:83]\n"
"WT lgkmcnt(12)\n"
"TR a[208:209],v12 offset:4096\n"
"MF v[52:55],a[212:215],a[56:59],v[52:55]\n"
"LD a[140:143],v1,s[76:79],s73 offen offset:1024 sc1\n"
"MF v[84:87],a[212:215],a[60:63],v[84:87]\n"
"WT lgkmcnt(11)\n"
"s_add_u32 s61, s61, 32768\n"
"TR a[210:211],v12 offset:6144\n"
"TR a[212:213],v13 offset:4096\n"
"MF v[56:59],a[216:219],a[56:59],v[56:59]\n"
"buffer_load_dwordx2 v[132:133], v11, s[40:43], s61 offen sc1\n"
"MF v[88:91],a[216:219],a[60:63],v[88:91]\n"
"WT lgkmcnt(11)\n"
"TR a[214:215],v13 offset:6144\n"
"TR a[216:217],v14 offset:4096\n"
"MF v[60:63],a[220:223],a[56:59],v[60:63]\n"
"LD v9,s[48:51],s65 offen offset:3072 lds\n"
"MF v[92:95],a[220:223],a[60:63],v[92:95]\n"
"WT lgkmcnt(11)\n"
"TR a[218:219],v14 offset:6144\n"
"TR a[220:221],v15 offset:4096\n"
"MF v[32:35],a[192:195],a[64:67],v[32:35]\n"
"s_mov_b32 m0, s62\n"
"TR a[222:223],v15 offset:6144\n"
"MF v[64:67],a[192:195],a[68:71],v[64:67]\n"
"WT lgkmcnt(12)\n"
"LD v1,s[40:43],s60 offen sc1 lds\n"
"MF v[36:39],a[196:199],a[64:67],v[36:39]\n"
"LD a[144:147],v1,s[76:79],s68 offen offset:2048 sc1\n"
"MF v[68:71],a[196:199],a[68:71],v[68:71]\n"
"WT lgkmcnt(10)\n"
"TR a[192:193],v12 offset:8192\n"
"TR a[194:195],v12 offset:10240\n"
"MF v[40:43],a[200:203],a[64:67],v[40:43]\n"
"TR a[196:197],v13 offset:8192\n"
"MF v[72:75],a[200:203],a[68:71],v[72:75]\n"
"WT lgkmcnt(11)\n"
"s_add_u32 s65, s65, 221184\n"
"s_add_u32 m0, s66, 81920\n"
"TR a[198:199],v13 offset:10240\n"
"TR a[200:201],v14 offset:8192\n"
"MF v[44:47],a[204:207],a[64:67],v[44:47]\n"
"LD v6,s[48:51],s65 offen offset:0 lds\n"
"MF v[76:79],a[204:207],a[68:71],v[76:79]\n"
"WT lgkmcnt(11)\n"
"TR a[202:203],v14 offset:10240\n"
"TR a[204:205],v15 offset:8192\n"
"MF v[48:51],a[208:211],a[64:67],v[48:51]\n"
"TR a[206:207],v15 offset:10240\n"
"MF v[80:83],a[208:211],a[68:71],v[80:83]\n"
"WT lgkmcnt(12)\n"
"TR a[208:209],v12 offset:12288\n"
"MF v[52:55],a[212:215],a[64:67],v[52:55]\n"
"LD a[148:151],v1,s[76:79],s73 offen offset:2048 sc1\n"
"MF v[84:87],a[212:215],a[68:71],v[84:87]\n"
"WT lgkmcnt(11)\n"
"s_add_u32 s61, s61, 32768\n"
"TR a[210:211],v12 offset:14336\n"
"TR a[212:213],v13 offset:12288\n"
"MF v[56:59],a[216:219],a[64:67],v[56:59]\n"
"buffer_load_dwordx2 v[134:135], v11, s[40:43], s61 offen sc1\n"
"MF v[88:91],a[216:219],a[68:71],v[88:91]\n"
"WT lgkmcnt(11)\n"
"TR a[214:215],v13 offset:14336\n"
"TR a[216:217],v14 offset:12288\n"
"MF v[60:63],a[220:223],a[64:67],v[60:63]\n"
"LD v7,s[48:51],s65 offen offset:1024 lds\n"
"MF v[92:95],a[220:223],a[68:71],v[92:95]\n"
"WT vmcnt(30) lgkmcnt(11)\n"
"TR a[218:219],v14 offset:14336\n"
"TR a[220:221],v15 offset:12288\n"
"MF v[32:35],a[192:195],a[72:75],v[32:35]\n"
"WT vmcnt(26)\n"
"TR a[222:223],v15 offset:14336\n"
"MF v[64:67],a[192:195],a[76:79],v[64:67]\n"
"WT lgkmcnt(12)\n"
"TR a[192:193],v12 offset:16384\n"
"MF v[36:39],a[196:199],a[72:75],v[36:39]\n"
"LD a[152:155],v1,s[76:79],s68 offen offset:3072 sc1\n"
"MF v[68:71],a[196:199],a[76:79],v[68:71]\n"
"WT lgkmcnt(11)\n"
"TR a[194:195],v12 offset:18432\n"
"TR a[196:197],v13 offset:16384\n"
"MF v[40:43],a[200:203],a[72:75],v[40:43]\n"
"TR a[198:199],v13 offset:18432\n"
"MF v[72:75],a[200:203],a[76:79],v[72:75]\n"
"WT lgkmcnt(12)\n"
"TR a[200:201],v14 offset:16384\n"
"MF v[44:47],a[204:207],a[72:75],v[44:47]\n"
"LD v8,s[48:51],s65 offen offset:2048 lds\n"
"MF v[76:79],a[204:207],a[76:79],v[76:79]\n"
"WT lgkmcnt(11)\n"
"TR a[202:203],v14 offset:18432\n"
"TR a[204:205],v15 offset:16384\n"
"MF v[48:51],a[208:211],a[72:75],v[48:51]\n"
"TR a[206:207],v15 offset:18432\n"
"MF v[80:83],a[208:211],a[76:79],v[80:83]\n"
"WT lgkmcnt(12)\n"
"TR a[208:209],v12 offset:20480\n"
"MF v[52:55],a[212:215],a[72:75],v[52:55]\n"
"LD a[156:159],v1,s[76:79],s73 offen offset:3072 sc1\n"
"MF v[84:87],a[212:215],a[76:79],v[84:87]\n"
"WT lgkmcnt(11)\n"
"s_add_u32 s61, s61, 32768\n"
"TR a[210:211],v12 offset:22528\n"
"TR a[212:213],v13 offset:20480\n"
"MF v[56:59],a[216:219],a[72:75],v[56:59]\n"
"buffer_load_dwordx2 v[136:137], v11, s[40:43], s61 offen sc1\n"
"MF v[88:91],a[216:219],a[76:79],v[88:91]\n"
"WT lgkmcnt(11)\n"
"TR a[214:215],v13 offset:22528\n"
"TR a[216:217],v14 offset:20480\n"
"MF v[60:63],a[220:223],a[72:75],v[60:63]\n"
"LD v9,s[48:51],s65 offen offset:3072 lds\n"
"MF v[92:95],a[220:223],a[76:79],v[92:95]\n"
"WT vmcnt(29) lgkmcnt(11)\n"
"TR a[218:219],v14 offset:22528\n"
"TR a[220:221],v15 offset:20480\n"
"MF v[32:35],a[192:195],a[80:83],v[32:35]\n"
"WT vmcnt(27)\n"
"WT vmcnt(10)\n"
"TR a[222:223],v15 offset:22528\n"
"MF v[64:67],a[192:195],a[84:87],v[64:67]\n"
"WT lgkmcnt(12)\n"
"s_add_u32 s68, s67, 16384\n"
"TR a[192:193],v12 offset:24576\n"
"MF v[36:39],a[196:199],a[80:83],v[36:39]\n"
"LD a[160:163],v1,s[76:79],s68 offen offset:0 sc1\n"
"MF v[68:71],a[196:199],a[84:87],v[68:71]\n"
"WT lgkmcnt(11)\n"
"TR a[194:195],v12 offset:26624\n"
"TR a[196:197],v13 offset:24576\n"
"MF v[40:43],a[200:203],a[80:83],v[40:43]\n"
"RD v[96:99],v10 offset:0\n"
"TR a[198:199],v13 offset:26624\n"
"MF v[72:75],a[200:203],a[84:87],v[72:75]\n"
"WT lgkmcnt(13)\n"
"RD v[100:103],v10 offset:64\n"
"TR a[200:201],v14 offset:24576\n"
"MF v[44:47],a[204:207],a[80:83],v[44:47]\n"
"RD v[104:107],v10 offset:128\n"
"TR a[202:203],v14 offset:26624\n"
"MF v[76:79],a[204:207],a[84:87],v[76:79]\n"
"WT lgkmcnt(15)\n"
"RD v[108:111],v10 offset:192\n"
"TR a[204:205],v15 offset:24576\n"
"MF v[48:51],a[208:211],a[80:83],v[48:51]\n"
"RD v[112:115],v10 offset:256\n"
"TR a[206:207],v15 offset:26624\n"
"MF v[80:83],a[208:211],a[84:87],v[80:83]\n"
"WT lgkmcnt(15)\n"
"s_add_u32 s73, s67, 36864\n"
"TR a[208:209],v12 offset:28672\n"
"MF v[52:55],a[212:215],a[80:83],v[52:55]\n"
"LD a[164:167],v1,s[76:79],s73 offen offset:0 sc1\n"
"MF v[84:87],a[212:215],a[84:87],v[84:87]\n"
"TR a[210:211],v12 offset:30720\n"
"TR a[212:213],v13 offset:28672\n"
"MF v[56:59],a[216:219],a[80:83],v[56:59]\n"
"RD v[116:119],v10 offset:320\n"
"TR a[214:215],v13 offset:30720\n"
"MF v[88:91],a[216:219],a[84:87],v[88:91]\n"
"WT lgkmcnt(15)\n"
"RD v[120:123],v10 offset:384\n"
"TR a[216:217],v14 offset:28672\n"
"MF v[60:63],a[220:223],a[80:83],v[60:63]\n"
"RD v[124:127],v10 offset:448\n"
"TR a[218:219],v14 offset:30720\n"
"MF v[92:95],a[220:223],a[84:87],v[92:95]\n"
"TR a[220:221],v15 offset:28672\n"
"MF v[32:35],a[192:195],a[88:91],v[32:35]\n"
"s_barrier\n"
"s_lshl_b32 s22, s34, 12\n"
"s_sub_u32 s56, s37, 20\n"
"s_min_u32 s58, s56, 9\n"
"s_and_b32 s56, s72, 4\n"
"s_lshl_b32 s14, s72, 1\n"
"s_cmp_ge_u32 s37, 20\n"
"s_cselect_b32 s59, s58, s37\n"
"s_cselect_b32 s14, s14, s56\n"
"s_mul_i32 s57, s59, 52\n"
"s_lshr_b32 s57, s57, 8\n"
"s_mul_i32 s58, s57, 5\n"
"s_sub_u32 s15, s59, s58\n"
"s_add_u32 s14, s14, s57\n"
"s_lshr_b32 s56, s34, 1\n"
"s_and_b32 s58, s34, 1\n"
"s_mul_i32 s65, s56, 40960\n"
"s_lshl_b32 s58, s58, 7\n"
"s_add_u32 s65, s65, s58\n"
"s_lshl_b32 s58, s15, 8\n"
"s_add_u32 s65, s65, s58\n"
"s_lshl_b32 s59, s14, 2\n"
"s_add_u32 s59, s59, s34\n"
"v_mov_b32 v10, v1\n"
"s_cmp_ge_u32 s37, 20\n"
"s_cbranch_scc1 L_s7b_40\n"
"s_and_b32 s16, s72, 3\n"
"s_lshl_b32 s56, s14, 1\n"
"v_bfe_u32 v22, v0, 3, 1\n"
"v_add_u32 v22, s56, v22\n"
"v_sub_u32 v23, 15, v22\n"
"v_min_u32 v22, v22, v23\n"
"s_lshl_b32 s57, s16, 1\n"
"v_and_b32 v21, 3, v0\n"
"v_lshlrev_b32 v21, 3, v21\n"
"v_lshl_add_u32 v21, v22, 5, v21\n"
"v_bfe_u32 v22, v0, 2, 1\n"
"v_add3_u32 v21, v21, v22, s57\n"
"v_lshlrev_b32 v21, 7, v21\n"
"v_add_u32 v20, 0x60000, v21\n"
"s_mul_i32 s56, s16, 1310720\n"
"s_add_u32 s52, s18, s56\n"
"s_addc_u32 s53, s19, 0\n"
"s_and_b32 s53, s53, 0xffff\n"
"s_mov_b32 s54, 0x140000\n"
"s_mov_b32 s55, 0x20000\n"
"s_lshl_b32 s56, s59, 14\n"
"s_lshl_b32 s57, s16, 22\n"
"s_add_u32 s56, s56, s57\n"
"s_add_u32 s56, s56, 0x4000000\n"
"s_add_u32 s44, s30, s56\n"
"s_addc_u32 s45, s31, 0\n"
"s_and_b32 s45, s45, 0xffff\n"
"s_mov_b32 s46, 0x384000\n"
"s_mov_b32 s47, 0x20000\n"
"s_mov_b32 s23, 81920\n"
"s_branch L_s7e_41\n"
"L_s7b_40:\n"
"s_mul_i32 s56, s57, 14\n"
"v_min_u32 v21, 13, v0\n"
"v_add_u32 v21, s56, v21\n"
"v_lshlrev_b32 v21, 3, v21\n"
"v_add_u32 v21, s72, v21\n"
"v_lshlrev_b32 v21, 2, v21\n"
"v_add_u32 v21, s34, v21\n"
"v_lshlrev_b32 v21, 7, v21\n"
"v_add_u32 v20, 0xc0000, v21\n"
"s_mov_b32 s52, s26\n"
"s_and_b32 s53, s27, 0xffff\n"
"s_mov_b32 s54, 0x21c000\n"
"s_mov_b32 s55, 0x20000\n"
"s_mul_i32 s56, s59, 110592\n"
"s_add_u32 s56, s56, 0x1100000\n"
"s_add_u32 s44, s30, s56\n"
"s_addc_u32 s45, s31, 0\n"
"s_and_b32 s45, s45, 0xffff\n"
"s_mov_b32 s46, 0x1b000\n"
"s_mov_b32 s47, 0x20000\n"
"v_add_u32 v11, 55296, v1\n"
"s_mov_b32 s23, 163840\n"
"L_s7e_41:\n"
"s_sub_u32 s65, s65, s23\n"
"TR a[222:223],v15 offset:30720\n"
"MF v[64:67],a[192:195],a[92:95],v[64:67]\n"
"WT lgkmcnt(15)\n"
"TR a[192:193],v12 offset:32768\n"
"MF v[36:39],a[196:199],a[88:91],v[36:39]\n"
"LD a[168:171],v1,s[76:79],s68 offen offset:1024 sc1\n"
"MF v[68:71],a[196:199],a[92:95],v[68:71]\n"
"TR a[194:195],v12 offset:34816\n"
"TR a[196:197],v13 offset:32768\n"
"MF v[40:43],a[200:203],a[88:91],v[40:43]\n"
"TR a[198:199],v13 offset:34816\n"
"MF v[72:75],a[200:203],a[92:95],v[72:75]\n"
"WT lgkmcnt(15)\n"
"s_add_u32 s65, s65, s23\n"
"s_add_u32 m0, s22, 98304\n"
"TR a[200:201],v14 offset:32768\n"
"MF v[44:47],a[204:207],a[88:91],v[44:47]\n"
"LD v2,s[52:55],s65 offen offset:0 lds\n"
"MF v[76:79],a[204:207],a[92:95],v[76:79]\n"
"WT lgkmcnt(14)\n"
"TR a[202:203],v14 offset:34816\n"
"TR a[204:205],v15 offset:32768\n"
"MF v[48:51],a[208:211],a[88:91],v[48:51]\n"
"TR a[206:207],v15 offset:34816\n"
"MF v[80:83],a[208:211],a[92:95],v[80:83]\n"
"WT lgkmcnt(14)\n"
"TR a[208:209],v12 offset:36864\n"
"MF v[52:55],a[212:215],a[88:91],v[52:55]\n"
"LD a[172:175],v1,s[76:79],s73 offen offset:1024 sc1\n"
"MF v[84:87],a[212:215],a[92:95],v[84:87]\n"
"WT lgkmcnt(11)\n"
"TR a[210:211],v12 offset:38912\n"
"TR a[212:213],v13 offset:36864\n"
"MF v[56:59],a[216:219],a[88:91],v[56:59]\n"
"TR a[214:215],v13 offset:38912\n"
"MF v[88:91],a[216:219],a[92:95],v[88:91]\n"
"WT lgkmcnt(12)\n"
"TR a[216:217],v14 offset:36864\n"
"MF v[60:63],a[220:223],a[88:91],v[60:63]\n"
"LD v3,s[52:55],s65 offen offset:1024 lds\n"
"MF v[92:95],a[220:223],a[92:95],v[92:95]\n"
"WT lgkmcnt(11)\n"
"TR a[218:219],v14 offset:38912\n"
"TR a[220:221],v15 offset:36864\n"
"MF v[32:35],a[192:195],a[128:131],v[32:35]\n"
"TR a[222:223],v15 offset:38912\n"
"MF v[64:67],a[192:195],a[132:135],v[64:67]\n"
"WT lgkmcnt(12)\n"
"TR a[192:193],v12 offset:40960\n"
"MF v[36:39],a[196:199],a[128:131],v[36:39]\n"
"LD a[176:179],v1,s[76:79],s68 offen offset:2048 sc1\n"
"MF v[68:71],a[196:199],a[132:135],v[68:71]\n"
"WT lgkmcnt(11)\n"
"TR a[194:195],v12 offset:43008\n"
"TR a[196:197],v13 offset:40960\n"
"MF v[40:43],a[200:203],a[128:131],v[40:43]\n"
"TR a[198:199],v13 offset:43008\n"
"MF v[72:75],a[200:203],a[132:135],v[72:75]\n"
"WT lgkmcnt(12)\n"
"TR a[200:201],v14 offset:40960\n"
"MF v[44:47],a[204:207],a[128:131],v[44:47]\n"
"LD v4,s[52:55],s65 offen offset:2048 lds\n"
"MF v[76:79],a[204:207],a[132:135],v[76:79]\n"
"WT lgkmcnt(11)\n"
"TR a[202:203],v14 offset:43008\n"
"TR a[204:205],v15 offset:40960\n"
"MF v[48:51],a[208:211],a[128:131],v[48:51]\n"
"TR a[206:207],v15 offset:43008\n"
"MF v[80:83],a[208:211],a[132:135],v[80:83]\n"
"WT lgkmcnt(12)\n"
"TR a[208:209],v12 offset:45056\n"
"MF v[52:55],a[212:215],a[128:131],v[52:55]\n"
"LD a[180:183],v1,s[76:79],s73 offen offset:2048 sc1\n"
"MF v[84:87],a[212:215],a[132:135],v[84:87]\n"
"WT lgkmcnt(11)\n"
"TR a[210:211],v12 offset:47104\n"
"TR a[212:213],v13 offset:45056\n"
"MF v[56:59],a[216:219],a[128:131],v[56:59]\n"
"TR a[214:215],v13 offset:47104\n"
"MF v[88:91],a[216:219],a[132:135],v[88:91]\n"
"WT lgkmcnt(12)\n"
"TR a[216:217],v14 offset:45056\n"
"MF v[60:63],a[220:223],a[128:131],v[60:63]\n"
"LD v5,s[52:55],s65 offen offset:3072 lds\n"
"MF v[92:95],a[220:223],a[132:135],v[92:95]\n"
"WT lgkmcnt(11)\n"
"TR a[218:219],v14 offset:47104\n"
"TR a[220:221],v15 offset:45056\n"
"MF v[32:35],a[192:195],a[136:139],v[32:35]\n"
"TR a[222:223],v15 offset:47104\n"
"MF v[64:67],a[192:195],a[140:143],v[64:67]\n"
"WT lgkmcnt(12)\n"
"TR a[192:193],v12 offset:49152\n"
"MF v[36:39],a[196:199],a[136:139],v[36:39]\n"
"LD a[184:187],v1,s[76:79],s68 offen offset:3072 sc1\n"
"MF v[68:71],a[196:199],a[140:143],v[68:71]\n"
"WT lgkmcnt(11)\n"
"TR a[194:195],v12 offset:51200\n"
"TR a[196:197],v13 offset:49152\n"
"MF v[40:43],a[200:203],a[136:139],v[40:43]\n"
"TR a[198:199],v13 offset:51200\n"
"MF v[72:75],a[200:203],a[140:143],v[72:75]\n"
"WT lgkmcnt(12)\n"
"s_add_u32 s65, s65, s23\n"
"s_add_u32 m0, s22, 114688\n"
"TR a[200:201],v14 offset:49152\n"
"MF v[44:47],a[204:207],a[136:139],v[44:47]\n"
"LD v2,s[52:55],s65 offen offset:0 lds\n"
"MF v[76:79],a[204:207],a[140:143],v[76:79]\n"
"WT lgkmcnt(11)\n"
"TR a[202:203],v14 offset:51200\n"
"TR a[204:205],v15 offset:49152\n"
"MF v[48:51],a[208:211],a[136:139],v[48:51]\n"
"TR a[206:207],v15 offset:51200\n"
"MF v[80:83],a[208:211],a[140:143],v[80:83]\n"
"WT lgkmcnt(12)\n"
"TR a[208:209],v12 offset:53248\n"
"MF v[52:55],a[212:215],a[136:139],v[52:55]\n"
"LD a[188:191],v1,s[76:79],s73 offen offset:3072 sc1\n"
"MF v[84:87],a[212:215],a[140:143],v[84:87]\n"
"WT lgkmcnt(11)\n"
"TR a[210:211],v12 offset:55296\n"
"TR a[212:213],v13 offset:53248\n"
"MF v[56:59],a[216:219],a[136:139],v[56:59]\n"
"TR a[214:215],v13 offset:55296\n"
"MF v[88:91],a[216:219],a[140:143],v[88:91]\n"
"WT lgkmcnt(12)\n"
"TR a[216:217],v14 offset:53248\n"
"MF v[60:63],a[220:223],a[136:139],v[60:63]\n"
"LD v3,s[52:55],s65 offen offset:1024 lds\n"
"MF v[92:95],a[220:223],a[140:143],v[92:95]\n"
"WT vmcnt(23) lgkmcnt(11)\n"
"TR a[218:219],v14 offset:55296\n"
"TR a[220:221],v15 offset:53248\n"
"MF v[32:35],a[192:195],a[144:147],v[32:35]\n"
"WT vmcnt(21)\n"
"TR a[222:223],v15 offset:55296\n"
"MF v[64:67],a[192:195],a[148:151],v[64:67]\n"
"WT lgkmcnt(12)\n"
"TR a[192:193],v12 offset:57344\n"
"MF v[36:39],a[196:199],a[144:147],v[36:39]\n"
"LD v4,s[52:55],s65 offen offset:2048 lds\n"
"MF v[68:71],a[196:199],a[148:151],v[68:71]\n"
"WT lgkmcnt(11)\n"
"TR a[194:195],v12 offset:59392\n"
"TR a[196:197],v13 offset:57344\n"
"MF v[40:43],a[200:203],a[144:147],v[40:43]\n"
"TR a[198:199],v13 offset:59392\n"
"MF v[72:75],a[200:203],a[148:151],v[72:75]\n"
"WT lgkmcnt(12)\n"
"TR a[200:201],v14 offset:57344\n"
"MF v[44:47],a[204:207],a[144:147],v[44:47]\n"
"LD v5,s[52:55],s65 offen offset:3072 lds\n"
"MF v[76:79],a[204:207],a[148:151],v[76:79]\n"
"WT lgkmcnt(11)\n"
"TR a[202:203],v14 offset:59392\n"
"TR a[204:205],v15 offset:57344\n"
"MF v[48:51],a[208:211],a[144:147],v[48:51]\n"
"TR a[206:207],v15 offset:59392\n"
"MF v[80:83],a[208:211],a[148:151],v[80:83]\n"
"WT lgkmcnt(12)\n"
"s_add_u32 s65, s65, s23\n"
"s_add_u32 m0, s22, 131072\n"
"TR a[208:209],v12 offset:61440\n"
"MF v[52:55],a[212:215],a[144:147],v[52:55]\n"
"LD v2,s[52:55],s65 offen offset:0 lds\n"
"MF v[84:87],a[212:215],a[148:151],v[84:87]\n"
"WT lgkmcnt(11)\n"
"TR a[210:211],v12 offset:63488\n"
"TR a[212:213],v13 offset:61440\n"
"MF v[56:59],a[216:219],a[144:147],v[56:59]\n"
"TR a[214:215],v13 offset:63488\n"
"MF v[88:91],a[216:219],a[148:151],v[88:91]\n"
"WT lgkmcnt(12)\n"
"TR a[216:217],v14 offset:61440\n"
"MF v[60:63],a[220:223],a[144:147],v[60:63]\n"
"LD v3,s[52:55],s65 offen offset:1024 lds\n"
"MF v[92:95],a[220:223],a[148:151],v[92:95]\n"
"WT vmcnt(22) lgkmcnt(11)\n"
"TR a[218:219],v14 offset:63488\n"
"TR a[220:221],v15 offset:61440\n"
"MF v[32:35],a[192:195],a[152:155],v[32:35]\n"
"WT vmcnt(20)\n"
"WT vmcnt(18)\n"
"s_barrier\n"
"TR a[222:223],v15 offset:63488\n"
"MF v[64:67],a[192:195],a[156:159],v[64:67]\n"
"WT lgkmcnt(12)\n"
"TR a[192:193],v24 offset:0\n"
"MF v[36:39],a[196:199],a[152:155],v[36:39]\n"
"LD v4,s[52:55],s65 offen offset:2048 lds\n"
"MF v[68:71],a[196:199],a[156:159],v[68:71]\n"
"WT lgkmcnt(11)\n"
"TR a[194:195],v24 offset:2048\n"
"TR a[196:197],v25 offset:0\n"
"MF v[40:43],a[200:203],a[152:155],v[40:43]\n"
"TR a[198:199],v25 offset:2048\n"
"MF v[72:75],a[200:203],a[156:159],v[72:75]\n"
"WT lgkmcnt(12)\n"
"TR a[200:201],v26 offset:0\n"
"MF v[44:47],a[204:207],a[152:155],v[44:47]\n"
"LD v5,s[52:55],s65 offen offset:3072 lds\n"
"MF v[76:79],a[204:207],a[156:159],v[76:79]\n"
"WT lgkmcnt(11)\n"
"TR a[202:203],v26 offset:2048\n"
"TR a[204:205],v27 offset:0\n"
"MF v[48:51],a[208:211],a[152:155],v[48:51]\n"
"TR a[206:207],v27 offset:2048\n"
"MF v[80:83],a[208:211],a[156:159],v[80:83]\n"
"WT lgkmcnt(12)\n"
"TR a[208:209],v24 offset:4096\n"
"MF v[52:55],a[212:215],a[152:155],v[52:55]\n"
"TR a[210:211],v24 offset:6144\n"
"MF v[84:87],a[212:215],a[156:159],v[84:87]\n"
"WT lgkmcnt(12)\n"
"TR a[212:213],v25 offset:4096\n"
"MF v[56:59],a[216:219],a[152:155],v[56:59]\n"
"TR a[214:215],v25 offset:6144\n"
"MF v[88:91],a[216:219],a[156:159],v[88:91]\n"
"WT lgkmcnt(12)\n"
"TR a[216:217],v26 offset:4096\n"
"MF v[60:63],a[220:223],a[152:155],v[60:63]\n"
"TR a[218:219],v26 offset:6144\n"
"MF v[92:95],a[220:223],a[156:159],v[92:95]\n"
"WT vmcnt(19) lgkmcnt(12)\n"
"TR a[220:221],v27 offset:4096\n"
"MF v[32:35],a[192:195],a[160:163],v[32:35]\n"
"WT vmcnt(18)\n"
"TR a[222:223],v27 offset:6144\n"
"MF v[64:67],a[192:195],a[164:167],v[64:67]\n"
"WT lgkmcnt(12)\n"
"s_add_u32 s65, s65, s23\n"
"s_add_u32 m0, s22, 147456\n"
"TR a[192:193],v24 offset:8192\n"
"MF v[36:39],a[196:199],a[160:163],v[36:39]\n"
"LD v2,s[52:55],s65 offen offset:0 lds\n"
"MF v[68:71],a[196:199],a[164:167],v[68:71]\n"
"WT lgkmcnt(11)\n"
"TR a[194:195],v24 offset:10240\n"
"TR a[196:197],v25 offset:8192\n"
"MF v[40:43],a[200:203],a[160:163],v[40:43]\n"
"TR a[198:199],v25 offset:10240\n"
"MF v[72:75],a[200:203],a[164:167],v[72:75]\n"
"WT lgkmcnt(12)\n"
"TR a[200:201],v26 offset:8192\n"
"MF v[44:47],a[204:207],a[160:163],v[44:47]\n"
"LD v3,s[52:55],s65 offen offset:1024 lds\n"
"MF v[76:79],a[204:207],a[164:167],v[76:79]\n"
"WT lgkmcnt(11)\n"
"TR a[202:203],v26 offset:10240\n"
"TR a[204:205],v27 offset:8192\n"
"MF v[48:51],a[208:211],a[160:163],v[48:51]\n"
"TR a[206:207],v27 offset:10240\n"
"MF v[80:83],a[208:211],a[164:167],v[80:83]\n"
"WT lgkmcnt(12)\n"
"TR a[208:209],v24 offset:12288\n"
"MF v[52:55],a[212:215],a[160:163],v[52:55]\n"
"LD v4,s[52:55],s65 offen offset:2048 lds\n"
"MF v[84:87],a[212:215],a[164:167],v[84:87]\n"
"WT lgkmcnt(11)\n"
"TR a[210:211],v24 offset:14336\n"
"TR a[212:213],v25 offset:12288\n"
"MF v[56:59],a[216:219],a[160:163],v[56:59]\n"
"TR a[214:215],v25 offset:14336\n"
"MF v[88:91],a[216:219],a[164:167],v[88:91]\n"
"WT lgkmcnt(12)\n"
"TR a[216:217],v26 offset:12288\n"
"MF v[60:63],a[220:223],a[160:163],v[60:63]\n"
"LD v5,s[52:55],s65 offen offset:3072 lds\n"
"MF v[92:95],a[220:223],a[164:167],v[92:95]\n"
"WT vmcnt(21) lgkmcnt(11)\n"
"TR a[218:219],v26 offset:14336\n"
"TR a[220:221],v27 offset:12288\n"
"MF v[32:35],a[192:195],a[168:171],v[32:35]\n"
"WT vmcnt(19)\n"
"TR a[222:223],v27 offset:14336\n"
"MF v[64:67],a[192:195],a[172:175],v[64:67]\n"
"WT lgkmcnt(12)\n"
"s_add_u32 s65, s65, s23\n"
"s_add_u32 m0, s22, 0\n"
"TR a[192:193],v24 offset:16384\n"
"MF v[36:39],a[196:199],a[168:171],v[36:39]\n"
"LD v2,s[52:55],s65 offen offset:0 lds\n"
"MF v[68:71],a[196:199],a[172:175],v[68:71]\n"
"WT lgkmcnt(11)\n"
"TR a[194:195],v24 offset:18432\n"
"TR a[196:197],v25 offset:16384\n"
"MF v[40:43],a[200:203],a[168:171],v[40:43]\n"
"TR a[198:199],v25 offset:18432\n"
"MF v[72:75],a[200:203],a[172:175],v[72:75]\n"
"WT lgkmcnt(12)\n"
"TR a[200:201],v26 offset:16384\n"
"MF v[44:47],a[204:207],a[168:171],v[44:47]\n"
"LD v3,s[52:55],s65 offen offset:1024 lds\n"
"MF v[76:79],a[204:207],a[172:175],v[76:79]\n"
"WT lgkmcnt(11)\n"
"TR a[202:203],v26 offset:18432\n"
"TR a[204:205],v27 offset:16384\n"
"MF v[48:51],a[208:211],a[168:171],v[48:51]\n"
"TR a[206:207],v27 offset:18432\n"
"MF v[80:83],a[208:211],a[172:175],v[80:83]\n"
"WT lgkmcnt(12)\n"
"TR a[208:209],v24 offset:20480\n"
"MF v[52:55],a[212:215],a[168:171],v[52:55]\n"
"LD v4,s[52:55],s65 offen offset:2048 lds\n"
"MF v[84:87],a[212:215],a[172:175],v[84:87]\n"
"WT lgkmcnt(11)\n"
"TR a[210:211],v24 offset:22528\n"
"TR a[212:213],v25 offset:20480\n"
"MF v[56:59],a[216:219],a[168:171],v[56:59]\n"
"TR a[214:215],v25 offset:22528\n"
"MF v[88:91],a[216:219],a[172:175],v[88:91]\n"
"WT lgkmcnt(12)\n"
"TR a[216:217],v26 offset:20480\n"
"MF v[60:63],a[220:223],a[168:171],v[60:63]\n"
"LD v5,s[52:55],s65 offen offset:3072 lds\n"
"MF v[92:95],a[220:223],a[172:175],v[92:95]\n"
"WT vmcnt(21) lgkmcnt(11)\n"
"TR a[218:219],v26 offset:22528\n"
"TR a[220:221],v27 offset:20480\n"
"MF v[32:35],a[192:195],a[176:179],v[32:35]\n"
"WT vmcnt(19)\n"
"TR a[222:223],v27 offset:22528\n"
"MF v[64:67],a[192:195],a[180:183],v[64:67]\n"
"WT lgkmcnt(12)\n"
"s_add_u32 s65, s65, s23\n"
"s_add_u32 m0, s22, 16384\n"
"TR a[192:193],v24 offset:24576\n"
"MF v[36:39],a[196:199],a[176:179],v[36:39]\n"
"LD v2,s[52:55],s65 offen offset:0 lds\n"
"MF v[68:71],a[196:199],a[180:183],v[68:71]\n"
"WT lgkmcnt(11)\n"
"TR a[194:195],v24 offset:26624\n"
"TR a[196:197],v25 offset:24576\n"
"MF v[40:43],a[200:203],a[176:179],v[40:43]\n"
"TR a[198:199],v25 offset:26624\n"
"MF v[72:75],a[200:203],a[180:183],v[72:75]\n"
"WT lgkmcnt(12)\n"
"TR a[200:201],v26 offset:24576\n"
"MF v[44:47],a[204:207],a[176:179],v[44:47]\n"
"LD v3,s[52:55],s65 offen offset:1024 lds\n"
"MF v[76:79],a[204:207],a[180:183],v[76:79]\n"
"WT lgkmcnt(11)\n"
"TR a[202:203],v26 offset:26624\n"
"TR a[204:205],v27 offset:24576\n"
"MF v[48:51],a[208:211],a[176:179],v[48:51]\n"
"buffer_load_dwordx2 v[18:19], v20, s[40:43], 0 offen sc1\n"
"TR a[206:207],v27 offset:26624\n"
"MF v[80:83],a[208:211],a[180:183],v[80:83]\n"
"WT lgkmcnt(12)\n"
"TR a[208:209],v24 offset:28672\n"
"MF v[52:55],a[212:215],a[176:179],v[52:55]\n"
"LD v4,s[52:55],s65 offen offset:2048 lds\n"
"MF v[84:87],a[212:215],a[180:183],v[84:87]\n"
"WT lgkmcnt(11)\n"
"TR a[210:211],v24 offset:30720\n"
"TR a[212:213],v25 offset:28672\n"
"MF v[56:59],a[216:219],a[176:179],v[56:59]\n"
"TR a[214:215],v25 offset:30720\n"
"MF v[88:91],a[216:219],a[180:183],v[88:91]\n"
"WT lgkmcnt(12)\n"
"TR a[216:217],v26 offset:28672\n"
"MF v[60:63],a[220:223],a[176:179],v[60:63]\n"
"LD v5,s[52:55],s65 offen offset:3072 lds\n"
"MF v[92:95],a[220:223],a[180:183],v[92:95]\n"
"WT vmcnt(22) lgkmcnt(11)\n"
"TR a[218:219],v26 offset:30720\n"
"TR a[220:221],v27 offset:28672\n"
"MF v[32:35],a[192:195],a[184:187],v[32:35]\n"
"WT vmcnt(20)\n"
"TR a[222:223],v27 offset:30720\n"
"MF v[64:67],a[192:195],a[188:191],v[64:67]\n"
"WT lgkmcnt(12)\n"
"s_add_u32 s65, s65, s23\n"
"s_add_u32 m0, s22, 32768\n"
"MF v[36:39],a[196:199],a[184:187],v[36:39]\n"
"LD v2,s[52:55],s65 offen offset:0 lds\n"
"MF v[68:71],a[196:199],a[188:191],v[68:71]\n"
"WT lgkmcnt(10)\n"
"MF v[40:43],a[200:203],a[184:187],v[40:43]\n"
"MF v[72:75],a[200:203],a[188:191],v[72:75]\n"
"WT lgkmcnt(8)\n"
"MF v[44:47],a[204:207],a[184:187],v[44:47]\n"
"LD v3,s[52:55],s65 offen offset:1024 lds\n"
"MF v[76:79],a[204:207],a[188:191],v[76:79]\n"
"WT lgkmcnt(6)\n"
"MF v[48:51],a[208:211],a[184:187],v[48:51]\n"
"MF v[80:83],a[208:211],a[188:191],v[80:83]\n"
"WT lgkmcnt(4)\n"
"MF v[52:55],a[212:215],a[184:187],v[52:55]\n"
"LD v4,s[52:55],s65 offen offset:2048 lds\n"
"MF v[84:87],a[212:215],a[188:191],v[84:87]\n"
"WT lgkmcnt(2)\n"
"MF v[56:59],a[216:219],a[184:187],v[56:59]\n"
"MF v[88:91],a[216:219],a[188:191],v[88:91]\n"
"WT lgkmcnt(0)\n"
"MF v[60:63],a[220:223],a[184:187],v[60:63]\n"
"LD v5,s[52:55],s65 offen offset:3072 lds\n"
"MF v[92:95],a[220:223],a[188:191],v[92:95]\n"
"WT vmcnt(6)\n"
"s_barrier\n"
"s_mov_b32 s50, 0\n"
"s_cmp_ge_u32 s37, 20\n"
"s_cbranch_scc1 L_gt_33\n"
"v_cmp_ne_u64 vcc, s[38:39], v[18:19]\n"
"s_nop 1\n"
"s_cmp_eq_u64 vcc, 0\n"
"s_cbranch_scc0 L_psp_43\n"
"L_pok_42:\n"
"s_getpc_b64 s[56:57]\n"
"L_pc_44:\n"
"s_and_b32 s57, s57, 0xffff\n"
"s_mov_b32 s58, L_codeend-L_pc_44\n"
"s_mov_b32 s59, 0x20000\n"
"v_and_b32 v23, 7, v0\n"
"v_lshlrev_b32 v23, 7, v23\n"
"s_sub_u32 s64, s37, 0\n"
"s_lshl_b32 s64, s64, 2\n"
"s_add_u32 s64, s64, s34\n"
"s_lshl_b32 s64, s64, 10\n"
"buffer_load_dword v140, v23, s[56:59], s64 offen\n"
"s_lshl_b32 s56, s70, 2\n"
"s_add_u32 s56, s56, s34\n"
"s_mul_i32 s56, s56, 110592\n"
"s_lshl_b32 s57, s71, 12\n"
"s_add_u32 s56, s56, s57\n"
"s_add_u32 s57, s56, 0x1100000\n"
"s_add_u32 s58, s57, 55296\n"
"v_pk_add_f32 v[128:129], v[128:129], v[130:131]\n"
"v_pk_add_f32 v[128:129], v[128:129], v[132:133]\n"
"v_pk_add_f32 v[128:129], v[128:129], v[134:135]\n"
"v_pk_add_f32 v[128:129], v[128:129], v[136:137]\n"
"v_mov_b64 v[130:131], v[128:129]\n"
"s_nop 1\n"
"v_permlane32_swap_b32 v128, v130\n"
"v_permlane32_swap_b32 v129, v131\n"
"v_pk_add_f32 v[128:129], v[128:129], v[130:131]\n"
"v_mul_f32 v128, 0x3acccccd, v128\n"
"v_mul_f32 v129, 0x3acccccd, v129\n"
"v_fma_f32 v129, -v128, v128, v129\n"
"v_max_f32 v129, 0, v129\n"
"v_add_f32 v129, 0x3727c5ac, v129\n"
"v_rsq_f32 v130, v129\n"
"s_nop 0\n"
"v_mul_f32 v131, v128, v130\n"
"v_sub_f32 v131, 0, v131\n"
"v_mov_b64 v[132:133], v[130:131]\n"
"s_nop 1\n"
"v_permlane16_swap_b32 v130, v132\n"
"v_permlane16_swap_b32 v131, v133\n"
"s_cmp_eq_u32 s71, 13\n"
"s_cselect_b32 s78, 0, s78\n"
"v_pk_mul_f32 v[32:33], v[32:33], v[130:131] op_sel_hi:[1,0]\n"
"v_pk_mul_f32 v[34:35], v[34:35], v[130:131] op_sel_hi:[1,0]\n"
"v_pk_mul_f32 v[36:37], v[36:37], v[130:131] op_sel_hi:[1,0]\n"
"v_pk_mul_f32 v[38:39], v[38:39], v[130:131] op_sel_hi:[1,0]\n"
"v_pk_mul_f32 v[40:41], v[40:41], v[130:131] op_sel_hi:[1,0]\n"
"v_pk_mul_f32 v[42:43], v[42:43], v[130:131] op_sel_hi:[1,0]\n"
"v_pk_mul_f32 v[44:45], v[44:45], v[130:131] op_sel_hi:[1,0]\n"
"v_pk_mul_f32 v[46:47], v[46:47], v[130:131] op_sel_hi:[1,0]\n"
"v_pk_fma_f32 v[32:33], v[96:97], v[130:131], v[32:33] op_sel:[0,1,0] op_sel_hi:[1,1,1]\n"
"v_pk_fma_f32 v[34:35], v[98:99], v[130:131], v[34:35] op_sel:[0,1,0] op_sel_hi:[1,1,1]\n"
"v_pk_fma_f32 v[36:37], v[100:101], v[130:131], v[36:37] op_sel:[0,1,0] op_sel_hi:[1,1,1]\n"
"v_pk_fma_f32 v[38:39], v[102:103], v[130:131], v[38:39] op_sel:[0,1,0] op_sel_hi:[1,1,1]\n"
"v_pk_fma_f32 v[40:41], v[104:105], v[130:131], v[40:41] op_sel:[0,1,0] op_sel_hi:[1,1,1]\n"
"v_pk_fma_f32 v[42:43], v[106:107], v[130:131], v[42:43] op_sel:[0,1,0] op_sel_hi:[1,1,1]\n"
"v_pk_fma_f32 v[44:45], v[108:109], v[130:131], v[44:45] op_sel:[0,1,0] op_sel_hi:[1,1,1]\n"
"v_pk_fma_f32 v[46:47], v[110:111], v[130:131], v[46:47] op_sel:[0,1,0] op_sel_hi:[1,1,1]\n"
"v_pk_mul_f32 v[192:193], v[32:33], v[32:33]\n"
"v_pk_mul_f32 v[194:195], v[34:35], v[34:35]\n"
"v_pk_mul_f32 v[196:197], v[36:37], v[36:37]\n"
"v_pk_mul_f32 v[198:199], v[38:39], v[38:39]\n"
"v_pk_mul_f32 v[200:201], v[40:41], v[40:41]\n"
"v_pk_mul_f32 v[202:203], v[42:43], v[42:43]\n"
"v_pk_mul_f32 v[204:205], v[44:45], v[44:45]\n"
"v_pk_mul_f32 v[206:207], v[46:47], v[46:47]\n"
"v_pk_fma_f32 v[192:193], v[192:193], s[74:75], s[74:75] op_sel:[0,0,1] op_sel_hi:[1,0,1]\n"
"v_pk_fma_f32 v[194:195], v[194:195], s[74:75], s[74:75] op_sel:[0,0,1] op_sel_hi:[1,0,1]\n"
"v_pk_fma_f32 v[196:197], v[196:197], s[74:75], s[74:75] op_sel:[0,0,1] op_sel_hi:[1,0,1]\n"
"v_pk_fma_f32 v[198:199], v[198:199], s[74:75], s[74:75] op_sel:[0,0,1] op_sel_hi:[1,0,1]\n"
"s_mov_b32 s60, 0\n"
"v_pk_fma_f32 v[200:201], v[200:201], s[74:75], s[74:75] op_sel:[0,0,1] op_sel_hi:[1,0,1]\n"
"LD a[0:3],v1,s[44:47],s60 offen offset:0 sc1\n"
"v_pk_fma_f32 v[202:203], v[202:203], s[74:75], s[74:75] op_sel:[0,0,1] op_sel_hi:[1,0,1]\n"
"v_pk_fma_f32 v[204:205], v[204:205], s[74:75], s[74:75] op_sel:[0,0,1] op_sel_hi:[1,0,1]\n"
"v_pk_fma_f32 v[206:207], v[206:207], s[74:75], s[74:75] op_sel:[0,0,1] op_sel_hi:[1,0,1]\n"
"v_pk_mul_f32 v[192:193], v[192:193], v[32:33]\n"
"v_pk_mul_f32 v[194:195], v[194:195], v[34:35]\n"
"v_pk_mul_f32 v[196:197], v[196:197], v[36:37]\n"
"v_pk_mul_f32 v[198:199], v[198:199], v[38:39]\n"
"v_pk_mul_f32 v[200:201], v[200:201], v[40:41]\n"
"v_pk_mul_f32 v[202:203], v[202:203], v[42:43]\n"
"v_pk_mul_f32 v[204:205], v[204:205], v[44:45]\n"
"v_pk_mul_f32 v[206:207], v[206:207], v[46:47]\n"
"EX v192,v192\n"
"s_mov_b32 s61, 4096\n"
"EX v193,v193\n"
"LD a[4:7],v1,s[44:47],s61 offen offset:0 sc1\n"
"EX v194,v194\n"
"EX v195,v195\n"
"EX v196,v196\n"
"EX v197,v197\n"
"EX v198,v198\n"
"EX v199,v199\n"
"EX v200,v200\n"
"EX v201,v201\n"
"EX v202,v202\n"
"EX v203,v203\n"
"EX v204,v204\n"
"EX v205,v205\n"
"s_mov_b32 s62, 8192\n"
"EX v206,v206\n"
"LD a[8:11],v1,s[44:47],s62 offen offset:0 sc1\n"
"EX v207,v207\n"
"v_pk_add_f32 v[192:193], v[192:193], 1.0 op_sel_hi:[1,0]\n"
"v_pk_add_f32 v[194:195], v[194:195], 1.0 op_sel_hi:[1,0]\n"
"v_pk_add_f32 v[196:197], v[196:197], 1.0 op_sel_hi:[1,0]\n"
"v_pk_add_f32 v[198:199], v[198:199], 1.0 op_sel_hi:[1,0]\n"
"v_pk_add_f32 v[200:201], v[200:201], 1.0 op_sel_hi:[1,0]\n"
"v_pk_add_f32 v[202:203], v[202:203], 1.0 op_sel_hi:[1,0]\n"
"v_pk_add_f32 v[204:205], v[204:205], 1.0 op_sel_hi:[1,0]\n"
"v_pk_add_f32 v[206:207], v[206:207], 1.0 op_sel_hi:[1,0]\n"
"v_rcp_f32 v192, v192\n"
"v_rcp_f32 v193, v193\n"
"v_rcp_f32 v194, v194\n"
"s_mov_b32 s63, 12288\n"
"v_rcp_f32 v195, v195\n"
"LD a[12:15],v1,s[44:47],s63 offen offset:0 sc1\n"
"v_rcp_f32 v196, v196\n"
"v_rcp_f32 v197, v197\n"
"v_rcp_f32 v198, v198\n"
"v_rcp_f32 v199, v199\n"
"v_rcp_f32 v200, v200\n"
"v_rcp_f32 v201, v201\n"
"v_rcp_f32 v202, v202\n"
"v_rcp_f32 v203, v203\n"
"v_rcp_f32 v204, v204\n"
"v_rcp_f32 v205, v205\n"
"v_rcp_f32 v206, v206\n"
"v_rcp_f32 v207, v207\n"
"v_pk_mul_f32 v[32:33], v[32:33], v[192:193]\n"
"LD a[16:19],v1,s[44:47],s60 offen offset:1024 sc1\n"
"v_pk_mul_f32 v[34:35], v[34:35], v[194:195]\n"
"v_pk_mul_f32 v[36:37], v[36:37], v[196:197]\n"
"v_pk_mul_f32 v[38:39], v[38:39], v[198:199]\n"
"v_pk_mul_f32 v[40:41], v[40:41], v[200:201]\n"
"v_pk_mul_f32 v[42:43], v[42:43], v[202:203]\n"
"v_pk_mul_f32 v[44:45], v[44:45], v[204:205]\n"
"v_pk_mul_f32 v[46:47], v[46:47], v[206:207]\n"
"CV v32,v32,v33\n"
"CV v33,v34,v35\n"
"CV v34,v36,v37\n"
"CV v35,v38,v39\n"
"ST v[32:35],v1,s[40:43],s57 offen offset:0 sc1\n"
"CV v40,v40,v41\n"
"CV v41,v42,v43\n"
"CV v42,v44,v45\n"
"CV v43,v46,v47\n"
"ST v[40:43],v1,s[40:43],s57 offen offset:1024 sc1\n"
"v_pk_mul_f32 v[64:65], v[64:65], v[132:133] op_sel_hi:[1,0]\n"
"v_pk_mul_f32 v[66:67], v[66:67], v[132:133] op_sel_hi:[1,0]\n"
"v_pk_mul_f32 v[68:69], v[68:69], v[132:133] op_sel_hi:[1,0]\n"
"v_pk_mul_f32 v[70:71], v[70:71], v[132:133] op_sel_hi:[1,0]\n"
"v_pk_mul_f32 v[72:73], v[72:73], v[132:133] op_sel_hi:[1,0]\n"
"v_pk_mul_f32 v[74:75], v[74:75], v[132:133] op_sel_hi:[1,0]\n"
"v_pk_mul_f32 v[76:77], v[76:77], v[132:133] op_sel_hi:[1,0]\n"
"v_pk_mul_f32 v[78:79], v[78:79], v[132:133] op_sel_hi:[1,0]\n"
"v_pk_fma_f32 v[64:65], v[96:97], v[132:133], v[64:65] op_sel:[0,1,0] op_sel_hi:[1,1,1]\n"
"v_pk_fma_f32 v[66:67], v[98:99], v[132:133], v[66:67] op_sel:[0,1,0] op_sel_hi:[1,1,1]\n"
"v_pk_fma_f32 v[68:69], v[100:101], v[132:133], v[68:69] op_sel:[0,1,0] op_sel_hi:[1,1,1]\n"
"v_pk_fma_f32 v[70:71], v[102:103], v[132:133], v[70:71] op_sel:[0,1,0] op_sel_hi:[1,1,1]\n"
"v_pk_fma_f32 v[72:73], v[104:105], v[132:133], v[72:73] op_sel:[0,1,0] op_sel_hi:[1,1,1]\n"
"v_pk_fma_f32 v[74:75], v[106:107], v[132:133], v[74:75] op_sel:[0,1,0] op_sel_hi:[1,1,1]\n"
"v_pk_fma_f32 v[76:77], v[108:109], v[132:133], v[76:77] op_sel:[0,1,0] op_sel_hi:[1,1,1]\n"
"v_pk_fma_f32 v[78:79], v[110:111], v[132:133], v[78:79] op_sel:[0,1,0] op_sel_hi:[1,1,1]\n"
"v_pk_mul_f32 v[192:193], v[64:65], v[64:65]\n"
"v_pk_mul_f32 v[194:195], v[66:67], v[66:67]\n"
"v_pk_mul_f32 v[196:197], v[68:69], v[68:69]\n"
"v_pk_mul_f32 v[198:199], v[70:71], v[70:71]\n"
"v_pk_mul_f32 v[200:201], v[72:73], v[72:73]\n"
"v_pk_mul_f32 v[202:203], v[74:75], v[74:75]\n"
"v_pk_mul_f32 v[204:205], v[76:77], v[76:77]\n"
"v_pk_mul_f32 v[206:207], v[78:79], v[78:79]\n"
"v_pk_fma_f32 v[192:193], v[192:193], s[74:75], s[74:75] op_sel:[0,0,1] op_sel_hi:[1,0,1]\n"
"v_pk_fma_f32 v[194:195], v[194:195], s[74:75], s[74:75] op_sel:[0,0,1] op_sel_hi:[1,0,1]\n"
"v_pk_fma_f32 v[196:197], v[196:197], s[74:75], s[74:75] op_sel:[0,0,1] op_sel_hi:[1,0,1]\n"
"v_pk_fma_f32 v[198:199], v[198:199], s[74:75], s[74:75] op_sel:[0,0,1] op_sel_hi:[1,0,1]\n"
"v_pk_fma_f32 v[200:201], v[200:201], s[74:75], s[74:75] op_sel:[0,0,1] op_sel_hi:[1,0,1]\n"
"LD a[20:23],v1,s[44:47],s61 offen offset:1024 sc1\n"
"v_pk_fma_f32 v[202:203], v[202:203], s[74:75], s[74:75] op_sel:[0,0,1] op_sel_hi:[1,0,1]\n"
"v_pk_fma_f32 v[204:205], v[204:205], s[74:75], s[74:75] op_sel:[0,0,1] op_sel_hi:[1,0,1]\n"
"v_pk_fma_f32 v[206:207], v[206:207], s[74:75], s[74:75] op_sel:[0,0,1] op_sel_hi:[1,0,1]\n"
"v_pk_mul_f32 v[192:193], v[192:193], v[64:65]\n"
"v_pk_mul_f32 v[194:195], v[194:195], v[66:67]\n"
"v_pk_mul_f32 v[196:197], v[196:197], v[68:69]\n"
"v_pk_mul_f32 v[198:199], v[198:199], v[70:71]\n"
"v_pk_mul_f32 v[200:201], v[200:201], v[72:73]\n"
"v_pk_mul_f32 v[202:203], v[202:203], v[74:75]\n"
"v_pk_mul_f32 v[204:205], v[204:205], v[76:77]\n"
"v_pk_mul_f32 v[206:207], v[206:207], v[78:79]\n"
"EX v192,v192\n"
"EX v193,v193\n"
"LD a[24:27],v1,s[44:47],s62 offen offset:1024 sc1\n"
"EX v194,v194\n"
"EX v195,v195\n"
"EX v196,v196\n"
"EX v197,v197\n"
"EX v198,v198\n"
"EX v199,v199\n"
"EX v200,v200\n"
"EX v201,v201\n"
"EX v202,v202\n"
"EX v203,v203\n"
"EX v204,v204\n"
"EX v205,v205\n"
"EX v206,v206\n"
"LD a[28:31],v1,s[44:47],s63 offen offset:1024 sc1\n"
"EX v207,v207\n"
"v_pk_add_f32 v[192:193], v[192:193], 1.0 op_sel_hi:[1,0]\n"
"v_pk_add_f32 v[194:195], v[194:195], 1.0 op_sel_hi:[1,0]\n"
"v_pk_add_f32 v[196:197], v[196:197], 1.0 op_sel_hi:[1,0]\n"
"v_pk_add_f32 v[198:199], v[198:199], 1.0 op_sel_hi:[1,0]\n"
"v_pk_add_f32 v[200:201], v[200:201], 1.0 op_sel_hi:[1,0]\n"
"v_pk_add_f32 v[202:203], v[202:203], 1.0 op_sel_hi:[1,0]\n"
"v_pk_add_f32 v[204:205], v[204:205], 1.0 op_sel_hi:[1,0]\n"
"v_pk_add_f32 v[206:207], v[206:207], 1.0 op_sel_hi:[1,0]\n"
"v_rcp_f32 v192, v192\n"
"v_rcp_f32 v193, v193\n"
"v_rcp_f32 v194, v194\n"
"v_rcp_f32 v195, v195\n"
"LD a[32:35],v1,s[44:47],s60 offen offset:2048 sc1\n"
"v_rcp_f32 v196, v196\n"
"v_rcp_f32 v197, v197\n"
"v_rcp_f32 v198, v198\n"
"v_rcp_f32 v199, v199\n"
"v_rcp_f32 v200, v200\n"
"v_rcp_f32 v201, v201\n"
"v_rcp_f32 v202, v202\n"
"v_rcp_f32 v203, v203\n"
"v_rcp_f32 v204, v204\n"
"v_rcp_f32 v205, v205\n"
"v_rcp_f32 v206, v206\n"
"v_rcp_f32 v207, v207\n"
"v_pk_mul_f32 v[64:65], v[64:65], v[192:193]\n"
"LD a[36:39],v1,s[44:47],s61 offen offset:2048 sc1\n"
"v_pk_mul_f32 v[66:67], v[66:67], v[194:195]\n"
"v_pk_mul_f32 v[68:69], v[68:69], v[196:197]\n"
"v_pk_mul_f32 v[70:71], v[70:71], v[198:199]\n"
"v_pk_mul_f32 v[72:73], v[72:73], v[200:201]\n"
"v_pk_mul_f32 v[74:75], v[74:75], v[202:203]\n"
"v_pk_mul_f32 v[76:77], v[76:77], v[204:205]\n"
"v_pk_mul_f32 v[78:79], v[78:79], v[206:207]\n"
"CV v64,v64,v65\n"
"CV v65,v66,v67\n"
"CV v66,v68,v69\n"
"CV v67,v70,v71\n"
"ST v[64:67],v1,s[40:43],s58 offen offset:0 sc1\n"
"CV v72,v72,v73\n"
"CV v73,v74,v75\n"
"CV v74,v76,v77\n"
"CV v75,v78,v79\n"
"ST v[72:75],v1,s[40:43],s58 offen offset:1024 sc1\n"
"v_pk_mul_f32 v[48:49], v[48:49], v[130:131] op_sel_hi:[1,0]\n"
"v_pk_mul_f32 v[50:51], v[50:51], v[130:131] op_sel_hi:[1,0]\n"
"v_pk_mul_f32 v[52:53], v[52:53], v[130:131] op_sel_hi:[1,0]\n"
"v_pk_mul_f32 v[54:55], v[54:55], v[130:131] op_sel_hi:[1,0]\n"
"v_pk_mul_f32 v[56:57], v[56:57], v[130:131] op_sel_hi:[1,0]\n"
"v_pk_mul_f32 v[58:59], v[58:59], v[130:131] op_sel_hi:[1,0]\n"
"v_pk_mul_f32 v[60:61], v[60:61], v[130:131] op_sel_hi:[1,0]\n"
"v_pk_mul_f32 v[62:63], v[62:63], v[130:131] op_sel_hi:[1,0]\n"
"v_pk_fma_f32 v[48:49], v[112:113], v[130:131], v[48:49] op_sel:[0,1,0] op_sel_hi:[1,1,1]\n"
"v_pk_fma_f32 v[50:51], v[114:115], v[130:131], v[50:51] op_sel:[0,1,0] op_sel_hi:[1,1,1]\n"
"v_pk_fma_f32 v[52:53], v[116:117], v[130:131], v[52:53] op_sel:[0,1,0] op_sel_hi:[1,1,1]\n"
"v_pk_fma_f32 v[54:55], v[118:119], v[130:131], v[54:55] op_sel:[0,1,0] op_sel_hi:[1,1,1]\n"
"v_pk_fma_f32 v[56:57], v[120:121], v[130:131], v[56:57] op_sel:[0,1,0] op_sel_hi:[1,1,1]\n"
"v_pk_fma_f32 v[58:59], v[122:123], v[130:131], v[58:59] op_sel:[0,1,0] op_sel_hi:[1,1,1]\n"
"v_pk_fma_f32 v[60:61], v[124:125], v[130:131], v[60:61] op_sel:[0,1,0] op_sel_hi:[1,1,1]\n"
"v_pk_fma_f32 v[62:63], v[126:127], v[130:131], v[62:63] op_sel:[0,1,0] op_sel_hi:[1,1,1]\n"
"v_pk_mul_f32 v[192:193], v[48:49], v[48:49]\n"
"v_pk_mul_f32 v[194:195], v[50:51], v[50:51]\n"
"v_pk_mul_f32 v[196:197], v[52:53], v[52:53]\n"
"v_pk_mul_f32 v[198:199], v[54:55], v[54:55]\n"
"v_pk_mul_f32 v[200:201], v[56:57], v[56:57]\n"
"v_pk_mul_f32 v[202:203], v[58:59], v[58:59]\n"
"v_pk_mul_f32 v[204:205], v[60:61], v[60:61]\n"
"v_pk_mul_f32 v[206:207], v[62:63], v[62:63]\n"
"v_pk_fma_f32 v[192:193], v[192:193], s[74:75], s[74:75] op_sel:[0,0,1] op_sel_hi:[1,0,1]\n"
"v_pk_fma_f32 v[194:195], v[194:195], s[74:75], s[74:75] op_sel:[0,0,1] op_sel_hi:[1,0,1]\n"
"v_pk_fma_f32 v[196:197], v[196:197], s[74:75], s[74:75] op_sel:[0,0,1] op_sel_hi:[1,0,1]\n"
"v_pk_fma_f32 v[198:199], v[198:199], s[74:75], s[74:75] op_sel:[0,0,1] op_sel_hi:[1,0,1]\n"
"v_pk_fma_f32 v[200:201], v[200:201], s[74:75], s[74:75] op_sel:[0,0,1] op_sel_hi:[1,0,1]\n"
"LD a[40:43],v1,s[44:47],s62 offen offset:2048 sc1\n"
"v_pk_fma_f32 v[202:203], v[202:203], s[74:75], s[74:75] op_sel:[0,0,1] op_sel_hi:[1,0,1]\n"
"v_pk_fma_f32 v[204:205], v[204:205], s[74:75], s[74:75] op_sel:[0,0,1] op_sel_hi:[1,0,1]\n"
"v_pk_fma_f32 v[206:207], v[206:207], s[74:75], s[74:75] op_sel:[0,0,1] op_sel_hi:[1,0,1]\n"
"v_pk_mul_f32 v[192:193], v[192:193], v[48:49]\n"
"v_pk_mul_f32 v[194:195], v[194:195], v[50:51]\n"
"v_pk_mul_f32 v[196:197], v[196:197], v[52:53]\n"
"v_pk_mul_f32 v[198:199], v[198:199], v[54:55]\n"
"v_pk_mul_f32 v[200:201], v[200:201], v[56:57]\n"
"v_pk_mul_f32 v[202:203], v[202:203], v[58:59]\n"
"v_pk_mul_f32 v[204:205], v[204:205], v[60:61]\n"
"v_pk_mul_f32 v[206:207], v[206:207], v[62:63]\n"
"EX v192,v192\n"
"EX v193,v193\n"
"LD a[44:47],v1,s[44:47],s63 offen offset:2048 sc1\n"
"EX v194,v194\n"
"EX v195,v195\n"
"EX v196,v196\n"
"EX v197,v197\n"
"EX v198,v198\n"
"EX v199,v199\n"
"EX v200,v200\n"
"EX v201,v201\n"
"EX v202,v202\n"
"EX v203,v203\n"
"EX v204,v204\n"
"EX v205,v205\n"
"EX v206,v206\n"
"LD a[48:51],v1,s[44:47],s60 offen offset:3072 sc1\n"
"EX v207,v207\n"
"v_pk_add_f32 v[192:193], v[192:193], 1.0 op_sel_hi:[1,0]\n"
"v_pk_add_f32 v[194:195], v[194:195], 1.0 op_sel_hi:[1,0]\n"
"v_pk_add_f32 v[196:197], v[196:197], 1.0 op_sel_hi:[1,0]\n"
"v_pk_add_f32 v[198:199], v[198:199], 1.0 op_sel_hi:[1,0]\n"
"v_pk_add_f32 v[200:201], v[200:201], 1.0 op_sel_hi:[1,0]\n"
"v_pk_add_f32 v[202:203], v[202:203], 1.0 op_sel_hi:[1,0]\n"
"v_pk_add_f32 v[204:205], v[204:205], 1.0 op_sel_hi:[1,0]\n"
"v_pk_add_f32 v[206:207], v[206:207], 1.0 op_sel_hi:[1,0]\n"
"v_rcp_f32 v192, v192\n"
"v_rcp_f32 v193, v193\n"
"v_rcp_f32 v194, v194\n"
"v_rcp_f32 v195, v195\n"
"LD a[52:55],v1,s[44:47],s61 offen offset:3072 sc1\n"
"v_rcp_f32 v196, v196\n"
"v_rcp_f32 v197, v197\n"
"v_rcp_f32 v198, v198\n"
"v_rcp_f32 v199, v199\n"
"v_rcp_f32 v200, v200\n"
"v_rcp_f32 v201, v201\n"
"v_rcp_f32 v202, v202\n"
"v_rcp_f32 v203, v203\n"
"v_rcp_f32 v204, v204\n"
"v_rcp_f32 v205, v205\n"
"v_rcp_f32 v206, v206\n"
"v_rcp_f32 v207, v207\n"
"v_pk_mul_f32 v[48:49], v[48:49], v[192:193]\n"
"LD a[56:59],v1,s[44:47],s62 offen offset:3072 sc1\n"
"v_pk_mul_f32 v[50:51], v[50:51], v[194:195]\n"
"v_pk_mul_f32 v[52:53], v[52:53], v[196:197]\n"
"v_pk_mul_f32 v[54:55], v[54:55], v[198:199]\n"
"v_pk_mul_f32 v[56:57], v[56:57], v[200:201]\n"
"v_pk_mul_f32 v[58:59], v[58:59], v[202:203]\n"
"v_pk_mul_f32 v[60:61], v[60:61], v[204:205]\n"
"v_pk_mul_f32 v[62:63], v[62:63], v[206:207]\n"
"WT vmcnt(5)\n"
"s_lshl_b32 s56, s33, 2\n"
"s_add_u32 s56, s56, s34\n"
"s_lshl_b32 s56, s56, 7\n"
"s_add_u32 s56, s56, 0xc0000\n"
"v_lshlrev_b32 v21, 3, v0\n"
"s_mov_b64 exec, 1\n"
"buffer_store_dwordx2 v[16:17], v21, s[40:43], s56 offen sc1\n"
"s_mov_b64 exec, -1\n"
"CV v48,v48,v49\n"
"CV v49,v50,v51\n"
"CV v50,v52,v53\n"
"CV v51,v54,v55\n"
"ST v[48:51],v1,s[76:79],s57 offen offset:2048 sc1\n"
"CV v56,v56,v57\n"
"CV v57,v58,v59\n"
"CV v58,v60,v61\n"
"CV v59,v62,v63\n"
"ST v[56:59],v1,s[76:79],s57 offen offset:3072 sc1\n"
"v_pk_mul_f32 v[80:81], v[80:81], v[132:133] op_sel_hi:[1,0]\n"
"v_pk_mul_f32 v[82:83], v[82:83], v[132:133] op_sel_hi:[1,0]\n"
"v_pk_mul_f32 v[84:85], v[84:85], v[132:133] op_sel_hi:[1,0]\n"
"v_pk_mul_f32 v[86:87], v[86:87], v[132:133] op_sel_hi:[1,0]\n"
"v_pk_mul_f32 v[88:89], v[88:89], v[132:133] op_sel_hi:[1,0]\n"
"v_pk_mul_f32 v[90:91], v[90:91], v[132:133] op_sel_hi:[1,0]\n"
"v_pk_mul_f32 v[92:93], v[92:93], v[132:133] op_sel_hi:[1,0]\n"
"v_pk_mul_f32 v[94:95], v[94:95], v[132:133] op_sel_hi:[1,0]\n"
"v_pk_fma_f32 v[80:81], v[112:113], v[132:133], v[80:81] op_sel:[0,1,0] op_sel_hi:[1,1,1]\n"
"v_pk_fma_f32 v[82:83], v[114:115], v[132:133], v[82:83] op_sel:[0,1,0] op_sel_hi:[1,1,1]\n"
"v_pk_fma_f32 v[84:85], v[116:117], v[132:133], v[84:85] op_sel:[0,1,0] op_sel_hi:[1,1,1]\n"
"v_pk_fma_f32 v[86:87], v[118:119], v[132:133], v[86:87] op_sel:[0,1,0] op_sel_hi:[1,1,1]\n"
"v_pk_fma_f32 v[88:89], v[120:121], v[132:133], v[88:89] op_sel:[0,1,0] op_sel_hi:[1,1,1]\n"
"v_pk_fma_f32 v[90:91], v[122:123], v[132:133], v[90:91] op_sel:[0,1,0] op_sel_hi:[1,1,1]\n"
"v_pk_fma_f32 v[92:93], v[124:125], v[132:133], v[92:93] op_sel:[0,1,0] op_sel_hi:[1,1,1]\n"
"v_pk_fma_f32 v[94:95], v[126:127], v[132:133], v[94:95] op_sel:[0,1,0] op_sel_hi:[1,1,1]\n"
"v_pk_mul_f32 v[192:193], v[80:81], v[80:81]\n"
"v_pk_mul_f32 v[194:195], v[82:83], v[82:83]\n"
"v_pk_mul_f32 v[196:197], v[84:85], v[84:85]\n"
"v_pk_mul_f32 v[198:199], v[86:87], v[86:87]\n"
"v_pk_mul_f32 v[200:201], v[88:89], v[88:89]\n"
"v_pk_mul_f32 v[202:203], v[90:91], v[90:91]\n"
"v_pk_mul_f32 v[204:205], v[92:93], v[92:93]\n"
"v_pk_mul_f32 v[206:207], v[94:95], v[94:95]\n"
"v_pk_fma_f32 v[192:193], v[192:193], s[74:75], s[74:75] op_sel:[0,0,1] op_sel_hi:[1,0,1]\n"
"v_pk_fma_f32 v[194:195], v[194:195], s[74:75], s[74:75] op_sel:[0,0,1] op_sel_hi:[1,0,1]\n"
"v_pk_fma_f32 v[196:197], v[196:197], s[74:75], s[74:75] op_sel:[0,0,1] op_sel_hi:[1,0,1]\n"
"v_pk_fma_f32 v[198:199], v[198:199], s[74:75], s[74:75] op_sel:[0,0,1] op_sel_hi:[1,0,1]\n"
"v_pk_fma_f32 v[200:201], v[200:201], s[74:75], s[74:75] op_sel:[0,0,1] op_sel_hi:[1,0,1]\n"
"LD a[60:63],v1,s[44:47],s63 offen offset:3072 sc1\n"
"v_pk_fma_f32 v[202:203], v[202:203], s[74:75], s[74:75] op_sel:[0,0,1] op_sel_hi:[1,0,1]\n"
"v_pk_fma_f32 v[204:205], v[204:205], s[74:75], s[74:75] op_sel:[0,0,1] op_sel_hi:[1,0,1]\n"
"v_pk_fma_f32 v[206:207], v[206:207], s[74:75], s[74:75] op_sel:[0,0,1] op_sel_hi:[1,0,1]\n"
"v_pk_mul_f32 v[192:193], v[192:193], v[80:81]\n"
"v_pk_mul_f32 v[194:195], v[194:195], v[82:83]\n"
"v_pk_mul_f32 v[196:197], v[196:197], v[84:85]\n"
"v_pk_mul_f32 v[198:199], v[198:199], v[86:87]\n"
"v_pk_mul_f32 v[200:201], v[200:201], v[88:89]\n"
"v_pk_mul_f32 v[202:203], v[202:203], v[90:91]\n"
"v_pk_mul_f32 v[204:205], v[204:205], v[92:93]\n"
"v_pk_mul_f32 v[206:207], v[206:207], v[94:95]\n"
"EX v192,v192\n"
"s_mov_b32 s60, 524288\n"
"EX v193,v193\n"
"LD a[64:67],v1,s[44:47],s60 offen offset:0 sc1\n"
"EX v194,v194\n"
"EX v195,v195\n"
"EX v196,v196\n"
"EX v197,v197\n"
"EX v198,v198\n"
"EX v199,v199\n"
"EX v200,v200\n"
"EX v201,v201\n"
"EX v202,v202\n"
"EX v203,v203\n"
"EX v204,v204\n"
"EX v205,v205\n"
"s_mov_b32 s61, 528384\n"
"EX v206,v206\n"
"LD a[68:71],v1,s[44:47],s61 offen offset:0 sc1\n"
"EX v207,v207\n"
"v_pk_add_f32 v[192:193], v[192:193], 1.0 op_sel_hi:[1,0]\n"
"v_pk_add_f32 v[194:195], v[194:195], 1.0 op_sel_hi:[1,0]\n"
"v_pk_add_f32 v[196:197], v[196:197], 1.0 op_sel_hi:[1,0]\n"
"v_pk_add_f32 v[198:199], v[198:199], 1.0 op_sel_hi:[1,0]\n"
"v_pk_add_f32 v[200:201], v[200:201], 1.0 op_sel_hi:[1,0]\n"
"v_pk_add_f32 v[202:203], v[202:203], 1.0 op_sel_hi:[1,0]\n"
"v_pk_add_f32 v[204:205], v[204:205], 1.0 op_sel_hi:[1,0]\n"
"v_pk_add_f32 v[206:207], v[206:207], 1.0 op_sel_hi:[1,0]\n"
"v_rcp_f32 v192, v192\n"
"v_rcp_f32 v193, v193\n"
"v_rcp_f32 v194, v194\n"
"s_mov_b32 s62, 532480\n"
"v_rcp_f32 v195, v195\n"
"LD a[72:75],v1,s[44:47],s62 offen offset:0 sc1\n"
"v_rcp_f32 v196, v196\n"
"v_rcp_f32 v197, v197\n"
"v_rcp_f32 v198, v198\n"
"v_rcp_f32 v199, v199\n"
"v_rcp_f32 v200, v200\n"
"v_rcp_f32 v201, v201\n"
"v_rcp_f32 v202, v202\n"
"v_rcp_f32 v203, v203\n"
"v_rcp_f32 v204, v204\n"
"v_rcp_f32 v205, v205\n"
"v_rcp_f32 v206, v206\n"
"v_rcp_f32 v207, v207\n"
"s_mov_b32 s63, 536576\n"
"v_pk_mul_f32 v[80:81], v[80:81], v[192:193]\n"
"LD a[76:79],v1,s[44:47],s63 offen offset:0 sc1\n"
"v_pk_mul_f32 v[82:83], v[82:83], v[194:195]\n"
"v_pk_mul_f32 v[84:85], v[84:85], v[196:197]\n"
"v_pk_mul_f32 v[86:87], v[86:87], v[198:199]\n"
"v_pk_mul_f32 v[88:89], v[88:89], v[200:201]\n"
"v_pk_mul_f32 v[90:91], v[90:91], v[202:203]\n"
"v_pk_mul_f32 v[92:93], v[92:93], v[204:205]\n"
"v_pk_mul_f32 v[94:95], v[94:95], v[206:207]\n"
"CV v80,v80,v81\n"
"CV v81,v82,v83\n"
"CV v82,v84,v85\n"
"CV v83,v86,v87\n"
"ST v[80:83],v1,s[76:79],s58 offen offset:2048 sc1\n"
"CV v88,v88,v89\n"
"CV v89,v90,v91\n"
"CV v90,v92,v93\n"
"CV v91,v94,v95\n"
"ST v[88:91],v1,s[76:79],s58 offen offset:3072 sc1\n"
"s_barrier\n"
"TR a[192:193],v24 offset:32768\n"
"TR a[194:195],v24 offset:34816\n"
"TR a[196:197],v24 offset:40960\n"
"TR a[198:199],v24 offset:43008\n"
"TR a[200:201],v25 offset:32768\n"
"TR a[202:203],v25 offset:34816\n"
"TR a[204:205],v25 offset:40960\n"
"TR a[206:207],v25 offset:43008\n"
"TR a[208:209],v26 offset:32768\n"
"TR a[210:211],v26 offset:34816\n"
"TR a[212:213],v26 offset:40960\n"
"TR a[214:215],v26 offset:43008\n"
"TR a[216:217],v27 offset:32768\n"
"TR a[218:219],v27 offset:34816\n"
"TR a[220:221],v27 offset:40960\n"
"TR a[222:223],v27 offset:43008\n"
"TR a[224:225],v24 offset:36864\n"
"TR a[226:227],v24 offset:38912\n"
"TR a[228:229],v24 offset:45056\n"
"TR a[230:231],v24 offset:47104\n"
"TR a[232:233],v25 offset:36864\n"
"TR a[234:235],v25 offset:38912\n"
"TR a[236:237],v25 offset:45056\n"
"TR a[238:239],v25 offset:47104\n"
"TR a[240:241],v26 offset:36864\n"
"TR a[242:243],v26 offset:38912\n"
"TR a[244:245],v26 offset:45056\n"
"TR a[246:247],v26 offset:47104\n"
"TR a[248:249],v27 offset:36864\n"
"TR a[250:251],v27 offset:38912\n"
"TR a[252:253],v27 offset:45056\n"
"TR a[254:255],v27 offset:47104\n"
"s_lshl_b32 s56, s33, 2\n"
"s_add_u32 s56, s56, s34\n"
"s_mul_i32 s64, s56, 16384\n"
"s_add_u32 s64, s64, 0x5000000\n"
"WT lgkmcnt(15)\n"
"MF v[128:131],a[192:195],a[0:3],0\n"
"MF v[128:131],a[196:199],a[16:19],v[128:131]\n"
"MF v[160:163],a[192:195],a[4:7],0\n"
"MF v[160:163],a[196:199],a[20:23],v[160:163]\n"
"MF v[192:195],a[192:195],a[8:11],0\n"
"MF v[192:195],a[196:199],a[24:27],v[192:195]\n"
"MF v[224:227],a[192:195],a[12:15],0\n"
"MF v[224:227],a[196:199],a[28:31],v[224:227]\n"
"TR a[192:193],v24 offset:49152\n"
"MF v[132:135],a[200:203],a[0:3],0\n"
"MF v[132:135],a[204:207],a[16:19],v[132:135]\n"
"TR a[194:195],v24 offset:51200\n"
"MF v[164:167],a[200:203],a[4:7],0\n"
"MF v[164:167],a[204:207],a[20:23],v[164:167]\n"
"TR a[196:197],v24 offset:57344\n"
"MF v[196:199],a[200:203],a[8:11],0\n"
"MF v[196:199],a[204:207],a[24:27],v[196:199]\n"
"TR a[198:199],v24 offset:59392\n"
"MF v[228:231],a[200:203],a[12:15],0\n"
"MF v[228:231],a[204:207],a[28:31],v[228:231]\n"
"TR a[200:201],v25 offset:49152\n"
"MF v[136:139],a[208:211],a[0:3],0\n"
"MF v[136:139],a[212:215],a[16:19],v[136:139]\n"
"WT vmcnt(0)\n"
"s_lshl_b32 s56, s33, 2\n"
"s_add_u32 s56, s56, s34\n"
"s_lshl_b32 s56, s56, 7\n"
"s_add_u32 s56, s56, 0xa0000\n"
"v_lshlrev_b32 v21, 3, v0\n"
"s_mov_b64 exec, 1\n"
"buffer_store_dwordx2 v[16:17], v21, s[40:43], s56 offen sc1\n"
"s_mov_b64 exec, -1\n"
"TR a[202:203],v25 offset:51200\n"
"MF v[168:171],a[208:211],a[4:7],0\n"
"MF v[168:171],a[212:215],a[20:23],v[168:171]\n"
"TR a[204:205],v25 offset:57344\n"
"MF v[200:203],a[208:211],a[8:11],0\n"
"MF v[200:203],a[212:215],a[24:27],v[200:203]\n"
"s_add_u32 s65, s65, s23\n"
"s_add_u32 m0, s22, 49152\n"
"TR a[206:207],v25 offset:59392\n"
"MF v[232:235],a[208:211],a[12:15],0\n"
"LD v2,s[52:55],s65 offen offset:0 lds\n"
"MF v[232:235],a[212:215],a[28:31],v[232:235]\n"
"TR a[208:209],v26 offset:49152\n"
"MF v[140:143],a[216:219],a[0:3],0\n"
"MF v[140:143],a[220:223],a[16:19],v[140:143]\n"
"TR a[210:211],v26 offset:51200\n"
"MF v[172:175],a[216:219],a[4:7],0\n"
"LD a[80:83],v1,s[44:47],s60 offen offset:1024 sc1\n"
"MF v[172:175],a[220:223],a[20:23],v[172:175]\n"
"TR a[212:213],v26 offset:57344\n"
"MF v[204:207],a[216:219],a[8:11],0\n"
"MF v[204:207],a[220:223],a[24:27],v[204:207]\n"
"TR a[214:215],v26 offset:59392\n"
"MF v[236:239],a[216:219],a[12:15],0\n"
"LD a[84:87],v1,s[44:47],s61 offen offset:1024 sc1\n"
"MF v[236:239],a[220:223],a[28:31],v[236:239]\n"
"WT lgkmcnt(15)\n"
"TR a[216:217],v27 offset:49152\n"
"MF v[144:147],a[224:227],a[0:3],0\n"
"MF v[144:147],a[228:231],a[16:19],v[144:147]\n"
"TR a[218:219],v27 offset:51200\n"
"MF v[176:179],a[224:227],a[4:7],0\n"
"LD a[88:91],v1,s[44:47],s62 offen offset:1024 sc1\n"
"MF v[176:179],a[228:231],a[20:23],v[176:179]\n"
"TR a[220:221],v27 offset:57344\n"
"MF v[208:211],a[224:227],a[8:11],0\n"
"MF v[208:211],a[228:231],a[24:27],v[208:211]\n"
"TR a[222:223],v27 offset:59392\n"
"MF v[240:243],a[224:227],a[12:15],0\n"
"LD a[92:95],v1,s[44:47],s63 offen offset:1024 sc1\n"
"MF v[240:243],a[228:231],a[28:31],v[240:243]\n"
"TR a[224:225],v24 offset:53248\n"
"MF v[148:151],a[232:235],a[0:3],0\n"
"MF v[148:151],a[236:239],a[16:19],v[148:151]\n"
"TR a[226:227],v24 offset:55296\n"
"MF v[180:183],a[232:235],a[4:7],0\n"
"LD v3,s[52:55],s65 offen offset:1024 lds\n"
"MF v[180:183],a[236:239],a[20:23],v[180:183]\n"
"TR a[228:229],v24 offset:61440\n"
"MF v[212:215],a[232:235],a[8:11],0\n"
"MF v[212:215],a[236:239],a[24:27],v[212:215]\n"
"TR a[230:231],v24 offset:63488\n"
"MF v[244:247],a[232:235],a[12:15],0\n"
"LD a[96:99],v1,s[44:47],s60 offen offset:2048 sc1\n"
"MF v[244:247],a[236:239],a[28:31],v[244:247]\n"
"TR a[232:233],v25 offset:53248\n"
"MF v[152:155],a[240:243],a[0:3],0\n"
"MF v[152:155],a[244:247],a[16:19],v[152:155]\n"
"TR a[234:235],v25 offset:55296\n"
"MF v[184:187],a[240:243],a[4:7],0\n"
"LD a[100:103],v1,s[44:47],s61 offen offset:2048 sc1\n"
"MF v[184:187],a[244:247],a[20:23],v[184:187]\n"
"TR a[236:237],v25 offset:61440\n"
"MF v[216:219],a[240:243],a[8:11],0\n"
"MF v[216:219],a[244:247],a[24:27],v[216:219]\n"
"TR a[238:239],v25 offset:63488\n"
"MF v[248:251],a[240:243],a[12:15],0\n"
"LD v4,s[52:55],s65 offen offset:2048 lds\n"
"MF v[248:251],a[244:247],a[28:31],v[248:251]\n"
"WT lgkmcnt(15)\n"
"TR a[240:241],v26 offset:53248\n"
"MF v[156:159],a[248:251],a[0:3],0\n"
"MF v[156:159],a[252:255],a[16:19],v[156:159]\n"
"TR a[242:243],v26 offset:55296\n"
"MF v[188:191],a[248:251],a[4:7],0\n"
"LD a[104:107],v1,s[44:47],s62 offen offset:2048 sc1\n"
"MF v[188:191],a[252:255],a[20:23],v[188:191]\n"
"TR a[244:245],v26 offset:61440\n"
"MF v[220:223],a[248:251],a[8:11],0\n"
"MF v[220:223],a[252:255],a[24:27],v[220:223]\n"
"TR a[246:247],v26 offset:63488\n"
"MF v[252:255],a[248:251],a[12:15],0\n"
"LD a[108:111],v1,s[44:47],s63 offen offset:2048 sc1\n"
"MF v[252:255],a[252:255],a[28:31],v[252:255]\n"
"TR a[248:249],v27 offset:53248\n"
"MF v[128:131],a[192:195],a[32:35],v[128:131]\n"
"MF v[128:131],a[196:199],a[48:51],v[128:131]\n"
"TR a[250:251],v27 offset:55296\n"
"MF v[160:163],a[192:195],a[36:39],v[160:163]\n"
"LD a[112:115],v1,s[44:47],s60 offen offset:3072 sc1\n"
"MF v[160:163],a[196:199],a[52:55],v[160:163]\n"
"TR a[252:253],v27 offset:61440\n"
"MF v[192:195],a[192:195],a[40:43],v[192:195]\n"
"MF v[192:195],a[196:199],a[56:59],v[192:195]\n"
"TR a[254:255],v27 offset:63488\n"
"MF v[224:227],a[192:195],a[44:47],v[224:227]\n"
"LD a[116:119],v1,s[44:47],s61 offen offset:3072 sc1\n"
"MF v[224:227],a[196:199],a[60:63],v[224:227]\n"
"TR a[192:193],v28 offset:0\n"
"MF v[132:135],a[200:203],a[32:35],v[132:135]\n"
"MF v[132:135],a[204:207],a[48:51],v[132:135]\n"
"TR a[194:195],v28 offset:2048\n"
"MF v[164:167],a[200:203],a[36:39],v[164:167]\n"
"LD v5,s[52:55],s65 offen offset:3072 lds\n"
"MF v[164:167],a[204:207],a[52:55],v[164:167]\n"
"TR a[196:197],v28 offset:8192\n"
"MF v[196:199],a[200:203],a[40:43],v[196:199]\n"
"MF v[196:199],a[204:207],a[56:59],v[196:199]\n"
"TR a[198:199],v28 offset:10240\n"
"MF v[228:231],a[200:203],a[44:47],v[228:231]\n"
"LD a[120:123],v1,s[44:47],s62 offen offset:3072 sc1\n"
"MF v[228:231],a[204:207],a[60:63],v[228:231]\n"
"WT lgkmcnt(15)\n"
"TR a[200:201],v28 offset:16384\n"
"MF v[136:139],a[208:211],a[32:35],v[136:139]\n"
"MF v[136:139],a[212:215],a[48:51],v[136:139]\n"
"TR a[202:203],v28 offset:18432\n"
"MF v[168:171],a[208:211],a[36:39],v[168:171]\n"
"LD a[124:127],v1,s[44:47],s63 offen offset:3072 sc1\n"
"MF v[168:171],a[212:215],a[52:55],v[168:171]\n"
"TR a[204:205],v28 offset:24576\n"
"MF v[200:203],a[208:211],a[40:43],v[200:203]\n"
"MF v[200:203],a[212:215],a[56:59],v[200:203]\n"
"s_add_u32 s65, s65, s23\n"
"s_add_u32 m0, s22, 65536\n"
"TR a[206:207],v28 offset:26624\n"
"MF v[232:235],a[208:211],a[44:47],v[232:235]\n"
"LD v2,s[52:55],s65 offen offset:0 lds\n"
"MF v[232:235],a[212:215],a[60:63],v[232:235]\n"
"TR a[208:209],v29 offset:0\n"
"MF v[140:143],a[216:219],a[32:35],v[140:143]\n"
"MF v[140:143],a[220:223],a[48:51],v[140:143]\n"
"s_mov_b32 s60, 1048576\n"
"TR a[210:211],v29 offset:2048\n"
"MF v[172:175],a[216:219],a[36:39],v[172:175]\n"
"LD a[128:131],v1,s[44:47],s60 offen offset:0 sc1\n"
"MF v[172:175],a[220:223],a[52:55],v[172:175]\n"
"TR a[212:213],v29 offset:8192\n"
"MF v[204:207],a[216:219],a[40:43],v[204:207]\n"
"MF v[204:207],a[220:223],a[56:59],v[204:207]\n"
"s_mov_b32 s61, 1052672\n"
"TR a[214:215],v29 offset:10240\n"
"MF v[236:239],a[216:219],a[44:47],v[236:239]\n"
"LD a[132:135],v1,s[44:47],s61 offen offset:0 sc1\n"
"MF v[236:239],a[220:223],a[60:63],v[236:239]\n"
"TR a[216:217],v29 offset:16384\n"
"MF v[144:147],a[224:227],a[32:35],v[144:147]\n"
"MF v[144:147],a[228:231],a[48:51],v[144:147]\n"
"s_mov_b32 s62, 1056768\n"
"TR a[218:219],v29 offset:18432\n"
"MF v[176:179],a[224:227],a[36:39],v[176:179]\n"
"LD a[136:139],v1,s[44:47],s62 offen offset:0 sc1\n"
"MF v[176:179],a[228:231],a[52:55],v[176:179]\n"
"TR a[220:221],v29 offset:24576\n"
"MF v[208:211],a[224:227],a[40:43],v[208:211]\n"
"MF v[208:211],a[228:231],a[56:59],v[208:211]\n"
"s_mov_b32 s63, 1060864\n"
"TR a[222:223],v29 offset:26624\n"
"MF v[240:243],a[224:227],a[44:47],v[240:243]\n"
"LD a[140:143],v1,s[44:47],s63 offen offset:0 sc1\n"
"MF v[240:243],a[228:231],a[60:63],v[240:243]\n"
"WT lgkmcnt(15)\n"
"TR a[224:225],v30 offset:0\n"
"MF v[148:151],a[232:235],a[32:35],v[148:151]\n"
"MF v[148:151],a[236:239],a[48:51],v[148:151]\n"
"TR a[226:227],v30 offset:2048\n"
"MF v[180:183],a[232:235],a[36:39],v[180:183]\n"
"LD v3,s[52:55],s65 offen offset:1024 lds\n"
"MF v[180:183],a[236:239],a[52:55],v[180:183]\n"
"TR a[228:229],v30 offset:8192\n"
"MF v[212:215],a[232:235],a[40:43],v[212:215]\n"
"MF v[212:215],a[236:239],a[56:59],v[212:215]\n"
"TR a[230:231],v30 offset:10240\n"
"MF v[244:247],a[232:235],a[44:47],v[244:247]\n"
"LD a[144:147],v1,s[44:47],s60 offen offset:1024 sc1\n"
"MF v[244:247],a[236:239],a[60:63],v[244:247]\n"
"TR a[232:233],v30 offset:16384\n"
"MF v[152:155],a[240:243],a[32:35],v[152:155]\n"
"MF v[152:155],a[244:247],a[48:51],v[152:155]\n"
"TR a[234:235],v30 offset:18432\n"
"MF v[184:187],a[240:243],a[36:39],v[184:187]\n"
"LD a[148:151],v1,s[44:47],s61 offen offset:1024 sc1\n"
"MF v[184:187],a[244:247],a[52:55],v[184:187]\n"
"TR a[236:237],v30 offset:24576\n"
"MF v[216:219],a[240:243],a[40:43],v[216:219]\n"
"MF v[216:219],a[244:247],a[56:59],v[216:219]\n"
"TR a[238:239],v30 offset:26624\n"
"MF v[248:251],a[240:243],a[44:47],v[248:251]\n"
"LD v4,s[52:55],s65 offen offset:2048 lds\n"
"MF v[248:251],a[244:247],a[60:63],v[248:251]\n"
"TR a[240:241],v31 offset:0\n"
"MF v[156:159],a[248:251],a[32:35],v[156:159]\n"
"MF v[156:159],a[252:255],a[48:51],v[156:159]\n"
"TR a[242:243],v31 offset:2048\n"
"MF v[188:191],a[248:251],a[36:39],v[188:191]\n"
"LD a[152:155],v1,s[44:47],s62 offen offset:1024 sc1\n"
"MF v[188:191],a[252:255],a[52:55],v[188:191]\n"
"TR a[244:245],v31 offset:8192\n"
"MF v[220:223],a[248:251],a[40:43],v[220:223]\n"
"MF v[220:223],a[252:255],a[56:59],v[220:223]\n"
"TR a[246:247],v31 offset:10240\n"
"MF v[252:255],a[248:251],a[44:47],v[252:255]\n"
"LD a[156:159],v1,s[44:47],s63 offen offset:1024 sc1\n"
"MF v[252:255],a[252:255],a[60:63],v[252:255]\n"
"WT lgkmcnt(15)\n"
"TR a[248:249],v31 offset:16384\n"
"MF v[128:131],a[192:195],a[64:67],v[128:131]\n"
"WT vmcnt(25)\n"
"MF v[128:131],a[196:199],a[80:83],v[128:131]\n"
"WT vmcnt(20)\n"
"TR a[250:251],v31 offset:18432\n"
"MF v[128:131],a[200:203],a[96:99],v[128:131]\n"
"WT vmcnt(15)\n"
"LD a[160:163],v1,s[44:47],s60 offen offset:2048 sc1\n"
"MF v[128:131],a[204:207],a[112:115],v[128:131]\n"
"TR a[252:253],v31 offset:24576\n"
"MF v[160:163],a[192:195],a[68:71],v[160:163]\n"
"MF v[160:163],a[196:199],a[84:87],v[160:163]\n"
"TR a[254:255],v31 offset:26624\n"
"MF v[160:163],a[200:203],a[100:103],v[160:163]\n"
"WT vmcnt(15)\n"
"LD a[164:167],v1,s[44:47],s61 offen offset:2048 sc1\n"
"MF v[160:163],a[204:207],a[116:119],v[160:163]\n"
"MF v[192:195],a[192:195],a[72:75],v[192:195]\n"
"MF v[192:195],a[196:199],a[88:91],v[192:195]\n"
"MF v[192:195],a[200:203],a[104:107],v[192:195]\n"
"WT vmcnt(14)\n"
"LD v5,s[52:55],s65 offen offset:3072 lds\n"
"MF v[192:195],a[204:207],a[120:123],v[192:195]\n"
"MF v[224:227],a[192:195],a[76:79],v[224:227]\n"
"MF v[224:227],a[196:199],a[92:95],v[224:227]\n"
"TR a[192:193],v28 offset:4096\n"
"MF v[224:227],a[200:203],a[108:111],v[224:227]\n"
"WT vmcnt(14)\n"
"LD a[168:171],v1,s[44:47],s62 offen offset:2048 sc1\n"
"MF v[224:227],a[204:207],a[124:127],v[224:227]\n"
"TR a[194:195],v28 offset:6144\n"
"MF v[132:135],a[208:211],a[64:67],v[132:135]\n"
"MF v[132:135],a[212:215],a[80:83],v[132:135]\n"
"WT lgkmcnt(15)\n"
"TR a[196:197],v28 offset:12288\n"
"MF v[132:135],a[216:219],a[96:99],v[132:135]\n"
"LD a[172:175],v1,s[44:47],s63 offen offset:2048 sc1\n"
"MF v[132:135],a[220:223],a[112:115],v[132:135]\n"
"TR a[198:199],v28 offset:14336\n"
"MF v[164:167],a[208:211],a[68:71],v[164:167]\n"
"MF v[164:167],a[212:215],a[84:87],v[164:167]\n"
"s_add_u32 s65, s65, s23\n"
"s_add_u32 m0, s22, 81920\n"
"TR a[200:201],v28 offset:20480\n"
"MF v[164:167],a[216:219],a[100:103],v[164:167]\n"
"LD v2,s[52:55],s65 offen offset:0 lds\n"
"MF v[164:167],a[220:223],a[116:119],v[164:167]\n"
"TR a[202:203],v28 offset:22528\n"
"MF v[196:199],a[208:211],a[72:75],v[196:199]\n"
"MF v[196:199],a[212:215],a[88:91],v[196:199]\n"
"TR a[204:205],v28 offset:28672\n"
"MF v[196:199],a[216:219],a[104:107],v[196:199]\n"
"LD a[176:179],v1,s[44:47],s60 offen offset:3072 sc1\n"
"MF v[196:199],a[220:223],a[120:123],v[196:199]\n"
"TR a[206:207],v28 offset:30720\n"
"MF v[228:231],a[208:211],a[76:79],v[228:231]\n"
"MF v[228:231],a[212:215],a[92:95],v[228:231]\n"
"TR a[208:209],v29 offset:4096\n"
"MF v[228:231],a[216:219],a[108:111],v[228:231]\n"
"LD a[180:183],v1,s[44:47],s61 offen offset:3072 sc1\n"
"MF v[228:231],a[220:223],a[124:127],v[228:231]\n"
"TR a[210:211],v29 offset:6144\n"
"MF v[136:139],a[224:227],a[64:67],v[136:139]\n"
"WT lgkmcnt(15)\n"
"MF v[136:139],a[228:231],a[80:83],v[136:139]\n"
"TR a[212:213],v29 offset:12288\n"
"MF v[136:139],a[232:235],a[96:99],v[136:139]\n"
"LD a[184:187],v1,s[44:47],s62 offen offset:3072 sc1\n"
"MF v[136:139],a[236:239],a[112:115],v[136:139]\n"
"TR a[214:215],v29 offset:14336\n"
"MF v[168:171],a[224:227],a[68:71],v[168:171]\n"
"MF v[168:171],a[228:231],a[84:87],v[168:171]\n"
"TR a[216:217],v29 offset:20480\n"
"MF v[168:171],a[232:235],a[100:103],v[168:171]\n"
"LD a[188:191],v1,s[44:47],s63 offen offset:3072 sc1\n"
"MF v[168:171],a[236:239],a[116:119],v[168:171]\n"
"TR a[218:219],v29 offset:22528\n"
"MF v[200:203],a[224:227],a[72:75],v[200:203]\n"
"MF v[200:203],a[228:231],a[88:91],v[200:203]\n"
"TR a[220:221],v29 offset:28672\n"
"MF v[200:203],a[232:235],a[104:107],v[200:203]\n"
"LD v3,s[52:55],s65 offen offset:1024 lds\n"
"MF v[200:203],a[236:239],a[120:123],v[200:203]\n"
"TR a[222:223],v29 offset:30720\n"
"MF v[232:235],a[224:227],a[76:79],v[232:235]\n"
"MF v[232:235],a[228:231],a[92:95],v[232:235]\n"
"s_mov_b32 s60, 1572864\n"
"TR a[224:225],v30 offset:4096\n"
"MF v[232:235],a[232:235],a[108:111],v[232:235]\n"
"LD a[0:3],v1,s[44:47],s60 offen offset:0 sc1\n"
"MF v[232:235],a[236:239],a[124:127],v[232:235]\n"
"TR a[226:227],v30 offset:6144\n"
"MF v[140:143],a[240:243],a[64:67],v[140:143]\n"
"WT lgkmcnt(15)\n"
"MF v[140:143],a[244:247],a[80:83],v[140:143]\n"
"s_mov_b32 s61, 1576960\n"
"TR a[228:229],v30 offset:12288\n"
"MF v[140:143],a[248:251],a[96:99],v[140:143]\n"
"LD a[4:7],v1,s[44:47],s61 offen offset:0 sc1\n"
"MF v[140:143],a[252:255],a[112:115],v[140:143]\n"
"TR a[230:231],v30 offset:14336\n"
"MF v[172:175],a[240:243],a[68:71],v[172:175]\n"
"MF v[172:175],a[244:247],a[84:87],v[172:175]\n"
"TR a[232:233],v30 offset:20480\n"
"MF v[172:175],a[248:251],a[100:103],v[172:175]\n"
"LD v4,s[52:55],s65 offen offset:2048 lds\n"
"MF v[172:175],a[252:255],a[116:119],v[172:175]\n"
"TR a[234:235],v30 offset:22528\n"
"MF v[204:207],a[240:243],a[72:75],v[204:207]\n"
"MF v[204:207],a[244:247],a[88:91],v[204:207]\n"
"s_mov_b32 s62, 1581056\n"
"TR a[236:237],v30 offset:28672\n"
"MF v[204:207],a[248:251],a[104:107],v[204:207]\n"
"LD a[8:11],v1,s[44:47],s62 offen offset:0 sc1\n"
"MF v[204:207],a[252:255],a[120:123],v[204:207]\n"
"TR a[238:239],v30 offset:30720\n"
"MF v[236:239],a[240:243],a[76:79],v[236:239]\n"
"MF v[236:239],a[244:247],a[92:95],v[236:239]\n"
"s_mov_b32 s63, 1585152\n"
"TR a[240:241],v31 offset:4096\n"
"MF v[236:239],a[248:251],a[108:111],v[236:239]\n"
"LD a[12:15],v1,s[44:47],s63 offen offset:0 sc1\n"
"MF v[236:239],a[252:255],a[124:127],v[236:239]\n"
"TR a[242:243],v31 offset:6144\n"
"MF v[144:147],a[192:195],a[64:67],v[144:147]\n"
"WT lgkmcnt(15)\n"
"s_barrier\n"
"MF v[144:147],a[196:199],a[80:83],v[144:147]\n"
"TR a[244:245],v31 offset:12288\n"
"MF v[144:147],a[200:203],a[96:99],v[144:147]\n"
"LD a[16:19],v1,s[44:47],s60 offen offset:1024 sc1\n"
"MF v[144:147],a[204:207],a[112:115],v[144:147]\n"
"TR a[246:247],v31 offset:14336\n"
"MF v[176:179],a[192:195],a[68:71],v[176:179]\n"
"MF v[176:179],a[196:199],a[84:87],v[176:179]\n"
"TR a[248:249],v31 offset:20480\n"
"MF v[176:179],a[200:203],a[100:103],v[176:179]\n"
"LD a[20:23],v1,s[44:47],s61 offen offset:1024 sc1\n"
"MF v[176:179],a[204:207],a[116:119],v[176:179]\n"
"TR a[250:251],v31 offset:22528\n"
"MF v[208:211],a[192:195],a[72:75],v[208:211]\n"
"MF v[208:211],a[196:199],a[88:91],v[208:211]\n"
"TR a[252:253],v31 offset:28672\n"
"MF v[208:211],a[200:203],a[104:107],v[208:211]\n"
"LD v5,s[52:55],s65 offen offset:3072 lds\n"
"MF v[208:211],a[204:207],a[120:123],v[208:211]\n"
"TR a[254:255],v31 offset:30720\n"
"MF v[240:243],a[192:195],a[76:79],v[240:243]\n"
"MF v[240:243],a[196:199],a[92:95],v[240:243]\n"
"TR a[192:193],v12 offset:0\n"
"MF v[240:243],a[200:203],a[108:111],v[240:243]\n"
"LD a[24:27],v1,s[44:47],s62 offen offset:1024 sc1\n"
"MF v[240:243],a[204:207],a[124:127],v[240:243]\n"
"TR a[194:195],v12 offset:2048\n"
"MF v[148:151],a[208:211],a[64:67],v[148:151]\n"
"WT lgkmcnt(15)\n"
"MF v[148:151],a[212:215],a[80:83],v[148:151]\n"
"TR a[196:197],v12 offset:8192\n"
"MF v[148:151],a[216:219],a[96:99],v[148:151]\n"
"LD a[28:31],v1,s[44:47],s63 offen offset:1024 sc1\n"
"MF v[148:151],a[220:223],a[112:115],v[148:151]\n"
"TR a[198:199],v12 offset:10240\n"
"MF v[180:183],a[208:211],a[68:71],v[180:183]\n"
"MF v[180:183],a[212:215],a[84:87],v[180:183]\n"
"TR a[200:201],v12 offset:16384\n"
"MF v[180:183],a[216:219],a[100:103],v[180:183]\n"
"MF v[180:183],a[220:223],a[116:119],v[180:183]\n"
"TR a[202:203],v12 offset:18432\n"
"MF v[212:215],a[208:211],a[72:75],v[212:215]\n"
"MF v[212:215],a[212:215],a[88:91],v[212:215]\n"
"s_add_u32 s65, s65, s23\n"
"s_add_u32 m0, s22, 98304\n"
"TR a[204:205],v12 offset:24576\n"
"MF v[212:215],a[216:219],a[104:107],v[212:215]\n"
"LD v2,s[52:55],s65 offen offset:0 lds\n"
"MF v[212:215],a[220:223],a[120:123],v[212:215]\n"
"TR a[206:207],v12 offset:26624\n"
"MF v[244:247],a[208:211],a[76:79],v[244:247]\n"
"MF v[244:247],a[212:215],a[92:95],v[244:247]\n"
"TR a[208:209],v13 offset:0\n"
"MF v[244:247],a[216:219],a[108:111],v[244:247]\n"
"LD a[32:35],v1,s[44:47],s60 offen offset:2048 sc1\n"
"MF v[244:247],a[220:223],a[124:127],v[244:247]\n"
"TR a[210:211],v13 offset:2048\n"
"MF v[152:155],a[224:227],a[64:67],v[152:155]\n"
"WT lgkmcnt(15)\n"
"MF v[152:155],a[228:231],a[80:83],v[152:155]\n"
"TR a[212:213],v13 offset:8192\n"
"MF v[152:155],a[232:235],a[96:99],v[152:155]\n"
"LD a[36:39],v1,s[44:47],s61 offen offset:2048 sc1\n"
"MF v[152:155],a[236:239],a[112:115],v[152:155]\n"
"TR a[214:215],v13 offset:10240\n"
"MF v[184:187],a[224:227],a[68:71],v[184:187]\n"
"MF v[184:187],a[228:231],a[84:87],v[184:187]\n"
"TR a[216:217],v13 offset:16384\n"
"MF v[184:187],a[232:235],a[100:103],v[184:187]\n"
"LD a[40:43],v1,s[44:47],s62 offen offset:2048 sc1\n"
"MF v[184:187],a[236:239],a[116:119],v[184:187]\n"
"TR a[218:219],v13 offset:18432\n"
"MF v[216:219],a[224:227],a[72:75],v[216:219]\n"
"MF v[216:219],a[228:231],a[88:91],v[216:219]\n"
"TR a[220:221],v13 offset:24576\n"
"MF v[216:219],a[232:235],a[104:107],v[216:219]\n"
"LD v3,s[52:55],s65 offen offset:1024 lds\n"
"MF v[216:219],a[236:239],a[120:123],v[216:219]\n"
"TR a[222:223],v13 offset:26624\n"
"MF v[248:251],a[224:227],a[76:79],v[248:251]\n"
"MF v[248:251],a[228:231],a[92:95],v[248:251]\n"
"TR a[224:225],v14 offset:0\n"
"MF v[248:251],a[232:235],a[108:111],v[248:251]\n"
"LD a[44:47],v1,s[44:47],s63 offen offset:2048 sc1\n"
"MF v[248:251],a[236:239],a[124:127],v[248:251]\n"
"TR a[226:227],v14 offset:2048\n"
"MF v[156:159],a[240:243],a[64:67],v[156:159]\n"
"WT lgkmcnt(15)\n"
"MF v[156:159],a[244:247],a[80:83],v[156:159]\n"
"TR a[228:229],v14 offset:8192\n"
"MF v[156:159],a[248:251],a[96:99],v[156:159]\n"
"LD a[48:51],v1,s[44:47],s60 offen offset:3072 sc1\n"
"MF v[156:159],a[252:255],a[112:115],v[156:159]\n"
"TR a[230:231],v14 offset:10240\n"
"MF v[188:191],a[240:243],a[68:71],v[188:191]\n"
"MF v[188:191],a[244:247],a[84:87],v[188:191]\n"
"TR a[232:233],v14 offset:16384\n"
"MF v[188:191],a[248:251],a[100:103],v[188:191]\n"
"MF v[188:191],a[252:255],a[116:119],v[188:191]\n"
"TR a[234:235],v14 offset:18432\n"
"MF v[220:223],a[240:243],a[72:75],v[220:223]\n"
"MF v[220:223],a[244:247],a[88:91],v[220:223]\n"
"TR a[236:237],v14 offset:24576\n"
"MF v[220:223],a[248:251],a[104:107],v[220:223]\n"
"LD v4,s[52:55],s65 offen offset:2048 lds\n"
"MF v[220:223],a[252:255],a[120:123],v[220:223]\n"
"TR a[238:239],v14 offset:26624\n"
"MF v[252:255],a[240:243],a[76:79],v[252:255]\n"
"MF v[252:255],a[244:247],a[92:95],v[252:255]\n"
"TR a[240:241],v15 offset:0\n"
"MF v[252:255],a[248:251],a[108:111],v[252:255]\n"
"LD a[52:55],v1,s[44:47],s61 offen offset:3072 sc1\n"
"MF v[252:255],a[252:255],a[124:127],v[252:255]\n"
"WT vmcnt(39)\n"
"TR a[242:243],v15 offset:2048\n"
"MF v[128:131],a[192:195],a[128:131],v[128:131]\n"
"WT vmcnt(34) lgkmcnt(15)\n"
"MF v[128:131],a[196:199],a[144:147],v[128:131]\n"
"WT vmcnt(29)\n"
"TR a[244:245],v15 offset:8192\n"
"MF v[128:131],a[200:203],a[160:163],v[128:131]\n"
"WT vmcnt(23)\n"
"LD a[56:59],v1,s[44:47],s62 offen offset:3072 sc1\n"
"MF v[128:131],a[204:207],a[176:179],v[128:131]\n"
"TR a[246:247],v15 offset:10240\n"
"MF v[160:163],a[192:195],a[132:135],v[160:163]\n"
"MF v[160:163],a[196:199],a[148:151],v[160:163]\n"
"TR a[248:249],v15 offset:16384\n"
"MF v[160:163],a[200:203],a[164:167],v[160:163]\n"
"WT vmcnt(23)\n"
"LD a[60:63],v1,s[44:47],s63 offen offset:3072 sc1\n"
"MF v[160:163],a[204:207],a[180:183],v[160:163]\n"
"TR a[250:251],v15 offset:18432\n"
"MF v[192:195],a[192:195],a[136:139],v[192:195]\n"
"MF v[192:195],a[196:199],a[152:155],v[192:195]\n"
"TR a[252:253],v15 offset:24576\n"
"MF v[192:195],a[200:203],a[168:171],v[192:195]\n"
"WT vmcnt(23)\n"
"LD v5,s[52:55],s65 offen offset:3072 lds\n"
"MF v[192:195],a[204:207],a[184:187],v[192:195]\n"
"TR a[254:255],v15 offset:26624\n"
"MF v[224:227],a[192:195],a[140:143],v[224:227]\n"
"MF v[224:227],a[196:199],a[156:159],v[224:227]\n"
"s_mov_b32 s60, 2097152\n"
"TR a[192:193],v12 offset:4096\n"
"MF v[224:227],a[200:203],a[172:175],v[224:227]\n"
"WT vmcnt(23)\n"
"LD a[64:67],v1,s[44:47],s60 offen offset:0 sc1\n"
"MF v[224:227],a[204:207],a[188:191],v[224:227]\n"
"TR a[194:195],v12 offset:6144\n"
"MF v[132:135],a[208:211],a[128:131],v[132:135]\n"
"WT lgkmcnt(15)\n"
"MF v[132:135],a[212:215],a[144:147],v[132:135]\n"
"s_mov_b32 s61, 2101248\n"
"TR a[196:197],v12 offset:12288\n"
"MF v[132:135],a[216:219],a[160:163],v[132:135]\n"
"LD a[68:71],v1,s[44:47],s61 offen offset:0 sc1\n"
"MF v[132:135],a[220:223],a[176:179],v[132:135]\n"
"TR a[198:199],v12 offset:14336\n"
"MF v[164:167],a[208:211],a[132:135],v[164:167]\n"
"MF v[164:167],a[212:215],a[148:151],v[164:167]\n"
"TR a[200:201],v12 offset:20480\n"
"MF v[164:167],a[216:219],a[164:167],v[164:167]\n"
"MF v[164:167],a[220:223],a[180:183],v[164:167]\n"
"TR a[202:203],v12 offset:22528\n"
"MF v[196:199],a[208:211],a[136:139],v[196:199]\n"
"MF v[196:199],a[212:215],a[152:155],v[196:199]\n"
"s_add_u32 s65, s65, s23\n"
"s_add_u32 m0, s22, 114688\n"
"TR a[204:205],v12 offset:28672\n"
"MF v[196:199],a[216:219],a[168:171],v[196:199]\n"
"LD v2,s[52:55],s65 offen offset:0 lds\n"
"MF v[196:199],a[220:223],a[184:187],v[196:199]\n"
"TR a[206:207],v12 offset:30720\n"
"MF v[228:231],a[208:211],a[140:143],v[228:231]\n"
"MF v[228:231],a[212:215],a[156:159],v[228:231]\n"
"s_mov_b32 s62, 2105344\n"
"TR a[208:209],v13 offset:4096\n"
"MF v[228:231],a[216:219],a[172:175],v[228:231]\n"
"LD a[72:75],v1,s[44:47],s62 offen offset:0 sc1\n"
"MF v[228:231],a[220:223],a[188:191],v[228:231]\n"
"TR a[210:211],v13 offset:6144\n"
"MF v[136:139],a[224:227],a[128:131],v[136:139]\n"
"WT lgkmcnt(15)\n"
"MF v[136:139],a[228:231],a[144:147],v[136:139]\n"
"s_mov_b32 s63, 2109440\n"
"TR a[212:213],v13 offset:12288\n"
"MF v[136:139],a[232:235],a[160:163],v[136:139]\n"
"LD a[76:79],v1,s[44:47],s63 offen offset:0 sc1\n"
"MF v[136:139],a[236:239],a[176:179],v[136:139]\n"
"TR a[214:215],v13 offset:14336\n"
"MF v[168:171],a[224:227],a[132:135],v[168:171]\n"
"MF v[168:171],a[228:231],a[148:151],v[168:171]\n"
"TR a[216:217],v13 offset:20480\n"
"MF v[168:171],a[232:235],a[164:167],v[168:171]\n"
"LD a[80:83],v1,s[44:47],s60 offen offset:1024 sc1\n"
"MF v[168:171],a[236:239],a[180:183],v[168:171]\n"
"TR a[218:219],v13 offset:22528\n"
"MF v[200:203],a[224:227],a[136:139],v[200:203]\n"
"MF v[200:203],a[228:231],a[152:155],v[200:203]\n"
"TR a[220:221],v13 offset:28672\n"
"MF v[200:203],a[232:235],a[168:171],v[200:203]\n"
"LD v3,s[52:55],s65 offen offset:1024 lds\n"
"MF v[200:203],a[236:239],a[184:187],v[200:203]\n"
"TR a[222:223],v13 offset:30720\n"
"MF v[232:235],a[224:227],a[140:143],v[232:235]\n"
"MF v[232:235],a[228:231],a[156:159],v[232:235]\n"
"TR a[224:225],v14 offset:4096\n"
"MF v[232:235],a[232:235],a[172:175],v[232:235]\n"
"LD a[84:87],v1,s[44:47],s61 offen offset:1024 sc1\n"
"MF v[232:235],a[236:239],a[188:191],v[232:235]\n"
"TR a[226:227],v14 offset:6144\n"
"MF v[140:143],a[240:243],a[128:131],v[140:143]\n"
"WT lgkmcnt(15)\n"
"MF v[140:143],a[244:247],a[144:147],v[140:143]\n"
"TR a[228:229],v14 offset:12288\n"
"MF v[140:143],a[248:251],a[160:163],v[140:143]\n"
"LD a[88:91],v1,s[44:47],s62 offen offset:1024 sc1\n"
"MF v[140:143],a[252:255],a[176:179],v[140:143]\n"
"TR a[230:231],v14 offset:14336\n"
"MF v[172:175],a[240:243],a[132:135],v[172:175]\n"
"MF v[172:175],a[244:247],a[148:151],v[172:175]\n"
"TR a[232:233],v14 offset:20480\n"
"MF v[172:175],a[248:251],a[164:167],v[172:175]\n"
"MF v[172:175],a[252:255],a[180:183],v[172:175]\n"
"TR a[234:235],v14 offset:22528\n"
"MF v[204:207],a[240:243],a[136:139],v[204:207]\n"
"MF v[204:207],a[244:247],a[152:155],v[204:207]\n"
"TR a[236:237],v14 offset:28672\n"
"MF v[204:207],a[248:251],a[168:171],v[204:207]\n"
"LD v4,s[52:55],s65 offen offset:2048 lds\n"
"MF v[204:207],a[252:255],a[184:187],v[204:207]\n"
"TR a[238:239],v14 offset:30720\n"
"MF v[236:239],a[240:243],a[140:143],v[236:239]\n"
"MF v[236:239],a[244:247],a[156:159],v[236:239]\n"
"TR a[240:241],v15 offset:4096\n"
"MF v[236:239],a[248:251],a[172:175],v[236:239]\n"
"LD a[92:95],v1,s[44:47],s63 offen offset:1024 sc1\n"
"MF v[236:239],a[252:255],a[188:191],v[236:239]\n"
"TR a[242:243],v15 offset:6144\n"
"MF v[144:147],a[192:195],a[128:131],v[144:147]\n"
"WT lgkmcnt(15)\n"
"s_barrier\n"
"MF v[144:147],a[196:199],a[144:147],v[144:147]\n"
"TR a[244:245],v15 offset:12288\n"
"MF v[144:147],a[200:203],a[160:163],v[144:147]\n"
"LD a[96:99],v1,s[44:47],s60 offen offset:2048 sc1\n"
"MF v[144:147],a[204:207],a[176:179],v[144:147]\n"
"TR a[246:247],v15 offset:14336\n"
"MF v[176:179],a[192:195],a[132:135],v[176:179]\n"
"MF v[176:179],a[196:199],a[148:151],v[176:179]\n"
"TR a[248:249],v15 offset:20480\n"
"MF v[176:179],a[200:203],a[164:167],v[176:179]\n"
"LD a[100:103],v1,s[44:47],s61 offen offset:2048 sc1\n"
"MF v[176:179],a[204:207],a[180:183],v[176:179]\n"
"TR a[250:251],v15 offset:22528\n"
"MF v[208:211],a[192:195],a[136:139],v[208:211]\n"
"MF v[208:211],a[196:199],a[152:155],v[208:211]\n"
"TR a[252:253],v15 offset:28672\n"
"MF v[208:211],a[200:203],a[168:171],v[208:211]\n"
"LD v5,s[52:55],s65 offen offset:3072 lds\n"
"MF v[208:211],a[204:207],a[184:187],v[208:211]\n"
"TR a[254:255],v15 offset:30720\n"
"MF v[240:243],a[192:195],a[140:143],v[240:243]\n"
"MF v[240:243],a[196:199],a[156:159],v[240:243]\n"
"TR a[192:193],v12 offset:32768\n"
"MF v[240:243],a[200:203],a[172:175],v[240:243]\n"
"LD a[104:107],v1,s[44:47],s62 offen offset:2048 sc1\n"
"MF v[240:243],a[204:207],a[188:191],v[240:243]\n"
"TR a[194:195],v12 offset:34816\n"
"MF v[148:151],a[208:211],a[128:131],v[148:151]\n"
"WT lgkmcnt(15)\n"
"MF v[148:151],a[212:215],a[144:147],v[148:151]\n"
"TR a[196:197],v12 offset:40960\n"
"MF v[148:151],a[216:219],a[160:163],v[148:151]\n"
"LD a[108:111],v1,s[44:47],s63 offen offset:2048 sc1\n"
"MF v[148:151],a[220:223],a[176:179],v[148:151]\n"
"TR a[198:199],v12 offset:43008\n"
"MF v[180:183],a[208:211],a[132:135],v[180:183]\n"
"MF v[180:183],a[212:215],a[148:151],v[180:183]\n"
"TR a[200:201],v12 offset:49152\n"
"MF v[180:183],a[216:219],a[164:167],v[180:183]\n"
"MF v[180:183],a[220:223],a[180:183],v[180:183]\n"
"TR a[202:203],v12 offset:51200\n"
"MF v[212:215],a[208:211],a[136:139],v[212:215]\n"
"MF v[212:215],a[212:215],a[152:155],v[212:215]\n"
"s_add_u32 s65, s65, s23\n"
"s_add_u32 m0, s22, 131072\n"
"TR a[204:205],v12 offset:57344\n"
"MF v[212:215],a[216:219],a[168:171],v[212:215]\n"
"LD v2,s[52:55],s65 offen offset:0 lds\n"
"MF v[212:215],a[220:223],a[184:187],v[212:215]\n"
"TR a[206:207],v12 offset:59392\n"
"MF v[244:247],a[208:211],a[140:143],v[244:247]\n"
"MF v[244:247],a[212:215],a[156:159],v[244:247]\n"
"TR a[208:209],v13 offset:32768\n"
"MF v[244:247],a[216:219],a[172:175],v[244:247]\n"
"LD a[112:115],v1,s[44:47],s60 offen offset:3072 sc1\n"
"MF v[244:247],a[220:223],a[188:191],v[244:247]\n"
"TR a[210:211],v13 offset:34816\n"
"MF v[152:155],a[224:227],a[128:131],v[152:155]\n"
"WT lgkmcnt(15)\n"
"MF v[152:155],a[228:231],a[144:147],v[152:155]\n"
"TR a[212:213],v13 offset:40960\n"
"MF v[152:155],a[232:235],a[160:163],v[152:155]\n"
"LD a[116:119],v1,s[44:47],s61 offen offset:3072 sc1\n"
"MF v[152:155],a[236:239],a[176:179],v[152:155]\n"
"TR a[214:215],v13 offset:43008\n"
"MF v[184:187],a[224:227],a[132:135],v[184:187]\n"
"MF v[184:187],a[228:231],a[148:151],v[184:187]\n"
"TR a[216:217],v13 offset:49152\n"
"MF v[184:187],a[232:235],a[164:167],v[184:187]\n"
"LD a[120:123],v1,s[44:47],s62 offen offset:3072 sc1\n"
"MF v[184:187],a[236:239],a[180:183],v[184:187]\n"
"TR a[218:219],v13 offset:51200\n"
"MF v[216:219],a[224:227],a[136:139],v[216:219]\n"
"MF v[216:219],a[228:231],a[152:155],v[216:219]\n"
"TR a[220:221],v13 offset:57344\n"
"MF v[216:219],a[232:235],a[168:171],v[216:219]\n"
"LD v3,s[52:55],s65 offen offset:1024 lds\n"
"MF v[216:219],a[236:239],a[184:187],v[216:219]\n"
"TR a[222:223],v13 offset:59392\n"
"MF v[248:251],a[224:227],a[140:143],v[248:251]\n"
"MF v[248:251],a[228:231],a[156:159],v[248:251]\n"
"TR a[224:225],v14 offset:32768\n"
"MF v[248:251],a[232:235],a[172:175],v[248:251]\n"
"LD a[124:127],v1,s[44:47],s63 offen offset:3072 sc1\n"
"MF v[248:251],a[236:239],a[188:191],v[248:251]\n"
"TR a[226:227],v14 offset:34816\n"
"MF v[156:159],a[240:243],a[128:131],v[156:159]\n"
"WT lgkmcnt(15)\n"
"MF v[156:159],a[244:247],a[144:147],v[156:159]\n"
"TR a[228:229],v14 offset:40960\n"
"MF v[156:159],a[248:251],a[160:163],v[156:159]\n"
"LD v4,s[52:55],s65 offen offset:2048 lds\n"
"MF v[156:159],a[252:255],a[176:179],v[156:159]\n"
"TR a[230:231],v14 offset:43008\n"
"MF v[188:191],a[240:243],a[132:135],v[188:191]\n"
"MF v[188:191],a[244:247],a[148:151],v[188:191]\n"
"TR a[232:233],v14 offset:49152\n"
"MF v[188:191],a[248:251],a[164:167],v[188:191]\n"
"MF v[188:191],a[252:255],a[180:183],v[188:191]\n"
"TR a[234:235],v14 offset:51200\n"
"MF v[220:223],a[240:243],a[136:139],v[220:223]\n"
"MF v[220:223],a[244:247],a[152:155],v[220:223]\n"
"TR a[236:237],v14 offset:57344\n"
"MF v[220:223],a[248:251],a[168:171],v[220:223]\n"
"LD v5,s[52:55],s65 offen offset:3072 lds\n"
"MF v[220:223],a[252:255],a[184:187],v[220:223]\n"
"TR a[238:239],v14 offset:59392\n"
"MF v[252:255],a[240:243],a[140:143],v[252:255]\n"
"MF v[252:255],a[244:247],a[156:159],v[252:255]\n"
"s_add_u32 s65, s65, s23\n"
"s_add_u32 m0, s22, 147456\n"
"TR a[240:241],v15 offset:32768\n"
"MF v[252:255],a[248:251],a[172:175],v[252:255]\n"
"LD v2,s[52:55],s65 offen offset:0 lds\n"
"MF v[252:255],a[252:255],a[188:191],v[252:255]\n"
"WT vmcnt(46)\n"
"TR a[242:243],v15 offset:34816\n"
"MF v[128:131],a[192:195],a[0:3],v[128:131]\n"
"WT vmcnt(41) lgkmcnt(15)\n"
"MF v[128:131],a[196:199],a[16:19],v[128:131]\n"
"WT vmcnt(35)\n"
"s_mov_b32 s60, 2621440\n"
"TR a[244:245],v15 offset:40960\n"
"MF v[128:131],a[200:203],a[32:35],v[128:131]\n"
"WT vmcnt(30)\n"
"LD a[128:131],v1,s[44:47],s60 offen offset:0 sc1\n"
"MF v[128:131],a[204:207],a[48:51],v[128:131]\n"
"TR a[246:247],v15 offset:43008\n"
"MF v[160:163],a[192:195],a[4:7],v[160:163]\n"
"MF v[160:163],a[196:199],a[20:23],v[160:163]\n"
"s_mov_b32 s61, 2625536\n"
"TR a[248:249],v15 offset:49152\n"
"MF v[160:163],a[200:203],a[36:39],v[160:163]\n"
"WT vmcnt(29)\n"
"LD a[132:135],v1,s[44:47],s61 offen offset:0 sc1\n"
"MF v[160:163],a[204:207],a[52:55],v[160:163]\n"
"TR a[250:251],v15 offset:51200\n"
"MF v[192:195],a[192:195],a[8:11],v[192:195]\n"
"MF v[192:195],a[196:199],a[24:27],v[192:195]\n"
"TR a[252:253],v15 offset:57344\n"
"MF v[192:195],a[200:203],a[40:43],v[192:195]\n"
"WT vmcnt(29)\n"
"LD v3,s[52:55],s65 offen offset:1024 lds\n"
"MF v[192:195],a[204:207],a[56:59],v[192:195]\n"
"TR a[254:255],v15 offset:59392\n"
"MF v[224:227],a[192:195],a[12:15],v[224:227]\n"
"MF v[224:227],a[196:199],a[28:31],v[224:227]\n"
"s_mov_b32 s62, 2629632\n"
"TR a[192:193],v12 offset:36864\n"
"MF v[224:227],a[200:203],a[44:47],v[224:227]\n"
"WT vmcnt(29)\n"
"LD a[136:139],v1,s[44:47],s62 offen offset:0 sc1\n"
"MF v[224:227],a[204:207],a[60:63],v[224:227]\n"
"TR a[194:195],v12 offset:38912\n"
"MF v[132:135],a[208:211],a[0:3],v[132:135]\n"
"WT lgkmcnt(15)\n"
"MF v[132:135],a[212:215],a[16:19],v[132:135]\n"
"s_mov_b32 s63, 2633728\n"
"TR a[196:197],v12 offset:45056\n"
"MF v[132:135],a[216:219],a[32:35],v[132:135]\n"
"LD a[140:143],v1,s[44:47],s63 offen offset:0 sc1\n"
"MF v[132:135],a[220:223],a[48:51],v[132:135]\n"
"TR a[198:199],v12 offset:47104\n"
"MF v[164:167],a[208:211],a[4:7],v[164:167]\n"
"MF v[164:167],a[212:215],a[20:23],v[164:167]\n"
"TR a[200:201],v12 offset:53248\n"
"MF v[164:167],a[216:219],a[36:39],v[164:167]\n"
"MF v[164:167],a[220:223],a[52:55],v[164:167]\n"
"TR a[202:203],v12 offset:55296\n"
"MF v[196:199],a[208:211],a[8:11],v[196:199]\n"
"MF v[196:199],a[212:215],a[24:27],v[196:199]\n"
"TR a[204:205],v12 offset:61440\n"
"MF v[196:199],a[216:219],a[40:43],v[196:199]\n"
"LD v4,s[52:55],s65 offen offset:2048 lds\n"
"MF v[196:199],a[220:223],a[56:59],v[196:199]\n"
"TR a[206:207],v12 offset:63488\n"
"MF v[228:231],a[208:211],a[12:15],v[228:231]\n"
"MF v[228:231],a[212:215],a[28:31],v[228:231]\n"
"TR a[208:209],v13 offset:36864\n"
"MF v[228:231],a[216:219],a[44:47],v[228:231]\n"
"LD a[144:147],v1,s[44:47],s60 offen offset:1024 sc1\n"
"MF v[228:231],a[220:223],a[60:63],v[228:231]\n"
"TR a[210:211],v13 offset:38912\n"
"MF v[136:139],a[224:227],a[0:3],v[136:139]\n"
"WT lgkmcnt(15)\n"
"MF v[136:139],a[228:231],a[16:19],v[136:139]\n"
"TR a[212:213],v13 offset:45056\n"
"MF v[136:139],a[232:235],a[32:35],v[136:139]\n"
"LD a[148:151],v1,s[44:47],s61 offen offset:1024 sc1\n"
"MF v[136:139],a[236:239],a[48:51],v[136:139]\n"
"TR a[214:215],v13 offset:47104\n"
"MF v[168:171],a[224:227],a[4:7],v[168:171]\n"
"MF v[168:171],a[228:231],a[20:23],v[168:171]\n"
"TR a[216:217],v13 offset:53248\n"
"MF v[168:171],a[232:235],a[36:39],v[168:171]\n"
"LD a[152:155],v1,s[44:47],s62 offen offset:1024 sc1\n"
"MF v[168:171],a[236:239],a[52:55],v[168:171]\n"
"TR a[218:219],v13 offset:55296\n"
"MF v[200:203],a[224:227],a[8:11],v[200:203]\n"
"MF v[200:203],a[228:231],a[24:27],v[200:203]\n"
"TR a[220:221],v13 offset:61440\n"
"MF v[200:203],a[232:235],a[40:43],v[200:203]\n"
"LD v5,s[52:55],s65 offen offset:3072 lds\n"
"MF v[200:203],a[236:239],a[56:59],v[200:203]\n"
"TR a[222:223],v13 offset:63488\n"
"MF v[232:235],a[224:227],a[12:15],v[232:235]\n"
"MF v[232:235],a[228:231],a[28:31],v[232:235]\n"
"TR a[224:225],v14 offset:36864\n"
"MF v[232:235],a[232:235],a[44:47],v[232:235]\n"
"LD a[156:159],v1,s[44:47],s63 offen offset:1024 sc1\n"
"MF v[232:235],a[236:239],a[60:63],v[232:235]\n"
"TR a[226:227],v14 offset:38912\n"
"MF v[140:143],a[240:243],a[0:3],v[140:143]\n"
"WT lgkmcnt(15)\n"
"MF v[140:143],a[244:247],a[16:19],v[140:143]\n"
"TR a[228:229],v14 offset:45056\n"
"MF v[140:143],a[248:251],a[32:35],v[140:143]\n"
"LD a[160:163],v1,s[44:47],s60 offen offset:2048 sc1\n"
"MF v[140:143],a[252:255],a[48:51],v[140:143]\n"
"TR a[230:231],v14 offset:47104\n"
"MF v[172:175],a[240:243],a[4:7],v[172:175]\n"
"MF v[172:175],a[244:247],a[20:23],v[172:175]\n"
"TR a[232:233],v14 offset:53248\n"
"MF v[172:175],a[248:251],a[36:39],v[172:175]\n"
"MF v[172:175],a[252:255],a[52:55],v[172:175]\n"
"TR a[234:235],v14 offset:55296\n"
"MF v[204:207],a[240:243],a[8:11],v[204:207]\n"
"MF v[204:207],a[244:247],a[24:27],v[204:207]\n"
"TR a[236:237],v14 offset:61440\n"
"MF v[204:207],a[248:251],a[40:43],v[204:207]\n"
"LD a[164:167],v1,s[44:47],s61 offen offset:2048 sc1\n"
"MF v[204:207],a[252:255],a[56:59],v[204:207]\n"
"TR a[238:239],v14 offset:63488\n"
"MF v[236:239],a[240:243],a[12:15],v[236:239]\n"
"MF v[236:239],a[244:247],a[28:31],v[236:239]\n"
"TR a[240:241],v15 offset:36864\n"
"MF v[236:239],a[248:251],a[44:47],v[236:239]\n"
"LD a[168:171],v1,s[44:47],s62 offen offset:2048 sc1\n"
"MF v[236:239],a[252:255],a[60:63],v[236:239]\n"
"TR a[242:243],v15 offset:38912\n"
"MF v[144:147],a[192:195],a[0:3],v[144:147]\n"
"WT lgkmcnt(15)\n"
"s_barrier\n"
"MF v[144:147],a[196:199],a[16:19],v[144:147]\n"
"TR a[244:245],v15 offset:45056\n"
"MF v[144:147],a[200:203],a[32:35],v[144:147]\n"
"LD a[172:175],v1,s[44:47],s63 offen offset:2048 sc1\n"
"MF v[144:147],a[204:207],a[48:51],v[144:147]\n"
"TR a[246:247],v15 offset:47104\n"
"MF v[176:179],a[192:195],a[4:7],v[176:179]\n"
"MF v[176:179],a[196:199],a[20:23],v[176:179]\n"
"TR a[248:249],v15 offset:53248\n"
"MF v[176:179],a[200:203],a[36:39],v[176:179]\n"
"LD a[176:179],v1,s[44:47],s60 offen offset:3072 sc1\n"
"MF v[176:179],a[204:207],a[52:55],v[176:179]\n"
"TR a[250:251],v15 offset:55296\n"
"MF v[208:211],a[192:195],a[8:11],v[208:211]\n"
"MF v[208:211],a[196:199],a[24:27],v[208:211]\n"
"s_add_u32 s65, s65, s23\n"
"s_add_u32 m0, s22, 0\n"
"TR a[252:253],v15 offset:61440\n"
"MF v[208:211],a[200:203],a[40:43],v[208:211]\n"
"LD v2,s[52:55],s65 offen offset:0 lds\n"
"MF v[208:211],a[204:207],a[56:59],v[208:211]\n"
"TR a[254:255],v15 offset:63488\n"
"MF v[240:243],a[192:195],a[12:15],v[240:243]\n"
"MF v[240:243],a[196:199],a[28:31],v[240:243]\n"
"TR a[192:193],v24 offset:0\n"
"MF v[240:243],a[200:203],a[44:47],v[240:243]\n"
"LD a[180:183],v1,s[44:47],s61 offen offset:3072 sc1\n"
"MF v[240:243],a[204:207],a[60:63],v[240:243]\n"
"TR a[194:195],v24 offset:2048\n"
"MF v[148:151],a[208:211],a[0:3],v[148:151]\n"
"WT lgkmcnt(15)\n"
"MF v[148:151],a[212:215],a[16:19],v[148:151]\n"
"TR a[196:197],v24 offset:8192\n"
"MF v[148:151],a[216:219],a[32:35],v[148:151]\n"
"LD a[184:187],v1,s[44:47],s62 offen offset:3072 sc1\n"
"MF v[148:151],a[220:223],a[48:51],v[148:151]\n"
"TR a[198:199],v24 offset:10240\n"
"MF v[180:183],a[208:211],a[4:7],v[180:183]\n"
"MF v[180:183],a[212:215],a[20:23],v[180:183]\n"
"TR a[200:201],v24 offset:16384\n"
"MF v[180:183],a[216:219],a[36:39],v[180:183]\n"
"MF v[180:183],a[220:223],a[52:55],v[180:183]\n"
"TR a[202:203],v24 offset:18432\n"
"MF v[212:215],a[208:211],a[8:11],v[212:215]\n"
"MF v[212:215],a[212:215],a[24:27],v[212:215]\n"
"TR a[204:205],v24 offset:24576\n"
"MF v[212:215],a[216:219],a[40:43],v[212:215]\n"
"LD v3,s[52:55],s65 offen offset:1024 lds\n"
"MF v[212:215],a[220:223],a[56:59],v[212:215]\n"
"TR a[206:207],v24 offset:26624\n"
"MF v[244:247],a[208:211],a[12:15],v[244:247]\n"
"MF v[244:247],a[212:215],a[28:31],v[244:247]\n"
"TR a[208:209],v25 offset:0\n"
"MF v[244:247],a[216:219],a[44:47],v[244:247]\n"
"LD a[188:191],v1,s[44:47],s63 offen offset:3072 sc1\n"
"MF v[244:247],a[220:223],a[60:63],v[244:247]\n"
"TR a[210:211],v25 offset:2048\n"
"MF v[152:155],a[224:227],a[0:3],v[152:155]\n"
"WT lgkmcnt(15)\n"
"MF v[152:155],a[228:231],a[16:19],v[152:155]\n"
"TR a[212:213],v25 offset:8192\n"
"MF v[152:155],a[232:235],a[32:35],v[152:155]\n"
"LD v4,s[52:55],s65 offen offset:2048 lds\n"
"MF v[152:155],a[236:239],a[48:51],v[152:155]\n"
"TR a[214:215],v25 offset:10240\n"
"MF v[184:187],a[224:227],a[4:7],v[184:187]\n"
"MF v[184:187],a[228:231],a[20:23],v[184:187]\n"
"TR a[216:217],v25 offset:16384\n"
"MF v[184:187],a[232:235],a[36:39],v[184:187]\n"
"LD v5,s[52:55],s65 offen offset:3072 lds\n"
"MF v[184:187],a[236:239],a[52:55],v[184:187]\n"
"TR a[218:219],v25 offset:18432\n"
"MF v[216:219],a[224:227],a[8:11],v[216:219]\n"
"MF v[216:219],a[228:231],a[24:27],v[216:219]\n"
"s_add_u32 s65, s65, s23\n"
"s_add_u32 m0, s22, 16384\n"
"TR a[220:221],v25 offset:24576\n"
"MF v[216:219],a[232:235],a[40:43],v[216:219]\n"
"LD v2,s[52:55],s65 offen offset:0 lds\n"
"MF v[216:219],a[236:239],a[56:59],v[216:219]\n"
"TR a[222:223],v25 offset:26624\n"
"MF v[248:251],a[224:227],a[12:15],v[248:251]\n"
"MF v[248:251],a[228:231],a[28:31],v[248:251]\n"
"TR a[224:225],v26 offset:0\n"
"MF v[248:251],a[232:235],a[44:47],v[248:251]\n"
"LD v3,s[52:55],s65 offen offset:1024 lds\n"
"MF v[248:251],a[236:239],a[60:63],v[248:251]\n"
"TR a[226:227],v26 offset:2048\n"
"MF v[156:159],a[240:243],a[0:3],v[156:159]\n"
"WT lgkmcnt(15)\n"
"MF v[156:159],a[244:247],a[16:19],v[156:159]\n"
"TR a[228:229],v26 offset:8192\n"
"MF v[156:159],a[248:251],a[32:35],v[156:159]\n"
"LD v4,s[52:55],s65 offen offset:2048 lds\n"
"MF v[156:159],a[252:255],a[48:51],v[156:159]\n"
"TR a[230:231],v26 offset:10240\n"
"MF v[188:191],a[240:243],a[4:7],v[188:191]\n"
"MF v[188:191],a[244:247],a[20:23],v[188:191]\n"
"TR a[232:233],v26 offset:16384\n"
"MF v[188:191],a[248:251],a[36:39],v[188:191]\n"
"MF v[188:191],a[252:255],a[52:55],v[188:191]\n"
"TR a[234:235],v26 offset:18432\n"
"MF v[220:223],a[240:243],a[8:11],v[220:223]\n"
"MF v[220:223],a[244:247],a[24:27],v[220:223]\n"
"TR a[236:237],v26 offset:24576\n"
"MF v[220:223],a[248:251],a[40:43],v[220:223]\n"
"LD v5,s[52:55],s65 offen offset:3072 lds\n"
"MF v[220:223],a[252:255],a[56:59],v[220:223]\n"
"TR a[238:239],v26 offset:26624\n"
"MF v[252:255],a[240:243],a[12:15],v[252:255]\n"
"MF v[252:255],a[244:247],a[28:31],v[252:255]\n"
"TR a[240:241],v27 offset:0\n"
"MF v[252:255],a[248:251],a[44:47],v[252:255]\n"
"MF v[252:255],a[252:255],a[60:63],v[252:255]\n"
"WT vmcnt(51)\n"
"TR a[242:243],v27 offset:2048\n"
"MF v[128:131],a[192:195],a[64:67],v[128:131]\n"
"WT vmcnt(46) lgkmcnt(15)\n"
"MF v[128:131],a[196:199],a[80:83],v[128:131]\n"
"WT vmcnt(40)\n"
"s_mov_b32 s60, 3145728\n"
"TR a[244:245],v27 offset:8192\n"
"MF v[128:131],a[200:203],a[96:99],v[128:131]\n"
"WT vmcnt(34)\n"
"LD a[0:3],v1,s[44:47],s60 offen offset:0 sc1\n"
"MF v[128:131],a[204:207],a[112:115],v[128:131]\n"
"TR a[246:247],v27 offset:10240\n"
"MF v[160:163],a[192:195],a[68:71],v[160:163]\n"
"MF v[160:163],a[196:199],a[84:87],v[160:163]\n"
"s_mov_b32 s61, 3149824\n"
"TR a[248:249],v27 offset:16384\n"
"MF v[160:163],a[200:203],a[100:103],v[160:163]\n"
"WT vmcnt(34)\n"
"LD a[4:7],v1,s[44:47],s61 offen offset:0 sc1\n"
"MF v[160:163],a[204:207],a[116:119],v[160:163]\n"
"TR a[250:251],v27 offset:18432\n"
"MF v[192:195],a[192:195],a[72:75],v[192:195]\n"
"MF v[192:195],a[196:199],a[88:91],v[192:195]\n"
"s_mov_b32 s62, 3153920\n"
"TR a[252:253],v27 offset:24576\n"
"MF v[192:195],a[200:203],a[104:107],v[192:195]\n"
"WT vmcnt(34)\n"
"LD a[8:11],v1,s[44:47],s62 offen offset:0 sc1\n"
"MF v[192:195],a[204:207],a[120:123],v[192:195]\n"
"TR a[254:255],v27 offset:26624\n"
"MF v[224:227],a[192:195],a[76:79],v[224:227]\n"
"MF v[224:227],a[196:199],a[92:95],v[224:227]\n"
"s_mov_b32 s63, 3158016\n"
"TR a[192:193],v24 offset:4096\n"
"MF v[224:227],a[200:203],a[108:111],v[224:227]\n"
"WT vmcnt(33)\n"
"LD a[12:15],v1,s[44:47],s63 offen offset:0 sc1\n"
"MF v[224:227],a[204:207],a[124:127],v[224:227]\n"
"TR a[194:195],v24 offset:6144\n"
"MF v[132:135],a[208:211],a[64:67],v[132:135]\n"
"WT lgkmcnt(15)\n"
"MF v[132:135],a[212:215],a[80:83],v[132:135]\n"
"TR a[196:197],v24 offset:12288\n"
"MF v[132:135],a[216:219],a[96:99],v[132:135]\n"
"LD a[16:19],v1,s[44:47],s60 offen offset:1024 sc1\n"
"MF v[132:135],a[220:223],a[112:115],v[132:135]\n"
"TR a[198:199],v24 offset:14336\n"
"MF v[164:167],a[208:211],a[68:71],v[164:167]\n"
"MF v[164:167],a[212:215],a[84:87],v[164:167]\n"
"TR a[200:201],v24 offset:20480\n"
"MF v[164:167],a[216:219],a[100:103],v[164:167]\n"
"MF v[164:167],a[220:223],a[116:119],v[164:167]\n"
"TR a[202:203],v24 offset:22528\n"
"MF v[196:199],a[208:211],a[72:75],v[196:199]\n"
"MF v[196:199],a[212:215],a[88:91],v[196:199]\n"
"TR a[204:205],v24 offset:28672\n"
"MF v[196:199],a[216:219],a[104:107],v[196:199]\n"
"LD a[20:23],v1,s[44:47],s61 offen offset:1024 sc1\n"
"MF v[196:199],a[220:223],a[120:123],v[196:199]\n"
"TR a[206:207],v24 offset:30720\n"
"MF v[228:231],a[208:211],a[76:79],v[228:231]\n"
"MF v[228:231],a[212:215],a[92:95],v[228:231]\n"
"TR a[208:209],v25 offset:4096\n"
"MF v[228:231],a[216:219],a[108:111],v[228:231]\n"
"LD a[24:27],v1,s[44:47],s62 offen offset:1024 sc1\n"
"MF v[228:231],a[220:223],a[124:127],v[228:231]\n"
"TR a[210:211],v25 offset:6144\n"
"MF v[136:139],a[224:227],a[64:67],v[136:139]\n"
"WT lgkmcnt(15)\n"
"MF v[136:139],a[228:231],a[80:83],v[136:139]\n"
"TR a[212:213],v25 offset:12288\n"
"MF v[136:139],a[232:235],a[96:99],v[136:139]\n"
"LD a[28:31],v1,s[44:47],s63 offen offset:1024 sc1\n"
"MF v[136:139],a[236:239],a[112:115],v[136:139]\n"
"TR a[214:215],v25 offset:14336\n"
"MF v[168:171],a[224:227],a[68:71],v[168:171]\n"
"MF v[168:171],a[228:231],a[84:87],v[168:171]\n"
"TR a[216:217],v25 offset:20480\n"
"MF v[168:171],a[232:235],a[100:103],v[168:171]\n"
"LD a[32:35],v1,s[44:47],s60 offen offset:2048 sc1\n"
"MF v[168:171],a[236:239],a[116:119],v[168:171]\n"
"TR a[218:219],v25 offset:22528\n"
"MF v[200:203],a[224:227],a[72:75],v[200:203]\n"
"MF v[200:203],a[228:231],a[88:91],v[200:203]\n"
"TR a[220:221],v25 offset:28672\n"
"MF v[200:203],a[232:235],a[104:107],v[200:203]\n"
"LD a[36:39],v1,s[44:47],s61 offen offset:2048 sc1\n"
"MF v[200:203],a[236:239],a[120:123],v[200:203]\n"
"TR a[222:223],v25 offset:30720\n"
"MF v[232:235],a[224:227],a[76:79],v[232:235]\n"
"MF v[232:235],a[228:231],a[92:95],v[232:235]\n"
"TR a[224:225],v26 offset:4096\n"
"MF v[232:235],a[232:235],a[108:111],v[232:235]\n"
"LD a[40:43],v1,s[44:47],s62 offen offset:2048 sc1\n"
"MF v[232:235],a[236:239],a[124:127],v[232:235]\n"
"TR a[226:227],v26 offset:6144\n"
"MF v[140:143],a[240:243],a[64:67],v[140:143]\n"
"WT lgkmcnt(15)\n"
"MF v[140:143],a[244:247],a[80:83],v[140:143]\n"
"TR a[228:229],v26 offset:12288\n"
"MF v[140:143],a[248:251],a[96:99],v[140:143]\n"
"LD a[44:47],v1,s[44:47],s63 offen offset:2048 sc1\n"
"MF v[140:143],a[252:255],a[112:115],v[140:143]\n"
"TR a[230:231],v26 offset:14336\n"
"MF v[172:175],a[240:243],a[68:71],v[172:175]\n"
"MF v[172:175],a[244:247],a[84:87],v[172:175]\n"
"TR a[232:233],v26 offset:20480\n"
"MF v[172:175],a[248:251],a[100:103],v[172:175]\n"
"MF v[172:175],a[252:255],a[116:119],v[172:175]\n"
"TR a[234:235],v26 offset:22528\n"
"MF v[204:207],a[240:243],a[72:75],v[204:207]\n"
"MF v[204:207],a[244:247],a[88:91],v[204:207]\n"
"TR a[236:237],v26 offset:28672\n"
"MF v[204:207],a[248:251],a[104:107],v[204:207]\n"
"LD a[48:51],v1,s[44:47],s60 offen offset:3072 sc1\n"
"MF v[204:207],a[252:255],a[120:123],v[204:207]\n"
"TR a[238:239],v26 offset:30720\n"
"MF v[236:239],a[240:243],a[76:79],v[236:239]\n"
"MF v[236:239],a[244:247],a[92:95],v[236:239]\n"
"TR a[240:241],v27 offset:4096\n"
"MF v[236:239],a[248:251],a[108:111],v[236:239]\n"
"LD a[52:55],v1,s[44:47],s61 offen offset:3072 sc1\n"
"MF v[236:239],a[252:255],a[124:127],v[236:239]\n"
"TR a[242:243],v27 offset:6144\n"
"MF v[144:147],a[192:195],a[64:67],v[144:147]\n"
"WT lgkmcnt(15)\n"
"s_barrier\n"
"MF v[144:147],a[196:199],a[80:83],v[144:147]\n"
"TR a[244:245],v27 offset:12288\n"
"MF v[144:147],a[200:203],a[96:99],v[144:147]\n"
"LD a[56:59],v1,s[44:47],s62 offen offset:3072 sc1\n"
"MF v[144:147],a[204:207],a[112:115],v[144:147]\n"
"TR a[246:247],v27 offset:14336\n"
"MF v[176:179],a[192:195],a[68:71],v[176:179]\n"
"MF v[176:179],a[196:199],a[84:87],v[176:179]\n"
"TR a[248:249],v27 offset:20480\n"
"MF v[176:179],a[200:203],a[100:103],v[176:179]\n"
"LD a[60:63],v1,s[44:47],s63 offen offset:3072 sc1\n"
"MF v[176:179],a[204:207],a[116:119],v[176:179]\n"
"TR a[250:251],v27 offset:22528\n"
"MF v[208:211],a[192:195],a[72:75],v[208:211]\n"
"MF v[208:211],a[196:199],a[88:91],v[208:211]\n"
"TR a[252:253],v27 offset:28672\n"
"MF v[208:211],a[200:203],a[104:107],v[208:211]\n"
"MF v[208:211],a[204:207],a[120:123],v[208:211]\n"
"TR a[254:255],v27 offset:30720\n"
"MF v[240:243],a[192:195],a[76:79],v[240:243]\n"
"MF v[240:243],a[196:199],a[92:95],v[240:243]\n"
"TR a[192:193],v24 offset:32768\n"
"MF v[240:243],a[200:203],a[108:111],v[240:243]\n"
"MF v[240:243],a[204:207],a[124:127],v[240:243]\n"
"TR a[194:195],v24 offset:34816\n"
"MF v[148:151],a[208:211],a[64:67],v[148:151]\n"
"WT lgkmcnt(15)\n"
"MF v[148:151],a[212:215],a[80:83],v[148:151]\n"
"TR a[196:197],v24 offset:40960\n"
"MF v[148:151],a[216:219],a[96:99],v[148:151]\n"
"MF v[148:151],a[220:223],a[112:115],v[148:151]\n"
"TR a[198:199],v24 offset:43008\n"
"MF v[180:183],a[208:211],a[68:71],v[180:183]\n"
"MF v[180:183],a[212:215],a[84:87],v[180:183]\n"
"TR a[200:201],v24 offset:49152\n"
"MF v[180:183],a[216:219],a[100:103],v[180:183]\n"
"MF v[180:183],a[220:223],a[116:119],v[180:183]\n"
"TR a[202:203],v24 offset:51200\n"
"MF v[212:215],a[208:211],a[72:75],v[212:215]\n"
"MF v[212:215],a[212:215],a[88:91],v[212:215]\n"
"TR a[204:205],v24 offset:57344\n"
"MF v[212:215],a[216:219],a[104:107],v[212:215]\n"
"MF v[212:215],a[220:223],a[120:123],v[212:215]\n"
"TR a[206:207],v24 offset:59392\n"
"MF v[244:247],a[208:211],a[76:79],v[244:247]\n"
"MF v[244:247],a[212:215],a[92:95],v[244:247]\n"
"TR a[208:209],v25 offset:32768\n"
"MF v[244:247],a[216:219],a[108:111],v[244:247]\n"
"MF v[244:247],a[220:223],a[124:127],v[244:247]\n"
"TR a[210:211],v25 offset:34816\n"
"MF v[152:155],a[224:227],a[64:67],v[152:155]\n"
"WT lgkmcnt(15)\n"
"MF v[152:155],a[228:231],a[80:83],v[152:155]\n"
"TR a[212:213],v25 offset:40960\n"
"MF v[152:155],a[232:235],a[96:99],v[152:155]\n"
"MF v[152:155],a[236:239],a[112:115],v[152:155]\n"
"TR a[214:215],v25 offset:43008\n"
"MF v[184:187],a[224:227],a[68:71],v[184:187]\n"
"MF v[184:187],a[228:231],a[84:87],v[184:187]\n"
"TR a[216:217],v25 offset:49152\n"
"MF v[184:187],a[232:235],a[100:103],v[184:187]\n"
"MF v[184:187],a[236:239],a[116:119],v[184:187]\n"
"TR a[218:219],v25 offset:51200\n"
"MF v[216:219],a[224:227],a[72:75],v[216:219]\n"
"MF v[216:219],a[228:231],a[88:91],v[216:219]\n"
"TR a[220:221],v25 offset:57344\n"
"MF v[216:219],a[232:235],a[104:107],v[216:219]\n"
"MF v[216:219],a[236:239],a[120:123],v[216:219]\n"
"TR a[222:223],v25 offset:59392\n"
"MF v[248:251],a[224:227],a[76:79],v[248:251]\n"
"MF v[248:251],a[228:231],a[92:95],v[248:251]\n"
"TR a[224:225],v26 offset:32768\n"
"MF v[248:251],a[232:235],a[108:111],v[248:251]\n"
"MF v[248:251],a[236:239],a[124:127],v[248:251]\n"
"TR a[226:227],v26 offset:34816\n"
"MF v[156:159],a[240:243],a[64:67],v[156:159]\n"
"WT lgkmcnt(15)\n"
"MF v[156:159],a[244:247],a[80:83],v[156:159]\n"
"TR a[228:229],v26 offset:40960\n"
"MF v[156:159],a[248:251],a[96:99],v[156:159]\n"
"MF v[156:159],a[252:255],a[112:115],v[156:159]\n"
"TR a[230:231],v26 offset:43008\n"
"MF v[188:191],a[240:243],a[68:71],v[188:191]\n"
"MF v[188:191],a[244:247],a[84:87],v[188:191]\n"
"TR a[232:233],v26 offset:49152\n"
"MF v[188:191],a[248:251],a[100:103],v[188:191]\n"
"MF v[188:191],a[252:255],a[116:119],v[188:191]\n"
"TR a[234:235],v26 offset:51200\n"
"MF v[220:223],a[240:243],a[72:75],v[220:223]\n"
"MF v[220:223],a[244:247],a[88:91],v[220:223]\n"
"TR a[236:237],v26 offset:57344\n"
"MF v[220:223],a[248:251],a[104:107],v[220:223]\n"
"MF v[220:223],a[252:255],a[120:123],v[220:223]\n"
"TR a[238:239],v26 offset:59392\n"
"MF v[252:255],a[240:243],a[76:79],v[252:255]\n"
"MF v[252:255],a[244:247],a[92:95],v[252:255]\n"
"TR a[240:241],v27 offset:32768\n"
"MF v[252:255],a[248:251],a[108:111],v[252:255]\n"
"MF v[252:255],a[252:255],a[124:127],v[252:255]\n"
"WT vmcnt(42)\n"
"TR a[242:243],v27 offset:34816\n"
"MF v[128:131],a[192:195],a[128:131],v[128:131]\n"
"WT vmcnt(36) lgkmcnt(15)\n"
"MF v[128:131],a[196:199],a[144:147],v[128:131]\n"
"WT vmcnt(31)\n"
"s_mov_b32 s60, 3670016\n"
"TR a[244:245],v27 offset:40960\n"
"MF v[128:131],a[200:203],a[160:163],v[128:131]\n"
"WT vmcnt(27)\n"
"LD a[64:67],v1,s[44:47],s60 offen offset:0 sc1\n"
"MF v[128:131],a[204:207],a[176:179],v[128:131]\n"
"TR a[246:247],v27 offset:43008\n"
"MF v[160:163],a[192:195],a[132:135],v[160:163]\n"
"MF v[160:163],a[196:199],a[148:151],v[160:163]\n"
"s_mov_b32 s61, 3674112\n"
"TR a[248:249],v27 offset:49152\n"
"MF v[160:163],a[200:203],a[164:167],v[160:163]\n"
"WT vmcnt(26)\n"
"LD a[68:71],v1,s[44:47],s61 offen offset:0 sc1\n"
"MF v[160:163],a[204:207],a[180:183],v[160:163]\n"
"TR a[250:251],v27 offset:51200\n"
"MF v[192:195],a[192:195],a[136:139],v[192:195]\n"
"MF v[192:195],a[196:199],a[152:155],v[192:195]\n"
"s_mov_b32 s62, 3678208\n"
"TR a[252:253],v27 offset:57344\n"
"MF v[192:195],a[200:203],a[168:171],v[192:195]\n"
"WT vmcnt(26)\n"
"LD a[72:75],v1,s[44:47],s62 offen offset:0 sc1\n"
"MF v[192:195],a[204:207],a[184:187],v[192:195]\n"
"TR a[254:255],v27 offset:59392\n"
"MF v[224:227],a[192:195],a[140:143],v[224:227]\n"
"MF v[224:227],a[196:199],a[156:159],v[224:227]\n"
"s_mov_b32 s63, 3682304\n"
"TR a[192:193],v24 offset:36864\n"
"MF v[224:227],a[200:203],a[172:175],v[224:227]\n"
"WT vmcnt(25)\n"
"LD a[76:79],v1,s[44:47],s63 offen offset:0 sc1\n"
"MF v[224:227],a[204:207],a[188:191],v[224:227]\n"
"TR a[194:195],v24 offset:38912\n"
"MF v[132:135],a[208:211],a[128:131],v[132:135]\n"
"WT lgkmcnt(15)\n"
"MF v[132:135],a[212:215],a[144:147],v[132:135]\n"
"TR a[196:197],v24 offset:45056\n"
"MF v[132:135],a[216:219],a[160:163],v[132:135]\n"
"LD a[80:83],v1,s[44:47],s60 offen offset:1024 sc1\n"
"MF v[132:135],a[220:223],a[176:179],v[132:135]\n"
"TR a[198:199],v24 offset:47104\n"
"MF v[164:167],a[208:211],a[132:135],v[164:167]\n"
"MF v[164:167],a[212:215],a[148:151],v[164:167]\n"
"TR a[200:201],v24 offset:53248\n"
"MF v[164:167],a[216:219],a[164:167],v[164:167]\n"
"LD a[84:87],v1,s[44:47],s61 offen offset:1024 sc1\n"
"MF v[164:167],a[220:223],a[180:183],v[164:167]\n"
"TR a[202:203],v24 offset:55296\n"
"MF v[196:199],a[208:211],a[136:139],v[196:199]\n"
"MF v[196:199],a[212:215],a[152:155],v[196:199]\n"
"TR a[204:205],v24 offset:61440\n"
"MF v[196:199],a[216:219],a[168:171],v[196:199]\n"
"LD a[88:91],v1,s[44:47],s62 offen offset:1024 sc1\n"
"MF v[196:199],a[220:223],a[184:187],v[196:199]\n"
"TR a[206:207],v24 offset:63488\n"
"MF v[228:231],a[208:211],a[140:143],v[228:231]\n"
"MF v[228:231],a[212:215],a[156:159],v[228:231]\n"
"TR a[208:209],v25 offset:36864\n"
"MF v[228:231],a[216:219],a[172:175],v[228:231]\n"
"LD a[92:95],v1,s[44:47],s63 offen offset:1024 sc1\n"
"MF v[228:231],a[220:223],a[188:191],v[228:231]\n"
"TR a[210:211],v25 offset:38912\n"
"MF v[136:139],a[224:227],a[128:131],v[136:139]\n"
"WT lgkmcnt(15)\n"
"MF v[136:139],a[228:231],a[144:147],v[136:139]\n"
"TR a[212:213],v25 offset:45056\n"
"MF v[136:139],a[232:235],a[160:163],v[136:139]\n"
"LD a[96:99],v1,s[44:47],s60 offen offset:2048 sc1\n"
"MF v[136:139],a[236:239],a[176:179],v[136:139]\n"
"TR a[214:215],v25 offset:47104\n"
"MF v[168:171],a[224:227],a[132:135],v[168:171]\n"
"MF v[168:171],a[228:231],a[148:151],v[168:171]\n"
"TR a[216:217],v25 offset:53248\n"
"MF v[168:171],a[232:235],a[164:167],v[168:171]\n"
"LD a[100:103],v1,s[44:47],s61 offen offset:2048 sc1\n"
"MF v[168:171],a[236:239],a[180:183],v[168:171]\n"
"TR a[218:219],v25 offset:55296\n"
"MF v[200:203],a[224:227],a[136:139],v[200:203]\n"
"MF v[200:203],a[228:231],a[152:155],v[200:203]\n"
"TR a[220:221],v25 offset:61440\n"
"MF v[200:203],a[232:235],a[168:171],v[200:203]\n"
"LD a[104:107],v1,s[44:47],s62 offen offset:2048 sc1\n"
"MF v[200:203],a[236:239],a[184:187],v[200:203]\n"
"TR a[222:223],v25 offset:63488\n"
"MF v[232:235],a[224:227],a[140:143],v[232:235]\n"
"MF v[232:235],a[228:231],a[156:159],v[232:235]\n"
"TR a[224:225],v26 offset:36864\n"
"MF v[232:235],a[232:235],a[172:175],v[232:235]\n"
"LD a[108:111],v1,s[44:47],s63 offen offset:2048 sc1\n"
"MF v[232:235],a[236:239],a[188:191],v[232:235]\n"
"TR a[226:227],v26 offset:38912\n"
"MF v[140:143],a[240:243],a[128:131],v[140:143]\n"
"WT lgkmcnt(15)\n"
"MF v[140:143],a[244:247],a[144:147],v[140:143]\n"
"TR a[228:229],v26 offset:45056\n"
"MF v[140:143],a[248:251],a[160:163],v[140:143]\n"
"LD a[112:115],v1,s[44:47],s60 offen offset:3072 sc1\n"
"MF v[140:143],a[252:255],a[176:179],v[140:143]\n"
"TR a[230:231],v26 offset:47104\n"
"MF v[172:175],a[240:243],a[132:135],v[172:175]\n"
"MF v[172:175],a[244:247],a[148:151],v[172:175]\n"
"TR a[232:233],v26 offset:53248\n"
"MF v[172:175],a[248:251],a[164:167],v[172:175]\n"
"LD a[116:119],v1,s[44:47],s61 offen offset:3072 sc1\n"
"MF v[172:175],a[252:255],a[180:183],v[172:175]\n"
"TR a[234:235],v26 offset:55296\n"
"MF v[204:207],a[240:243],a[136:139],v[204:207]\n"
"MF v[204:207],a[244:247],a[152:155],v[204:207]\n"
"TR a[236:237],v26 offset:61440\n"
"MF v[204:207],a[248:251],a[168:171],v[204:207]\n"
"LD a[120:123],v1,s[44:47],s62 offen offset:3072 sc1\n"
"MF v[204:207],a[252:255],a[184:187],v[204:207]\n"
"TR a[238:239],v26 offset:63488\n"
"MF v[236:239],a[240:243],a[140:143],v[236:239]\n"
"MF v[236:239],a[244:247],a[156:159],v[236:239]\n"
"TR a[240:241],v27 offset:36864\n"
"MF v[236:239],a[248:251],a[172:175],v[236:239]\n"
"LD a[124:127],v1,s[44:47],s63 offen offset:3072 sc1\n"
"MF v[236:239],a[252:255],a[188:191],v[236:239]\n"
"TR a[242:243],v27 offset:38912\n"
"MF v[144:147],a[192:195],a[128:131],v[144:147]\n"
"WT lgkmcnt(15)\n"
"WT vmcnt(32)\n"
"s_barrier\n"
"MF v[144:147],a[196:199],a[144:147],v[144:147]\n"
"TR a[244:245],v27 offset:45056\n"
"MF v[144:147],a[200:203],a[160:163],v[144:147]\n"
"MF v[144:147],a[204:207],a[176:179],v[144:147]\n"
"TR a[246:247],v27 offset:47104\n"
"MF v[176:179],a[192:195],a[132:135],v[176:179]\n"
"MF v[176:179],a[196:199],a[148:151],v[176:179]\n"
"TR a[248:249],v27 offset:53248\n"
"MF v[176:179],a[200:203],a[164:167],v[176:179]\n"
"MF v[176:179],a[204:207],a[180:183],v[176:179]\n"
"TR a[250:251],v27 offset:55296\n"
"MF v[208:211],a[192:195],a[136:139],v[208:211]\n"
"MF v[208:211],a[196:199],a[152:155],v[208:211]\n"
"TR a[252:253],v27 offset:61440\n"
"MF v[208:211],a[200:203],a[168:171],v[208:211]\n"
"MF v[208:211],a[204:207],a[184:187],v[208:211]\n"
"TR a[254:255],v27 offset:63488\n"
"MF v[240:243],a[192:195],a[140:143],v[240:243]\n"
"MF v[240:243],a[196:199],a[156:159],v[240:243]\n"
"TR a[192:193],v28 offset:0\n"
"MF v[240:243],a[200:203],a[172:175],v[240:243]\n"
"MF v[240:243],a[204:207],a[188:191],v[240:243]\n"
"TR a[194:195],v28 offset:2048\n"
"MF v[148:151],a[208:211],a[128:131],v[148:151]\n"
"WT lgkmcnt(15)\n"
"MF v[148:151],a[212:215],a[144:147],v[148:151]\n"
"TR a[196:197],v28 offset:8192\n"
"MF v[148:151],a[216:219],a[160:163],v[148:151]\n"
"MF v[148:151],a[220:223],a[176:179],v[148:151]\n"
"TR a[198:199],v28 offset:10240\n"
"MF v[180:183],a[208:211],a[132:135],v[180:183]\n"
"MF v[180:183],a[212:215],a[148:151],v[180:183]\n"
"TR a[200:201],v28 offset:16384\n"
"MF v[180:183],a[216:219],a[164:167],v[180:183]\n"
"MF v[180:183],a[220:223],a[180:183],v[180:183]\n"
"TR a[202:203],v28 offset:18432\n"
"MF v[212:215],a[208:211],a[136:139],v[212:215]\n"
"MF v[212:215],a[212:215],a[152:155],v[212:215]\n"
"TR a[204:205],v28 offset:24576\n"
"MF v[212:215],a[216:219],a[168:171],v[212:215]\n"
"MF v[212:215],a[220:223],a[184:187],v[212:215]\n"
"TR a[206:207],v28 offset:26624\n"
"MF v[244:247],a[208:211],a[140:143],v[244:247]\n"
"MF v[244:247],a[212:215],a[156:159],v[244:247]\n"
"TR a[208:209],v12 offset:0\n"
"MF v[244:247],a[216:219],a[172:175],v[244:247]\n"
"MF v[244:247],a[220:223],a[188:191],v[244:247]\n"
"TR a[210:211],v12 offset:2048\n"
"MF v[152:155],a[224:227],a[128:131],v[152:155]\n"
"WT lgkmcnt(15)\n"
"MF v[152:155],a[228:231],a[144:147],v[152:155]\n"
"TR a[212:213],v12 offset:8192\n"
"MF v[152:155],a[232:235],a[160:163],v[152:155]\n"
"MF v[152:155],a[236:239],a[176:179],v[152:155]\n"
"TR a[214:215],v12 offset:10240\n"
"MF v[184:187],a[224:227],a[132:135],v[184:187]\n"
"MF v[184:187],a[228:231],a[148:151],v[184:187]\n"
"TR a[216:217],v12 offset:16384\n"
"MF v[184:187],a[232:235],a[164:167],v[184:187]\n"
"MF v[184:187],a[236:239],a[180:183],v[184:187]\n"
"TR a[218:219],v12 offset:18432\n"
"MF v[216:219],a[224:227],a[136:139],v[216:219]\n"
"MF v[216:219],a[228:231],a[152:155],v[216:219]\n"
"TR a[220:221],v12 offset:24576\n"
"MF v[216:219],a[232:235],a[168:171],v[216:219]\n"
"MF v[216:219],a[236:239],a[184:187],v[216:219]\n"
"TR a[222:223],v12 offset:26624\n"
"MF v[248:251],a[224:227],a[140:143],v[248:251]\n"
"MF v[248:251],a[228:231],a[156:159],v[248:251]\n"
"TR a[224:225],v29 offset:0\n"
"MF v[248:251],a[232:235],a[172:175],v[248:251]\n"
"MF v[248:251],a[236:239],a[188:191],v[248:251]\n"
"TR a[226:227],v29 offset:2048\n"
"MF v[156:159],a[240:243],a[128:131],v[156:159]\n"
"WT lgkmcnt(15)\n"
"MF v[156:159],a[244:247],a[144:147],v[156:159]\n"
"TR a[228:229],v29 offset:8192\n"
"MF v[156:159],a[248:251],a[160:163],v[156:159]\n"
"MF v[156:159],a[252:255],a[176:179],v[156:159]\n"
"TR a[230:231],v29 offset:10240\n"
"MF v[188:191],a[240:243],a[132:135],v[188:191]\n"
"MF v[188:191],a[244:247],a[148:151],v[188:191]\n"
"TR a[232:233],v29 offset:16384\n"
"MF v[188:191],a[248:251],a[164:167],v[188:191]\n"
"MF v[188:191],a[252:255],a[180:183],v[188:191]\n"
"TR a[234:235],v29 offset:18432\n"
"MF v[220:223],a[240:243],a[136:139],v[220:223]\n"
"MF v[220:223],a[244:247],a[152:155],v[220:223]\n"
"TR a[236:237],v29 offset:24576\n"
"MF v[220:223],a[248:251],a[168:171],v[220:223]\n"
"MF v[220:223],a[252:255],a[184:187],v[220:223]\n"
"TR a[238:239],v29 offset:26624\n"
"MF v[252:255],a[240:243],a[140:143],v[252:255]\n"
"MF v[252:255],a[244:247],a[156:159],v[252:255]\n"
"TR a[240:241],v13 offset:0\n"
"MF v[252:255],a[248:251],a[172:175],v[252:255]\n"
"MF v[252:255],a[252:255],a[188:191],v[252:255]\n"
"WT vmcnt(31)\n"
"TR a[242:243],v13 offset:2048\n"
"MF v[128:131],a[192:195],a[0:3],v[128:131]\n"
"WT vmcnt(27) lgkmcnt(15)\n"
"MF v[128:131],a[196:199],a[16:19],v[128:131]\n"
"WT vmcnt(23)\n"
"TR a[244:245],v13 offset:8192\n"
"MF v[128:131],a[200:203],a[32:35],v[128:131]\n"
"WT vmcnt(19)\n"
"MF v[128:131],a[204:207],a[48:51],v[128:131]\n"
"WT vmcnt(15)\n"
"TR a[246:247],v13 offset:10240\n"
"MF v[128:131],a[208:211],a[64:67],v[128:131]\n"
"WT vmcnt(11) lgkmcnt(15)\n"
"MF v[128:131],a[212:215],a[80:83],v[128:131]\n"
"WT vmcnt(7) lgkmcnt(14)\n"
"TR a[248:249],v13 offset:16384\n"
"MF v[128:131],a[216:219],a[96:99],v[128:131]\n"
"WT vmcnt(3) lgkmcnt(13)\n"
"MF v[128:131],a[220:223],a[112:115],v[128:131]\n"
"TR a[250:251],v13 offset:18432\n"
"MF v[160:163],a[192:195],a[4:7],v[160:163]\n"
"MF v[160:163],a[196:199],a[20:23],v[160:163]\n"
"TR a[252:253],v13 offset:24576\n"
"MF v[160:163],a[200:203],a[36:39],v[160:163]\n"
"MF v[160:163],a[204:207],a[52:55],v[160:163]\n"
"TR a[254:255],v13 offset:26624\n"
"MF v[160:163],a[208:211],a[68:71],v[160:163]\n"
"MF v[160:163],a[212:215],a[84:87],v[160:163]\n"
"MF v[160:163],a[216:219],a[100:103],v[160:163]\n"
"WT vmcnt(2)\n"
"MF v[160:163],a[220:223],a[116:119],v[160:163]\n"
"MF v[192:195],a[192:195],a[8:11],v[192:195]\n"
"MF v[192:195],a[196:199],a[24:27],v[192:195]\n"
"MF v[192:195],a[200:203],a[40:43],v[192:195]\n"
"MF v[192:195],a[204:207],a[56:59],v[192:195]\n"
"MF v[192:195],a[208:211],a[72:75],v[192:195]\n"
"MF v[192:195],a[212:215],a[88:91],v[192:195]\n"
"MF v[192:195],a[216:219],a[104:107],v[192:195]\n"
"WT vmcnt(1)\n"
"MF v[192:195],a[220:223],a[120:123],v[192:195]\n"
"MF v[224:227],a[192:195],a[12:15],v[224:227]\n"
"MF v[224:227],a[196:199],a[28:31],v[224:227]\n"
"TR a[192:193],v30 offset:0\n"
"MF v[224:227],a[200:203],a[44:47],v[224:227]\n"
"MF v[224:227],a[204:207],a[60:63],v[224:227]\n"
"TR a[194:195],v30 offset:2048\n"
"MF v[224:227],a[208:211],a[76:79],v[224:227]\n"
"TR a[196:197],v30 offset:8192\n"
"MF v[224:227],a[212:215],a[92:95],v[224:227]\n"
"TR a[198:199],v30 offset:10240\n"
"MF v[224:227],a[216:219],a[108:111],v[224:227]\n"
"WT vmcnt(0)\n"
"TR a[200:201],v30 offset:16384\n"
"MF v[224:227],a[220:223],a[124:127],v[224:227]\n"
"WT lgkmcnt(15)\n"
"TR a[202:203],v30 offset:18432\n"
"MF v[132:135],a[224:227],a[0:3],v[132:135]\n"
"TR a[204:205],v30 offset:24576\n"
"MF v[132:135],a[228:231],a[16:19],v[132:135]\n"
"TR a[206:207],v30 offset:26624\n"
"MF v[132:135],a[232:235],a[32:35],v[132:135]\n"
"WT lgkmcnt(15)\n"
"TR a[208:209],v14 offset:0\n"
"MF v[132:135],a[236:239],a[48:51],v[132:135]\n"
"WT lgkmcnt(15)\n"
"TR a[210:211],v14 offset:2048\n"
"MF v[132:135],a[240:243],a[64:67],v[132:135]\n"
"WT lgkmcnt(14)\n"
"MF v[132:135],a[244:247],a[80:83],v[132:135]\n"
"WT lgkmcnt(12)\n"
"TR a[212:213],v14 offset:8192\n"
"MF v[132:135],a[248:251],a[96:99],v[132:135]\n"
"WT lgkmcnt(11)\n"
"MF v[132:135],a[252:255],a[112:115],v[132:135]\n"
"TR a[214:215],v14 offset:10240\n"
"MF v[164:167],a[224:227],a[4:7],v[164:167]\n"
"MF v[164:167],a[228:231],a[20:23],v[164:167]\n"
"TR a[216:217],v14 offset:16384\n"
"MF v[164:167],a[232:235],a[36:39],v[164:167]\n"
"MF v[164:167],a[236:239],a[52:55],v[164:167]\n"
"TR a[218:219],v14 offset:18432\n"
"MF v[164:167],a[240:243],a[68:71],v[164:167]\n"
"MF v[164:167],a[244:247],a[84:87],v[164:167]\n"
"TR a[220:221],v14 offset:24576\n"
"MF v[164:167],a[248:251],a[100:103],v[164:167]\n"
"CV v128,v128,v129\n"
"CV v129,v130,v131\n"
"MF v[164:167],a[252:255],a[116:119],v[164:167]\n"
"TR a[222:223],v14 offset:26624\n"
"MF v[196:199],a[224:227],a[8:11],v[196:199]\n"
"CV v130,v132,v133\n"
"CV v131,v134,v135\n"
"MF v[196:199],a[228:231],a[24:27],v[196:199]\n"
"s_add_u32 s66, s64, 0\n"
"MF v[196:199],a[232:235],a[40:43],v[196:199]\n"
"ST v[128:131],v1,s[40:43],s66 offen offset:0 sc1\n"
"MF v[196:199],a[236:239],a[56:59],v[196:199]\n"
"MF v[196:199],a[240:243],a[72:75],v[196:199]\n"
"MF v[196:199],a[244:247],a[88:91],v[196:199]\n"
"MF v[196:199],a[248:251],a[104:107],v[196:199]\n"
"MF v[196:199],a[252:255],a[120:123],v[196:199]\n"
"MF v[228:231],a[224:227],a[12:15],v[228:231]\n"
"MF v[228:231],a[228:231],a[28:31],v[228:231]\n"
"TR a[224:225],v31 offset:0\n"
"MF v[228:231],a[232:235],a[44:47],v[228:231]\n"
"MF v[228:231],a[236:239],a[60:63],v[228:231]\n"
"TR a[226:227],v31 offset:2048\n"
"MF v[228:231],a[240:243],a[76:79],v[228:231]\n"
"TR a[228:229],v31 offset:8192\n"
"MF v[228:231],a[244:247],a[92:95],v[228:231]\n"
"TR a[230:231],v31 offset:10240\n"
"MF v[228:231],a[248:251],a[108:111],v[228:231]\n"
"CV v160,v160,v161\n"
"CV v161,v162,v163\n"
"MF v[228:231],a[252:255],a[124:127],v[228:231]\n"
"WT lgkmcnt(15)\n"
"TR a[232:233],v31 offset:16384\n"
"MF v[136:139],a[192:195],a[0:3],v[136:139]\n"
"CV v162,v164,v165\n"
"CV v163,v166,v167\n"
"MF v[136:139],a[196:199],a[16:19],v[136:139]\n"
"WT lgkmcnt(15)\n"
"s_add_u32 s66, s64, 4096\n"
"TR a[234:235],v31 offset:18432\n"
"MF v[136:139],a[200:203],a[32:35],v[136:139]\n"
"WT lgkmcnt(14)\n"
"ST v[160:163],v1,s[40:43],s66 offen offset:0 sc1\n"
"MF v[136:139],a[204:207],a[48:51],v[136:139]\n"
"WT lgkmcnt(12)\n"
"TR a[236:237],v31 offset:24576\n"
"MF v[136:139],a[208:211],a[64:67],v[136:139]\n"
"WT lgkmcnt(11)\n"
"TR a[238:239],v31 offset:26624\n"
"MF v[136:139],a[212:215],a[80:83],v[136:139]\n"
"WT lgkmcnt(10)\n"
"TR a[240:241],v15 offset:0\n"
"MF v[136:139],a[216:219],a[96:99],v[136:139]\n"
"WT lgkmcnt(9)\n"
"TR a[242:243],v15 offset:2048\n"
"MF v[136:139],a[220:223],a[112:115],v[136:139]\n"
"TR a[244:245],v15 offset:8192\n"
"MF v[168:171],a[192:195],a[4:7],v[168:171]\n"
"MF v[168:171],a[196:199],a[20:23],v[168:171]\n"
"TR a[246:247],v15 offset:10240\n"
"MF v[168:171],a[200:203],a[36:39],v[168:171]\n"
"MF v[168:171],a[204:207],a[52:55],v[168:171]\n"
"TR a[248:249],v15 offset:16384\n"
"MF v[168:171],a[208:211],a[68:71],v[168:171]\n"
"MF v[168:171],a[212:215],a[84:87],v[168:171]\n"
"TR a[250:251],v15 offset:18432\n"
"MF v[168:171],a[216:219],a[100:103],v[168:171]\n"
"CV v192,v192,v193\n"
"CV v193,v194,v195\n"
"MF v[168:171],a[220:223],a[116:119],v[168:171]\n"
"TR a[252:253],v15 offset:24576\n"
"MF v[200:203],a[192:195],a[8:11],v[200:203]\n"
"CV v194,v196,v197\n"
"CV v195,v198,v199\n"
"MF v[200:203],a[196:199],a[24:27],v[200:203]\n"
"s_add_u32 s66, s64, 8192\n"
"TR a[254:255],v15 offset:26624\n"
"MF v[200:203],a[200:203],a[40:43],v[200:203]\n"
"ST v[192:195],v1,s[40:43],s66 offen offset:0 sc1\n"
"MF v[200:203],a[204:207],a[56:59],v[200:203]\n"
"MF v[200:203],a[208:211],a[72:75],v[200:203]\n"
"MF v[200:203],a[212:215],a[88:91],v[200:203]\n"
"MF v[200:203],a[216:219],a[104:107],v[200:203]\n"
"MF v[200:203],a[220:223],a[120:123],v[200:203]\n"
"MF v[232:235],a[192:195],a[12:15],v[232:235]\n"
"MF v[232:235],a[196:199],a[28:31],v[232:235]\n"
"TR a[192:193],v28 offset:4096\n"
"MF v[232:235],a[200:203],a[44:47],v[232:235]\n"
"MF v[232:235],a[204:207],a[60:63],v[232:235]\n"
"TR a[194:195],v28 offset:6144\n"
"MF v[232:235],a[208:211],a[76:79],v[232:235]\n"
"TR a[196:197],v28 offset:12288\n"
"MF v[232:235],a[212:215],a[92:95],v[232:235]\n"
"TR a[198:199],v28 offset:14336\n"
"MF v[232:235],a[216:219],a[108:111],v[232:235]\n"
"CV v224,v224,v225\n"
"CV v225,v226,v227\n"
"MF v[232:235],a[220:223],a[124:127],v[232:235]\n"
"WT lgkmcnt(15)\n"
"TR a[200:201],v28 offset:20480\n"
"MF v[140:143],a[224:227],a[0:3],v[140:143]\n"
"CV v226,v228,v229\n"
"CV v227,v230,v231\n"
"MF v[140:143],a[228:231],a[16:19],v[140:143]\n"
"WT lgkmcnt(15)\n"
"s_add_u32 s66, s64, 12288\n"
"TR a[202:203],v28 offset:22528\n"
"MF v[140:143],a[232:235],a[32:35],v[140:143]\n"
"WT lgkmcnt(14)\n"
"ST v[224:227],v1,s[40:43],s66 offen offset:0 sc1\n"
"MF v[140:143],a[236:239],a[48:51],v[140:143]\n"
"WT lgkmcnt(12)\n"
"TR a[204:205],v28 offset:28672\n"
"MF v[140:143],a[240:243],a[64:67],v[140:143]\n"
"WT lgkmcnt(11)\n"
"TR a[206:207],v28 offset:30720\n"
"MF v[140:143],a[244:247],a[80:83],v[140:143]\n"
"WT lgkmcnt(10)\n"
"TR a[208:209],v12 offset:4096\n"
"MF v[140:143],a[248:251],a[96:99],v[140:143]\n"
"WT lgkmcnt(9)\n"
"TR a[210:211],v12 offset:6144\n"
"MF v[140:143],a[252:255],a[112:115],v[140:143]\n"
"TR a[212:213],v12 offset:12288\n"
"MF v[172:175],a[224:227],a[4:7],v[172:175]\n"
"MF v[172:175],a[228:231],a[20:23],v[172:175]\n"
"TR a[214:215],v12 offset:14336\n"
"MF v[172:175],a[232:235],a[36:39],v[172:175]\n"
"MF v[172:175],a[236:239],a[52:55],v[172:175]\n"
"TR a[216:217],v12 offset:20480\n"
"MF v[172:175],a[240:243],a[68:71],v[172:175]\n"
"MF v[172:175],a[244:247],a[84:87],v[172:175]\n"
"TR a[218:219],v12 offset:22528\n"
"MF v[172:175],a[248:251],a[100:103],v[172:175]\n"
"CV v136,v136,v137\n"
"CV v137,v138,v139\n"
"MF v[172:175],a[252:255],a[116:119],v[172:175]\n"
"TR a[220:221],v12 offset:28672\n"
"MF v[204:207],a[224:227],a[8:11],v[204:207]\n"
"CV v138,v140,v141\n"
"CV v139,v142,v143\n"
"MF v[204:207],a[228:231],a[24:27],v[204:207]\n"
"s_add_u32 s66, s64, 0\n"
"TR a[222:223],v12 offset:30720\n"
"MF v[204:207],a[232:235],a[40:43],v[204:207]\n"
"ST v[136:139],v1,s[40:43],s66 offen offset:1024 sc1\n"
"MF v[204:207],a[236:239],a[56:59],v[204:207]\n"
"MF v[204:207],a[240:243],a[72:75],v[204:207]\n"
"MF v[204:207],a[244:247],a[88:91],v[204:207]\n"
"MF v[204:207],a[248:251],a[104:107],v[204:207]\n"
"MF v[204:207],a[252:255],a[120:123],v[204:207]\n"
"MF v[236:239],a[224:227],a[12:15],v[236:239]\n"
"MF v[236:239],a[228:231],a[28:31],v[236:239]\n"
"TR a[224:225],v29 offset:4096\n"
"MF v[236:239],a[232:235],a[44:47],v[236:239]\n"
"MF v[236:239],a[236:239],a[60:63],v[236:239]\n"
"TR a[226:227],v29 offset:6144\n"
"MF v[236:239],a[240:243],a[76:79],v[236:239]\n"
"TR a[228:229],v29 offset:12288\n"
"MF v[236:239],a[244:247],a[92:95],v[236:239]\n"
"TR a[230:231],v29 offset:14336\n"
"MF v[236:239],a[248:251],a[108:111],v[236:239]\n"
"CV v168,v168,v169\n"
"CV v169,v170,v171\n"
"MF v[236:239],a[252:255],a[124:127],v[236:239]\n"
"WT lgkmcnt(15)\n"
"TR a[232:233],v29 offset:20480\n"
"MF v[144:147],a[192:195],a[0:3],v[144:147]\n"
"CV v170,v172,v173\n"
"CV v171,v174,v175\n"
"MF v[144:147],a[196:199],a[16:19],v[144:147]\n"
"WT lgkmcnt(15)\n"
"s_add_u32 s66, s64, 4096\n"
"TR a[234:235],v29 offset:22528\n"
"MF v[144:147],a[200:203],a[32:35],v[144:147]\n"
"WT lgkmcnt(14)\n"
"ST v[168:171],v1,s[40:43],s66 offen offset:1024 sc1\n"
"MF v[144:147],a[204:207],a[48:51],v[144:147]\n"
"WT lgkmcnt(12)\n"
"TR a[236:237],v29 offset:28672\n"
"MF v[144:147],a[208:211],a[64:67],v[144:147]\n"
"WT lgkmcnt(11)\n"
"TR a[238:239],v29 offset:30720\n"
"MF v[144:147],a[212:215],a[80:83],v[144:147]\n"
"WT lgkmcnt(10)\n"
"TR a[240:241],v13 offset:4096\n"
"MF v[144:147],a[216:219],a[96:99],v[144:147]\n"
"WT lgkmcnt(9)\n"
"TR a[242:243],v13 offset:6144\n"
"MF v[144:147],a[220:223],a[112:115],v[144:147]\n"
"TR a[244:245],v13 offset:12288\n"
"MF v[176:179],a[192:195],a[4:7],v[176:179]\n"
"MF v[176:179],a[196:199],a[20:23],v[176:179]\n"
"TR a[246:247],v13 offset:14336\n"
"MF v[176:179],a[200:203],a[36:39],v[176:179]\n"
"MF v[176:179],a[204:207],a[52:55],v[176:179]\n"
"TR a[248:249],v13 offset:20480\n"
"MF v[176:179],a[208:211],a[68:71],v[176:179]\n"
"MF v[176:179],a[212:215],a[84:87],v[176:179]\n"
"TR a[250:251],v13 offset:22528\n"
"MF v[176:179],a[216:219],a[100:103],v[176:179]\n"
"CV v200,v200,v201\n"
"CV v201,v202,v203\n"
"MF v[176:179],a[220:223],a[116:119],v[176:179]\n"
"TR a[252:253],v13 offset:28672\n"
"MF v[208:211],a[192:195],a[8:11],v[208:211]\n"
"CV v202,v204,v205\n"
"CV v203,v206,v207\n"
"MF v[208:211],a[196:199],a[24:27],v[208:211]\n"
"s_add_u32 s66, s64, 8192\n"
"TR a[254:255],v13 offset:30720\n"
"MF v[208:211],a[200:203],a[40:43],v[208:211]\n"
"ST v[200:203],v1,s[40:43],s66 offen offset:1024 sc1\n"
"MF v[208:211],a[204:207],a[56:59],v[208:211]\n"
"MF v[208:211],a[208:211],a[72:75],v[208:211]\n"
"MF v[208:211],a[212:215],a[88:91],v[208:211]\n"
"MF v[208:211],a[216:219],a[104:107],v[208:211]\n"
"MF v[208:211],a[220:223],a[120:123],v[208:211]\n"
"MF v[240:243],a[192:195],a[12:15],v[240:243]\n"
"MF v[240:243],a[196:199],a[28:31],v[240:243]\n"
"TR a[192:193],v30 offset:4096\n"
"MF v[240:243],a[200:203],a[44:47],v[240:243]\n"
"MF v[240:243],a[204:207],a[60:63],v[240:243]\n"
"TR a[194:195],v30 offset:6144\n"
"MF v[240:243],a[208:211],a[76:79],v[240:243]\n"
"TR a[196:197],v30 offset:12288\n"
"MF v[240:243],a[212:215],a[92:95],v[240:243]\n"
"TR a[198:199],v30 offset:14336\n"
"MF v[240:243],a[216:219],a[108:111],v[240:243]\n"
"CV v232,v232,v233\n"
"CV v233,v234,v235\n"
"MF v[240:243],a[220:223],a[124:127],v[240:243]\n"
"WT lgkmcnt(15)\n"
"TR a[200:201],v30 offset:20480\n"
"MF v[148:151],a[224:227],a[0:3],v[148:151]\n"
"CV v234,v236,v237\n"
"CV v235,v238,v239\n"
"MF v[148:151],a[228:231],a[16:19],v[148:151]\n"
"WT lgkmcnt(15)\n"
"s_add_u32 s66, s64, 12288\n"
"TR a[202:203],v30 offset:22528\n"
"MF v[148:151],a[232:235],a[32:35],v[148:151]\n"
"WT lgkmcnt(14)\n"
"ST v[232:235],v1,s[40:43],s66 offen offset:1024 sc1\n"
"MF v[148:151],a[236:239],a[48:51],v[148:151]\n"
"WT lgkmcnt(12)\n"
"TR a[204:205],v30 offset:28672\n"
"MF v[148:151],a[240:243],a[64:67],v[148:151]\n"
"WT lgkmcnt(11)\n"
"TR a[206:207],v30 offset:30720\n"
"MF v[148:151],a[244:247],a[80:83],v[148:151]\n"
"WT lgkmcnt(10)\n"
"TR a[208:209],v14 offset:4096\n"
"MF v[148:151],a[248:251],a[96:99],v[148:151]\n"
"WT lgkmcnt(9)\n"
"TR a[210:211],v14 offset:6144\n"
"MF v[148:151],a[252:255],a[112:115],v[148:151]\n"
"TR a[212:213],v14 offset:12288\n"
"MF v[180:183],a[224:227],a[4:7],v[180:183]\n"
"MF v[180:183],a[228:231],a[20:23],v[180:183]\n"
"TR a[214:215],v14 offset:14336\n"
"MF v[180:183],a[232:235],a[36:39],v[180:183]\n"
"MF v[180:183],a[236:239],a[52:55],v[180:183]\n"
"TR a[216:217],v14 offset:20480\n"
"MF v[180:183],a[240:243],a[68:71],v[180:183]\n"
"MF v[180:183],a[244:247],a[84:87],v[180:183]\n"
"TR a[218:219],v14 offset:22528\n"
"MF v[180:183],a[248:251],a[100:103],v[180:183]\n"
"CV v144,v144,v145\n"
"CV v145,v146,v147\n"
"MF v[180:183],a[252:255],a[116:119],v[180:183]\n"
"TR a[220:221],v14 offset:28672\n"
"MF v[212:215],a[224:227],a[8:11],v[212:215]\n"
"CV v146,v148,v149\n"
"CV v147,v150,v151\n"
"MF v[212:215],a[228:231],a[24:27],v[212:215]\n"
"s_add_u32 s66, s64, 0\n"
"TR a[222:223],v14 offset:30720\n"
"MF v[212:215],a[232:235],a[40:43],v[212:215]\n"
"ST v[144:147],v1,s[40:43],s66 offen offset:2048 sc1\n"
"MF v[212:215],a[236:239],a[56:59],v[212:215]\n"
"MF v[212:215],a[240:243],a[72:75],v[212:215]\n"
"MF v[212:215],a[244:247],a[88:91],v[212:215]\n"
"MF v[212:215],a[248:251],a[104:107],v[212:215]\n"
"MF v[212:215],a[252:255],a[120:123],v[212:215]\n"
"MF v[244:247],a[224:227],a[12:15],v[244:247]\n"
"MF v[244:247],a[228:231],a[28:31],v[244:247]\n"
"TR a[224:225],v31 offset:4096\n"
"MF v[244:247],a[232:235],a[44:47],v[244:247]\n"
"MF v[244:247],a[236:239],a[60:63],v[244:247]\n"
"TR a[226:227],v31 offset:6144\n"
"MF v[244:247],a[240:243],a[76:79],v[244:247]\n"
"TR a[228:229],v31 offset:12288\n"
"MF v[244:247],a[244:247],a[92:95],v[244:247]\n"
"TR a[230:231],v31 offset:14336\n"
"MF v[244:247],a[248:251],a[108:111],v[244:247]\n"
"CV v176,v176,v177\n"
"CV v177,v178,v179\n"
"MF v[244:247],a[252:255],a[124:127],v[244:247]\n"
"WT lgkmcnt(15)\n"
"TR a[232:233],v31 offset:20480\n"
"MF v[152:155],a[192:195],a[0:3],v[152:155]\n"
"CV v178,v180,v181\n"
"CV v179,v182,v183\n"
"MF v[152:155],a[196:199],a[16:19],v[152:155]\n"
"WT lgkmcnt(15)\n"
"s_add_u32 s66, s64, 4096\n"
"TR a[234:235],v31 offset:22528\n"
"MF v[152:155],a[200:203],a[32:35],v[152:155]\n"
"WT lgkmcnt(14)\n"
"ST v[176:179],v1,s[40:43],s66 offen offset:2048 sc1\n"
"MF v[152:155],a[204:207],a[48:51],v[152:155]\n"
"WT lgkmcnt(12)\n"
"TR a[236:237],v31 offset:28672\n"
"MF v[152:155],a[208:211],a[64:67],v[152:155]\n"
"WT lgkmcnt(11)\n"
"TR a[238:239],v31 offset:30720\n"
"MF v[152:155],a[212:215],a[80:83],v[152:155]\n"
"WT lgkmcnt(10)\n"
"TR a[240:241],v15 offset:4096\n"
"MF v[152:155],a[216:219],a[96:99],v[152:155]\n"
"WT lgkmcnt(9)\n"
"TR a[242:243],v15 offset:6144\n"
"MF v[152:155],a[220:223],a[112:115],v[152:155]\n"
"TR a[244:245],v15 offset:12288\n"
"MF v[184:187],a[192:195],a[4:7],v[184:187]\n"
"MF v[184:187],a[196:199],a[20:23],v[184:187]\n"
"TR a[246:247],v15 offset:14336\n"
"MF v[184:187],a[200:203],a[36:39],v[184:187]\n"
"MF v[184:187],a[204:207],a[52:55],v[184:187]\n"
"TR a[248:249],v15 offset:20480\n"
"MF v[184:187],a[208:211],a[68:71],v[184:187]\n"
"MF v[184:187],a[212:215],a[84:87],v[184:187]\n"
"TR a[250:251],v15 offset:22528\n"
"MF v[184:187],a[216:219],a[100:103],v[184:187]\n"
"CV v208,v208,v209\n"
"CV v209,v210,v211\n"
"MF v[184:187],a[220:223],a[116:119],v[184:187]\n"
"TR a[252:253],v15 offset:28672\n"
"MF v[216:219],a[192:195],a[8:11],v[216:219]\n"
"CV v210,v212,v213\n"
"CV v211,v214,v215\n"
"MF v[216:219],a[196:199],a[24:27],v[216:219]\n"
"s_add_u32 s66, s64, 8192\n"
"TR a[254:255],v15 offset:30720\n"
"MF v[216:219],a[200:203],a[40:43],v[216:219]\n"
"ST v[208:211],v1,s[40:43],s66 offen offset:2048 sc1\n"
"MF v[216:219],a[204:207],a[56:59],v[216:219]\n"
"MF v[216:219],a[208:211],a[72:75],v[216:219]\n"
"MF v[216:219],a[212:215],a[88:91],v[216:219]\n"
"MF v[216:219],a[216:219],a[104:107],v[216:219]\n"
"MF v[216:219],a[220:223],a[120:123],v[216:219]\n"
"MF v[248:251],a[192:195],a[12:15],v[248:251]\n"
"MF v[248:251],a[196:199],a[28:31],v[248:251]\n"
"MF v[248:251],a[200:203],a[44:47],v[248:251]\n"
"MF v[248:251],a[204:207],a[60:63],v[248:251]\n"
"MF v[248:251],a[208:211],a[76:79],v[248:251]\n"
"MF v[248:251],a[212:215],a[92:95],v[248:251]\n"
"MF v[248:251],a[216:219],a[108:111],v[248:251]\n"
"CV v240,v240,v241\n"
"CV v241,v242,v243\n"
"MF v[248:251],a[220:223],a[124:127],v[248:251]\n"
"WT lgkmcnt(14)\n"
"MF v[156:159],a[224:227],a[0:3],v[156:159]\n"
"WT lgkmcnt(12)\n"
"CV v242,v244,v245\n"
"CV v243,v246,v247\n"
"MF v[156:159],a[228:231],a[16:19],v[156:159]\n"
"WT lgkmcnt(10)\n"
"s_add_u32 s66, s64, 12288\n"
"MF v[156:159],a[232:235],a[32:35],v[156:159]\n"
"WT lgkmcnt(8)\n"
"ST v[240:243],v1,s[40:43],s66 offen offset:2048 sc1\n"
"MF v[156:159],a[236:239],a[48:51],v[156:159]\n"
"WT lgkmcnt(6)\n"
"MF v[156:159],a[240:243],a[64:67],v[156:159]\n"
"WT lgkmcnt(4)\n"
"MF v[156:159],a[244:247],a[80:83],v[156:159]\n"
"WT lgkmcnt(2)\n"
"MF v[156:159],a[248:251],a[96:99],v[156:159]\n"
"WT lgkmcnt(0)\n"
"MF v[156:159],a[252:255],a[112:115],v[156:159]\n"
"MF v[188:191],a[224:227],a[4:7],v[188:191]\n"
"MF v[188:191],a[228:231],a[20:23],v[188:191]\n"
"MF v[188:191],a[232:235],a[36:39],v[188:191]\n"
"MF v[188:191],a[236:239],a[52:55],v[188:191]\n"
"MF v[188:191],a[240:243],a[68:71],v[188:191]\n"
"MF v[188:191],a[244:247],a[84:87],v[188:191]\n"
"MF v[188:191],a[248:251],a[100:103],v[188:191]\n"
"CV v152,v152,v153\n"
"CV v153,v154,v155\n"
"MF v[188:191],a[252:255],a[116:119],v[188:191]\n"
"MF v[220:223],a[224:227],a[8:11],v[220:223]\n"
"CV v154,v156,v157\n"
"CV v155,v158,v159\n"
"MF v[220:223],a[228:231],a[24:27],v[220:223]\n"
"s_add_u32 s66, s64, 0\n"
"MF v[220:223],a[232:235],a[40:43],v[220:223]\n"
"ST v[152:155],v1,s[40:43],s66 offen offset:3072 sc1\n"
"MF v[220:223],a[236:239],a[56:59],v[220:223]\n"
"MF v[220:223],a[240:243],a[72:75],v[220:223]\n"
"MF v[220:223],a[244:247],a[88:91],v[220:223]\n"
"MF v[220:223],a[248:251],a[104:107],v[220:223]\n"
"CV v184,v184,v185\n"
"CV v185,v186,v187\n"
"MF v[220:223],a[252:255],a[120:123],v[220:223]\n"
"MF v[252:255],a[224:227],a[12:15],v[252:255]\n"
"CV v186,v188,v189\n"
"CV v187,v190,v191\n"
"MF v[252:255],a[228:231],a[28:31],v[252:255]\n"
"s_add_u32 s66, s64, 4096\n"
"MF v[252:255],a[232:235],a[44:47],v[252:255]\n"
"ST v[184:187],v1,s[40:43],s66 offen offset:3072 sc1\n"
"MF v[252:255],a[236:239],a[60:63],v[252:255]\n"
"MF v[252:255],a[240:243],a[76:79],v[252:255]\n"
"MF v[252:255],a[244:247],a[92:95],v[252:255]\n"
"MF v[252:255],a[248:251],a[108:111],v[252:255]\n"
"MF v[252:255],a[252:255],a[124:127],v[252:255]\n"
"CV v216,v216,v217\n"
"CV v217,v218,v219\n"
"CV v218,v220,v221\n"
"CV v219,v222,v223\n"
"s_add_u32 s66, s64, 8192\n"
"ST v[216:219],v1,s[40:43],s66 offen offset:3072 sc1\n"
"CV v248,v248,v249\n"
"CV v249,v250,v251\n"
"CV v250,v252,v253\n"
"CV v251,v254,v255\n"
"s_add_u32 s66, s64, 12288\n"
"ST v[248:251],v1,s[40:43],s66 offen offset:3072 sc1\n"
"WT vmcnt(0)\n"
"s_lshl_b32 s56, s33, 2\n"
"s_add_u32 s56, s56, s34\n"
"s_lshl_b32 s56, s56, 7\n"
"s_add_u32 s56, s56, 0x80000\n"
"v_lshlrev_b32 v21, 3, v0\n"
"s_mov_b64 exec, 1\n"
"buffer_store_dwordx2 v[16:17], v21, s[40:43], s56 offen sc1\n"
"s_mov_b64 exec, -1\n"
"s_branch L_end_35\n"
"L_no6_32:\n"
"WT vmcnt(0) lgkmcnt(0)\n"
"s_barrier\n"
"s_cmp_lg_u32 s34, 0\n"
"s_cbranch_scc1 L_pub_45\n"
"s_mov_b64 exec, 1\n"
"s_lshl_b32 s56, s33, 7\n"
"v_lshl_add_u32 v21, v0, 3, s56\n"
"s_mov_b32 s57, 0x60000\n"
"buffer_store_dwordx2 v[16:17], v21, s[40:43], s57 offen sc1\n"
"s_mov_b64 exec, -1\n"
"L_pub_45:\n"
"s_cmp_ge_u32 s37, 30\n"
"s_cbranch_scc1 L_idle_38\n"
"s_lshl_b32 s22, s34, 12\n"
"s_sub_u32 s56, s37, 20\n"
"s_min_u32 s58, s56, 9\n"
"s_and_b32 s56, s72, 4\n"
"s_lshl_b32 s14, s72, 1\n"
"s_cmp_ge_u32 s37, 20\n"
"s_cselect_b32 s59, s58, s37\n"
"s_cselect_b32 s14, s14, s56\n"
"s_mul_i32 s57, s59, 52\n"
"s_lshr_b32 s57, s57, 8\n"
"s_mul_i32 s58, s57, 5\n"
"s_sub_u32 s15, s59, s58\n"
"s_add_u32 s14, s14, s57\n"
"s_lshr_b32 s56, s34, 1\n"
"s_and_b32 s58, s34, 1\n"
"s_mul_i32 s65, s56, 40960\n"
"s_lshl_b32 s58, s58, 7\n"
"s_add_u32 s65, s65, s58\n"
"s_lshl_b32 s58, s15, 8\n"
"s_add_u32 s65, s65, s58\n"
"s_lshl_b32 s59, s14, 2\n"
"s_add_u32 s59, s59, s34\n"
"v_mov_b32 v10, v1\n"
"s_cmp_ge_u32 s37, 20\n"
"s_cbranch_scc1 L_s7b_46\n"
"s_and_b32 s16, s72, 3\n"
"s_lshl_b32 s56, s14, 1\n"
"v_bfe_u32 v22, v0, 3, 1\n"
"v_add_u32 v22, s56, v22\n"
"v_sub_u32 v23, 15, v22\n"
"v_min_u32 v22, v22, v23\n"
"s_lshl_b32 s57, s16, 1\n"
"v_and_b32 v21, 3, v0\n"
"v_lshlrev_b32 v21, 3, v21\n"
"v_lshl_add_u32 v21, v22, 5, v21\n"
"v_bfe_u32 v22, v0, 2, 1\n"
"v_add3_u32 v21, v21, v22, s57\n"
"v_lshlrev_b32 v21, 7, v21\n"
"v_add_u32 v20, 0x60000, v21\n"
"s_mul_i32 s56, s16, 1310720\n"
"s_add_u32 s52, s18, s56\n"
"s_addc_u32 s53, s19, 0\n"
"s_and_b32 s53, s53, 0xffff\n"
"s_mov_b32 s54, 0x140000\n"
"s_mov_b32 s55, 0x20000\n"
"s_lshl_b32 s56, s59, 14\n"
"s_lshl_b32 s57, s16, 22\n"
"s_add_u32 s56, s56, s57\n"
"s_add_u32 s56, s56, 0x4000000\n"
"s_add_u32 s44, s30, s56\n"
"s_addc_u32 s45, s31, 0\n"
"s_and_b32 s45, s45, 0xffff\n"
"s_mov_b32 s46, 0x384000\n"
"s_mov_b32 s47, 0x20000\n"
"s_mov_b32 s23, 81920\n"
"s_branch L_s7e_47\n"
"L_s7b_46:\n"
"s_mul_i32 s56, s57, 14\n"
"v_min_u32 v21, 13, v0\n"
"v_add_u32 v21, s56, v21\n"
"v_lshlrev_b32 v21, 3, v21\n"
"v_add_u32 v21, s72, v21\n"
"v_lshlrev_b32 v21, 2, v21\n"
"v_add_u32 v21, s34, v21\n"
"v_lshlrev_b32 v21, 7, v21\n"
"v_add_u32 v20, 0xc0000, v21\n"
"s_mov_b32 s52, s26\n"
"s_and_b32 s53, s27, 0xffff\n"
"s_mov_b32 s54, 0x21c000\n"
"s_mov_b32 s55, 0x20000\n"
"s_mul_i32 s56, s59, 110592\n"
"s_add_u32 s56, s56, 0x1100000\n"
"s_add_u32 s44, s30, s56\n"
"s_addc_u32 s45, s31, 0\n"
"s_and_b32 s45, s45, 0xffff\n"
"s_mov_b32 s46, 0x1b000\n"
"s_mov_b32 s47, 0x20000\n"
"v_add_u32 v11, 55296, v1\n"
"s_mov_b32 s23, 163840\n"
"L_s7e_47:\n"
"s_sub_u32 s65, s65, s23\n"
"s_add_u32 s65, s65, s23\n"
"s_add_u32 m0, s22, 98304\n"
"s_nop 0\n"
"LD v2,s[52:55],s65 offen offset:0 lds\n"
"LD v3,s[52:55],s65 offen offset:1024 lds\n"
"LD v4,s[52:55],s65 offen offset:2048 lds\n"
"LD v5,s[52:55],s65 offen offset:3072 lds\n"
"s_add_u32 s65, s65, s23\n"
"s_add_u32 m0, s22, 114688\n"
"s_nop 0\n"
"LD v2,s[52:55],s65 offen offset:0 lds\n"
"LD v3,s[52:55],s65 offen offset:1024 lds\n"
"LD v4,s[52:55],s65 offen offset:2048 lds\n"
"LD v5,s[52:55],s65 offen offset:3072 lds\n"
"s_add_u32 s65, s65, s23\n"
"s_add_u32 m0, s22, 131072\n"
"s_nop 0\n"
"LD v2,s[52:55],s65 offen offset:0 lds\n"
"LD v3,s[52:55],s65 offen offset:1024 lds\n"
"LD v4,s[52:55],s65 offen offset:2048 lds\n"
"LD v5,s[52:55],s65 offen offset:3072 lds\n"
"s_add_u32 s65, s65, s23\n"
"s_add_u32 m0, s22, 147456\n"
"s_nop 0\n"
"LD v2,s[52:55],s65 offen offset:0 lds\n"
"LD v3,s[52:55],s65 offen offset:1024 lds\n"
"LD v4,s[52:55],s65 offen offset:2048 lds\n"
"LD v5,s[52:55],s65 offen offset:3072 lds\n"
"s_add_u32 s65, s65, s23\n"
"s_add_u32 m0, s22, 0\n"
"s_nop 0\n"
"LD v2,s[52:55],s65 offen offset:0 lds\n"
"LD v3,s[52:55],s65 offen offset:1024 lds\n"
"LD v4,s[52:55],s65 offen offset:2048 lds\n"
"LD v5,s[52:55],s65 offen offset:3072 lds\n"
"s_add_u32 s65, s65, s23\n"
"s_add_u32 m0, s22, 16384\n"
"s_nop 0\n"
"LD v2,s[52:55],s65 offen offset:0 lds\n"
"LD v3,s[52:55],s65 offen offset:1024 lds\n"
"LD v4,s[52:55],s65 offen offset:2048 lds\n"
"LD v5,s[52:55],s65 offen offset:3072 lds\n"
"s_add_u32 s65, s65, s23\n"
"s_add_u32 m0, s22, 32768\n"
"s_nop 0\n"
"LD v2,s[52:55],s65 offen offset:0 lds\n"
"LD v3,s[52:55],s65 offen offset:1024 lds\n"
"LD v4,s[52:55],s65 offen offset:2048 lds\n"
"LD v5,s[52:55],s65 offen offset:3072 lds\n"
"s_lshl_b32 s56, s14, 2\n"
"s_add_u32 s56, s56, s34\n"
"s_mul_i32 s59, s56, 40960\n"
"s_lshl_b32 s57, s15, 8\n"
"s_add_u32 s62, s59, s57\n"
"s_add_u32 s63, s62, 20480\n"
"s_lshl_b32 s57, s15, 12\n"
"s_add_u32 s59, s59, s57\n"
"s_add_u32 s60, s59, 0xa00000\n"
"s_add_u32 s61, s60, 20480\n"
"LD v[160:163],v1,s[40:43],s60 offen offset:0 sc1\n"
"LD v[164:167],v1,s[40:43],s60 offen offset:1024 sc1\n"
"LD v[168:171],v1,s[40:43],s60 offen offset:2048 sc1\n"
"LD v[172:175],v1,s[40:43],s60 offen offset:3072 sc1\n"
"LD v[176:179],v1,s[40:43],s61 offen offset:0 sc1\n"
"LD v[180:183],v1,s[40:43],s61 offen offset:1024 sc1\n"
"LD v[184:187],v1,s[40:43],s61 offen offset:2048 sc1\n"
"LD v[188:191],v1,s[40:43],s61 offen offset:3072 sc1\n"
"s_add_u32 s65, s65, s23\n"
"s_add_u32 m0, s22, 49152\n"
"s_nop 0\n"
"LD v2,s[52:55],s65 offen offset:0 lds\n"
"LD v3,s[52:55],s65 offen offset:1024 lds\n"
"LD v4,s[52:55],s65 offen offset:2048 lds\n"
"LD v5,s[52:55],s65 offen offset:3072 lds\n"
"s_add_u32 s65, s65, s23\n"
"s_add_u32 m0, s22, 65536\n"
"s_nop 0\n"
"LD v2,s[52:55],s65 offen offset:0 lds\n"
"LD v3,s[52:55],s65 offen offset:1024 lds\n"
"LD v4,s[52:55],s65 offen offset:2048 lds\n"
"LD v5,s[52:55],s65 offen offset:3072 lds\n"
"s_add_u32 s65, s65, s23\n"
"s_add_u32 m0, s22, 81920\n"
"s_nop 0\n"
"LD v2,s[52:55],s65 offen offset:0 lds\n"
"LD v3,s[52:55],s65 offen offset:1024 lds\n"
"LD v4,s[52:55],s65 offen offset:2048 lds\n"
"LD v5,s[52:55],s65 offen offset:3072 lds\n"
"WT vmcnt(0)\n"
"v_lshlrev_b32 v192, 16, v160\n"
"v_and_b32 v193, 0xffff0000, v160\n"
"v_lshlrev_b32 v194, 16, v161\n"
"v_and_b32 v195, 0xffff0000, v161\n"
"v_lshlrev_b32 v196, 16, v162\n"
"v_and_b32 v197, 0xffff0000, v162\n"
"v_lshlrev_b32 v198, 16, v163\n"
"v_and_b32 v199, 0xffff0000, v163\n"
"v_lshlrev_b32 v200, 16, v164\n"
"v_and_b32 v201, 0xffff0000, v164\n"
"v_lshlrev_b32 v202, 16, v165\n"
"v_and_b32 v203, 0xffff0000, v165\n"
"v_lshlrev_b32 v204, 16, v166\n"
"v_and_b32 v205, 0xffff0000, v166\n"
"v_lshlrev_b32 v206, 16, v167\n"
"v_and_b32 v207, 0xffff0000, v167\n"
"v_lshlrev_b32 v208, 16, v168\n"
"v_and_b32 v209, 0xffff0000, v168\n"
"v_lshlrev_b32 v210, 16, v169\n"
"v_and_b32 v211, 0xffff0000, v169\n"
"v_lshlrev_b32 v212, 16, v170\n"
"v_and_b32 v213, 0xffff0000, v170\n"
"v_lshlrev_b32 v214, 16, v171\n"
"v_and_b32 v215, 0xffff0000, v171\n"
"v_lshlrev_b32 v216, 16, v172\n"
"v_and_b32 v217, 0xffff0000, v172\n"
"v_lshlrev_b32 v218, 16, v173\n"
"v_and_b32 v219, 0xffff0000, v173\n"
"v_lshlrev_b32 v220, 16, v174\n"
"v_and_b32 v221, 0xffff0000, v174\n"
"v_lshlrev_b32 v222, 16, v175\n"
"v_and_b32 v223, 0xffff0000, v175\n"
"v_lshlrev_b32 v224, 16, v176\n"
"v_and_b32 v225, 0xffff0000, v176\n"
"v_lshlrev_b32 v226, 16, v177\n"
"v_and_b32 v227, 0xffff0000, v177\n"
"v_lshlrev_b32 v228, 16, v178\n"
"v_and_b32 v229, 0xffff0000, v178\n"
"v_lshlrev_b32 v230, 16, v179\n"
"v_and_b32 v231, 0xffff0000, v179\n"
"v_lshlrev_b32 v232, 16, v180\n"
"v_and_b32 v233, 0xffff0000, v180\n"
"v_lshlrev_b32 v234, 16, v181\n"
"v_and_b32 v235, 0xffff0000, v181\n"
"v_lshlrev_b32 v236, 16, v182\n"
"v_and_b32 v237, 0xffff0000, v182\n"
"v_lshlrev_b32 v238, 16, v183\n"
"v_and_b32 v239, 0xffff0000, v183\n"
"v_lshlrev_b32 v240, 16, v184\n"
"v_and_b32 v241, 0xffff0000, v184\n"
"v_lshlrev_b32 v242, 16, v185\n"
"v_and_b32 v243, 0xffff0000, v185\n"
"v_lshlrev_b32 v244, 16, v186\n"
"v_and_b32 v245, 0xffff0000, v186\n"
"v_lshlrev_b32 v246, 16, v187\n"
"v_and_b32 v247, 0xffff0000, v187\n"
"v_lshlrev_b32 v248, 16, v188\n"
"v_and_b32 v249, 0xffff0000, v188\n"
"v_lshlrev_b32 v250, 16, v189\n"
"v_and_b32 v251, 0xffff0000, v189\n"
"v_lshlrev_b32 v252, 16, v190\n"
"v_and_b32 v253, 0xffff0000, v190\n"
"v_lshlrev_b32 v254, 16, v191\n"
"v_and_b32 v255, 0xffff0000, v191\n"
"s_branch L_gt2_34\n"
"L_gt_33:\n"
"s_lshl_b32 s56, s14, 2\n"
"s_add_u32 s56, s56, s34\n"
"s_mul_i32 s59, s56, 40960\n"
"s_lshl_b32 s57, s15, 8\n"
"s_add_u32 s62, s59, s57\n"
"s_add_u32 s63, s62, 20480\n"
"s_lshl_b32 s57, s15, 12\n"
"s_add_u32 s59, s59, s57\n"
"s_add_u32 s60, s59, 0xa00000\n"
"s_add_u32 s61, s60, 20480\n"
"s_lshl_b32 s56, s70, 2\n"
"s_add_u32 s56, s56, s34\n"
"s_mul_i32 s56, s56, 110592\n"
"s_lshl_b32 s57, s71, 12\n"
"s_add_u32 s56, s56, s57\n"
"s_add_u32 s57, s56, 0x1100000\n"
"s_add_u32 s58, s57, 55296\n"
"v_pk_add_f32 v[128:129], v[128:129], v[130:131]\n"
"v_pk_add_f32 v[128:129], v[128:129], v[132:133]\n"
"v_pk_add_f32 v[128:129], v[128:129], v[134:135]\n"
"v_pk_add_f32 v[128:129], v[128:129], v[136:137]\n"
"v_mov_b64 v[130:131], v[128:129]\n"
"s_nop 1\n"
"v_permlane32_swap_b32 v128, v130\n"
"v_permlane32_swap_b32 v129, v131\n"
"v_pk_add_f32 v[128:129], v[128:129], v[130:131]\n"
"v_mul_f32 v128, 0x3acccccd, v128\n"
"v_mul_f32 v129, 0x3acccccd, v129\n"
"v_fma_f32 v129, -v128, v128, v129\n"
"v_max_f32 v129, 0, v129\n"
"v_add_f32 v129, 0x3727c5ac, v129\n"
"v_rsq_f32 v130, v129\n"
"s_nop 0\n"
"v_mul_f32 v131, v128, v130\n"
"v_sub_f32 v131, 0, v131\n"
"v_mov_b64 v[132:133], v[130:131]\n"
"s_nop 1\n"
"v_permlane16_swap_b32 v130, v132\n"
"v_permlane16_swap_b32 v131, v133\n"
"s_cmp_eq_u32 s71, 13\n"
"s_cselect_b32 s78, 0, s78\n"
"v_pk_mul_f32 v[32:33], v[32:33], v[130:131] op_sel_hi:[1,0]\n"
"v_pk_mul_f32 v[34:35], v[34:35], v[130:131] op_sel_hi:[1,0]\n"
"v_pk_mul_f32 v[36:37], v[36:37], v[130:131] op_sel_hi:[1,0]\n"
"v_pk_mul_f32 v[38:39], v[38:39], v[130:131] op_sel_hi:[1,0]\n"
"v_pk_mul_f32 v[40:41], v[40:41], v[130:131] op_sel_hi:[1,0]\n"
"v_pk_mul_f32 v[42:43], v[42:43], v[130:131] op_sel_hi:[1,0]\n"
"v_pk_mul_f32 v[44:45], v[44:45], v[130:131] op_sel_hi:[1,0]\n"
"v_pk_mul_f32 v[46:47], v[46:47], v[130:131] op_sel_hi:[1,0]\n"
"v_pk_fma_f32 v[32:33], v[96:97], v[130:131], v[32:33] op_sel:[0,1,0] op_sel_hi:[1,1,1]\n"
"v_pk_fma_f32 v[34:35], v[98:99], v[130:131], v[34:35] op_sel:[0,1,0] op_sel_hi:[1,1,1]\n"
"v_pk_fma_f32 v[36:37], v[100:101], v[130:131], v[36:37] op_sel:[0,1,0] op_sel_hi:[1,1,1]\n"
"v_pk_fma_f32 v[38:39], v[102:103], v[130:131], v[38:39] op_sel:[0,1,0] op_sel_hi:[1,1,1]\n"
"v_pk_fma_f32 v[40:41], v[104:105], v[130:131], v[40:41] op_sel:[0,1,0] op_sel_hi:[1,1,1]\n"
"v_pk_fma_f32 v[42:43], v[106:107], v[130:131], v[42:43] op_sel:[0,1,0] op_sel_hi:[1,1,1]\n"
"v_pk_fma_f32 v[44:45], v[108:109], v[130:131], v[44:45] op_sel:[0,1,0] op_sel_hi:[1,1,1]\n"
"v_pk_fma_f32 v[46:47], v[110:111], v[130:131], v[46:47] op_sel:[0,1,0] op_sel_hi:[1,1,1]\n"
"v_pk_mul_f32 v[192:193], v[32:33], v[32:33]\n"
"v_pk_mul_f32 v[194:195], v[34:35], v[34:35]\n"
"v_pk_mul_f32 v[196:197], v[36:37], v[36:37]\n"
"v_pk_mul_f32 v[198:199], v[38:39], v[38:39]\n"
"v_pk_mul_f32 v[200:201], v[40:41], v[40:41]\n"
"v_pk_mul_f32 v[202:203], v[42:43], v[42:43]\n"
"v_pk_mul_f32 v[204:205], v[44:45], v[44:45]\n"
"v_pk_mul_f32 v[206:207], v[46:47], v[46:47]\n"
"v_pk_fma_f32 v[192:193], v[192:193], s[74:75], s[74:75] op_sel:[0,0,1] op_sel_hi:[1,0,1]\n"
"v_pk_fma_f32 v[194:195], v[194:195], s[74:75], s[74:75] op_sel:[0,0,1] op_sel_hi:[1,0,1]\n"
"v_pk_fma_f32 v[196:197], v[196:197], s[74:75], s[74:75] op_sel:[0,0,1] op_sel_hi:[1,0,1]\n"
"v_pk_fma_f32 v[198:199], v[198:199], s[74:75], s[74:75] op_sel:[0,0,1] op_sel_hi:[1,0,1]\n"
"v_pk_fma_f32 v[200:201], v[200:201], s[74:75], s[74:75] op_sel:[0,0,1] op_sel_hi:[1,0,1]\n"
"LD v[160:163],v1,s[40:43],s60 offen offset:0 sc1\n"
"v_pk_fma_f32 v[202:203], v[202:203], s[74:75], s[74:75] op_sel:[0,0,1] op_sel_hi:[1,0,1]\n"
"v_pk_fma_f32 v[204:205], v[204:205], s[74:75], s[74:75] op_sel:[0,0,1] op_sel_hi:[1,0,1]\n"
"v_pk_fma_f32 v[206:207], v[206:207], s[74:75], s[74:75] op_sel:[0,0,1] op_sel_hi:[1,0,1]\n"
"v_pk_mul_f32 v[192:193], v[192:193], v[32:33]\n"
"v_pk_mul_f32 v[194:195], v[194:195], v[34:35]\n"
"v_pk_mul_f32 v[196:197], v[196:197], v[36:37]\n"
"v_pk_mul_f32 v[198:199], v[198:199], v[38:39]\n"
"v_pk_mul_f32 v[200:201], v[200:201], v[40:41]\n"
"v_pk_mul_f32 v[202:203], v[202:203], v[42:43]\n"
"v_pk_mul_f32 v[204:205], v[204:205], v[44:45]\n"
"v_pk_mul_f32 v[206:207], v[206:207], v[46:47]\n"
"EX v192,v192\n"
"EX v193,v193\n"
"LD v[164:167],v1,s[40:43],s60 offen offset:1024 sc1\n"
"EX v194,v194\n"
"EX v195,v195\n"
"EX v196,v196\n"
"EX v197,v197\n"
"EX v198,v198\n"
"EX v199,v199\n"
"EX v200,v200\n"
"EX v201,v201\n"
"EX v202,v202\n"
"EX v203,v203\n"
"EX v204,v204\n"
"EX v205,v205\n"
"EX v206,v206\n"
"LD v[168:171],v1,s[40:43],s60 offen offset:2048 sc1\n"
"EX v207,v207\n"
"v_pk_add_f32 v[192:193], v[192:193], 1.0 op_sel_hi:[1,0]\n"
"v_pk_add_f32 v[194:195], v[194:195], 1.0 op_sel_hi:[1,0]\n"
"v_pk_add_f32 v[196:197], v[196:197], 1.0 op_sel_hi:[1,0]\n"
"v_pk_add_f32 v[198:199], v[198:199], 1.0 op_sel_hi:[1,0]\n"
"v_pk_add_f32 v[200:201], v[200:201], 1.0 op_sel_hi:[1,0]\n"
"v_pk_add_f32 v[202:203], v[202:203], 1.0 op_sel_hi:[1,0]\n"
"v_pk_add_f32 v[204:205], v[204:205], 1.0 op_sel_hi:[1,0]\n"
"v_pk_add_f32 v[206:207], v[206:207], 1.0 op_sel_hi:[1,0]\n"
"v_rcp_f32 v192, v192\n"
"v_rcp_f32 v193, v193\n"
"v_rcp_f32 v194, v194\n"
"v_rcp_f32 v195, v195\n"
"LD v[172:175],v1,s[40:43],s60 offen offset:3072 sc1\n"
"v_rcp_f32 v196, v196\n"
"v_rcp_f32 v197, v197\n"
"v_rcp_f32 v198, v198\n"
"v_rcp_f32 v199, v199\n"
"v_rcp_f32 v200, v200\n"
"v_rcp_f32 v201, v201\n"
"v_rcp_f32 v202, v202\n"
"v_rcp_f32 v203, v203\n"
"v_rcp_f32 v204, v204\n"
"v_rcp_f32 v205, v205\n"
"v_rcp_f32 v206, v206\n"
"v_rcp_f32 v207, v207\n"
"v_pk_mul_f32 v[32:33], v[32:33], v[192:193]\n"
"LD v[176:179],v1,s[40:43],s61 offen offset:0 sc1\n"
"v_pk_mul_f32 v[34:35], v[34:35], v[194:195]\n"
"v_pk_mul_f32 v[36:37], v[36:37], v[196:197]\n"
"v_pk_mul_f32 v[38:39], v[38:39], v[198:199]\n"
"v_pk_mul_f32 v[40:41], v[40:41], v[200:201]\n"
"v_pk_mul_f32 v[42:43], v[42:43], v[202:203]\n"
"v_pk_mul_f32 v[44:45], v[44:45], v[204:205]\n"
"v_pk_mul_f32 v[46:47], v[46:47], v[206:207]\n"
"CV v32,v32,v33\n"
"CV v33,v34,v35\n"
"CV v34,v36,v37\n"
"CV v35,v38,v39\n"
"ST v[32:35],v1,s[40:43],s57 offen offset:0 sc1\n"
"CV v40,v40,v41\n"
"CV v41,v42,v43\n"
"CV v42,v44,v45\n"
"CV v43,v46,v47\n"
"ST v[40:43],v1,s[40:43],s57 offen offset:1024 sc1\n"
"v_pk_mul_f32 v[64:65], v[64:65], v[132:133] op_sel_hi:[1,0]\n"
"v_pk_mul_f32 v[66:67], v[66:67], v[132:133] op_sel_hi:[1,0]\n"
"v_pk_mul_f32 v[68:69], v[68:69], v[132:133] op_sel_hi:[1,0]\n"
"v_pk_mul_f32 v[70:71], v[70:71], v[132:133] op_sel_hi:[1,0]\n"
"v_pk_mul_f32 v[72:73], v[72:73], v[132:133] op_sel_hi:[1,0]\n"
"v_pk_mul_f32 v[74:75], v[74:75], v[132:133] op_sel_hi:[1,0]\n"
"v_pk_mul_f32 v[76:77], v[76:77], v[132:133] op_sel_hi:[1,0]\n"
"v_pk_mul_f32 v[78:79], v[78:79], v[132:133] op_sel_hi:[1,0]\n"
"v_pk_fma_f32 v[64:65], v[96:97], v[132:133], v[64:65] op_sel:[0,1,0] op_sel_hi:[1,1,1]\n"
"v_pk_fma_f32 v[66:67], v[98:99], v[132:133], v[66:67] op_sel:[0,1,0] op_sel_hi:[1,1,1]\n"
"v_pk_fma_f32 v[68:69], v[100:101], v[132:133], v[68:69] op_sel:[0,1,0] op_sel_hi:[1,1,1]\n"
"v_pk_fma_f32 v[70:71], v[102:103], v[132:133], v[70:71] op_sel:[0,1,0] op_sel_hi:[1,1,1]\n"
"v_pk_fma_f32 v[72:73], v[104:105], v[132:133], v[72:73] op_sel:[0,1,0] op_sel_hi:[1,1,1]\n"
"v_pk_fma_f32 v[74:75], v[106:107], v[132:133], v[74:75] op_sel:[0,1,0] op_sel_hi:[1,1,1]\n"
"v_pk_fma_f32 v[76:77], v[108:109], v[132:133], v[76:77] op_sel:[0,1,0] op_sel_hi:[1,1,1]\n"
"v_pk_fma_f32 v[78:79], v[110:111], v[132:133], v[78:79] op_sel:[0,1,0] op_sel_hi:[1,1,1]\n"
"v_pk_mul_f32 v[192:193], v[64:65], v[64:65]\n"
"v_pk_mul_f32 v[194:195], v[66:67], v[66:67]\n"
"v_pk_mul_f32 v[196:197], v[68:69], v[68:69]\n"
"v_pk_mul_f32 v[198:199], v[70:71], v[70:71]\n"
"v_pk_mul_f32 v[200:201], v[72:73], v[72:73]\n"
"v_pk_mul_f32 v[202:203], v[74:75], v[74:75]\n"
"v_pk_mul_f32 v[204:205], v[76:77], v[76:77]\n"
"v_pk_mul_f32 v[206:207], v[78:79], v[78:79]\n"
"v_pk_fma_f32 v[192:193], v[192:193], s[74:75], s[74:75] op_sel:[0,0,1] op_sel_hi:[1,0,1]\n"
"v_pk_fma_f32 v[194:195], v[194:195], s[74:75], s[74:75] op_sel:[0,0,1] op_sel_hi:[1,0,1]\n"
"v_pk_fma_f32 v[196:197], v[196:197], s[74:75], s[74:75] op_sel:[0,0,1] op_sel_hi:[1,0,1]\n"
"v_pk_fma_f32 v[198:199], v[198:199], s[74:75], s[74:75] op_sel:[0,0,1] op_sel_hi:[1,0,1]\n"
"v_pk_fma_f32 v[200:201], v[200:201], s[74:75], s[74:75] op_sel:[0,0,1] op_sel_hi:[1,0,1]\n"
"LD v[180:183],v1,s[40:43],s61 offen offset:1024 sc1\n"
"v_pk_fma_f32 v[202:203], v[202:203], s[74:75], s[74:75] op_sel:[0,0,1] op_sel_hi:[1,0,1]\n"
"v_pk_fma_f32 v[204:205], v[204:205], s[74:75], s[74:75] op_sel:[0,0,1] op_sel_hi:[1,0,1]\n"
"v_pk_fma_f32 v[206:207], v[206:207], s[74:75], s[74:75] op_sel:[0,0,1] op_sel_hi:[1,0,1]\n"
"v_pk_mul_f32 v[192:193], v[192:193], v[64:65]\n"
"v_pk_mul_f32 v[194:195], v[194:195], v[66:67]\n"
"v_pk_mul_f32 v[196:197], v[196:197], v[68:69]\n"
"v_pk_mul_f32 v[198:199], v[198:199], v[70:71]\n"
"v_pk_mul_f32 v[200:201], v[200:201], v[72:73]\n"
"v_pk_mul_f32 v[202:203], v[202:203], v[74:75]\n"
"v_pk_mul_f32 v[204:205], v[204:205], v[76:77]\n"
"v_pk_mul_f32 v[206:207], v[206:207], v[78:79]\n"
"EX v192,v192\n"
"EX v193,v193\n"
"LD v[184:187],v1,s[40:43],s61 offen offset:2048 sc1\n"
"EX v194,v194\n"
"EX v195,v195\n"
"EX v196,v196\n"
"EX v197,v197\n"
"EX v198,v198\n"
"EX v199,v199\n"
"EX v200,v200\n"
"EX v201,v201\n"
"EX v202,v202\n"
"EX v203,v203\n"
"EX v204,v204\n"
"EX v205,v205\n"
"EX v206,v206\n"
"LD v[188:191],v1,s[40:43],s61 offen offset:3072 sc1\n"
"EX v207,v207\n"
"v_pk_add_f32 v[192:193], v[192:193], 1.0 op_sel_hi:[1,0]\n"
"v_pk_add_f32 v[194:195], v[194:195], 1.0 op_sel_hi:[1,0]\n"
"v_pk_add_f32 v[196:197], v[196:197], 1.0 op_sel_hi:[1,0]\n"
"v_pk_add_f32 v[198:199], v[198:199], 1.0 op_sel_hi:[1,0]\n"
"v_pk_add_f32 v[200:201], v[200:201], 1.0 op_sel_hi:[1,0]\n"
"v_pk_add_f32 v[202:203], v[202:203], 1.0 op_sel_hi:[1,0]\n"
"v_pk_add_f32 v[204:205], v[204:205], 1.0 op_sel_hi:[1,0]\n"
"v_pk_add_f32 v[206:207], v[206:207], 1.0 op_sel_hi:[1,0]\n"
"v_rcp_f32 v192, v192\n"
"v_rcp_f32 v193, v193\n"
"v_rcp_f32 v194, v194\n"
"s_add_u32 s65, s65, s23\n"
"s_add_u32 m0, s22, 49152\n"
"v_rcp_f32 v195, v195\n"
"LD v2,s[52:55],s65 offen offset:0 lds\n"
"v_rcp_f32 v196, v196\n"
"v_rcp_f32 v197, v197\n"
"v_rcp_f32 v198, v198\n"
"v_rcp_f32 v199, v199\n"
"v_rcp_f32 v200, v200\n"
"v_rcp_f32 v201, v201\n"
"v_rcp_f32 v202, v202\n"
"v_rcp_f32 v203, v203\n"
"v_rcp_f32 v204, v204\n"
"v_rcp_f32 v205, v205\n"
"v_rcp_f32 v206, v206\n"
"v_rcp_f32 v207, v207\n"
"v_pk_mul_f32 v[64:65], v[64:65], v[192:193]\n"
"LD v3,s[52:55],s65 offen offset:1024 lds\n"
"v_pk_mul_f32 v[66:67], v[66:67], v[194:195]\n"
"v_pk_mul_f32 v[68:69], v[68:69], v[196:197]\n"
"v_pk_mul_f32 v[70:71], v[70:71], v[198:199]\n"
"v_pk_mul_f32 v[72:73], v[72:73], v[200:201]\n"
"v_pk_mul_f32 v[74:75], v[74:75], v[202:203]\n"
"v_pk_mul_f32 v[76:77], v[76:77], v[204:205]\n"
"v_pk_mul_f32 v[78:79], v[78:79], v[206:207]\n"
"CV v64,v64,v65\n"
"CV v65,v66,v67\n"
"CV v66,v68,v69\n"
"CV v67,v70,v71\n"
"ST v[64:67],v1,s[40:43],s58 offen offset:0 sc1\n"
"CV v72,v72,v73\n"
"CV v73,v74,v75\n"
"CV v74,v76,v77\n"
"CV v75,v78,v79\n"
"ST v[72:75],v1,s[40:43],s58 offen offset:1024 sc1\n"
"v_pk_mul_f32 v[48:49], v[48:49], v[130:131] op_sel_hi:[1,0]\n"
"v_pk_mul_f32 v[50:51], v[50:51], v[130:131] op_sel_hi:[1,0]\n"
"v_pk_mul_f32 v[52:53], v[52:53], v[130:131] op_sel_hi:[1,0]\n"
"v_pk_mul_f32 v[54:55], v[54:55], v[130:131] op_sel_hi:[1,0]\n"
"v_pk_mul_f32 v[56:57], v[56:57], v[130:131] op_sel_hi:[1,0]\n"
"v_pk_mul_f32 v[58:59], v[58:59], v[130:131] op_sel_hi:[1,0]\n"
"v_pk_mul_f32 v[60:61], v[60:61], v[130:131] op_sel_hi:[1,0]\n"
"v_pk_mul_f32 v[62:63], v[62:63], v[130:131] op_sel_hi:[1,0]\n"
"v_pk_fma_f32 v[48:49], v[112:113], v[130:131], v[48:49] op_sel:[0,1,0] op_sel_hi:[1,1,1]\n"
"v_pk_fma_f32 v[50:51], v[114:115], v[130:131], v[50:51] op_sel:[0,1,0] op_sel_hi:[1,1,1]\n"
"v_pk_fma_f32 v[52:53], v[116:117], v[130:131], v[52:53] op_sel:[0,1,0] op_sel_hi:[1,1,1]\n"
"v_pk_fma_f32 v[54:55], v[118:119], v[130:131], v[54:55] op_sel:[0,1,0] op_sel_hi:[1,1,1]\n"
"v_pk_fma_f32 v[56:57], v[120:121], v[130:131], v[56:57] op_sel:[0,1,0] op_sel_hi:[1,1,1]\n"
"v_pk_fma_f32 v[58:59], v[122:123], v[130:131], v[58:59] op_sel:[0,1,0] op_sel_hi:[1,1,1]\n"
"v_pk_fma_f32 v[60:61], v[124:125], v[130:131], v[60:61] op_sel:[0,1,0] op_sel_hi:[1,1,1]\n"
"v_pk_fma_f32 v[62:63], v[126:127], v[130:131], v[62:63] op_sel:[0,1,0] op_sel_hi:[1,1,1]\n"
"v_pk_mul_f32 v[192:193], v[48:49], v[48:49]\n"
"v_pk_mul_f32 v[194:195], v[50:51], v[50:51]\n"
"v_pk_mul_f32 v[196:197], v[52:53], v[52:53]\n"
"v_pk_mul_f32 v[198:199], v[54:55], v[54:55]\n"
"v_pk_mul_f32 v[200:201], v[56:57], v[56:57]\n"
"v_pk_mul_f32 v[202:203], v[58:59], v[58:59]\n"
"v_pk_mul_f32 v[204:205], v[60:61], v[60:61]\n"
"v_pk_mul_f32 v[206:207], v[62:63], v[62:63]\n"
"v_pk_fma_f32 v[192:193], v[192:193], s[74:75], s[74:75] op_sel:[0,0,1] op_sel_hi:[1,0,1]\n"
"v_pk_fma_f32 v[194:195], v[194:195], s[74:75], s[74:75] op_sel:[0,0,1] op_sel_hi:[1,0,1]\n"
"v_pk_fma_f32 v[196:197], v[196:197], s[74:75], s[74:75] op_sel:[0,0,1] op_sel_hi:[1,0,1]\n"
"v_pk_fma_f32 v[198:199], v[198:199], s[74:75], s[74:75] op_sel:[0,0,1] op_sel_hi:[1,0,1]\n"
"v_pk_fma_f32 v[200:201], v[200:201], s[74:75], s[74:75] op_sel:[0,0,1] op_sel_hi:[1,0,1]\n"
"LD v4,s[52:55],s65 offen offset:2048 lds\n"
"v_pk_fma_f32 v[202:203], v[202:203], s[74:75], s[74:75] op_sel:[0,0,1] op_sel_hi:[1,0,1]\n"
"v_pk_fma_f32 v[204:205], v[204:205], s[74:75], s[74:75] op_sel:[0,0,1] op_sel_hi:[1,0,1]\n"
"v_pk_fma_f32 v[206:207], v[206:207], s[74:75], s[74:75] op_sel:[0,0,1] op_sel_hi:[1,0,1]\n"
"v_pk_mul_f32 v[192:193], v[192:193], v[48:49]\n"
"v_pk_mul_f32 v[194:195], v[194:195], v[50:51]\n"
"v_pk_mul_f32 v[196:197], v[196:197], v[52:53]\n"
"v_pk_mul_f32 v[198:199], v[198:199], v[54:55]\n"
"v_pk_mul_f32 v[200:201], v[200:201], v[56:57]\n"
"v_pk_mul_f32 v[202:203], v[202:203], v[58:59]\n"
"v_pk_mul_f32 v[204:205], v[204:205], v[60:61]\n"
"v_pk_mul_f32 v[206:207], v[206:207], v[62:63]\n"
"EX v192,v192\n"
"EX v193,v193\n"
"LD v5,s[52:55],s65 offen offset:3072 lds\n"
"EX v194,v194\n"
"EX v195,v195\n"
"EX v196,v196\n"
"EX v197,v197\n"
"EX v198,v198\n"
"EX v199,v199\n"
"EX v200,v200\n"
"EX v201,v201\n"
"EX v202,v202\n"
"EX v203,v203\n"
"EX v204,v204\n"
"EX v205,v205\n"
"s_add_u32 s65, s65, s23\n"
"s_add_u32 m0, s22, 65536\n"
"EX v206,v206\n"
"LD v2,s[52:55],s65 offen offset:0 lds\n"
"EX v207,v207\n"
"v_pk_add_f32 v[192:193], v[192:193], 1.0 op_sel_hi:[1,0]\n"
"v_pk_add_f32 v[194:195], v[194:195], 1.0 op_sel_hi:[1,0]\n"
"v_pk_add_f32 v[196:197], v[196:197], 1.0 op_sel_hi:[1,0]\n"
"v_pk_add_f32 v[198:199], v[198:199], 1.0 op_sel_hi:[1,0]\n"
"v_pk_add_f32 v[200:201], v[200:201], 1.0 op_sel_hi:[1,0]\n"
"v_pk_add_f32 v[202:203], v[202:203], 1.0 op_sel_hi:[1,0]\n"
"v_pk_add_f32 v[204:205], v[204:205], 1.0 op_sel_hi:[1,0]\n"
"v_pk_add_f32 v[206:207], v[206:207], 1.0 op_sel_hi:[1,0]\n"
"v_rcp_f32 v192, v192\n"
"v_rcp_f32 v193, v193\n"
"v_rcp_f32 v194, v194\n"
"v_rcp_f32 v195, v195\n"
"LD v3,s[52:55],s65 offen offset:1024 lds\n"
"v_rcp_f32 v196, v196\n"
"v_rcp_f32 v197, v197\n"
"v_rcp_f32 v198, v198\n"
"v_rcp_f32 v199, v199\n"
"v_rcp_f32 v200, v200\n"
"v_rcp_f32 v201, v201\n"
"v_rcp_f32 v202, v202\n"
"v_rcp_f32 v203, v203\n"
"v_rcp_f32 v204, v204\n"
"v_rcp_f32 v205, v205\n"
"v_rcp_f32 v206, v206\n"
"v_rcp_f32 v207, v207\n"
"v_pk_mul_f32 v[48:49], v[48:49], v[192:193]\n"
"LD v4,s[52:55],s65 offen offset:2048 lds\n"
"v_pk_mul_f32 v[50:51], v[50:51], v[194:195]\n"
"v_pk_mul_f32 v[52:53], v[52:53], v[196:197]\n"
"v_pk_mul_f32 v[54:55], v[54:55], v[198:199]\n"
"v_pk_mul_f32 v[56:57], v[56:57], v[200:201]\n"
"v_pk_mul_f32 v[58:59], v[58:59], v[202:203]\n"
"v_pk_mul_f32 v[60:61], v[60:61], v[204:205]\n"
"v_pk_mul_f32 v[62:63], v[62:63], v[206:207]\n"
"WT vmcnt(5)\n"
"s_lshl_b32 s56, s33, 2\n"
"s_add_u32 s56, s56, s34\n"
"s_lshl_b32 s56, s56, 7\n"
"s_add_u32 s56, s56, 0xc0000\n"
"v_lshlrev_b32 v21, 3, v0\n"
"s_mov_b64 exec, 1\n"
"buffer_store_dwordx2 v[16:17], v21, s[40:43], s56 offen sc1\n"
"s_mov_b64 exec, -1\n"
"CV v48,v48,v49\n"
"CV v49,v50,v51\n"
"CV v50,v52,v53\n"
"CV v51,v54,v55\n"
"ST v[48:51],v1,s[76:79],s57 offen offset:2048 sc1\n"
"CV v56,v56,v57\n"
"CV v57,v58,v59\n"
"CV v58,v60,v61\n"
"CV v59,v62,v63\n"
"ST v[56:59],v1,s[76:79],s57 offen offset:3072 sc1\n"
"v_pk_mul_f32 v[80:81], v[80:81], v[132:133] op_sel_hi:[1,0]\n"
"v_pk_mul_f32 v[82:83], v[82:83], v[132:133] op_sel_hi:[1,0]\n"
"v_pk_mul_f32 v[84:85], v[84:85], v[132:133] op_sel_hi:[1,0]\n"
"v_pk_mul_f32 v[86:87], v[86:87], v[132:133] op_sel_hi:[1,0]\n"
"v_pk_mul_f32 v[88:89], v[88:89], v[132:133] op_sel_hi:[1,0]\n"
"v_pk_mul_f32 v[90:91], v[90:91], v[132:133] op_sel_hi:[1,0]\n"
"v_pk_mul_f32 v[92:93], v[92:93], v[132:133] op_sel_hi:[1,0]\n"
"v_pk_mul_f32 v[94:95], v[94:95], v[132:133] op_sel_hi:[1,0]\n"
"v_pk_fma_f32 v[80:81], v[112:113], v[132:133], v[80:81] op_sel:[0,1,0] op_sel_hi:[1,1,1]\n"
"v_pk_fma_f32 v[82:83], v[114:115], v[132:133], v[82:83] op_sel:[0,1,0] op_sel_hi:[1,1,1]\n"
"v_pk_fma_f32 v[84:85], v[116:117], v[132:133], v[84:85] op_sel:[0,1,0] op_sel_hi:[1,1,1]\n"
"v_pk_fma_f32 v[86:87], v[118:119], v[132:133], v[86:87] op_sel:[0,1,0] op_sel_hi:[1,1,1]\n"
"v_pk_fma_f32 v[88:89], v[120:121], v[132:133], v[88:89] op_sel:[0,1,0] op_sel_hi:[1,1,1]\n"
"v_pk_fma_f32 v[90:91], v[122:123], v[132:133], v[90:91] op_sel:[0,1,0] op_sel_hi:[1,1,1]\n"
"v_pk_fma_f32 v[92:93], v[124:125], v[132:133], v[92:93] op_sel:[0,1,0] op_sel_hi:[1,1,1]\n"
"v_pk_fma_f32 v[94:95], v[126:127], v[132:133], v[94:95] op_sel:[0,1,0] op_sel_hi:[1,1,1]\n"
"v_pk_mul_f32 v[192:193], v[80:81], v[80:81]\n"
"v_pk_mul_f32 v[194:195], v[82:83], v[82:83]\n"
"v_pk_mul_f32 v[196:197], v[84:85], v[84:85]\n"
"v_pk_mul_f32 v[198:199], v[86:87], v[86:87]\n"
"v_pk_mul_f32 v[200:201], v[88:89], v[88:89]\n"
"v_pk_mul_f32 v[202:203], v[90:91], v[90:91]\n"
"v_pk_mul_f32 v[204:205], v[92:93], v[92:93]\n"
"v_pk_mul_f32 v[206:207], v[94:95], v[94:95]\n"
"v_pk_fma_f32 v[192:193], v[192:193], s[74:75], s[74:75] op_sel:[0,0,1] op_sel_hi:[1,0,1]\n"
"v_pk_fma_f32 v[194:195], v[194:195], s[74:75], s[74:75] op_sel:[0,0,1] op_sel_hi:[1,0,1]\n"
"v_pk_fma_f32 v[196:197], v[196:197], s[74:75], s[74:75] op_sel:[0,0,1] op_sel_hi:[1,0,1]\n"
"v_pk_fma_f32 v[198:199], v[198:199], s[74:75], s[74:75] op_sel:[0,0,1] op_sel_hi:[1,0,1]\n"
"v_pk_fma_f32 v[200:201], v[200:201], s[74:75], s[74:75] op_sel:[0,0,1] op_sel_hi:[1,0,1]\n"
"LD v5,s[52:55],s65 offen offset:3072 lds\n"
"v_pk_fma_f32 v[202:203], v[202:203], s[74:75], s[74:75] op_sel:[0,0,1] op_sel_hi:[1,0,1]\n"
"v_pk_fma_f32 v[204:205], v[204:205], s[74:75], s[74:75] op_sel:[0,0,1] op_sel_hi:[1,0,1]\n"
"v_pk_fma_f32 v[206:207], v[206:207], s[74:75], s[74:75] op_sel:[0,0,1] op_sel_hi:[1,0,1]\n"
"v_pk_mul_f32 v[192:193], v[192:193], v[80:81]\n"
"v_pk_mul_f32 v[194:195], v[194:195], v[82:83]\n"
"v_pk_mul_f32 v[196:197], v[196:197], v[84:85]\n"
"v_pk_mul_f32 v[198:199], v[198:199], v[86:87]\n"
"v_pk_mul_f32 v[200:201], v[200:201], v[88:89]\n"
"v_pk_mul_f32 v[202:203], v[202:203], v[90:91]\n"
"v_pk_mul_f32 v[204:205], v[204:205], v[92:93]\n"
"v_pk_mul_f32 v[206:207], v[206:207], v[94:95]\n"
"EX v192,v192\n"
"s_add_u32 s65, s65, s23\n"
"s_add_u32 m0, s22, 81920\n"
"EX v193,v193\n"
"LD v2,s[52:55],s65 offen offset:0 lds\n"
"EX v194,v194\n"
"EX v195,v195\n"
"EX v196,v196\n"
"EX v197,v197\n"
"EX v198,v198\n"
"EX v199,v199\n"
"EX v200,v200\n"
"EX v201,v201\n"
"EX v202,v202\n"
"EX v203,v203\n"
"EX v204,v204\n"
"EX v205,v205\n"
"EX v206,v206\n"
"LD v3,s[52:55],s65 offen offset:1024 lds\n"
"EX v207,v207\n"
"v_pk_add_f32 v[192:193], v[192:193], 1.0 op_sel_hi:[1,0]\n"
"v_pk_add_f32 v[194:195], v[194:195], 1.0 op_sel_hi:[1,0]\n"
"v_pk_add_f32 v[196:197], v[196:197], 1.0 op_sel_hi:[1,0]\n"
"v_pk_add_f32 v[198:199], v[198:199], 1.0 op_sel_hi:[1,0]\n"
"v_pk_add_f32 v[200:201], v[200:201], 1.0 op_sel_hi:[1,0]\n"
"v_pk_add_f32 v[202:203], v[202:203], 1.0 op_sel_hi:[1,0]\n"
"v_pk_add_f32 v[204:205], v[204:205], 1.0 op_sel_hi:[1,0]\n"
"v_pk_add_f32 v[206:207], v[206:207], 1.0 op_sel_hi:[1,0]\n"
"v_rcp_f32 v192, v192\n"
"v_rcp_f32 v193, v193\n"
"v_rcp_f32 v194, v194\n"
"v_rcp_f32 v195, v195\n"
"LD v4,s[52:55],s65 offen offset:2048 lds\n"
"v_rcp_f32 v196, v196\n"
"v_rcp_f32 v197, v197\n"
"v_rcp_f32 v198, v198\n"
"v_rcp_f32 v199, v199\n"
"v_rcp_f32 v200, v200\n"
"v_rcp_f32 v201, v201\n"
"v_rcp_f32 v202, v202\n"
"v_rcp_f32 v203, v203\n"
"v_rcp_f32 v204, v204\n"
"v_rcp_f32 v205, v205\n"
"v_rcp_f32 v206, v206\n"
"v_rcp_f32 v207, v207\n"
"v_pk_mul_f32 v[80:81], v[80:81], v[192:193]\n"
"LD v5,s[52:55],s65 offen offset:3072 lds\n"
"v_pk_mul_f32 v[82:83], v[82:83], v[194:195]\n"
"v_pk_mul_f32 v[84:85], v[84:85], v[196:197]\n"
"v_pk_mul_f32 v[86:87], v[86:87], v[198:199]\n"
"v_pk_mul_f32 v[88:89], v[88:89], v[200:201]\n"
"v_pk_mul_f32 v[90:91], v[90:91], v[202:203]\n"
"v_pk_mul_f32 v[92:93], v[92:93], v[204:205]\n"
"v_pk_mul_f32 v[94:95], v[94:95], v[206:207]\n"
"CV v80,v80,v81\n"
"CV v81,v82,v83\n"
"CV v82,v84,v85\n"
"CV v83,v86,v87\n"
"ST v[80:83],v1,s[76:79],s58 offen offset:2048 sc1\n"
"CV v88,v88,v89\n"
"CV v89,v90,v91\n"
"CV v90,v92,v93\n"
"CV v91,v94,v95\n"
"ST v[88:91],v1,s[76:79],s58 offen offset:3072 sc1\n"
"v_lshlrev_b32 v192, 16, v160\n"
"v_and_b32 v193, 0xffff0000, v160\n"
"v_lshlrev_b32 v194, 16, v161\n"
"v_and_b32 v195, 0xffff0000, v161\n"
"v_lshlrev_b32 v196, 16, v162\n"
"v_and_b32 v197, 0xffff0000, v162\n"
"v_lshlrev_b32 v198, 16, v163\n"
"v_and_b32 v199, 0xffff0000, v163\n"
"v_lshlrev_b32 v200, 16, v164\n"
"v_and_b32 v201, 0xffff0000, v164\n"
"v_lshlrev_b32 v202, 16, v165\n"
"v_and_b32 v203, 0xffff0000, v165\n"
"v_lshlrev_b32 v204, 16, v166\n"
"v_and_b32 v205, 0xffff0000, v166\n"
"v_lshlrev_b32 v206, 16, v167\n"
"v_and_b32 v207, 0xffff0000, v167\n"
"v_lshlrev_b32 v208, 16, v168\n"
"v_and_b32 v209, 0xffff0000, v168\n"
"v_lshlrev_b32 v210, 16, v169\n"
"v_and_b32 v211, 0xffff0000, v169\n"
"v_lshlrev_b32 v212, 16, v170\n"
"v_and_b32 v213, 0xffff0000, v170\n"
"v_lshlrev_b32 v214, 16, v171\n"
"v_and_b32 v215, 0xffff0000, v171\n"
"v_lshlrev_b32 v216, 16, v172\n"
"v_and_b32 v217, 0xffff0000, v172\n"
"v_lshlrev_b32 v218, 16, v173\n"
"v_and_b32 v219, 0xffff0000, v173\n"
"v_lshlrev_b32 v220, 16, v174\n"
"v_and_b32 v221, 0xffff0000, v174\n"
"v_lshlrev_b32 v222, 16, v175\n"
"v_and_b32 v223, 0xffff0000, v175\n"
"v_lshlrev_b32 v224, 16, v176\n"
"v_and_b32 v225, 0xffff0000, v176\n"
"v_lshlrev_b32 v226, 16, v177\n"
"v_and_b32 v227, 0xffff0000, v177\n"
"v_lshlrev_b32 v228, 16, v178\n"
"v_and_b32 v229, 0xffff0000, v178\n"
"v_lshlrev_b32 v230, 16, v179\n"
"v_and_b32 v231, 0xffff0000, v179\n"
"v_lshlrev_b32 v232, 16, v180\n"
"v_and_b32 v233, 0xffff0000, v180\n"
"v_lshlrev_b32 v234, 16, v181\n"
"v_and_b32 v235, 0xffff0000, v181\n"
"v_lshlrev_b32 v236, 16, v182\n"
"v_and_b32 v237, 0xffff0000, v182\n"
"v_lshlrev_b32 v238, 16, v183\n"
"v_and_b32 v239, 0xffff0000, v183\n"
"v_lshlrev_b32 v240, 16, v184\n"
"v_and_b32 v241, 0xffff0000, v184\n"
"v_lshlrev_b32 v242, 16, v185\n"
"v_and_b32 v243, 0xffff0000, v185\n"
"v_lshlrev_b32 v244, 16, v186\n"
"v_and_b32 v245, 0xffff0000, v186\n"
"v_lshlrev_b32 v246, 16, v187\n"
"v_and_b32 v247, 0xffff0000, v187\n"
"v_lshlrev_b32 v248, 16, v188\n"
"v_and_b32 v249, 0xffff0000, v188\n"
"v_lshlrev_b32 v250, 16, v189\n"
"v_and_b32 v251, 0xffff0000, v189\n"
"v_lshlrev_b32 v252, 16, v190\n"
"v_and_b32 v253, 0xffff0000, v190\n"
"v_lshlrev_b32 v254, 16, v191\n"
"v_and_b32 v255, 0xffff0000, v191\n"
"WT vmcnt(2)\n"
"L_gt2_34:\n"
"buffer_load_dwordx2 v[18:19], v20, s[40:43], 0 offen sc1\n"
"s_barrier\n"
"TR a[160:161],v24 offset:32768\n"
"TR a[162:163],v24 offset:34816\n"
"TR a[164:165],v25 offset:32768\n"
"TR a[166:167],v25 offset:34816\n"
"TR a[168:169],v26 offset:32768\n"
"TR a[170:171],v26 offset:34816\n"
"TR a[172:173],v27 offset:32768\n"
"TR a[174:175],v27 offset:34816\n"
"TR a[176:177],v24 offset:36864\n"
"TR a[178:179],v24 offset:38912\n"
"TR a[180:181],v25 offset:36864\n"
"TR a[182:183],v25 offset:38912\n"
"TR a[184:185],v26 offset:36864\n"
"TR a[186:187],v26 offset:38912\n"
"TR a[188:189],v27 offset:36864\n"
"TR a[190:191],v27 offset:38912\n"
"s_add_u32 s60, s60, 0x400000\n"
"s_add_u32 s61, s61, 0x400000\n"
"v_and_b32 v21, 15, v0\n"
"v_lshrrev_b32 v22, 2, v21\n"
"v_lshrrev_b32 v23, 4, v0\n"
"v_cmp_eq_u32 vcc, v22, v23\n"
"v_and_b32 v22, 1, v0\n"
"v_lshlrev_b32 v22, 4, v22\n"
"v_mov_b32 v23, 0x3f80\n"
"v_lshlrev_b32 v23, v22, v23\n"
"v_cndmask_b32 v23, 0, v23, vcc\n"
"v_and_b32 v21, 2, v0\n"
"v_cmp_eq_u32 vcc, 0, v21\n"
"s_nop 1\n"
"v_cndmask_b32 v22, 0, v23, vcc\n"
"v_cndmask_b32 v23, v23, 0, vcc\n"
"AW a144,v22\n"
"AW a145,v23\n"
"AW a146,0\n"
"AW a147,0\n"
"AW a148,0\n"
"AW a149,0\n"
"AW a150,v22\n"
"AW a151,v23\n"
"WT vmcnt(0)\n"
"v_cmp_ne_u64 vcc, s[38:39], v[18:19]\n"
"s_nop 1\n"
"s_cmp_eq_u64 vcc, 0\n"
"s_cbranch_scc0 L_psp_49\n"
"L_pok_48:\n"
"s_lshl_b32 s56, s33, 2\n"
"s_add_u32 s56, s56, s34\n"
"s_lshl_b32 s56, s56, 7\n"
"s_add_u32 s56, s56, 0xa0000\n"
"v_lshlrev_b32 v21, 3, v0\n"
"s_mov_b64 exec, 1\n"
"buffer_store_dwordx2 v[16:17], v21, s[40:43], s56 offen sc1\n"
"s_mov_b64 exec, -1\n"
"s_mov_b32 s64, 0\n"
"LD a[0:3],v10,s[44:47],s64 offen offset:0 sc1\n"
"LD a[4:7],v11,s[44:47],s64 offen offset:0 sc1\n"
"LD a[8:11],v10,s[44:47],s64 offen offset:1024 sc1\n"
"LD a[12:15],v11,s[44:47],s64 offen offset:1024 sc1\n"
"s_mov_b32 s64, 4096\n"
"LD a[16:19],v10,s[44:47],s64 offen offset:0 sc1\n"
"LD a[20:23],v11,s[44:47],s64 offen offset:0 sc1\n"
"v_add_u32 v6, 0xfffe0000, v20\n"
"s_bfe_u32 s56, s14, 0x20001\n"
"s_mul_i32 s56, s56, 5\n"
"s_add_u32 s56, s56, s15\n"
"s_lshl_b32 s56, s56, 3\n"
"s_lshr_b32 s57, s14, 3\n"
"s_lshl_b32 s57, s57, 2\n"
"s_add_u32 s56, s56, s57\n"
"s_lshl_b32 s56, s56, 2\n"
"s_and_b32 s57, s14, 1\n"
"s_lshl_b32 s57, s57, 1\n"
"s_add_u32 s56, s56, s57\n"
"s_lshr_b32 s57, s34, 1\n"
"s_add_u32 s56, s56, s57\n"
"v_and_b32 v21, 3, v0\n"
"v_lshlrev_b32 v21, 2, v21\n"
"v_add_u32 v21, s56, v21\n"
"v_lshlrev_b32 v21, 7, v21\n"
"v_add_u32 v20, 0xa0000, v21\n"
"v_add_u32 v7, 0xfffe0000, v20\n"
"v_cmp_gt_u32 vcc, 16, v0\n"
"s_nop 1\n"
"v_cndmask_b32 v20, v20, v6, vcc\n"
"s_mul_i32 s67, s56, 16384\n"
"s_and_b32 s57, s34, 1\n"
"s_lshl_b32 s57, s57, 13\n"
"s_add_u32 s67, s67, s57\n"
"s_add_u32 s67, s67, 0x5000000\n"
"v_and_b32 v22, 15, v0\n"
"v_mul_u32_u24 v22, 1280, v22\n"
"v_lshrrev_b32 v23, 4, v0\n"
"v_and_b32 v21, 1, v23\n"
"v_lshlrev_b32 v21, 5, v21\n"
"v_lshrrev_b32 v23, 1, v23\n"
"v_lshl_add_u32 v21, v23, 4, v21\n"
"v_add_u32 v22, v21, v22\n"
"LD a[24:27],v10,s[44:47],s64 offen offset:1024 sc1\n"
"LD a[28:31],v11,s[44:47],s64 offen offset:1024 sc1\n"
"s_mov_b32 s64, 8192\n"
"LD a[32:35],v10,s[44:47],s64 offen offset:0 sc1\n"
"LD a[36:39],v11,s[44:47],s64 offen offset:0 sc1\n"
"LD a[40:43],v10,s[44:47],s64 offen offset:1024 sc1\n"
"LD a[44:47],v11,s[44:47],s64 offen offset:1024 sc1\n"
"WT vmcnt(11) lgkmcnt(14)\n"
"MF v[192:195],a[160:163],a[0:3],v[192:195]\n"
"WT vmcnt(10)\n"
"MF v[224:227],a[160:163],a[4:7],v[224:227]\n"
"WT lgkmcnt(12)\n"
"s_mov_b32 s64, 12288\n"
"TR a[160:161],v24 offset:40960\n"
"MF v[196:199],a[164:167],a[0:3],v[196:199]\n"
"LD a[48:51],v10,s[44:47],s64 offen offset:0 sc1\n"
"MF v[228:231],a[164:167],a[4:7],v[228:231]\n"
"WT lgkmcnt(11)\n"
"TR a[162:163],v24 offset:43008\n"
"TR a[164:165],v25 offset:40960\n"
"MF v[200:203],a[168:171],a[0:3],v[200:203]\n"
"TR a[166:167],v25 offset:43008\n"
"MF v[232:235],a[168:171],a[4:7],v[232:235]\n"
"WT lgkmcnt(12)\n"
"TR a[168:169],v26 offset:40960\n"
"MF v[204:207],a[172:175],a[0:3],v[204:207]\n"
"TR a[170:171],v26 offset:43008\n"
"MF v[236:239],a[172:175],a[4:7],v[236:239]\n"
"WT lgkmcnt(12)\n"
"TR a[172:173],v27 offset:40960\n"
"MF v[208:211],a[176:179],a[0:3],v[208:211]\n"
"LD a[52:55],v11,s[44:47],s64 offen offset:0 sc1\n"
"MF v[240:243],a[176:179],a[4:7],v[240:243]\n"
"WT lgkmcnt(11)\n"
"TR a[174:175],v27 offset:43008\n"
"TR a[176:177],v24 offset:45056\n"
"MF v[212:215],a[180:183],a[0:3],v[212:215]\n"
"TR a[178:179],v24 offset:47104\n"
"MF v[244:247],a[180:183],a[4:7],v[244:247]\n"
"WT lgkmcnt(12)\n"
"TR a[180:181],v25 offset:45056\n"
"MF v[216:219],a[184:187],a[0:3],v[216:219]\n"
"LD a[56:59],v10,s[44:47],s64 offen offset:1024 sc1\n"
"MF v[248:251],a[184:187],a[4:7],v[248:251]\n"
"WT lgkmcnt(11)\n"
"TR a[182:183],v25 offset:47104\n"
"TR a[184:185],v26 offset:45056\n"
"MF v[220:223],a[188:191],a[0:3],v[220:223]\n"
"TR a[186:187],v26 offset:47104\n"
"MF v[252:255],a[188:191],a[4:7],v[252:255]\n"
"WT vmcnt(12) lgkmcnt(12)\n"
"TR a[188:189],v27 offset:45056\n"
"MF v[192:195],a[160:163],a[8:11],v[192:195]\n"
"WT vmcnt(11)\n"
"TR a[190:191],v27 offset:47104\n"
"MF v[224:227],a[160:163],a[12:15],v[224:227]\n"
"WT lgkmcnt(12)\n"
"TR a[160:161],v24 offset:49152\n"
"MF v[196:199],a[164:167],a[8:11],v[196:199]\n"
"LD a[60:63],v11,s[44:47],s64 offen offset:1024 sc1\n"
"MF v[228:231],a[164:167],a[12:15],v[228:231]\n"
"WT lgkmcnt(11)\n"
"TR a[162:163],v24 offset:51200\n"
"TR a[164:165],v25 offset:49152\n"
"MF v[200:203],a[168:171],a[8:11],v[200:203]\n"
"TR a[166:167],v25 offset:51200\n"
"MF v[232:235],a[168:171],a[12:15],v[232:235]\n"
"WT lgkmcnt(12)\n"
"TR a[168:169],v26 offset:49152\n"
"MF v[204:207],a[172:175],a[8:11],v[204:207]\n"
"TR a[170:171],v26 offset:51200\n"
"MF v[236:239],a[172:175],a[12:15],v[236:239]\n"
"WT lgkmcnt(12)\n"
"s_mov_b32 s64, 16384\n"
"TR a[172:173],v27 offset:49152\n"
"MF v[208:211],a[176:179],a[8:11],v[208:211]\n"
"LD a[64:67],v10,s[44:47],s64 offen offset:0 sc1\n"
"MF v[240:243],a[176:179],a[12:15],v[240:243]\n"
"WT lgkmcnt(11)\n"
"TR a[174:175],v27 offset:51200\n"
"TR a[176:177],v24 offset:53248\n"
"MF v[212:215],a[180:183],a[8:11],v[212:215]\n"
"TR a[178:179],v24 offset:55296\n"
"MF v[244:247],a[180:183],a[12:15],v[244:247]\n"
"WT lgkmcnt(12)\n"
"TR a[180:181],v25 offset:53248\n"
"MF v[216:219],a[184:187],a[8:11],v[216:219]\n"
"LD a[68:71],v11,s[44:47],s64 offen offset:0 sc1\n"
"MF v[248:251],a[184:187],a[12:15],v[248:251]\n"
"WT lgkmcnt(11)\n"
"TR a[182:183],v25 offset:55296\n"
"TR a[184:185],v26 offset:53248\n"
"MF v[220:223],a[188:191],a[8:11],v[220:223]\n"
"TR a[186:187],v26 offset:55296\n"
"MF v[252:255],a[188:191],a[12:15],v[252:255]\n"
"WT vmcnt(13) lgkmcnt(12)\n"
"TR a[188:189],v27 offset:53248\n"
"MF v[192:195],a[160:163],a[16:19],v[192:195]\n"
"WT vmcnt(12)\n"
"TR a[190:191],v27 offset:55296\n"
"MF v[224:227],a[160:163],a[20:23],v[224:227]\n"
"WT lgkmcnt(12)\n"
"TR a[160:161],v24 offset:57344\n"
"MF v[196:199],a[164:167],a[16:19],v[196:199]\n"
"LD a[72:75],v10,s[44:47],s64 offen offset:1024 sc1\n"
"MF v[228:231],a[164:167],a[20:23],v[228:231]\n"
"WT lgkmcnt(11)\n"
"TR a[162:163],v24 offset:59392\n"
"TR a[164:165],v25 offset:57344\n"
"MF v[200:203],a[168:171],a[16:19],v[200:203]\n"
"TR a[166:167],v25 offset:59392\n"
"MF v[232:235],a[168:171],a[20:23],v[232:235]\n"
"WT lgkmcnt(12)\n"
"TR a[168:169],v26 offset:57344\n"
"MF v[204:207],a[172:175],a[16:19],v[204:207]\n"
"TR a[170:171],v26 offset:59392\n"
"MF v[236:239],a[172:175],a[20:23],v[236:239]\n"
"WT lgkmcnt(12)\n"
"TR a[172:173],v27 offset:57344\n"
"MF v[208:211],a[176:179],a[16:19],v[208:211]\n"
"LD a[76:79],v11,s[44:47],s64 offen offset:1024 sc1\n"
"MF v[240:243],a[176:179],a[20:23],v[240:243]\n"
"WT lgkmcnt(11)\n"
"TR a[174:175],v27 offset:59392\n"
"TR a[176:177],v24 offset:61440\n"
"MF v[212:215],a[180:183],a[16:19],v[212:215]\n"
"TR a[178:179],v24 offset:63488\n"
"MF v[244:247],a[180:183],a[20:23],v[244:247]\n"
"WT lgkmcnt(12)\n"
"s_mov_b32 s64, 20480\n"
"TR a[180:181],v25 offset:61440\n"
"MF v[216:219],a[184:187],a[16:19],v[216:219]\n"
"LD a[80:83],v10,s[44:47],s64 offen offset:0 sc1\n"
"MF v[248:251],a[184:187],a[20:23],v[248:251]\n"
"WT lgkmcnt(11)\n"
"TR a[182:183],v25 offset:63488\n"
"TR a[184:185],v26 offset:61440\n"
"MF v[220:223],a[188:191],a[16:19],v[220:223]\n"
"TR a[186:187],v26 offset:63488\n"
"MF v[252:255],a[188:191],a[20:23],v[252:255]\n"
"WT vmcnt(14) lgkmcnt(12)\n"
"TR a[188:189],v27 offset:61440\n"
"MF v[192:195],a[160:163],a[24:27],v[192:195]\n"
"WT vmcnt(13)\n"
"s_barrier\n"
"TR a[190:191],v27 offset:63488\n"
"MF v[224:227],a[160:163],a[28:31],v[224:227]\n"
"WT lgkmcnt(12)\n"
"TR a[160:161],v28 offset:0\n"
"MF v[196:199],a[164:167],a[24:27],v[196:199]\n"
"LD a[84:87],v11,s[44:47],s64 offen offset:0 sc1\n"
"MF v[228:231],a[164:167],a[28:31],v[228:231]\n"
"WT lgkmcnt(11)\n"
"TR a[162:163],v28 offset:2048\n"
"TR a[164:165],v29 offset:0\n"
"MF v[200:203],a[168:171],a[24:27],v[200:203]\n"
"TR a[166:167],v29 offset:2048\n"
"MF v[232:235],a[168:171],a[28:31],v[232:235]\n"
"WT lgkmcnt(12)\n"
"s_add_u32 s65, s65, s23\n"
"s_add_u32 m0, s22, 98304\n"
"TR a[168:169],v30 offset:0\n"
"MF v[204:207],a[172:175],a[24:27],v[204:207]\n"
"LD v2,s[52:55],s65 offen offset:0 lds\n"
"MF v[236:239],a[172:175],a[28:31],v[236:239]\n"
"WT lgkmcnt(11)\n"
"TR a[170:171],v30 offset:2048\n"
"TR a[172:173],v31 offset:0\n"
"MF v[208:211],a[176:179],a[24:27],v[208:211]\n"
"TR a[174:175],v31 offset:2048\n"
"MF v[240:243],a[176:179],a[28:31],v[240:243]\n"
"WT lgkmcnt(12)\n"
"TR a[176:177],v28 offset:4096\n"
"MF v[212:215],a[180:183],a[24:27],v[212:215]\n"
"LD a[88:91],v10,s[44:47],s64 offen offset:1024 sc1\n"
"MF v[244:247],a[180:183],a[28:31],v[244:247]\n"
"WT lgkmcnt(11)\n"
"TR a[178:179],v28 offset:6144\n"
"TR a[180:181],v29 offset:4096\n"
"MF v[216:219],a[184:187],a[24:27],v[216:219]\n"
"TR a[182:183],v29 offset:6144\n"
"MF v[248:251],a[184:187],a[28:31],v[248:251]\n"
"WT lgkmcnt(12)\n"
"TR a[184:185],v30 offset:4096\n"
"MF v[220:223],a[188:191],a[24:27],v[220:223]\n"
"LD v3,s[52:55],s65 offen offset:1024 lds\n"
"MF v[252:255],a[188:191],a[28:31],v[252:255]\n"
"WT vmcnt(16) lgkmcnt(11)\n"
"TR a[186:187],v30 offset:6144\n"
"TR a[188:189],v31 offset:4096\n"
"MF v[192:195],a[160:163],a[32:35],v[192:195]\n"
"WT vmcnt(15)\n"
"TR a[190:191],v31 offset:6144\n"
"MF v[224:227],a[160:163],a[36:39],v[224:227]\n"
"WT lgkmcnt(12)\n"
"TR a[160:161],v28 offset:8192\n"
"MF v[196:199],a[164:167],a[32:35],v[196:199]\n"
"LD a[92:95],v11,s[44:47],s64 offen offset:1024 sc1\n"
"MF v[228:231],a[164:167],a[36:39],v[228:231]\n"
"WT lgkmcnt(11)\n"
"TR a[162:163],v28 offset:10240\n"
"TR a[164:165],v29 offset:8192\n"
"MF v[200:203],a[168:171],a[32:35],v[200:203]\n"
"TR a[166:167],v29 offset:10240\n"
"MF v[232:235],a[168:171],a[36:39],v[232:235]\n"
"WT lgkmcnt(12)\n"
"TR a[168:169],v30 offset:8192\n"
"MF v[204:207],a[172:175],a[32:35],v[204:207]\n"
"LD v4,s[52:55],s65 offen offset:2048 lds\n"
"MF v[236:239],a[172:175],a[36:39],v[236:239]\n"
"WT lgkmcnt(11)\n"
"TR a[170:171],v30 offset:10240\n"
"TR a[172:173],v31 offset:8192\n"
"MF v[208:211],a[176:179],a[32:35],v[208:211]\n"
"TR a[174:175],v31 offset:10240\n"
"MF v[240:243],a[176:179],a[36:39],v[240:243]\n"
"WT lgkmcnt(12)\n"
"s_mov_b32 s64, 24576\n"
"TR a[176:177],v28 offset:12288\n"
"MF v[212:215],a[180:183],a[32:35],v[212:215]\n"
"LD a[96:99],v10,s[44:47],s64 offen offset:0 sc1\n"
"MF v[244:247],a[180:183],a[36:39],v[244:247]\n"
"WT lgkmcnt(11)\n"
"TR a[178:179],v28 offset:14336\n"
"TR a[180:181],v29 offset:12288\n"
"MF v[216:219],a[184:187],a[32:35],v[216:219]\n"
"TR a[182:183],v29 offset:14336\n"
"MF v[248:251],a[184:187],a[36:39],v[248:251]\n"
"WT lgkmcnt(12)\n"
"TR a[184:185],v30 offset:12288\n"
"MF v[220:223],a[188:191],a[32:35],v[220:223]\n"
"LD v5,s[52:55],s65 offen offset:3072 lds\n"
"MF v[252:255],a[188:191],a[36:39],v[252:255]\n"
"WT vmcnt(18) lgkmcnt(11)\n"
"TR a[186:187],v30 offset:14336\n"
"TR a[188:189],v31 offset:12288\n"
"MF v[192:195],a[160:163],a[40:43],v[192:195]\n"
"WT vmcnt(17)\n"
"TR a[190:191],v31 offset:14336\n"
"MF v[224:227],a[160:163],a[44:47],v[224:227]\n"
"WT lgkmcnt(12)\n"
"TR a[160:161],v28 offset:16384\n"
"MF v[196:199],a[164:167],a[40:43],v[196:199]\n"
"LD a[100:103],v11,s[44:47],s64 offen offset:0 sc1\n"
"MF v[228:231],a[164:167],a[44:47],v[228:231]\n"
"WT lgkmcnt(11)\n"
"TR a[162:163],v28 offset:18432\n"
"TR a[164:165],v29 offset:16384\n"
"MF v[200:203],a[168:171],a[40:43],v[200:203]\n"
"TR a[166:167],v29 offset:18432\n"
"MF v[232:235],a[168:171],a[44:47],v[232:235]\n"
"WT lgkmcnt(12)\n"
"TR a[168:169],v30 offset:16384\n"
"MF v[204:207],a[172:175],a[40:43],v[204:207]\n"
"TR a[170:171],v30 offset:18432\n"
"MF v[236:239],a[172:175],a[44:47],v[236:239]\n"
"WT lgkmcnt(12)\n"
"TR a[172:173],v31 offset:16384\n"
"MF v[208:211],a[176:179],a[40:43],v[208:211]\n"
"TR a[174:175],v31 offset:18432\n"
"MF v[240:243],a[176:179],a[44:47],v[240:243]\n"
"WT lgkmcnt(12)\n"
"TR a[176:177],v28 offset:20480\n"
"MF v[212:215],a[180:183],a[40:43],v[212:215]\n"
"TR a[178:179],v28 offset:22528\n"
"MF v[244:247],a[180:183],a[44:47],v[244:247]\n"
"WT lgkmcnt(12)\n"
"TR a[180:181],v29 offset:20480\n"
"MF v[216:219],a[184:187],a[40:43],v[216:219]\n"
"LD a[104:107],v10,s[44:47],s64 offen offset:1024 sc1\n"
"MF v[248:251],a[184:187],a[44:47],v[248:251]\n"
"WT lgkmcnt(11)\n"
"TR a[182:183],v29 offset:22528\n"
"TR a[184:185],v30 offset:20480\n"
"MF v[220:223],a[188:191],a[40:43],v[220:223]\n"
"TR a[186:187],v30 offset:22528\n"
"MF v[252:255],a[188:191],a[44:47],v[252:255]\n"
"WT vmcnt(18) lgkmcnt(12)\n"
"TR a[188:189],v31 offset:20480\n"
"MF v[192:195],a[160:163],a[48:51],v[192:195]\n"
"WT vmcnt(17)\n"
"TR a[190:191],v31 offset:22528\n"
"MF v[224:227],a[160:163],a[52:55],v[224:227]\n"
"WT lgkmcnt(12)\n"
"TR a[160:161],v28 offset:24576\n"
"MF v[196:199],a[164:167],a[48:51],v[196:199]\n"
"LD a[108:111],v11,s[44:47],s64 offen offset:1024 sc1\n"
"MF v[228:231],a[164:167],a[52:55],v[228:231]\n"
"WT lgkmcnt(11)\n"
"TR a[162:163],v28 offset:26624\n"
"TR a[164:165],v29 offset:24576\n"
"MF v[200:203],a[168:171],a[48:51],v[200:203]\n"
"TR a[166:167],v29 offset:26624\n"
"MF v[232:235],a[168:171],a[52:55],v[232:235]\n"
"WT lgkmcnt(12)\n"
"TR a[168:169],v30 offset:24576\n"
"MF v[204:207],a[172:175],a[48:51],v[204:207]\n"
"TR a[170:171],v30 offset:26624\n"
"MF v[236:239],a[172:175],a[52:55],v[236:239]\n"
"WT lgkmcnt(12)\n"
"TR a[172:173],v31 offset:24576\n"
"MF v[208:211],a[176:179],a[48:51],v[208:211]\n"
"TR a[174:175],v31 offset:26624\n"
"MF v[240:243],a[176:179],a[52:55],v[240:243]\n"
"WT lgkmcnt(12)\n"
"s_mov_b32 s64, 28672\n"
"TR a[176:177],v28 offset:28672\n"
"MF v[212:215],a[180:183],a[48:51],v[212:215]\n"
"LD a[0:3],v10,s[44:47],s64 offen offset:0 sc1\n"
"MF v[244:247],a[180:183],a[52:55],v[244:247]\n"
"WT lgkmcnt(11)\n"
"TR a[178:179],v28 offset:30720\n"
"TR a[180:181],v29 offset:28672\n"
"MF v[216:219],a[184:187],a[48:51],v[216:219]\n"
"TR a[182:183],v29 offset:30720\n"
"MF v[248:251],a[184:187],a[52:55],v[248:251]\n"
"WT lgkmcnt(12)\n"
"TR a[184:185],v30 offset:28672\n"
"MF v[220:223],a[188:191],a[48:51],v[220:223]\n"
"TR a[186:187],v30 offset:30720\n"
"MF v[252:255],a[188:191],a[52:55],v[252:255]\n"
"WT vmcnt(18) lgkmcnt(12)\n"
"TR a[188:189],v31 offset:28672\n"
"MF v[192:195],a[160:163],a[56:59],v[192:195]\n"
"WT vmcnt(17)\n"
"s_barrier\n"
"TR a[190:191],v31 offset:30720\n"
"MF v[224:227],a[160:163],a[60:63],v[224:227]\n"
"WT lgkmcnt(12)\n"
"TR a[160:161],v12 offset:0\n"
"MF v[196:199],a[164:167],a[56:59],v[196:199]\n"
"LD a[4:7],v11,s[44:47],s64 offen offset:0 sc1\n"
"MF v[228:231],a[164:167],a[60:63],v[228:231]\n"
"WT lgkmcnt(11)\n"
"TR a[162:163],v12 offset:2048\n"
"TR a[164:165],v13 offset:0\n"
"MF v[200:203],a[168:171],a[56:59],v[200:203]\n"
"TR a[166:167],v13 offset:2048\n"
"MF v[232:235],a[168:171],a[60:63],v[232:235]\n"
"WT lgkmcnt(12)\n"
"s_add_u32 s65, s65, s23\n"
"s_add_u32 m0, s22, 114688\n"
"TR a[168:169],v14 offset:0\n"
"MF v[204:207],a[172:175],a[56:59],v[204:207]\n"
"LD v2,s[52:55],s65 offen offset:0 lds\n"
"MF v[236:239],a[172:175],a[60:63],v[236:239]\n"
"WT lgkmcnt(11)\n"
"TR a[170:171],v14 offset:2048\n"
"TR a[172:173],v15 offset:0\n"
"MF v[208:211],a[176:179],a[56:59],v[208:211]\n"
"TR a[174:175],v15 offset:2048\n"
"MF v[240:243],a[176:179],a[60:63],v[240:243]\n"
"WT lgkmcnt(12)\n"
"TR a[176:177],v12 offset:4096\n"
"MF v[212:215],a[180:183],a[56:59],v[212:215]\n"
"LD a[8:11],v10,s[44:47],s64 offen offset:1024 sc1\n"
"MF v[244:247],a[180:183],a[60:63],v[244:247]\n"
"WT lgkmcnt(11)\n"
"TR a[178:179],v12 offset:6144\n"
"TR a[180:181],v13 offset:4096\n"
"MF v[216:219],a[184:187],a[56:59],v[216:219]\n"
"TR a[182:183],v13 offset:6144\n"
"MF v[248:251],a[184:187],a[60:63],v[248:251]\n"
"WT lgkmcnt(12)\n"
"TR a[184:185],v14 offset:4096\n"
"MF v[220:223],a[188:191],a[56:59],v[220:223]\n"
"LD v3,s[52:55],s65 offen offset:1024 lds\n"
"MF v[252:255],a[188:191],a[60:63],v[252:255]\n"
"WT vmcnt(20) lgkmcnt(11)\n"
"TR a[186:187],v14 offset:6144\n"
"TR a[188:189],v15 offset:4096\n"
"MF v[192:195],a[160:163],a[64:67],v[192:195]\n"
"WT vmcnt(19)\n"
"buffer_load_dwordx2 v[18:19], v20, s[40:43], 0 offen sc1\n"
"TR a[190:191],v15 offset:6144\n"
"MF v[224:227],a[160:163],a[68:71],v[224:227]\n"
"WT lgkmcnt(12)\n"
"TR a[160:161],v12 offset:8192\n"
"MF v[196:199],a[164:167],a[64:67],v[196:199]\n"
"LD a[12:15],v11,s[44:47],s64 offen offset:1024 sc1\n"
"MF v[228:231],a[164:167],a[68:71],v[228:231]\n"
"WT lgkmcnt(11)\n"
"TR a[162:163],v12 offset:10240\n"
"TR a[164:165],v13 offset:8192\n"
"MF v[200:203],a[168:171],a[64:67],v[200:203]\n"
"TR a[166:167],v13 offset:10240\n"
"MF v[232:235],a[168:171],a[68:71],v[232:235]\n"
"WT lgkmcnt(12)\n"
"TR a[168:169],v14 offset:8192\n"
"MF v[204:207],a[172:175],a[64:67],v[204:207]\n"
"LD v4,s[52:55],s65 offen offset:2048 lds\n"
"MF v[236:239],a[172:175],a[68:71],v[236:239]\n"
"WT lgkmcnt(11)\n"
"TR a[170:171],v14 offset:10240\n"
"TR a[172:173],v15 offset:8192\n"
"MF v[208:211],a[176:179],a[64:67],v[208:211]\n"
"TR a[174:175],v15 offset:10240\n"
"MF v[240:243],a[176:179],a[68:71],v[240:243]\n"
"WT lgkmcnt(12)\n"
"s_mov_b32 s64, 32768\n"
"TR a[176:177],v12 offset:12288\n"
"MF v[212:215],a[180:183],a[64:67],v[212:215]\n"
"LD a[16:19],v10,s[44:47],s64 offen offset:0 sc1\n"
"MF v[244:247],a[180:183],a[68:71],v[244:247]\n"
"WT lgkmcnt(11)\n"
"TR a[178:179],v12 offset:14336\n"
"TR a[180:181],v13 offset:12288\n"
"MF v[216:219],a[184:187],a[64:67],v[216:219]\n"
"TR a[182:183],v13 offset:14336\n"
"MF v[248:251],a[184:187],a[68:71],v[248:251]\n"
"WT lgkmcnt(12)\n"
"TR a[184:185],v14 offset:12288\n"
"MF v[220:223],a[188:191],a[64:67],v[220:223]\n"
"LD v5,s[52:55],s65 offen offset:3072 lds\n"
"MF v[252:255],a[188:191],a[68:71],v[252:255]\n"
"WT vmcnt(23) lgkmcnt(11)\n"
"TR a[186:187],v14 offset:14336\n"
"TR a[188:189],v15 offset:12288\n"
"MF v[192:195],a[160:163],a[72:75],v[192:195]\n"
"WT vmcnt(22)\n"
"TR a[190:191],v15 offset:14336\n"
"MF v[224:227],a[160:163],a[76:79],v[224:227]\n"
"WT lgkmcnt(12)\n"
"TR a[160:161],v12 offset:16384\n"
"MF v[196:199],a[164:167],a[72:75],v[196:199]\n"
"LD a[20:23],v11,s[44:47],s64 offen offset:0 sc1\n"
"MF v[228:231],a[164:167],a[76:79],v[228:231]\n"
"WT lgkmcnt(11)\n"
"TR a[162:163],v12 offset:18432\n"
"TR a[164:165],v13 offset:16384\n"
"MF v[200:203],a[168:171],a[72:75],v[200:203]\n"
"TR a[166:167],v13 offset:18432\n"
"MF v[232:235],a[168:171],a[76:79],v[232:235]\n"
"WT lgkmcnt(12)\n"
"s_add_u32 s65, s65, s23\n"
"s_add_u32 m0, s22, 131072\n"
"TR a[168:169],v14 offset:16384\n"
"MF v[204:207],a[172:175],a[72:75],v[204:207]\n"
"LD v2,s[52:55],s65 offen offset:0 lds\n"
"MF v[236:239],a[172:175],a[76:79],v[236:239]\n"
"WT lgkmcnt(11)\n"
"TR a[170:171],v14 offset:18432\n"
"TR a[172:173],v15 offset:16384\n"
"MF v[208:211],a[176:179],a[72:75],v[208:211]\n"
"TR a[174:175],v15 offset:18432\n"
"MF v[240:243],a[176:179],a[76:79],v[240:243]\n"
"WT lgkmcnt(12)\n"
"TR a[176:177],v12 offset:20480\n"
"MF v[212:215],a[180:183],a[72:75],v[212:215]\n"
"LD a[24:27],v10,s[44:47],s64 offen offset:1024 sc1\n"
"MF v[244:247],a[180:183],a[76:79],v[244:247]\n"
"WT lgkmcnt(11)\n"
"TR a[178:179],v12 offset:22528\n"
"TR a[180:181],v13 offset:20480\n"
"MF v[216:219],a[184:187],a[72:75],v[216:219]\n"
"TR a[182:183],v13 offset:22528\n"
"MF v[248:251],a[184:187],a[76:79],v[248:251]\n"
"WT lgkmcnt(12)\n"
"TR a[184:185],v14 offset:20480\n"
"MF v[220:223],a[188:191],a[72:75],v[220:223]\n"
"LD v3,s[52:55],s65 offen offset:1024 lds\n"
"MF v[252:255],a[188:191],a[76:79],v[252:255]\n"
"WT vmcnt(25) lgkmcnt(11)\n"
"TR a[186:187],v14 offset:22528\n"
"TR a[188:189],v15 offset:20480\n"
"MF v[192:195],a[160:163],a[80:83],v[192:195]\n"
"WT vmcnt(24)\n"
"TR a[190:191],v15 offset:22528\n"
"MF v[224:227],a[160:163],a[84:87],v[224:227]\n"
"WT lgkmcnt(12)\n"
"TR a[160:161],v12 offset:24576\n"
"MF v[196:199],a[164:167],a[80:83],v[196:199]\n"
"LD a[28:31],v11,s[44:47],s64 offen offset:1024 sc1\n"
"MF v[228:231],a[164:167],a[84:87],v[228:231]\n"
"WT lgkmcnt(11)\n"
"TR a[162:163],v12 offset:26624\n"
"TR a[164:165],v13 offset:24576\n"
"MF v[200:203],a[168:171],a[80:83],v[200:203]\n"
"TR a[166:167],v13 offset:26624\n"
"MF v[232:235],a[168:171],a[84:87],v[232:235]\n"
"WT lgkmcnt(12)\n"
"TR a[168:169],v14 offset:24576\n"
"MF v[204:207],a[172:175],a[80:83],v[204:207]\n"
"TR a[170:171],v14 offset:26624\n"
"MF v[236:239],a[172:175],a[84:87],v[236:239]\n"
"WT lgkmcnt(12)\n"
"TR a[172:173],v15 offset:24576\n"
"MF v[208:211],a[176:179],a[80:83],v[208:211]\n"
"LD v4,s[52:55],s65 offen offset:2048 lds\n"
"MF v[240:243],a[176:179],a[84:87],v[240:243]\n"
"WT lgkmcnt(11)\n"
"TR a[174:175],v15 offset:26624\n"
"TR a[176:177],v12 offset:28672\n"
"MF v[212:215],a[180:183],a[80:83],v[212:215]\n"
"TR a[178:179],v12 offset:30720\n"
"MF v[244:247],a[180:183],a[84:87],v[244:247]\n"
"WT lgkmcnt(12)\n"
"s_mov_b32 s64, 36864\n"
"TR a[180:181],v13 offset:28672\n"
"MF v[216:219],a[184:187],a[80:83],v[216:219]\n"
"LD a[32:35],v10,s[44:47],s64 offen offset:0 sc1\n"
"MF v[248:251],a[184:187],a[84:87],v[248:251]\n"
"WT lgkmcnt(11)\n"
"TR a[182:183],v13 offset:30720\n"
"TR a[184:185],v14 offset:28672\n"
"MF v[220:223],a[188:191],a[80:83],v[220:223]\n"
"TR a[186:187],v14 offset:30720\n"
"MF v[252:255],a[188:191],a[84:87],v[252:255]\n"
"WT vmcnt(25) lgkmcnt(12)\n"
"TR a[188:189],v15 offset:28672\n"
"MF v[192:195],a[160:163],a[88:91],v[192:195]\n"
"WT vmcnt(23)\n"
"s_barrier\n"
"TR a[190:191],v15 offset:30720\n"
"MF v[224:227],a[160:163],a[92:95],v[224:227]\n"
"WT lgkmcnt(12)\n"
"TR a[160:161],v12 offset:32768\n"
"MF v[196:199],a[164:167],a[88:91],v[196:199]\n"
"LD a[36:39],v11,s[44:47],s64 offen offset:0 sc1\n"
"MF v[228:231],a[164:167],a[92:95],v[228:231]\n"
"WT lgkmcnt(11)\n"
"TR a[162:163],v12 offset:34816\n"
"TR a[164:165],v13 offset:32768\n"
"MF v[200:203],a[168:171],a[88:91],v[200:203]\n"
"TR a[166:167],v13 offset:34816\n"
"MF v[232:235],a[168:171],a[92:95],v[232:235]\n"
"WT lgkmcnt(12)\n"
"TR a[168:169],v14 offset:32768\n"
"MF v[204:207],a[172:175],a[88:91],v[204:207]\n"
"LD v5,s[52:55],s65 offen offset:3072 lds\n"
"MF v[236:239],a[172:175],a[92:95],v[236:239]\n"
"WT lgkmcnt(11)\n"
"TR a[170:171],v14 offset:34816\n"
"TR a[172:173],v15 offset:32768\n"
"MF v[208:211],a[176:179],a[88:91],v[208:211]\n"
"TR a[174:175],v15 offset:34816\n"
"MF v[240:243],a[176:179],a[92:95],v[240:243]\n"
"WT lgkmcnt(12)\n"
"TR a[176:177],v12 offset:36864\n"
"MF v[212:215],a[180:183],a[88:91],v[212:215]\n"
"LD a[40:43],v10,s[44:47],s64 offen offset:1024 sc1\n"
"MF v[244:247],a[180:183],a[92:95],v[244:247]\n"
"WT lgkmcnt(11)\n"
"TR a[178:179],v12 offset:38912\n"
"TR a[180:181],v13 offset:36864\n"
"MF v[216:219],a[184:187],a[88:91],v[216:219]\n"
"TR a[182:183],v13 offset:38912\n"
"MF v[248:251],a[184:187],a[92:95],v[248:251]\n"
"WT lgkmcnt(12)\n"
"s_add_u32 s65, s65, s23\n"
"s_add_u32 m0, s22, 147456\n"
"TR a[184:185],v14 offset:36864\n"
"MF v[220:223],a[188:191],a[88:91],v[220:223]\n"
"LD v2,s[52:55],s65 offen offset:0 lds\n"
"MF v[252:255],a[188:191],a[92:95],v[252:255]\n"
"WT vmcnt(25) lgkmcnt(11)\n"
"TR a[186:187],v14 offset:38912\n"
"TR a[188:189],v15 offset:36864\n"
"MF v[192:195],a[160:163],a[96:99],v[192:195]\n"
"WT vmcnt(23)\n"
"TR a[190:191],v15 offset:38912\n"
"MF v[224:227],a[160:163],a[100:103],v[224:227]\n"
"WT lgkmcnt(12)\n"
"TR a[160:161],v12 offset:40960\n"
"MF v[196:199],a[164:167],a[96:99],v[196:199]\n"
"LD a[44:47],v11,s[44:47],s64 offen offset:1024 sc1\n"
"MF v[228:231],a[164:167],a[100:103],v[228:231]\n"
"WT lgkmcnt(11)\n"
"TR a[162:163],v12 offset:43008\n"
"TR a[164:165],v13 offset:40960\n"
"MF v[200:203],a[168:171],a[96:99],v[200:203]\n"
"TR a[166:167],v13 offset:43008\n"
"MF v[232:235],a[168:171],a[100:103],v[232:235]\n"
"WT lgkmcnt(12)\n"
"TR a[168:169],v14 offset:40960\n"
"MF v[204:207],a[172:175],a[96:99],v[204:207]\n"
"LD v3,s[52:55],s65 offen offset:1024 lds\n"
"MF v[236:239],a[172:175],a[100:103],v[236:239]\n"
"WT lgkmcnt(11)\n"
"TR a[170:171],v14 offset:43008\n"
"TR a[172:173],v15 offset:40960\n"
"MF v[208:211],a[176:179],a[96:99],v[208:211]\n"
"TR a[174:175],v15 offset:43008\n"
"MF v[240:243],a[176:179],a[100:103],v[240:243]\n"
"WT lgkmcnt(12)\n"
"s_mov_b32 s64, 40960\n"
"TR a[176:177],v12 offset:45056\n"
"MF v[212:215],a[180:183],a[96:99],v[212:215]\n"
"LD a[48:51],v10,s[44:47],s64 offen offset:0 sc1\n"
"MF v[244:247],a[180:183],a[100:103],v[244:247]\n"
"WT lgkmcnt(11)\n"
"TR a[178:179],v12 offset:47104\n"
"TR a[180:181],v13 offset:45056\n"
"MF v[216:219],a[184:187],a[96:99],v[216:219]\n"
"TR a[182:183],v13 offset:47104\n"
"MF v[248:251],a[184:187],a[100:103],v[248:251]\n"
"WT lgkmcnt(12)\n"
"TR a[184:185],v14 offset:45056\n"
"MF v[220:223],a[188:191],a[96:99],v[220:223]\n"
"LD v4,s[52:55],s65 offen offset:2048 lds\n"
"MF v[252:255],a[188:191],a[100:103],v[252:255]\n"
"WT vmcnt(26) lgkmcnt(11)\n"
"TR a[186:187],v14 offset:47104\n"
"TR a[188:189],v15 offset:45056\n"
"MF v[192:195],a[160:163],a[104:107],v[192:195]\n"
"WT vmcnt(25)\n"
"WT vmcnt(19)\n"
"v_cmp_ne_u64 vcc, s[38:39], v[18:19]\n"
"s_nop 1\n"
"s_cmp_eq_u64 vcc, 0\n"
"s_cbranch_scc0 L_psp_51\n"
"L_pok_50:\n"
"v_mov_b32 v20, v7\n"
"TR a[190:191],v15 offset:47104\n"
"MF v[224:227],a[160:163],a[108:111],v[224:227]\n"
"WT lgkmcnt(12)\n"
"TR a[160:161],v12 offset:49152\n"
"MF v[196:199],a[164:167],a[104:107],v[196:199]\n"
"LD a[52:55],v11,s[44:47],s64 offen offset:0 sc1\n"
"MF v[228:231],a[164:167],a[108:111],v[228:231]\n"
"WT lgkmcnt(11)\n"
"TR a[162:163],v12 offset:51200\n"
"TR a[164:165],v13 offset:49152\n"
"MF v[200:203],a[168:171],a[104:107],v[200:203]\n"
"TR a[166:167],v13 offset:51200\n"
"MF v[232:235],a[168:171],a[108:111],v[232:235]\n"
"WT lgkmcnt(12)\n"
"TR a[168:169],v14 offset:49152\n"
"MF v[204:207],a[172:175],a[104:107],v[204:207]\n"
"LD v5,s[52:55],s65 offen offset:3072 lds\n"
"MF v[236:239],a[172:175],a[108:111],v[236:239]\n"
"WT lgkmcnt(11)\n"
"TR a[170:171],v14 offset:51200\n"
"TR a[172:173],v15 offset:49152\n"
"MF v[208:211],a[176:179],a[104:107],v[208:211]\n"
"TR a[174:175],v15 offset:51200\n"
"MF v[240:243],a[176:179],a[108:111],v[240:243]\n"
"WT lgkmcnt(12)\n"
"TR a[176:177],v12 offset:53248\n"
"MF v[212:215],a[180:183],a[104:107],v[212:215]\n"
"LD a[56:59],v10,s[44:47],s64 offen offset:1024 sc1\n"
"MF v[244:247],a[180:183],a[108:111],v[244:247]\n"
"WT lgkmcnt(11)\n"
"TR a[178:179],v12 offset:55296\n"
"TR a[180:181],v13 offset:53248\n"
"MF v[216:219],a[184:187],a[104:107],v[216:219]\n"
"TR a[182:183],v13 offset:55296\n"
"MF v[248:251],a[184:187],a[108:111],v[248:251]\n"
"WT lgkmcnt(12)\n"
"s_sub_u32 s65, s65, 2048000\n"
"s_add_u32 m0, s22, 0\n"
"TR a[184:185],v14 offset:53248\n"
"MF v[220:223],a[188:191],a[104:107],v[220:223]\n"
"LD v2,s[52:55],s65 offen offset:0 lds\n"
"MF v[252:255],a[188:191],a[108:111],v[252:255]\n"
"WT lgkmcnt(11)\n"
"TR a[186:187],v14 offset:55296\n"
"TR a[188:189],v15 offset:53248\n"
"MF v[192:195],a[160:163],a[0:3],v[192:195]\n"
"TR a[190:191],v15 offset:55296\n"
"MF v[224:227],a[160:163],a[4:7],v[224:227]\n"
"WT lgkmcnt(12)\n"
"TR a[160:161],v12 offset:57344\n"
"MF v[196:199],a[164:167],a[0:3],v[196:199]\n"
"LD a[60:63],v11,s[44:47],s64 offen offset:1024 sc1\n"
"MF v[228:231],a[164:167],a[4:7],v[228:231]\n"
"WT lgkmcnt(11)\n"
"TR a[162:163],v12 offset:59392\n"
"TR a[164:165],v13 offset:57344\n"
"MF v[200:203],a[168:171],a[0:3],v[200:203]\n"
"TR a[166:167],v13 offset:59392\n"
"MF v[232:235],a[168:171],a[4:7],v[232:235]\n"
"WT lgkmcnt(12)\n"
"TR a[168:169],v14 offset:57344\n"
"MF v[204:207],a[172:175],a[0:3],v[204:207]\n"
"LD v3,s[52:55],s65 offen offset:1024 lds\n"
"MF v[236:239],a[172:175],a[4:7],v[236:239]\n"
"WT lgkmcnt(11)\n"
"TR a[170:171],v14 offset:59392\n"
"TR a[172:173],v15 offset:57344\n"
"MF v[208:211],a[176:179],a[0:3],v[208:211]\n"
"TR a[174:175],v15 offset:59392\n"
"MF v[240:243],a[176:179],a[4:7],v[240:243]\n"
"WT lgkmcnt(12)\n"
"s_mov_b32 s64, 45056\n"
"TR a[176:177],v12 offset:61440\n"
"MF v[212:215],a[180:183],a[0:3],v[212:215]\n"
"LD a[64:67],v10,s[44:47],s64 offen offset:0 sc1\n"
"MF v[244:247],a[180:183],a[4:7],v[244:247]\n"
"WT lgkmcnt(11)\n"
"TR a[178:179],v12 offset:63488\n"
"TR a[180:181],v13 offset:61440\n"
"MF v[216:219],a[184:187],a[0:3],v[216:219]\n"
"TR a[182:183],v13 offset:63488\n"
"MF v[248:251],a[184:187],a[4:7],v[248:251]\n"
"WT lgkmcnt(12)\n"
"TR a[184:185],v14 offset:61440\n"
"MF v[220:223],a[188:191],a[0:3],v[220:223]\n"
"LD v4,s[52:55],s65 offen offset:2048 lds\n"
"MF v[252:255],a[188:191],a[4:7],v[252:255]\n"
"WT lgkmcnt(11)\n"
"TR a[186:187],v14 offset:63488\n"
"TR a[188:189],v15 offset:61440\n"
"MF v[192:195],a[160:163],a[8:11],v[192:195]\n"
"WT vmcnt(26)\n"
"s_barrier\n"
"TR a[190:191],v15 offset:63488\n"
"MF v[224:227],a[160:163],a[12:15],v[224:227]\n"
"WT lgkmcnt(12)\n"
"TR a[160:161],v24 offset:0\n"
"MF v[196:199],a[164:167],a[8:11],v[196:199]\n"
"LD a[68:71],v11,s[44:47],s64 offen offset:0 sc1\n"
"MF v[228:231],a[164:167],a[12:15],v[228:231]\n"
"WT lgkmcnt(11)\n"
"TR a[162:163],v24 offset:2048\n"
"TR a[164:165],v25 offset:0\n"
"MF v[200:203],a[168:171],a[8:11],v[200:203]\n"
"TR a[166:167],v25 offset:2048\n"
"MF v[232:235],a[168:171],a[12:15],v[232:235]\n"
"WT lgkmcnt(12)\n"
"TR a[168:169],v26 offset:0\n"
"MF v[204:207],a[172:175],a[8:11],v[204:207]\n"
"TR a[170:171],v26 offset:2048\n"
"MF v[236:239],a[172:175],a[12:15],v[236:239]\n"
"WT lgkmcnt(12)\n"
"TR a[172:173],v27 offset:0\n"
"MF v[208:211],a[176:179],a[8:11],v[208:211]\n"
"LD v5,s[52:55],s65 offen offset:3072 lds\n"
"MF v[240:243],a[176:179],a[12:15],v[240:243]\n"
"WT lgkmcnt(11)\n"
"TR a[174:175],v27 offset:2048\n"
"TR a[176:177],v24 offset:4096\n"
"MF v[212:215],a[180:183],a[8:11],v[212:215]\n"
"TR a[178:179],v24 offset:6144\n"
"MF v[244:247],a[180:183],a[12:15],v[244:247]\n"
"WT lgkmcnt(12)\n"
"TR a[180:181],v25 offset:4096\n"
"MF v[216:219],a[184:187],a[8:11],v[216:219]\n"
"LD a[72:75],v10,s[44:47],s64 offen offset:1024 sc1\n"
"MF v[248:251],a[184:187],a[12:15],v[248:251]\n"
"WT lgkmcnt(11)\n"
"TR a[182:183],v25 offset:6144\n"
"TR a[184:185],v26 offset:4096\n"
"MF v[220:223],a[188:191],a[8:11],v[220:223]\n"
"TR a[186:187],v26 offset:6144\n"
"MF v[252:255],a[188:191],a[12:15],v[252:255]\n"
"WT vmcnt(27) lgkmcnt(12)\n"
"TR a[188:189],v27 offset:4096\n"
"MF v[192:195],a[160:163],a[16:19],v[192:195]\n"
"WT vmcnt(25)\n"
"TR a[190:191],v27 offset:6144\n"
"MF v[224:227],a[160:163],a[20:23],v[224:227]\n"
"WT lgkmcnt(12)\n"
"TR a[160:161],v24 offset:8192\n"
"MF v[196:199],a[164:167],a[16:19],v[196:199]\n"
"LD a[76:79],v11,s[44:47],s64 offen offset:1024 sc1\n"
"MF v[228:231],a[164:167],a[20:23],v[228:231]\n"
"WT lgkmcnt(11)\n"
"TR a[162:163],v24 offset:10240\n"
"TR a[164:165],v25 offset:8192\n"
"MF v[200:203],a[168:171],a[16:19],v[200:203]\n"
"TR a[166:167],v25 offset:10240\n"
"MF v[232:235],a[168:171],a[20:23],v[232:235]\n"
"WT lgkmcnt(12)\n"
"s_add_u32 s65, s65, s23\n"
"s_add_u32 m0, s22, 16384\n"
"TR a[168:169],v26 offset:8192\n"
"MF v[204:207],a[172:175],a[16:19],v[204:207]\n"
"LD v2,s[52:55],s65 offen offset:0 lds\n"
"MF v[236:239],a[172:175],a[20:23],v[236:239]\n"
"WT lgkmcnt(11)\n"
"TR a[170:171],v26 offset:10240\n"
"TR a[172:173],v27 offset:8192\n"
"MF v[208:211],a[176:179],a[16:19],v[208:211]\n"
"TR a[174:175],v27 offset:10240\n"
"MF v[240:243],a[176:179],a[20:23],v[240:243]\n"
"WT lgkmcnt(12)\n"
"s_mov_b32 s64, 49152\n"
"TR a[176:177],v24 offset:12288\n"
"MF v[212:215],a[180:183],a[16:19],v[212:215]\n"
"LD a[80:83],v10,s[44:47],s64 offen offset:0 sc1\n"
"MF v[244:247],a[180:183],a[20:23],v[244:247]\n"
"WT lgkmcnt(11)\n"
"TR a[178:179],v24 offset:14336\n"
"TR a[180:181],v25 offset:12288\n"
"MF v[216:219],a[184:187],a[16:19],v[216:219]\n"
"TR a[182:183],v25 offset:14336\n"
"MF v[248:251],a[184:187],a[20:23],v[248:251]\n"
"WT lgkmcnt(12)\n"
"TR a[184:185],v26 offset:12288\n"
"MF v[220:223],a[188:191],a[16:19],v[220:223]\n"
"LD v3,s[52:55],s65 offen offset:1024 lds\n"
"MF v[252:255],a[188:191],a[20:23],v[252:255]\n"
"WT vmcnt(27) lgkmcnt(11)\n"
"TR a[186:187],v26 offset:14336\n"
"TR a[188:189],v27 offset:12288\n"
"MF v[192:195],a[160:163],a[24:27],v[192:195]\n"
"WT vmcnt(25)\n"
"TR a[190:191],v27 offset:14336\n"
"MF v[224:227],a[160:163],a[28:31],v[224:227]\n"
"WT lgkmcnt(12)\n"
"TR a[160:161],v24 offset:16384\n"
"MF v[196:199],a[164:167],a[24:27],v[196:199]\n"
"LD a[84:87],v11,s[44:47],s64 offen offset:0 sc1\n"
"MF v[228:231],a[164:167],a[28:31],v[228:231]\n"
"WT lgkmcnt(11)\n"
"TR a[162:163],v24 offset:18432\n"
"TR a[164:165],v25 offset:16384\n"
"MF v[200:203],a[168:171],a[24:27],v[200:203]\n"
"TR a[166:167],v25 offset:18432\n"
"MF v[232:235],a[168:171],a[28:31],v[232:235]\n"
"WT lgkmcnt(12)\n"
"TR a[168:169],v26 offset:16384\n"
"MF v[204:207],a[172:175],a[24:27],v[204:207]\n"
"LD v4,s[52:55],s65 offen offset:2048 lds\n"
"MF v[236:239],a[172:175],a[28:31],v[236:239]\n"
"WT lgkmcnt(11)\n"
"TR a[170:171],v26 offset:18432\n"
"TR a[172:173],v27 offset:16384\n"
"MF v[208:211],a[176:179],a[24:27],v[208:211]\n"
"TR a[174:175],v27 offset:18432\n"
"MF v[240:243],a[176:179],a[28:31],v[240:243]\n"
"WT lgkmcnt(12)\n"
"TR a[176:177],v24 offset:20480\n"
"MF v[212:215],a[180:183],a[24:27],v[212:215]\n"
"LD a[88:91],v10,s[44:47],s64 offen offset:1024 sc1\n"
"MF v[244:247],a[180:183],a[28:31],v[244:247]\n"
"WT lgkmcnt(11)\n"
"TR a[178:179],v24 offset:22528\n"
"TR a[180:181],v25 offset:20480\n"
"MF v[216:219],a[184:187],a[24:27],v[216:219]\n"
"TR a[182:183],v25 offset:22528\n"
"MF v[248:251],a[184:187],a[28:31],v[248:251]\n"
"WT lgkmcnt(12)\n"
"TR a[184:185],v26 offset:20480\n"
"MF v[220:223],a[188:191],a[24:27],v[220:223]\n"
"LD v5,s[52:55],s65 offen offset:3072 lds\n"
"MF v[252:255],a[188:191],a[28:31],v[252:255]\n"
"WT vmcnt(27) lgkmcnt(11)\n"
"TR a[186:187],v26 offset:22528\n"
"TR a[188:189],v27 offset:20480\n"
"MF v[192:195],a[160:163],a[32:35],v[192:195]\n"
"WT vmcnt(26)\n"
"TR a[190:191],v27 offset:22528\n"
"MF v[224:227],a[160:163],a[36:39],v[224:227]\n"
"WT lgkmcnt(12)\n"
"TR a[160:161],v24 offset:24576\n"
"MF v[196:199],a[164:167],a[32:35],v[196:199]\n"
"LD a[92:95],v11,s[44:47],s64 offen offset:1024 sc1\n"
"MF v[228:231],a[164:167],a[36:39],v[228:231]\n"
"WT lgkmcnt(11)\n"
"TR a[162:163],v24 offset:26624\n"
"TR a[164:165],v25 offset:24576\n"
"MF v[200:203],a[168:171],a[32:35],v[200:203]\n"
"TR a[166:167],v25 offset:26624\n"
"MF v[232:235],a[168:171],a[36:39],v[232:235]\n"
"WT lgkmcnt(12)\n"
"s_add_u32 s65, s65, s23\n"
"s_add_u32 m0, s22, 32768\n"
"TR a[168:169],v26 offset:24576\n"
"MF v[204:207],a[172:175],a[32:35],v[204:207]\n"
"LD v2,s[52:55],s65 offen offset:0 lds\n"
"MF v[236:239],a[172:175],a[36:39],v[236:239]\n"
"WT lgkmcnt(11)\n"
"TR a[170:171],v26 offset:26624\n"
"TR a[172:173],v27 offset:24576\n"
"MF v[208:211],a[176:179],a[32:35],v[208:211]\n"
"TR a[174:175],v27 offset:26624\n"
"MF v[240:243],a[176:179],a[36:39],v[240:243]\n"
"WT lgkmcnt(12)\n"
"s_mov_b32 s64, 53248\n"
"TR a[176:177],v24 offset:28672\n"
"MF v[212:215],a[180:183],a[32:35],v[212:215]\n"
"LD a[96:99],v10,s[44:47],s64 offen offset:0 sc1\n"
"MF v[244:247],a[180:183],a[36:39],v[244:247]\n"
"WT lgkmcnt(11)\n"
"TR a[178:179],v24 offset:30720\n"
"TR a[180:181],v25 offset:28672\n"
"MF v[216:219],a[184:187],a[32:35],v[216:219]\n"
"TR a[182:183],v25 offset:30720\n"
"MF v[248:251],a[184:187],a[36:39],v[248:251]\n"
"WT lgkmcnt(12)\n"
"TR a[184:185],v26 offset:28672\n"
"MF v[220:223],a[188:191],a[32:35],v[220:223]\n"
"LD v3,s[52:55],s65 offen offset:1024 lds\n"
"MF v[252:255],a[188:191],a[36:39],v[252:255]\n"
"WT vmcnt(28) lgkmcnt(11)\n"
"TR a[186:187],v26 offset:30720\n"
"TR a[188:189],v27 offset:28672\n"
"MF v[192:195],a[160:163],a[40:43],v[192:195]\n"
"WT vmcnt(26)\n"
"s_barrier\n"
"TR a[190:191],v27 offset:30720\n"
"MF v[224:227],a[160:163],a[44:47],v[224:227]\n"
"WT lgkmcnt(12)\n"
"TR a[160:161],v24 offset:32768\n"
"MF v[196:199],a[164:167],a[40:43],v[196:199]\n"
"LD a[100:103],v11,s[44:47],s64 offen offset:0 sc1\n"
"MF v[228:231],a[164:167],a[44:47],v[228:231]\n"
"WT lgkmcnt(11)\n"
"TR a[162:163],v24 offset:34816\n"
"TR a[164:165],v25 offset:32768\n"
"MF v[200:203],a[168:171],a[40:43],v[200:203]\n"
"TR a[166:167],v25 offset:34816\n"
"MF v[232:235],a[168:171],a[44:47],v[232:235]\n"
"WT lgkmcnt(12)\n"
"TR a[168:169],v26 offset:32768\n"
"MF v[204:207],a[172:175],a[40:43],v[204:207]\n"
"LD v4,s[52:55],s65 offen offset:2048 lds\n"
"MF v[236:239],a[172:175],a[44:47],v[236:239]\n"
"WT lgkmcnt(11)\n"
"TR a[170:171],v26 offset:34816\n"
"TR a[172:173],v27 offset:32768\n"
"MF v[208:211],a[176:179],a[40:43],v[208:211]\n"
"TR a[174:175],v27 offset:34816\n"
"MF v[240:243],a[176:179],a[44:47],v[240:243]\n"
"WT lgkmcnt(12)\n"
"TR a[176:177],v24 offset:36864\n"
"MF v[212:215],a[180:183],a[40:43],v[212:215]\n"
"LD a[104:107],v10,s[44:47],s64 offen offset:1024 sc1\n"
"MF v[244:247],a[180:183],a[44:47],v[244:247]\n"
"WT lgkmcnt(11)\n"
"TR a[178:179],v24 offset:38912\n"
"TR a[180:181],v25 offset:36864\n"
"MF v[216:219],a[184:187],a[40:43],v[216:219]\n"
"TR a[182:183],v25 offset:38912\n"
"MF v[248:251],a[184:187],a[44:47],v[248:251]\n"
"WT lgkmcnt(12)\n"
"TR a[184:185],v26 offset:36864\n"
"MF v[220:223],a[188:191],a[40:43],v[220:223]\n"
"LD v5,s[52:55],s65 offen offset:3072 lds\n"
"MF v[252:255],a[188:191],a[44:47],v[252:255]\n"
"WT vmcnt(28) lgkmcnt(11)\n"
"TR a[186:187],v26 offset:38912\n"
"TR a[188:189],v27 offset:36864\n"
"MF v[192:195],a[160:163],a[48:51],v[192:195]\n"
"WT vmcnt(26)\n"
"TR a[190:191],v27 offset:38912\n"
"MF v[224:227],a[160:163],a[52:55],v[224:227]\n"
"WT lgkmcnt(12)\n"
"TR a[160:161],v24 offset:40960\n"
"MF v[196:199],a[164:167],a[48:51],v[196:199]\n"
"LD a[108:111],v11,s[44:47],s64 offen offset:1024 sc1\n"
"MF v[228:231],a[164:167],a[52:55],v[228:231]\n"
"WT lgkmcnt(11)\n"
"TR a[162:163],v24 offset:43008\n"
"TR a[164:165],v25 offset:40960\n"
"MF v[200:203],a[168:171],a[48:51],v[200:203]\n"
"TR a[166:167],v25 offset:43008\n"
"MF v[232:235],a[168:171],a[52:55],v[232:235]\n"
"WT lgkmcnt(12)\n"
"TR a[168:169],v26 offset:40960\n"
"MF v[204:207],a[172:175],a[48:51],v[204:207]\n"
"TR a[170:171],v26 offset:43008\n"
"MF v[236:239],a[172:175],a[52:55],v[236:239]\n"
"WT lgkmcnt(12)\n"
"s_add_u32 s65, s65, s23\n"
"s_add_u32 m0, s22, 49152\n"
"TR a[172:173],v27 offset:40960\n"
"MF v[208:211],a[176:179],a[48:51],v[208:211]\n"
"LD v2,s[52:55],s65 offen offset:0 lds\n"
"MF v[240:243],a[176:179],a[52:55],v[240:243]\n"
"WT lgkmcnt(11)\n"
"TR a[174:175],v27 offset:43008\n"
"TR a[176:177],v24 offset:45056\n"
"MF v[212:215],a[180:183],a[48:51],v[212:215]\n"
"TR a[178:179],v24 offset:47104\n"
"MF v[244:247],a[180:183],a[52:55],v[244:247]\n"
"WT lgkmcnt(12)\n"
"s_mov_b32 s64, 2048\n"
"TR a[180:181],v25 offset:45056\n"
"MF v[216:219],a[184:187],a[48:51],v[216:219]\n"
"LD a[0:3],v10,s[44:47],s64 offen offset:0 sc1\n"
"MF v[248:251],a[184:187],a[52:55],v[248:251]\n"
"WT lgkmcnt(11)\n"
"TR a[182:183],v25 offset:47104\n"
"TR a[184:185],v26 offset:45056\n"
"MF v[220:223],a[188:191],a[48:51],v[220:223]\n"
"TR a[186:187],v26 offset:47104\n"
"MF v[252:255],a[188:191],a[52:55],v[252:255]\n"
"WT vmcnt(27) lgkmcnt(12)\n"
"TR a[188:189],v27 offset:45056\n"
"MF v[192:195],a[160:163],a[56:59],v[192:195]\n"
"WT vmcnt(25)\n"
"TR a[190:191],v27 offset:47104\n"
"MF v[224:227],a[160:163],a[60:63],v[224:227]\n"
"WT lgkmcnt(12)\n"
"TR a[160:161],v24 offset:49152\n"
"MF v[196:199],a[164:167],a[56:59],v[196:199]\n"
"LD a[4:7],v11,s[44:47],s64 offen offset:0 sc1\n"
"MF v[228:231],a[164:167],a[60:63],v[228:231]\n"
"WT lgkmcnt(11)\n"
"TR a[162:163],v24 offset:51200\n"
"TR a[164:165],v25 offset:49152\n"
"MF v[200:203],a[168:171],a[56:59],v[200:203]\n"
"TR a[166:167],v25 offset:51200\n"
"MF v[232:235],a[168:171],a[60:63],v[232:235]\n"
"WT lgkmcnt(12)\n"
"TR a[168:169],v26 offset:49152\n"
"MF v[204:207],a[172:175],a[56:59],v[204:207]\n"
"LD v3,s[52:55],s65 offen offset:1024 lds\n"
"MF v[236:239],a[172:175],a[60:63],v[236:239]\n"
"WT lgkmcnt(11)\n"
"TR a[170:171],v26 offset:51200\n"
"TR a[172:173],v27 offset:49152\n"
"MF v[208:211],a[176:179],a[56:59],v[208:211]\n"
"TR a[174:175],v27 offset:51200\n"
"MF v[240:243],a[176:179],a[60:63],v[240:243]\n"
"WT lgkmcnt(12)\n"
"TR a[176:177],v24 offset:53248\n"
"MF v[212:215],a[180:183],a[56:59],v[212:215]\n"
"LD a[8:11],v10,s[44:47],s64 offen offset:1024 sc1\n"
"MF v[244:247],a[180:183],a[60:63],v[244:247]\n"
"WT lgkmcnt(11)\n"
"TR a[178:179],v24 offset:55296\n"
"TR a[180:181],v25 offset:53248\n"
"MF v[216:219],a[184:187],a[56:59],v[216:219]\n"
"TR a[182:183],v25 offset:55296\n"
"MF v[248:251],a[184:187],a[60:63],v[248:251]\n"
"WT lgkmcnt(12)\n"
"TR a[184:185],v26 offset:53248\n"
"MF v[220:223],a[188:191],a[56:59],v[220:223]\n"
"LD v4,s[52:55],s65 offen offset:2048 lds\n"
"MF v[252:255],a[188:191],a[60:63],v[252:255]\n"
"WT vmcnt(27) lgkmcnt(11)\n"
"TR a[186:187],v26 offset:55296\n"
"TR a[188:189],v27 offset:53248\n"
"MF v[192:195],a[160:163],a[64:67],v[192:195]\n"
"WT vmcnt(25)\n"
"TR a[190:191],v27 offset:55296\n"
"MF v[224:227],a[160:163],a[68:71],v[224:227]\n"
"WT lgkmcnt(12)\n"
"TR a[160:161],v24 offset:57344\n"
"MF v[196:199],a[164:167],a[64:67],v[196:199]\n"
"LD a[12:15],v11,s[44:47],s64 offen offset:1024 sc1\n"
"MF v[228:231],a[164:167],a[68:71],v[228:231]\n"
"WT lgkmcnt(11)\n"
"TR a[162:163],v24 offset:59392\n"
"TR a[164:165],v25 offset:57344\n"
"MF v[200:203],a[168:171],a[64:67],v[200:203]\n"
"TR a[166:167],v25 offset:59392\n"
"MF v[232:235],a[168:171],a[68:71],v[232:235]\n"
"WT lgkmcnt(12)\n"
"TR a[168:169],v26 offset:57344\n"
"MF v[204:207],a[172:175],a[64:67],v[204:207]\n"
"LD v5,s[52:55],s65 offen offset:3072 lds\n"
"MF v[236:239],a[172:175],a[68:71],v[236:239]\n"
"WT lgkmcnt(11)\n"
"TR a[170:171],v26 offset:59392\n"
"TR a[172:173],v27 offset:57344\n"
"MF v[208:211],a[176:179],a[64:67],v[208:211]\n"
"TR a[174:175],v27 offset:59392\n"
"MF v[240:243],a[176:179],a[68:71],v[240:243]\n"
"WT lgkmcnt(12)\n"
"s_mov_b32 s64, 6144\n"
"TR a[176:177],v24 offset:61440\n"
"MF v[212:215],a[180:183],a[64:67],v[212:215]\n"
"LD a[16:19],v10,s[44:47],s64 offen offset:0 sc1\n"
"MF v[244:247],a[180:183],a[68:71],v[244:247]\n"
"WT lgkmcnt(11)\n"
"TR a[178:179],v24 offset:63488\n"
"TR a[180:181],v25 offset:61440\n"
"MF v[216:219],a[184:187],a[64:67],v[216:219]\n"
"TR a[182:183],v25 offset:63488\n"
"MF v[248:251],a[184:187],a[68:71],v[248:251]\n"
"WT lgkmcnt(12)\n"
"s_add_u32 s65, s65, s23\n"
"s_add_u32 m0, s22, 65536\n"
"TR a[184:185],v26 offset:61440\n"
"MF v[220:223],a[188:191],a[64:67],v[220:223]\n"
"LD v2,s[52:55],s65 offen offset:0 lds\n"
"MF v[252:255],a[188:191],a[68:71],v[252:255]\n"
"WT vmcnt(27) lgkmcnt(11)\n"
"TR a[186:187],v26 offset:63488\n"
"TR a[188:189],v27 offset:61440\n"
"MF v[192:195],a[160:163],a[72:75],v[192:195]\n"
"WT vmcnt(26)\n"
"s_barrier\n"
"TR a[190:191],v27 offset:63488\n"
"MF v[224:227],a[160:163],a[76:79],v[224:227]\n"
"WT lgkmcnt(12)\n"
"TR a[160:161],v28 offset:0\n"
"MF v[196:199],a[164:167],a[72:75],v[196:199]\n"
"LD a[20:23],v11,s[44:47],s64 offen offset:0 sc1\n"
"MF v[228:231],a[164:167],a[76:79],v[228:231]\n"
"WT lgkmcnt(11)\n"
"TR a[162:163],v28 offset:2048\n"
"TR a[164:165],v29 offset:0\n"
"MF v[200:203],a[168:171],a[72:75],v[200:203]\n"
"TR a[166:167],v29 offset:2048\n"
"MF v[232:235],a[168:171],a[76:79],v[232:235]\n"
"WT lgkmcnt(12)\n"
"TR a[168:169],v30 offset:0\n"
"MF v[204:207],a[172:175],a[72:75],v[204:207]\n"
"LD v3,s[52:55],s65 offen offset:1024 lds\n"
"MF v[236:239],a[172:175],a[76:79],v[236:239]\n"
"WT lgkmcnt(11)\n"
"TR a[170:171],v30 offset:2048\n"
"TR a[172:173],v31 offset:0\n"
"MF v[208:211],a[176:179],a[72:75],v[208:211]\n"
"TR a[174:175],v31 offset:2048\n"
"MF v[240:243],a[176:179],a[76:79],v[240:243]\n"
"WT lgkmcnt(12)\n"
"TR a[176:177],v28 offset:4096\n"
"MF v[212:215],a[180:183],a[72:75],v[212:215]\n"
"LD a[24:27],v10,s[44:47],s64 offen offset:1024 sc1\n"
"MF v[244:247],a[180:183],a[76:79],v[244:247]\n"
"WT lgkmcnt(11)\n"
"TR a[178:179],v28 offset:6144\n"
"TR a[180:181],v29 offset:4096\n"
"MF v[216:219],a[184:187],a[72:75],v[216:219]\n"
"TR a[182:183],v29 offset:6144\n"
"MF v[248:251],a[184:187],a[76:79],v[248:251]\n"
"WT lgkmcnt(12)\n"
"TR a[184:185],v30 offset:4096\n"
"MF v[220:223],a[188:191],a[72:75],v[220:223]\n"
"LD v4,s[52:55],s65 offen offset:2048 lds\n"
"MF v[252:255],a[188:191],a[76:79],v[252:255]\n"
"WT vmcnt(28) lgkmcnt(11)\n"
"TR a[186:187],v30 offset:6144\n"
"TR a[188:189],v31 offset:4096\n"
"MF v[192:195],a[160:163],a[80:83],v[192:195]\n"
"WT vmcnt(26)\n"
"TR a[190:191],v31 offset:6144\n"
"MF v[224:227],a[160:163],a[84:87],v[224:227]\n"
"WT lgkmcnt(12)\n"
"TR a[160:161],v28 offset:8192\n"
"MF v[196:199],a[164:167],a[80:83],v[196:199]\n"
"LD a[28:31],v11,s[44:47],s64 offen offset:1024 sc1\n"
"MF v[228:231],a[164:167],a[84:87],v[228:231]\n"
"WT lgkmcnt(11)\n"
"TR a[162:163],v28 offset:10240\n"
"TR a[164:165],v29 offset:8192\n"
"MF v[200:203],a[168:171],a[80:83],v[200:203]\n"
"TR a[166:167],v29 offset:10240\n"
"MF v[232:235],a[168:171],a[84:87],v[232:235]\n"
"WT lgkmcnt(12)\n"
"TR a[168:169],v30 offset:8192\n"
"MF v[204:207],a[172:175],a[80:83],v[204:207]\n"
"LD v5,s[52:55],s65 offen offset:3072 lds\n"
"MF v[236:239],a[172:175],a[84:87],v[236:239]\n"
"WT lgkmcnt(11)\n"
"TR a[170:171],v30 offset:10240\n"
"TR a[172:173],v31 offset:8192\n"
"MF v[208:211],a[176:179],a[80:83],v[208:211]\n"
"TR a[174:175],v31 offset:10240\n"
"MF v[240:243],a[176:179],a[84:87],v[240:243]\n"
"WT lgkmcnt(12)\n"
"s_mov_b32 s64, 10240\n"
"TR a[176:177],v28 offset:12288\n"
"MF v[212:215],a[180:183],a[80:83],v[212:215]\n"
"LD a[32:35],v10,s[44:47],s64 offen offset:0 sc1\n"
"MF v[244:247],a[180:183],a[84:87],v[244:247]\n"
"WT lgkmcnt(11)\n"
"TR a[178:179],v28 offset:14336\n"
"TR a[180:181],v29 offset:12288\n"
"MF v[216:219],a[184:187],a[80:83],v[216:219]\n"
"TR a[182:183],v29 offset:14336\n"
"MF v[248:251],a[184:187],a[84:87],v[248:251]\n"
"WT lgkmcnt(12)\n"
"s_add_u32 s65, s65, s23\n"
"s_add_u32 m0, s22, 81920\n"
"TR a[184:185],v30 offset:12288\n"
"MF v[220:223],a[188:191],a[80:83],v[220:223]\n"
"LD v2,s[52:55],s65 offen offset:0 lds\n"
"MF v[252:255],a[188:191],a[84:87],v[252:255]\n"
"WT vmcnt(28) lgkmcnt(11)\n"
"TR a[186:187],v30 offset:14336\n"
"TR a[188:189],v31 offset:12288\n"
"MF v[192:195],a[160:163],a[88:91],v[192:195]\n"
"WT vmcnt(26)\n"
"TR a[190:191],v31 offset:14336\n"
"MF v[224:227],a[160:163],a[92:95],v[224:227]\n"
"WT lgkmcnt(12)\n"
"TR a[160:161],v28 offset:16384\n"
"MF v[196:199],a[164:167],a[88:91],v[196:199]\n"
"LD a[36:39],v11,s[44:47],s64 offen offset:0 sc1\n"
"MF v[228:231],a[164:167],a[92:95],v[228:231]\n"
"WT lgkmcnt(11)\n"
"TR a[162:163],v28 offset:18432\n"
"TR a[164:165],v29 offset:16384\n"
"MF v[200:203],a[168:171],a[88:91],v[200:203]\n"
"TR a[166:167],v29 offset:18432\n"
"MF v[232:235],a[168:171],a[92:95],v[232:235]\n"
"WT lgkmcnt(12)\n"
"TR a[168:169],v30 offset:16384\n"
"MF v[204:207],a[172:175],a[88:91],v[204:207]\n"
"TR a[170:171],v30 offset:18432\n"
"MF v[236:239],a[172:175],a[92:95],v[236:239]\n"
"WT lgkmcnt(12)\n"
"TR a[172:173],v31 offset:16384\n"
"MF v[208:211],a[176:179],a[88:91],v[208:211]\n"
"LD v3,s[52:55],s65 offen offset:1024 lds\n"
"MF v[240:243],a[176:179],a[92:95],v[240:243]\n"
"WT lgkmcnt(11)\n"
"TR a[174:175],v31 offset:18432\n"
"TR a[176:177],v28 offset:20480\n"
"MF v[212:215],a[180:183],a[88:91],v[212:215]\n"
"TR a[178:179],v28 offset:22528\n"
"MF v[244:247],a[180:183],a[92:95],v[244:247]\n"
"WT lgkmcnt(12)\n"
"TR a[180:181],v29 offset:20480\n"
"MF v[216:219],a[184:187],a[88:91],v[216:219]\n"
"LD a[40:43],v10,s[44:47],s64 offen offset:1024 sc1\n"
"MF v[248:251],a[184:187],a[92:95],v[248:251]\n"
"WT lgkmcnt(11)\n"
"TR a[182:183],v29 offset:22528\n"
"TR a[184:185],v30 offset:20480\n"
"MF v[220:223],a[188:191],a[88:91],v[220:223]\n"
"TR a[186:187],v30 offset:22528\n"
"MF v[252:255],a[188:191],a[92:95],v[252:255]\n"
"WT vmcnt(27) lgkmcnt(12)\n"
"TR a[188:189],v31 offset:20480\n"
"MF v[192:195],a[160:163],a[96:99],v[192:195]\n"
"WT vmcnt(25)\n"
"TR a[190:191],v31 offset:22528\n"
"MF v[224:227],a[160:163],a[100:103],v[224:227]\n"
"WT lgkmcnt(12)\n"
"TR a[160:161],v28 offset:24576\n"
"MF v[196:199],a[164:167],a[96:99],v[196:199]\n"
"LD a[44:47],v11,s[44:47],s64 offen offset:1024 sc1\n"
"MF v[228:231],a[164:167],a[100:103],v[228:231]\n"
"WT lgkmcnt(11)\n"
"TR a[162:163],v28 offset:26624\n"
"TR a[164:165],v29 offset:24576\n"
"MF v[200:203],a[168:171],a[96:99],v[200:203]\n"
"TR a[166:167],v29 offset:26624\n"
"MF v[232:235],a[168:171],a[100:103],v[232:235]\n"
"WT lgkmcnt(12)\n"
"TR a[168:169],v30 offset:24576\n"
"MF v[204:207],a[172:175],a[96:99],v[204:207]\n"
"LD v4,s[52:55],s65 offen offset:2048 lds\n"
"MF v[236:239],a[172:175],a[100:103],v[236:239]\n"
"WT lgkmcnt(11)\n"
"TR a[170:171],v30 offset:26624\n"
"TR a[172:173],v31 offset:24576\n"
"MF v[208:211],a[176:179],a[96:99],v[208:211]\n"
"TR a[174:175],v31 offset:26624\n"
"MF v[240:243],a[176:179],a[100:103],v[240:243]\n"
"WT lgkmcnt(12)\n"
"s_mov_b32 s64, 14336\n"
"TR a[176:177],v28 offset:28672\n"
"MF v[212:215],a[180:183],a[96:99],v[212:215]\n"
"LD a[48:51],v10,s[44:47],s64 offen offset:0 sc1\n"
"MF v[244:247],a[180:183],a[100:103],v[244:247]\n"
"WT lgkmcnt(11)\n"
"TR a[178:179],v28 offset:30720\n"
"TR a[180:181],v29 offset:28672\n"
"MF v[216:219],a[184:187],a[96:99],v[216:219]\n"
"TR a[182:183],v29 offset:30720\n"
"MF v[248:251],a[184:187],a[100:103],v[248:251]\n"
"WT lgkmcnt(12)\n"
"TR a[184:185],v30 offset:28672\n"
"MF v[220:223],a[188:191],a[96:99],v[220:223]\n"
"LD v5,s[52:55],s65 offen offset:3072 lds\n"
"MF v[252:255],a[188:191],a[100:103],v[252:255]\n"
"WT vmcnt(27) lgkmcnt(11)\n"
"TR a[186:187],v30 offset:30720\n"
"TR a[188:189],v31 offset:28672\n"
"MF v[192:195],a[160:163],a[104:107],v[192:195]\n"
"WT vmcnt(25)\n"
"s_barrier\n"
"TR a[190:191],v31 offset:30720\n"
"MF v[224:227],a[160:163],a[108:111],v[224:227]\n"
"WT lgkmcnt(12)\n"
"TR a[160:161],v12 offset:0\n"
"MF v[196:199],a[164:167],a[104:107],v[196:199]\n"
"LD a[52:55],v11,s[44:47],s64 offen offset:0 sc1\n"
"MF v[228:231],a[164:167],a[108:111],v[228:231]\n"
"WT lgkmcnt(11)\n"
"TR a[162:163],v12 offset:2048\n"
"TR a[164:165],v13 offset:0\n"
"MF v[200:203],a[168:171],a[104:107],v[200:203]\n"
"TR a[166:167],v13 offset:2048\n"
"MF v[232:235],a[168:171],a[108:111],v[232:235]\n"
"WT lgkmcnt(12)\n"
"s_add_u32 s65, s65, s23\n"
"s_add_u32 m0, s22, 98304\n"
"TR a[168:169],v14 offset:0\n"
"MF v[204:207],a[172:175],a[104:107],v[204:207]\n"
"LD v2,s[52:55],s65 offen offset:0 lds\n"
"MF v[236:239],a[172:175],a[108:111],v[236:239]\n"
"WT lgkmcnt(11)\n"
"TR a[170:171],v14 offset:2048\n"
"TR a[172:173],v15 offset:0\n"
"MF v[208:211],a[176:179],a[104:107],v[208:211]\n"
"TR a[174:175],v15 offset:2048\n"
"MF v[240:243],a[176:179],a[108:111],v[240:243]\n"
"WT lgkmcnt(12)\n"
"TR a[176:177],v12 offset:4096\n"
"MF v[212:215],a[180:183],a[104:107],v[212:215]\n"
"LD a[56:59],v10,s[44:47],s64 offen offset:1024 sc1\n"
"MF v[244:247],a[180:183],a[108:111],v[244:247]\n"
"WT lgkmcnt(11)\n"
"TR a[178:179],v12 offset:6144\n"
"TR a[180:181],v13 offset:4096\n"
"MF v[216:219],a[184:187],a[104:107],v[216:219]\n"
"TR a[182:183],v13 offset:6144\n"
"MF v[248:251],a[184:187],a[108:111],v[248:251]\n"
"WT lgkmcnt(12)\n"
"TR a[184:185],v14 offset:4096\n"
"MF v[220:223],a[188:191],a[104:107],v[220:223]\n"
"LD v3,s[52:55],s65 offen offset:1024 lds\n"
"MF v[252:255],a[188:191],a[108:111],v[252:255]\n"
"WT vmcnt(27) lgkmcnt(11)\n"
"TR a[186:187],v14 offset:6144\n"
"TR a[188:189],v15 offset:4096\n"
"MF v[192:195],a[160:163],a[0:3],v[192:195]\n"
"WT vmcnt(26)\n"
"TR a[190:191],v15 offset:6144\n"
"MF v[224:227],a[160:163],a[4:7],v[224:227]\n"
"WT lgkmcnt(12)\n"
"TR a[160:161],v12 offset:8192\n"
"MF v[196:199],a[164:167],a[0:3],v[196:199]\n"
"LD a[60:63],v11,s[44:47],s64 offen offset:1024 sc1\n"
"MF v[228:231],a[164:167],a[4:7],v[228:231]\n"
"WT lgkmcnt(11)\n"
"TR a[162:163],v12 offset:10240\n"
"TR a[164:165],v13 offset:8192\n"
"MF v[200:203],a[168:171],a[0:3],v[200:203]\n"
"TR a[166:167],v13 offset:10240\n"
"MF v[232:235],a[168:171],a[4:7],v[232:235]\n"
"WT lgkmcnt(12)\n"
"TR a[168:169],v14 offset:8192\n"
"MF v[204:207],a[172:175],a[0:3],v[204:207]\n"
"LD v4,s[52:55],s65 offen offset:2048 lds\n"
"MF v[236:239],a[172:175],a[4:7],v[236:239]\n"
"WT lgkmcnt(11)\n"
"TR a[170:171],v14 offset:10240\n"
"TR a[172:173],v15 offset:8192\n"
"MF v[208:211],a[176:179],a[0:3],v[208:211]\n"
"TR a[174:175],v15 offset:10240\n"
"MF v[240:243],a[176:179],a[4:7],v[240:243]\n"
"WT lgkmcnt(12)\n"
"s_mov_b32 s64, 18432\n"
"TR a[176:177],v12 offset:12288\n"
"MF v[212:215],a[180:183],a[0:3],v[212:215]\n"
"LD a[64:67],v10,s[44:47],s64 offen offset:0 sc1\n"
"MF v[244:247],a[180:183],a[4:7],v[244:247]\n"
"WT lgkmcnt(11)\n"
"TR a[178:179],v12 offset:14336\n"
"TR a[180:181],v13 offset:12288\n"
"MF v[216:219],a[184:187],a[0:3],v[216:219]\n"
"TR a[182:183],v13 offset:14336\n"
"MF v[248:251],a[184:187],a[4:7],v[248:251]\n"
"WT lgkmcnt(12)\n"
"TR a[184:185],v14 offset:12288\n"
"MF v[220:223],a[188:191],a[0:3],v[220:223]\n"
"LD v5,s[52:55],s65 offen offset:3072 lds\n"
"MF v[252:255],a[188:191],a[4:7],v[252:255]\n"
"WT vmcnt(28) lgkmcnt(11)\n"
"TR a[186:187],v14 offset:14336\n"
"TR a[188:189],v15 offset:12288\n"
"MF v[192:195],a[160:163],a[8:11],v[192:195]\n"
"WT vmcnt(26)\n"
"TR a[190:191],v15 offset:14336\n"
"MF v[224:227],a[160:163],a[12:15],v[224:227]\n"
"WT lgkmcnt(12)\n"
"TR a[160:161],v12 offset:16384\n"
"MF v[196:199],a[164:167],a[8:11],v[196:199]\n"
"LD a[68:71],v11,s[44:47],s64 offen offset:0 sc1\n"
"MF v[228:231],a[164:167],a[12:15],v[228:231]\n"
"WT lgkmcnt(11)\n"
"TR a[162:163],v12 offset:18432\n"
"TR a[164:165],v13 offset:16384\n"
"MF v[200:203],a[168:171],a[8:11],v[200:203]\n"
"TR a[166:167],v13 offset:18432\n"
"MF v[232:235],a[168:171],a[12:15],v[232:235]\n"
"WT lgkmcnt(12)\n"
"s_add_u32 s65, s65, s23\n"
"s_add_u32 m0, s22, 114688\n"
"TR a[168:169],v14 offset:16384\n"
"MF v[204:207],a[172:175],a[8:11],v[204:207]\n"
"LD v2,s[52:55],s65 offen offset:0 lds\n"
"MF v[236:239],a[172:175],a[12:15],v[236:239]\n"
"WT lgkmcnt(11)\n"
"TR a[170:171],v14 offset:18432\n"
"TR a[172:173],v15 offset:16384\n"
"MF v[208:211],a[176:179],a[8:11],v[208:211]\n"
"TR a[174:175],v15 offset:18432\n"
"MF v[240:243],a[176:179],a[12:15],v[240:243]\n"
"WT lgkmcnt(12)\n"
"TR a[176:177],v12 offset:20480\n"
"MF v[212:215],a[180:183],a[8:11],v[212:215]\n"
"LD a[72:75],v10,s[44:47],s64 offen offset:1024 sc1\n"
"MF v[244:247],a[180:183],a[12:15],v[244:247]\n"
"WT lgkmcnt(11)\n"
"TR a[178:179],v12 offset:22528\n"
"TR a[180:181],v13 offset:20480\n"
"MF v[216:219],a[184:187],a[8:11],v[216:219]\n"
"TR a[182:183],v13 offset:22528\n"
"MF v[248:251],a[184:187],a[12:15],v[248:251]\n"
"WT lgkmcnt(12)\n"
"TR a[184:185],v14 offset:20480\n"
"MF v[220:223],a[188:191],a[8:11],v[220:223]\n"
"LD v3,s[52:55],s65 offen offset:1024 lds\n"
"MF v[252:255],a[188:191],a[12:15],v[252:255]\n"
"WT vmcnt(28) lgkmcnt(11)\n"
"TR a[186:187],v14 offset:22528\n"
"TR a[188:189],v15 offset:20480\n"
"MF v[192:195],a[160:163],a[16:19],v[192:195]\n"
"WT vmcnt(26)\n"
"TR a[190:191],v15 offset:22528\n"
"MF v[224:227],a[160:163],a[20:23],v[224:227]\n"
"WT lgkmcnt(12)\n"
"TR a[160:161],v12 offset:24576\n"
"MF v[196:199],a[164:167],a[16:19],v[196:199]\n"
"LD a[76:79],v11,s[44:47],s64 offen offset:1024 sc1\n"
"MF v[228:231],a[164:167],a[20:23],v[228:231]\n"
"WT lgkmcnt(11)\n"
"TR a[162:163],v12 offset:26624\n"
"TR a[164:165],v13 offset:24576\n"
"MF v[200:203],a[168:171],a[16:19],v[200:203]\n"
"TR a[166:167],v13 offset:26624\n"
"MF v[232:235],a[168:171],a[20:23],v[232:235]\n"
"WT lgkmcnt(12)\n"
"TR a[168:169],v14 offset:24576\n"
"MF v[204:207],a[172:175],a[16:19],v[204:207]\n"
"TR a[170:171],v14 offset:26624\n"
"MF v[236:239],a[172:175],a[20:23],v[236:239]\n"
"WT lgkmcnt(12)\n"
"TR a[172:173],v15 offset:24576\n"
"MF v[208:211],a[176:179],a[16:19],v[208:211]\n"
"LD v4,s[52:55],s65 offen offset:2048 lds\n"
"MF v[240:243],a[176:179],a[20:23],v[240:243]\n"
"WT lgkmcnt(11)\n"
"TR a[174:175],v15 offset:26624\n"
"TR a[176:177],v12 offset:28672\n"
"MF v[212:215],a[180:183],a[16:19],v[212:215]\n"
"TR a[178:179],v12 offset:30720\n"
"MF v[244:247],a[180:183],a[20:23],v[244:247]\n"
"WT lgkmcnt(12)\n"
"s_mov_b32 s64, 22528\n"
"TR a[180:181],v13 offset:28672\n"
"MF v[216:219],a[184:187],a[16:19],v[216:219]\n"
"LD a[80:83],v10,s[44:47],s64 offen offset:0 sc1\n"
"MF v[248:251],a[184:187],a[20:23],v[248:251]\n"
"WT lgkmcnt(11)\n"
"TR a[182:183],v13 offset:30720\n"
"TR a[184:185],v14 offset:28672\n"
"MF v[220:223],a[188:191],a[16:19],v[220:223]\n"
"TR a[186:187],v14 offset:30720\n"
"MF v[252:255],a[188:191],a[20:23],v[252:255]\n"
"WT vmcnt(27) lgkmcnt(12)\n"
"TR a[188:189],v15 offset:28672\n"
"MF v[192:195],a[160:163],a[24:27],v[192:195]\n"
"WT vmcnt(25)\n"
"s_barrier\n"
"TR a[190:191],v15 offset:30720\n"
"MF v[224:227],a[160:163],a[28:31],v[224:227]\n"
"WT lgkmcnt(12)\n"
"TR a[160:161],v12 offset:32768\n"
"MF v[196:199],a[164:167],a[24:27],v[196:199]\n"
"LD a[84:87],v11,s[44:47],s64 offen offset:0 sc1\n"
"MF v[228:231],a[164:167],a[28:31],v[228:231]\n"
"WT lgkmcnt(11)\n"
"TR a[162:163],v12 offset:34816\n"
"TR a[164:165],v13 offset:32768\n"
"MF v[200:203],a[168:171],a[24:27],v[200:203]\n"
"TR a[166:167],v13 offset:34816\n"
"MF v[232:235],a[168:171],a[28:31],v[232:235]\n"
"WT lgkmcnt(12)\n"
"TR a[168:169],v14 offset:32768\n"
"MF v[204:207],a[172:175],a[24:27],v[204:207]\n"
"LD v5,s[52:55],s65 offen offset:3072 lds\n"
"MF v[236:239],a[172:175],a[28:31],v[236:239]\n"
"WT lgkmcnt(11)\n"
"TR a[170:171],v14 offset:34816\n"
"TR a[172:173],v15 offset:32768\n"
"MF v[208:211],a[176:179],a[24:27],v[208:211]\n"
"TR a[174:175],v15 offset:34816\n"
"MF v[240:243],a[176:179],a[28:31],v[240:243]\n"
"WT lgkmcnt(12)\n"
"TR a[176:177],v12 offset:36864\n"
"MF v[212:215],a[180:183],a[24:27],v[212:215]\n"
"LD a[88:91],v10,s[44:47],s64 offen offset:1024 sc1\n"
"MF v[244:247],a[180:183],a[28:31],v[244:247]\n"
"WT lgkmcnt(11)\n"
"TR a[178:179],v12 offset:38912\n"
"TR a[180:181],v13 offset:36864\n"
"MF v[216:219],a[184:187],a[24:27],v[216:219]\n"
"TR a[182:183],v13 offset:38912\n"
"MF v[248:251],a[184:187],a[28:31],v[248:251]\n"
"WT lgkmcnt(12)\n"
"s_add_u32 s65, s65, s23\n"
"s_add_u32 m0, s22, 131072\n"
"TR a[184:185],v14 offset:36864\n"
"MF v[220:223],a[188:191],a[24:27],v[220:223]\n"
"LD v2,s[52:55],s65 offen offset:0 lds\n"
"MF v[252:255],a[188:191],a[28:31],v[252:255]\n"
"WT vmcnt(27) lgkmcnt(11)\n"
"TR a[186:187],v14 offset:38912\n"
"TR a[188:189],v15 offset:36864\n"
"MF v[192:195],a[160:163],a[32:35],v[192:195]\n"
"WT vmcnt(25)\n"
"TR a[190:191],v15 offset:38912\n"
"MF v[224:227],a[160:163],a[36:39],v[224:227]\n"
"WT lgkmcnt(12)\n"
"TR a[160:161],v12 offset:40960\n"
"MF v[196:199],a[164:167],a[32:35],v[196:199]\n"
"LD a[92:95],v11,s[44:47],s64 offen offset:1024 sc1\n"
"MF v[228:231],a[164:167],a[36:39],v[228:231]\n"
"WT lgkmcnt(11)\n"
"TR a[162:163],v12 offset:43008\n"
"TR a[164:165],v13 offset:40960\n"
"MF v[200:203],a[168:171],a[32:35],v[200:203]\n"
"TR a[166:167],v13 offset:43008\n"
"MF v[232:235],a[168:171],a[36:39],v[232:235]\n"
"WT lgkmcnt(12)\n"
"TR a[168:169],v14 offset:40960\n"
"MF v[204:207],a[172:175],a[32:35],v[204:207]\n"
"LD v3,s[52:55],s65 offen offset:1024 lds\n"
"MF v[236:239],a[172:175],a[36:39],v[236:239]\n"
"WT lgkmcnt(11)\n"
"TR a[170:171],v14 offset:43008\n"
"TR a[172:173],v15 offset:40960\n"
"MF v[208:211],a[176:179],a[32:35],v[208:211]\n"
"TR a[174:175],v15 offset:43008\n"
"MF v[240:243],a[176:179],a[36:39],v[240:243]\n"
"WT lgkmcnt(12)\n"
"s_mov_b32 s64, 26624\n"
"TR a[176:177],v12 offset:45056\n"
"MF v[212:215],a[180:183],a[32:35],v[212:215]\n"
"LD a[96:99],v10,s[44:47],s64 offen offset:0 sc1\n"
"MF v[244:247],a[180:183],a[36:39],v[244:247]\n"
"WT lgkmcnt(11)\n"
"TR a[178:179],v12 offset:47104\n"
"TR a[180:181],v13 offset:45056\n"
"MF v[216:219],a[184:187],a[32:35],v[216:219]\n"
"TR a[182:183],v13 offset:47104\n"
"MF v[248:251],a[184:187],a[36:39],v[248:251]\n"
"WT lgkmcnt(12)\n"
"TR a[184:185],v14 offset:45056\n"
"MF v[220:223],a[188:191],a[32:35],v[220:223]\n"
"LD v4,s[52:55],s65 offen offset:2048 lds\n"
"MF v[252:255],a[188:191],a[36:39],v[252:255]\n"
"WT vmcnt(27) lgkmcnt(11)\n"
"TR a[186:187],v14 offset:47104\n"
"TR a[188:189],v15 offset:45056\n"
"MF v[192:195],a[160:163],a[40:43],v[192:195]\n"
"WT vmcnt(26)\n"
"TR a[190:191],v15 offset:47104\n"
"MF v[224:227],a[160:163],a[44:47],v[224:227]\n"
"WT lgkmcnt(12)\n"
"TR a[160:161],v12 offset:49152\n"
"MF v[196:199],a[164:167],a[40:43],v[196:199]\n"
"LD a[100:103],v11,s[44:47],s64 offen offset:0 sc1\n"
"MF v[228:231],a[164:167],a[44:47],v[228:231]\n"
"WT lgkmcnt(11)\n"
"TR a[162:163],v12 offset:51200\n"
"TR a[164:165],v13 offset:49152\n"
"MF v[200:203],a[168:171],a[40:43],v[200:203]\n"
"TR a[166:167],v13 offset:51200\n"
"MF v[232:235],a[168:171],a[44:47],v[232:235]\n"
"WT lgkmcnt(12)\n"
"TR a[168:169],v14 offset:49152\n"
"MF v[204:207],a[172:175],a[40:43],v[204:207]\n"
"LD v5,s[52:55],s65 offen offset:3072 lds\n"
"MF v[236:239],a[172:175],a[44:47],v[236:239]\n"
"WT lgkmcnt(11)\n"
"TR a[170:171],v14 offset:51200\n"
"TR a[172:173],v15 offset:49152\n"
"MF v[208:211],a[176:179],a[40:43],v[208:211]\n"
"TR a[174:175],v15 offset:51200\n"
"MF v[240:243],a[176:179],a[44:47],v[240:243]\n"
"WT lgkmcnt(12)\n"
"TR a[176:177],v12 offset:53248\n"
"MF v[212:215],a[180:183],a[40:43],v[212:215]\n"
"LD a[104:107],v10,s[44:47],s64 offen offset:1024 sc1\n"
"MF v[244:247],a[180:183],a[44:47],v[244:247]\n"
"WT lgkmcnt(11)\n"
"TR a[178:179],v12 offset:55296\n"
"TR a[180:181],v13 offset:53248\n"
"MF v[216:219],a[184:187],a[40:43],v[216:219]\n"
"TR a[182:183],v13 offset:55296\n"
"MF v[248:251],a[184:187],a[44:47],v[248:251]\n"
"WT lgkmcnt(12)\n"
"s_add_u32 s65, s65, s23\n"
"s_add_u32 m0, s22, 147456\n"
"TR a[184:185],v14 offset:53248\n"
"MF v[220:223],a[188:191],a[40:43],v[220:223]\n"
"LD v2,s[52:55],s65 offen offset:0 lds\n"
"MF v[252:255],a[188:191],a[44:47],v[252:255]\n"
"WT vmcnt(28) lgkmcnt(11)\n"
"TR a[186:187],v14 offset:55296\n"
"TR a[188:189],v15 offset:53248\n"
"MF v[192:195],a[160:163],a[48:51],v[192:195]\n"
"WT vmcnt(26)\n"
"TR a[190:191],v15 offset:55296\n"
"MF v[224:227],a[160:163],a[52:55],v[224:227]\n"
"WT lgkmcnt(12)\n"
"TR a[160:161],v12 offset:57344\n"
"MF v[196:199],a[164:167],a[48:51],v[196:199]\n"
"LD a[108:111],v11,s[44:47],s64 offen offset:1024 sc1\n"
"MF v[228:231],a[164:167],a[52:55],v[228:231]\n"
"WT lgkmcnt(11)\n"
"TR a[162:163],v12 offset:59392\n"
"TR a[164:165],v13 offset:57344\n"
"MF v[200:203],a[168:171],a[48:51],v[200:203]\n"
"TR a[166:167],v13 offset:59392\n"
"MF v[232:235],a[168:171],a[52:55],v[232:235]\n"
"WT lgkmcnt(12)\n"
"TR a[168:169],v14 offset:57344\n"
"MF v[204:207],a[172:175],a[48:51],v[204:207]\n"
"LD v3,s[52:55],s65 offen offset:1024 lds\n"
"MF v[236:239],a[172:175],a[52:55],v[236:239]\n"
"WT lgkmcnt(11)\n"
"TR a[170:171],v14 offset:59392\n"
"TR a[172:173],v15 offset:57344\n"
"MF v[208:211],a[176:179],a[48:51],v[208:211]\n"
"TR a[174:175],v15 offset:59392\n"
"MF v[240:243],a[176:179],a[52:55],v[240:243]\n"
"WT lgkmcnt(12)\n"
"s_mov_b32 s64, 30720\n"
"TR a[176:177],v12 offset:61440\n"
"MF v[212:215],a[180:183],a[48:51],v[212:215]\n"
"LD a[0:3],v10,s[44:47],s64 offen offset:0 sc1\n"
"MF v[244:247],a[180:183],a[52:55],v[244:247]\n"
"WT lgkmcnt(11)\n"
"TR a[178:179],v12 offset:63488\n"
"TR a[180:181],v13 offset:61440\n"
"MF v[216:219],a[184:187],a[48:51],v[216:219]\n"
"TR a[182:183],v13 offset:63488\n"
"MF v[248:251],a[184:187],a[52:55],v[248:251]\n"
"WT lgkmcnt(12)\n"
"TR a[184:185],v14 offset:61440\n"
"MF v[220:223],a[188:191],a[48:51],v[220:223]\n"
"LD v4,s[52:55],s65 offen offset:2048 lds\n"
"MF v[252:255],a[188:191],a[52:55],v[252:255]\n"
"WT vmcnt(28) lgkmcnt(11)\n"
"TR a[186:187],v14 offset:63488\n"
"TR a[188:189],v15 offset:61440\n"
"MF v[192:195],a[160:163],a[56:59],v[192:195]\n"
"WT vmcnt(26)\n"
"s_barrier\n"
"TR a[190:191],v15 offset:63488\n"
"MF v[224:227],a[160:163],a[60:63],v[224:227]\n"
"WT lgkmcnt(12)\n"
"TR a[160:161],v24 offset:0\n"
"MF v[196:199],a[164:167],a[56:59],v[196:199]\n"
"LD a[4:7],v11,s[44:47],s64 offen offset:0 sc1\n"
"MF v[228:231],a[164:167],a[60:63],v[228:231]\n"
"WT lgkmcnt(11)\n"
"TR a[162:163],v24 offset:2048\n"
"TR a[164:165],v25 offset:0\n"
"MF v[200:203],a[168:171],a[56:59],v[200:203]\n"
"TR a[166:167],v25 offset:2048\n"
"MF v[232:235],a[168:171],a[60:63],v[232:235]\n"
"WT lgkmcnt(12)\n"
"TR a[168:169],v26 offset:0\n"
"MF v[204:207],a[172:175],a[56:59],v[204:207]\n"
"TR a[170:171],v26 offset:2048\n"
"MF v[236:239],a[172:175],a[60:63],v[236:239]\n"
"WT lgkmcnt(12)\n"
"TR a[172:173],v27 offset:0\n"
"MF v[208:211],a[176:179],a[56:59],v[208:211]\n"
"LD v5,s[52:55],s65 offen offset:3072 lds\n"
"MF v[240:243],a[176:179],a[60:63],v[240:243]\n"
"WT lgkmcnt(11)\n"
"TR a[174:175],v27 offset:2048\n"
"TR a[176:177],v24 offset:4096\n"
"MF v[212:215],a[180:183],a[56:59],v[212:215]\n"
"TR a[178:179],v24 offset:6144\n"
"MF v[244:247],a[180:183],a[60:63],v[244:247]\n"
"WT lgkmcnt(12)\n"
"TR a[180:181],v25 offset:4096\n"
"MF v[216:219],a[184:187],a[56:59],v[216:219]\n"
"LD a[8:11],v10,s[44:47],s64 offen offset:1024 sc1\n"
"MF v[248:251],a[184:187],a[60:63],v[248:251]\n"
"WT lgkmcnt(11)\n"
"TR a[182:183],v25 offset:6144\n"
"TR a[184:185],v26 offset:4096\n"
"MF v[220:223],a[188:191],a[56:59],v[220:223]\n"
"TR a[186:187],v26 offset:6144\n"
"MF v[252:255],a[188:191],a[60:63],v[252:255]\n"
"WT vmcnt(27) lgkmcnt(12)\n"
"TR a[188:189],v27 offset:4096\n"
"MF v[192:195],a[160:163],a[64:67],v[192:195]\n"
"WT vmcnt(25)\n"
"TR a[190:191],v27 offset:6144\n"
"MF v[224:227],a[160:163],a[68:71],v[224:227]\n"
"WT lgkmcnt(12)\n"
"TR a[160:161],v24 offset:8192\n"
"MF v[196:199],a[164:167],a[64:67],v[196:199]\n"
"LD a[12:15],v11,s[44:47],s64 offen offset:1024 sc1\n"
"MF v[228:231],a[164:167],a[68:71],v[228:231]\n"
"WT lgkmcnt(11)\n"
"TR a[162:163],v24 offset:10240\n"
"TR a[164:165],v25 offset:8192\n"
"MF v[200:203],a[168:171],a[64:67],v[200:203]\n"
"TR a[166:167],v25 offset:10240\n"
"MF v[232:235],a[168:171],a[68:71],v[232:235]\n"
"WT lgkmcnt(12)\n"
"s_add_u32 s65, s65, s23\n"
"s_add_u32 m0, s22, 0\n"
"TR a[168:169],v26 offset:8192\n"
"MF v[204:207],a[172:175],a[64:67],v[204:207]\n"
"LD v2,s[52:55],s65 offen offset:0 lds\n"
"MF v[236:239],a[172:175],a[68:71],v[236:239]\n"
"WT lgkmcnt(11)\n"
"TR a[170:171],v26 offset:10240\n"
"TR a[172:173],v27 offset:8192\n"
"MF v[208:211],a[176:179],a[64:67],v[208:211]\n"
"TR a[174:175],v27 offset:10240\n"
"MF v[240:243],a[176:179],a[68:71],v[240:243]\n"
"WT lgkmcnt(12)\n"
"s_mov_b32 s64, 34816\n"
"TR a[176:177],v24 offset:12288\n"
"MF v[212:215],a[180:183],a[64:67],v[212:215]\n"
"LD a[16:19],v10,s[44:47],s64 offen offset:0 sc1\n"
"MF v[244:247],a[180:183],a[68:71],v[244:247]\n"
"WT lgkmcnt(11)\n"
"TR a[178:179],v24 offset:14336\n"
"TR a[180:181],v25 offset:12288\n"
"MF v[216:219],a[184:187],a[64:67],v[216:219]\n"
"TR a[182:183],v25 offset:14336\n"
"MF v[248:251],a[184:187],a[68:71],v[248:251]\n"
"WT lgkmcnt(12)\n"
"TR a[184:185],v26 offset:12288\n"
"MF v[220:223],a[188:191],a[64:67],v[220:223]\n"
"LD v3,s[52:55],s65 offen offset:1024 lds\n"
"MF v[252:255],a[188:191],a[68:71],v[252:255]\n"
"WT vmcnt(27) lgkmcnt(11)\n"
"TR a[186:187],v26 offset:14336\n"
"TR a[188:189],v27 offset:12288\n"
"MF v[192:195],a[160:163],a[72:75],v[192:195]\n"
"WT vmcnt(25)\n"
"TR a[190:191],v27 offset:14336\n"
"MF v[224:227],a[160:163],a[76:79],v[224:227]\n"
"WT lgkmcnt(12)\n"
"TR a[160:161],v24 offset:16384\n"
"MF v[196:199],a[164:167],a[72:75],v[196:199]\n"
"LD a[20:23],v11,s[44:47],s64 offen offset:0 sc1\n"
"MF v[228:231],a[164:167],a[76:79],v[228:231]\n"
"WT lgkmcnt(11)\n"
"TR a[162:163],v24 offset:18432\n"
"TR a[164:165],v25 offset:16384\n"
"MF v[200:203],a[168:171],a[72:75],v[200:203]\n"
"TR a[166:167],v25 offset:18432\n"
"MF v[232:235],a[168:171],a[76:79],v[232:235]\n"
"WT lgkmcnt(12)\n"
"TR a[168:169],v26 offset:16384\n"
"MF v[204:207],a[172:175],a[72:75],v[204:207]\n"
"LD v4,s[52:55],s65 offen offset:2048 lds\n"
"MF v[236:239],a[172:175],a[76:79],v[236:239]\n"
"WT lgkmcnt(11)\n"
"TR a[170:171],v26 offset:18432\n"
"TR a[172:173],v27 offset:16384\n"
"MF v[208:211],a[176:179],a[72:75],v[208:211]\n"
"TR a[174:175],v27 offset:18432\n"
"MF v[240:243],a[176:179],a[76:79],v[240:243]\n"
"WT lgkmcnt(12)\n"
"TR a[176:177],v24 offset:20480\n"
"MF v[212:215],a[180:183],a[72:75],v[212:215]\n"
"LD a[24:27],v10,s[44:47],s64 offen offset:1024 sc1\n"
"MF v[244:247],a[180:183],a[76:79],v[244:247]\n"
"WT lgkmcnt(11)\n"
"TR a[178:179],v24 offset:22528\n"
"TR a[180:181],v25 offset:20480\n"
"MF v[216:219],a[184:187],a[72:75],v[216:219]\n"
"TR a[182:183],v25 offset:22528\n"
"MF v[248:251],a[184:187],a[76:79],v[248:251]\n"
"WT lgkmcnt(12)\n"
"TR a[184:185],v26 offset:20480\n"
"MF v[220:223],a[188:191],a[72:75],v[220:223]\n"
"LD v5,s[52:55],s65 offen offset:3072 lds\n"
"MF v[252:255],a[188:191],a[76:79],v[252:255]\n"
"WT vmcnt(27) lgkmcnt(11)\n"
"TR a[186:187],v26 offset:22528\n"
"TR a[188:189],v27 offset:20480\n"
"MF v[192:195],a[160:163],a[80:83],v[192:195]\n"
"WT vmcnt(26)\n"
"TR a[190:191],v27 offset:22528\n"
"MF v[224:227],a[160:163],a[84:87],v[224:227]\n"
"WT lgkmcnt(12)\n"
"TR a[160:161],v24 offset:24576\n"
"MF v[196:199],a[164:167],a[80:83],v[196:199]\n"
"LD a[28:31],v11,s[44:47],s64 offen offset:1024 sc1\n"
"MF v[228:231],a[164:167],a[84:87],v[228:231]\n"
"WT lgkmcnt(11)\n"
"TR a[162:163],v24 offset:26624\n"
"TR a[164:165],v25 offset:24576\n"
"MF v[200:203],a[168:171],a[80:83],v[200:203]\n"
"TR a[166:167],v25 offset:26624\n"
"MF v[232:235],a[168:171],a[84:87],v[232:235]\n"
"WT lgkmcnt(12)\n"
"s_add_u32 s65, s65, s23\n"
"s_add_u32 m0, s22, 16384\n"
"TR a[168:169],v26 offset:24576\n"
"MF v[204:207],a[172:175],a[80:83],v[204:207]\n"
"LD v2,s[52:55],s65 offen offset:0 lds\n"
"MF v[236:239],a[172:175],a[84:87],v[236:239]\n"
"WT lgkmcnt(11)\n"
"TR a[170:171],v26 offset:26624\n"
"TR a[172:173],v27 offset:24576\n"
"MF v[208:211],a[176:179],a[80:83],v[208:211]\n"
"TR a[174:175],v27 offset:26624\n"
"MF v[240:243],a[176:179],a[84:87],v[240:243]\n"
"WT lgkmcnt(12)\n"
"s_mov_b32 s64, 38912\n"
"TR a[176:177],v24 offset:28672\n"
"MF v[212:215],a[180:183],a[80:83],v[212:215]\n"
"LD a[32:35],v10,s[44:47],s64 offen offset:0 sc1\n"
"MF v[244:247],a[180:183],a[84:87],v[244:247]\n"
"WT lgkmcnt(11)\n"
"TR a[178:179],v24 offset:30720\n"
"TR a[180:181],v25 offset:28672\n"
"MF v[216:219],a[184:187],a[80:83],v[216:219]\n"
"TR a[182:183],v25 offset:30720\n"
"MF v[248:251],a[184:187],a[84:87],v[248:251]\n"
"WT lgkmcnt(12)\n"
"TR a[184:185],v26 offset:28672\n"
"MF v[220:223],a[188:191],a[80:83],v[220:223]\n"
"LD v3,s[52:55],s65 offen offset:1024 lds\n"
"MF v[252:255],a[188:191],a[84:87],v[252:255]\n"
"WT vmcnt(28) lgkmcnt(11)\n"
"TR a[186:187],v26 offset:30720\n"
"TR a[188:189],v27 offset:28672\n"
"MF v[192:195],a[160:163],a[88:91],v[192:195]\n"
"WT vmcnt(26)\n"
"s_barrier\n"
"TR a[190:191],v27 offset:30720\n"
"MF v[224:227],a[160:163],a[92:95],v[224:227]\n"
"WT lgkmcnt(12)\n"
"TR a[160:161],v24 offset:32768\n"
"MF v[196:199],a[164:167],a[88:91],v[196:199]\n"
"LD a[36:39],v11,s[44:47],s64 offen offset:0 sc1\n"
"MF v[228:231],a[164:167],a[92:95],v[228:231]\n"
"WT lgkmcnt(11)\n"
"TR a[162:163],v24 offset:34816\n"
"TR a[164:165],v25 offset:32768\n"
"MF v[200:203],a[168:171],a[88:91],v[200:203]\n"
"TR a[166:167],v25 offset:34816\n"
"MF v[232:235],a[168:171],a[92:95],v[232:235]\n"
"WT lgkmcnt(12)\n"
"TR a[168:169],v26 offset:32768\n"
"MF v[204:207],a[172:175],a[88:91],v[204:207]\n"
"LD v4,s[52:55],s65 offen offset:2048 lds\n"
"MF v[236:239],a[172:175],a[92:95],v[236:239]\n"
"WT lgkmcnt(11)\n"
"TR a[170:171],v26 offset:34816\n"
"TR a[172:173],v27 offset:32768\n"
"MF v[208:211],a[176:179],a[88:91],v[208:211]\n"
"TR a[174:175],v27 offset:34816\n"
"MF v[240:243],a[176:179],a[92:95],v[240:243]\n"
"WT lgkmcnt(12)\n"
"TR a[176:177],v24 offset:36864\n"
"MF v[212:215],a[180:183],a[88:91],v[212:215]\n"
"LD a[40:43],v10,s[44:47],s64 offen offset:1024 sc1\n"
"MF v[244:247],a[180:183],a[92:95],v[244:247]\n"
"WT lgkmcnt(11)\n"
"TR a[178:179],v24 offset:38912\n"
"TR a[180:181],v25 offset:36864\n"
"MF v[216:219],a[184:187],a[88:91],v[216:219]\n"
"TR a[182:183],v25 offset:38912\n"
"MF v[248:251],a[184:187],a[92:95],v[248:251]\n"
"WT lgkmcnt(12)\n"
"TR a[184:185],v26 offset:36864\n"
"MF v[220:223],a[188:191],a[88:91],v[220:223]\n"
"LD v5,s[52:55],s65 offen offset:3072 lds\n"
"MF v[252:255],a[188:191],a[92:95],v[252:255]\n"
"WT vmcnt(28) lgkmcnt(11)\n"
"TR a[186:187],v26 offset:38912\n"
"TR a[188:189],v27 offset:36864\n"
"MF v[192:195],a[160:163],a[96:99],v[192:195]\n"
"WT vmcnt(26)\n"
"TR a[190:191],v27 offset:38912\n"
"MF v[224:227],a[160:163],a[100:103],v[224:227]\n"
"WT lgkmcnt(12)\n"
"TR a[160:161],v24 offset:40960\n"
"MF v[196:199],a[164:167],a[96:99],v[196:199]\n"
"LD a[44:47],v11,s[44:47],s64 offen offset:1024 sc1\n"
"MF v[228:231],a[164:167],a[100:103],v[228:231]\n"
"WT lgkmcnt(11)\n"
"TR a[162:163],v24 offset:43008\n"
"TR a[164:165],v25 offset:40960\n"
"MF v[200:203],a[168:171],a[96:99],v[200:203]\n"
"TR a[166:167],v25 offset:43008\n"
"MF v[232:235],a[168:171],a[100:103],v[232:235]\n"
"WT lgkmcnt(12)\n"
"TR a[168:169],v26 offset:40960\n"
"MF v[204:207],a[172:175],a[96:99],v[204:207]\n"
"TR a[170:171],v26 offset:43008\n"
"MF v[236:239],a[172:175],a[100:103],v[236:239]\n"
"WT lgkmcnt(12)\n"
"s_add_u32 s65, s65, s23\n"
"s_add_u32 m0, s22, 32768\n"
"TR a[172:173],v27 offset:40960\n"
"MF v[208:211],a[176:179],a[96:99],v[208:211]\n"
"LD v2,s[52:55],s65 offen offset:0 lds\n"
"MF v[240:243],a[176:179],a[100:103],v[240:243]\n"
"WT lgkmcnt(11)\n"
"TR a[174:175],v27 offset:43008\n"
"TR a[176:177],v24 offset:45056\n"
"MF v[212:215],a[180:183],a[96:99],v[212:215]\n"
"TR a[178:179],v24 offset:47104\n"
"MF v[244:247],a[180:183],a[100:103],v[244:247]\n"
"WT lgkmcnt(12)\n"
"s_mov_b32 s64, 43008\n"
"TR a[180:181],v25 offset:45056\n"
"MF v[216:219],a[184:187],a[96:99],v[216:219]\n"
"LD a[48:51],v10,s[44:47],s64 offen offset:0 sc1\n"
"MF v[248:251],a[184:187],a[100:103],v[248:251]\n"
"WT lgkmcnt(11)\n"
"TR a[182:183],v25 offset:47104\n"
"TR a[184:185],v26 offset:45056\n"
"MF v[220:223],a[188:191],a[96:99],v[220:223]\n"
"TR a[186:187],v26 offset:47104\n"
"MF v[252:255],a[188:191],a[100:103],v[252:255]\n"
"WT vmcnt(27) lgkmcnt(12)\n"
"TR a[188:189],v27 offset:45056\n"
"MF v[192:195],a[160:163],a[104:107],v[192:195]\n"
"WT vmcnt(25)\n"
"TR a[190:191],v27 offset:47104\n"
"MF v[224:227],a[160:163],a[108:111],v[224:227]\n"
"WT lgkmcnt(12)\n"
"TR a[160:161],v24 offset:49152\n"
"MF v[196:199],a[164:167],a[104:107],v[196:199]\n"
"LD a[52:55],v11,s[44:47],s64 offen offset:0 sc1\n"
"MF v[228:231],a[164:167],a[108:111],v[228:231]\n"
"WT lgkmcnt(11)\n"
"TR a[162:163],v24 offset:51200\n"
"TR a[164:165],v25 offset:49152\n"
"MF v[200:203],a[168:171],a[104:107],v[200:203]\n"
"TR a[166:167],v25 offset:51200\n"
"MF v[232:235],a[168:171],a[108:111],v[232:235]\n"
"WT lgkmcnt(12)\n"
"TR a[168:169],v26 offset:49152\n"
"MF v[204:207],a[172:175],a[104:107],v[204:207]\n"
"LD v3,s[52:55],s65 offen offset:1024 lds\n"
"MF v[236:239],a[172:175],a[108:111],v[236:239]\n"
"WT lgkmcnt(11)\n"
"TR a[170:171],v26 offset:51200\n"
"TR a[172:173],v27 offset:49152\n"
"MF v[208:211],a[176:179],a[104:107],v[208:211]\n"
"TR a[174:175],v27 offset:51200\n"
"MF v[240:243],a[176:179],a[108:111],v[240:243]\n"
"WT lgkmcnt(12)\n"
"TR a[176:177],v24 offset:53248\n"
"MF v[212:215],a[180:183],a[104:107],v[212:215]\n"
"LD a[56:59],v10,s[44:47],s64 offen offset:1024 sc1\n"
"MF v[244:247],a[180:183],a[108:111],v[244:247]\n"
"WT lgkmcnt(11)\n"
"TR a[178:179],v24 offset:55296\n"
"TR a[180:181],v25 offset:53248\n"
"MF v[216:219],a[184:187],a[104:107],v[216:219]\n"
"TR a[182:183],v25 offset:55296\n"
"MF v[248:251],a[184:187],a[108:111],v[248:251]\n"
"WT lgkmcnt(12)\n"
"TR a[184:185],v26 offset:53248\n"
"MF v[220:223],a[188:191],a[104:107],v[220:223]\n"
"LD v4,s[52:55],s65 offen offset:2048 lds\n"
"MF v[252:255],a[188:191],a[108:111],v[252:255]\n"
"WT vmcnt(27) lgkmcnt(11)\n"
"TR a[186:187],v26 offset:55296\n"
"TR a[188:189],v27 offset:53248\n"
"MF v[192:195],a[160:163],a[0:3],v[192:195]\n"
"WT vmcnt(25)\n"
"TR a[190:191],v27 offset:55296\n"
"MF v[224:227],a[160:163],a[4:7],v[224:227]\n"
"WT lgkmcnt(12)\n"
"TR a[160:161],v24 offset:57344\n"
"MF v[196:199],a[164:167],a[0:3],v[196:199]\n"
"LD a[60:63],v11,s[44:47],s64 offen offset:1024 sc1\n"
"MF v[228:231],a[164:167],a[4:7],v[228:231]\n"
"WT lgkmcnt(11)\n"
"TR a[162:163],v24 offset:59392\n"
"TR a[164:165],v25 offset:57344\n"
"MF v[200:203],a[168:171],a[0:3],v[200:203]\n"
"TR a[166:167],v25 offset:59392\n"
"MF v[232:235],a[168:171],a[4:7],v[232:235]\n"
"WT lgkmcnt(12)\n"
"TR a[168:169],v26 offset:57344\n"
"MF v[204:207],a[172:175],a[0:3],v[204:207]\n"
"LD v5,s[52:55],s65 offen offset:3072 lds\n"
"MF v[236:239],a[172:175],a[4:7],v[236:239]\n"
"WT lgkmcnt(11)\n"
"TR a[170:171],v26 offset:59392\n"
"TR a[172:173],v27 offset:57344\n"
"MF v[208:211],a[176:179],a[0:3],v[208:211]\n"
"TR a[174:175],v27 offset:59392\n"
"MF v[240:243],a[176:179],a[4:7],v[240:243]\n"
"WT lgkmcnt(12)\n"
"s_mov_b32 s64, 47104\n"
"TR a[176:177],v24 offset:61440\n"
"MF v[212:215],a[180:183],a[0:3],v[212:215]\n"
"LD a[64:67],v10,s[44:47],s64 offen offset:0 sc1\n"
"MF v[244:247],a[180:183],a[4:7],v[244:247]\n"
"WT lgkmcnt(11)\n"
"TR a[178:179],v24 offset:63488\n"
"TR a[180:181],v25 offset:61440\n"
"MF v[216:219],a[184:187],a[0:3],v[216:219]\n"
"TR a[182:183],v25 offset:63488\n"
"MF v[248:251],a[184:187],a[4:7],v[248:251]\n"
"WT lgkmcnt(12)\n"
"TR a[184:185],v26 offset:61440\n"
"MF v[220:223],a[188:191],a[0:3],v[220:223]\n"
"TR a[186:187],v26 offset:63488\n"
"MF v[252:255],a[188:191],a[4:7],v[252:255]\n"
"WT vmcnt(26) lgkmcnt(12)\n"
"TR a[188:189],v27 offset:61440\n"
"MF v[192:195],a[160:163],a[8:11],v[192:195]\n"
"WT vmcnt(25)\n"
"s_barrier\n"
"TR a[190:191],v27 offset:63488\n"
"MF v[224:227],a[160:163],a[12:15],v[224:227]\n"
"WT lgkmcnt(12)\n"
"TR a[160:161],v28 offset:0\n"
"MF v[196:199],a[164:167],a[8:11],v[196:199]\n"
"LD a[68:71],v11,s[44:47],s64 offen offset:0 sc1\n"
"MF v[228:231],a[164:167],a[12:15],v[228:231]\n"
"WT lgkmcnt(11)\n"
"TR a[162:163],v28 offset:2048\n"
"TR a[164:165],v29 offset:0\n"
"MF v[200:203],a[168:171],a[8:11],v[200:203]\n"
"TR a[166:167],v29 offset:2048\n"
"MF v[232:235],a[168:171],a[12:15],v[232:235]\n"
"WT lgkmcnt(12)\n"
"TR a[168:169],v30 offset:0\n"
"MF v[204:207],a[172:175],a[8:11],v[204:207]\n"
"TR a[170:171],v30 offset:2048\n"
"MF v[236:239],a[172:175],a[12:15],v[236:239]\n"
"WT lgkmcnt(12)\n"
"TR a[172:173],v31 offset:0\n"
"MF v[208:211],a[176:179],a[8:11],v[208:211]\n"
"TR a[174:175],v31 offset:2048\n"
"MF v[240:243],a[176:179],a[12:15],v[240:243]\n"
"WT lgkmcnt(12)\n"
"TR a[176:177],v28 offset:4096\n"
"MF v[212:215],a[180:183],a[8:11],v[212:215]\n"
"LD a[72:75],v10,s[44:47],s64 offen offset:1024 sc1\n"
"MF v[244:247],a[180:183],a[12:15],v[244:247]\n"
"WT lgkmcnt(11)\n"
"TR a[178:179],v28 offset:6144\n"
"TR a[180:181],v29 offset:4096\n"
"MF v[216:219],a[184:187],a[8:11],v[216:219]\n"
"TR a[182:183],v29 offset:6144\n"
"MF v[248:251],a[184:187],a[12:15],v[248:251]\n"
"WT lgkmcnt(12)\n"
"TR a[184:185],v30 offset:4096\n"
"MF v[220:223],a[188:191],a[8:11],v[220:223]\n"
"TR a[186:187],v30 offset:6144\n"
"MF v[252:255],a[188:191],a[12:15],v[252:255]\n"
"WT vmcnt(25) lgkmcnt(12)\n"
"TR a[188:189],v31 offset:4096\n"
"MF v[192:195],a[160:163],a[16:19],v[192:195]\n"
"WT vmcnt(23)\n"
"TR a[190:191],v31 offset:6144\n"
"MF v[224:227],a[160:163],a[20:23],v[224:227]\n"
"WT lgkmcnt(12)\n"
"TR a[160:161],v28 offset:8192\n"
"MF v[196:199],a[164:167],a[16:19],v[196:199]\n"
"LD a[76:79],v11,s[44:47],s64 offen offset:1024 sc1\n"
"MF v[228:231],a[164:167],a[20:23],v[228:231]\n"
"WT lgkmcnt(11)\n"
"TR a[162:163],v28 offset:10240\n"
"TR a[164:165],v29 offset:8192\n"
"MF v[200:203],a[168:171],a[16:19],v[200:203]\n"
"TR a[166:167],v29 offset:10240\n"
"MF v[232:235],a[168:171],a[20:23],v[232:235]\n"
"WT lgkmcnt(12)\n"
"TR a[168:169],v30 offset:8192\n"
"MF v[204:207],a[172:175],a[16:19],v[204:207]\n"
"TR a[170:171],v30 offset:10240\n"
"MF v[236:239],a[172:175],a[20:23],v[236:239]\n"
"WT lgkmcnt(12)\n"
"TR a[172:173],v31 offset:8192\n"
"MF v[208:211],a[176:179],a[16:19],v[208:211]\n"
"TR a[174:175],v31 offset:10240\n"
"MF v[240:243],a[176:179],a[20:23],v[240:243]\n"
"WT lgkmcnt(12)\n"
"s_mov_b32 s64, 51200\n"
"TR a[176:177],v28 offset:12288\n"
"MF v[212:215],a[180:183],a[16:19],v[212:215]\n"
"LD a[80:83],v10,s[44:47],s64 offen offset:0 sc1\n"
"MF v[244:247],a[180:183],a[20:23],v[244:247]\n"
"WT lgkmcnt(11)\n"
"TR a[178:179],v28 offset:14336\n"
"TR a[180:181],v29 offset:12288\n"
"MF v[216:219],a[184:187],a[16:19],v[216:219]\n"
"TR a[182:183],v29 offset:14336\n"
"MF v[248:251],a[184:187],a[20:23],v[248:251]\n"
"WT lgkmcnt(12)\n"
"TR a[184:185],v30 offset:12288\n"
"MF v[220:223],a[188:191],a[16:19],v[220:223]\n"
"TR a[186:187],v30 offset:14336\n"
"MF v[252:255],a[188:191],a[20:23],v[252:255]\n"
"WT vmcnt(23) lgkmcnt(12)\n"
"TR a[188:189],v31 offset:12288\n"
"MF v[192:195],a[160:163],a[24:27],v[192:195]\n"
"WT vmcnt(21)\n"
"TR a[190:191],v31 offset:14336\n"
"MF v[224:227],a[160:163],a[28:31],v[224:227]\n"
"WT lgkmcnt(12)\n"
"TR a[160:161],v28 offset:16384\n"
"MF v[196:199],a[164:167],a[24:27],v[196:199]\n"
"LD a[84:87],v11,s[44:47],s64 offen offset:0 sc1\n"
"MF v[228:231],a[164:167],a[28:31],v[228:231]\n"
"WT lgkmcnt(11)\n"
"TR a[162:163],v28 offset:18432\n"
"TR a[164:165],v29 offset:16384\n"
"MF v[200:203],a[168:171],a[24:27],v[200:203]\n"
"TR a[166:167],v29 offset:18432\n"
"MF v[232:235],a[168:171],a[28:31],v[232:235]\n"
"WT lgkmcnt(12)\n"
"TR a[168:169],v30 offset:16384\n"
"MF v[204:207],a[172:175],a[24:27],v[204:207]\n"
"TR a[170:171],v30 offset:18432\n"
"MF v[236:239],a[172:175],a[28:31],v[236:239]\n"
"WT lgkmcnt(12)\n"
"TR a[172:173],v31 offset:16384\n"
"MF v[208:211],a[176:179],a[24:27],v[208:211]\n"
"TR a[174:175],v31 offset:18432\n"
"MF v[240:243],a[176:179],a[28:31],v[240:243]\n"
"WT lgkmcnt(12)\n"
"TR a[176:177],v28 offset:20480\n"
"MF v[212:215],a[180:183],a[24:27],v[212:215]\n"
"TR a[178:179],v28 offset:22528\n"
"MF v[244:247],a[180:183],a[28:31],v[244:247]\n"
"WT lgkmcnt(12)\n"
"TR a[180:181],v29 offset:20480\n"
"MF v[216:219],a[184:187],a[24:27],v[216:219]\n"
"LD a[88:91],v10,s[44:47],s64 offen offset:1024 sc1\n"
"MF v[248:251],a[184:187],a[28:31],v[248:251]\n"
"WT lgkmcnt(11)\n"
"TR a[182:183],v29 offset:22528\n"
"TR a[184:185],v30 offset:20480\n"
"MF v[220:223],a[188:191],a[24:27],v[220:223]\n"
"TR a[186:187],v30 offset:22528\n"
"MF v[252:255],a[188:191],a[28:31],v[252:255]\n"
"WT vmcnt(21) lgkmcnt(12)\n"
"TR a[188:189],v31 offset:20480\n"
"MF v[192:195],a[160:163],a[32:35],v[192:195]\n"
"WT vmcnt(19)\n"
"TR a[190:191],v31 offset:22528\n"
"MF v[224:227],a[160:163],a[36:39],v[224:227]\n"
"WT lgkmcnt(12)\n"
"TR a[160:161],v28 offset:24576\n"
"MF v[196:199],a[164:167],a[32:35],v[196:199]\n"
"LD a[92:95],v11,s[44:47],s64 offen offset:1024 sc1\n"
"MF v[228:231],a[164:167],a[36:39],v[228:231]\n"
"WT lgkmcnt(11)\n"
"TR a[162:163],v28 offset:26624\n"
"TR a[164:165],v29 offset:24576\n"
"MF v[200:203],a[168:171],a[32:35],v[200:203]\n"
"TR a[166:167],v29 offset:26624\n"
"MF v[232:235],a[168:171],a[36:39],v[232:235]\n"
"WT lgkmcnt(12)\n"
"TR a[168:169],v30 offset:24576\n"
"MF v[204:207],a[172:175],a[32:35],v[204:207]\n"
"LD v[160:163],v1,s[40:43],s60 offen offset:0 sc1\n"
"MF v[236:239],a[172:175],a[36:39],v[236:239]\n"
"WT lgkmcnt(11)\n"
"TR a[170:171],v30 offset:26624\n"
"TR a[172:173],v31 offset:24576\n"
"MF v[208:211],a[176:179],a[32:35],v[208:211]\n"
"TR a[174:175],v31 offset:26624\n"
"MF v[240:243],a[176:179],a[36:39],v[240:243]\n"
"WT lgkmcnt(12)\n"
"TR a[176:177],v28 offset:28672\n"
"MF v[212:215],a[180:183],a[32:35],v[212:215]\n"
"LD v[164:167],v1,s[40:43],s60 offen offset:1024 sc1\n"
"MF v[244:247],a[180:183],a[36:39],v[244:247]\n"
"WT lgkmcnt(11)\n"
"TR a[178:179],v28 offset:30720\n"
"TR a[180:181],v29 offset:28672\n"
"MF v[216:219],a[184:187],a[32:35],v[216:219]\n"
"TR a[182:183],v29 offset:30720\n"
"MF v[248:251],a[184:187],a[36:39],v[248:251]\n"
"WT lgkmcnt(12)\n"
"TR a[184:185],v30 offset:28672\n"
"MF v[220:223],a[188:191],a[32:35],v[220:223]\n"
"LD v[168:171],v1,s[40:43],s60 offen offset:2048 sc1\n"
"MF v[252:255],a[188:191],a[36:39],v[252:255]\n"
"WT vmcnt(21) lgkmcnt(11)\n"
"TR a[186:187],v30 offset:30720\n"
"TR a[188:189],v31 offset:28672\n"
"MF v[192:195],a[160:163],a[40:43],v[192:195]\n"
"WT vmcnt(19)\n"
"s_barrier\n"
"TR a[190:191],v31 offset:30720\n"
"MF v[224:227],a[160:163],a[44:47],v[224:227]\n"
"WT lgkmcnt(12)\n"
"TR a[160:161],v12 offset:0\n"
"MF v[196:199],a[164:167],a[40:43],v[196:199]\n"
"LD v[172:175],v1,s[40:43],s60 offen offset:3072 sc1\n"
"MF v[228:231],a[164:167],a[44:47],v[228:231]\n"
"WT lgkmcnt(11)\n"
"TR a[162:163],v12 offset:2048\n"
"TR a[164:165],v13 offset:0\n"
"MF v[200:203],a[168:171],a[40:43],v[200:203]\n"
"TR a[166:167],v13 offset:2048\n"
"MF v[232:235],a[168:171],a[44:47],v[232:235]\n"
"WT lgkmcnt(12)\n"
"TR a[168:169],v14 offset:0\n"
"MF v[204:207],a[172:175],a[40:43],v[204:207]\n"
"LD v[176:179],v1,s[40:43],s61 offen offset:0 sc1\n"
"MF v[236:239],a[172:175],a[44:47],v[236:239]\n"
"WT lgkmcnt(11)\n"
"TR a[170:171],v14 offset:2048\n"
"TR a[172:173],v15 offset:0\n"
"MF v[208:211],a[176:179],a[40:43],v[208:211]\n"
"TR a[174:175],v15 offset:2048\n"
"MF v[240:243],a[176:179],a[44:47],v[240:243]\n"
"WT lgkmcnt(12)\n"
"TR a[176:177],v12 offset:4096\n"
"MF v[212:215],a[180:183],a[40:43],v[212:215]\n"
"LD v[180:183],v1,s[40:43],s61 offen offset:1024 sc1\n"
"MF v[244:247],a[180:183],a[44:47],v[244:247]\n"
"WT lgkmcnt(11)\n"
"TR a[178:179],v12 offset:6144\n"
"TR a[180:181],v13 offset:4096\n"
"MF v[216:219],a[184:187],a[40:43],v[216:219]\n"
"TR a[182:183],v13 offset:6144\n"
"MF v[248:251],a[184:187],a[44:47],v[248:251]\n"
"WT lgkmcnt(12)\n"
"TR a[184:185],v14 offset:4096\n"
"MF v[220:223],a[188:191],a[40:43],v[220:223]\n"
"LD v[184:187],v1,s[40:43],s61 offen offset:2048 sc1\n"
"MF v[252:255],a[188:191],a[44:47],v[252:255]\n"
"WT vmcnt(21) lgkmcnt(11)\n"
"TR a[186:187],v14 offset:6144\n"
"TR a[188:189],v15 offset:4096\n"
"MF v[192:195],a[160:163],a[48:51],v[192:195]\n"
"WT vmcnt(20)\n"
"TR a[190:191],v15 offset:6144\n"
"MF v[224:227],a[160:163],a[52:55],v[224:227]\n"
"WT lgkmcnt(12)\n"
"TR a[160:161],v12 offset:8192\n"
"MF v[196:199],a[164:167],a[48:51],v[196:199]\n"
"LD v[188:191],v1,s[40:43],s61 offen offset:3072 sc1\n"
"MF v[228:231],a[164:167],a[52:55],v[228:231]\n"
"WT lgkmcnt(11)\n"
"TR a[162:163],v12 offset:10240\n"
"TR a[164:165],v13 offset:8192\n"
"MF v[200:203],a[168:171],a[48:51],v[200:203]\n"
"TR a[166:167],v13 offset:10240\n"
"MF v[232:235],a[168:171],a[52:55],v[232:235]\n"
"WT lgkmcnt(12)\n"
"TR a[168:169],v14 offset:8192\n"
"MF v[204:207],a[172:175],a[48:51],v[204:207]\n"
"TR a[170:171],v14 offset:10240\n"
"MF v[236:239],a[172:175],a[52:55],v[236:239]\n"
"WT lgkmcnt(12)\n"
"TR a[172:173],v15 offset:8192\n"
"MF v[208:211],a[176:179],a[48:51],v[208:211]\n"
"TR a[174:175],v15 offset:10240\n"
"MF v[240:243],a[176:179],a[52:55],v[240:243]\n"
"WT lgkmcnt(12)\n"
"TR a[176:177],v12 offset:12288\n"
"MF v[212:215],a[180:183],a[48:51],v[212:215]\n"
"TR a[178:179],v12 offset:14336\n"
"MF v[244:247],a[180:183],a[52:55],v[244:247]\n"
"WT lgkmcnt(12)\n"
"TR a[180:181],v13 offset:12288\n"
"MF v[216:219],a[184:187],a[48:51],v[216:219]\n"
"TR a[182:183],v13 offset:14336\n"
"MF v[248:251],a[184:187],a[52:55],v[248:251]\n"
"WT lgkmcnt(12)\n"
"TR a[184:185],v14 offset:12288\n"
"MF v[220:223],a[188:191],a[48:51],v[220:223]\n"
"TR a[186:187],v14 offset:14336\n"
"MF v[252:255],a[188:191],a[52:55],v[252:255]\n"
"WT vmcnt(19) lgkmcnt(12)\n"
"TR a[188:189],v15 offset:12288\n"
"MF v[192:195],a[160:163],a[56:59],v[192:195]\n"
"WT vmcnt(17)\n"
"TR a[190:191],v15 offset:14336\n"
"MF v[224:227],a[160:163],a[60:63],v[224:227]\n"
"WT lgkmcnt(12)\n"
"s_add_u32 s66, s67, 0\n"
"TR a[160:161],v12 offset:16384\n"
"MF v[196:199],a[164:167],a[56:59],v[196:199]\n"
"LD a[192:195],v1,s[40:43],s66 offen offset:0 sc1\n"
"MF v[228:231],a[164:167],a[60:63],v[228:231]\n"
"WT lgkmcnt(11)\n"
"TR a[162:163],v12 offset:18432\n"
"TR a[164:165],v13 offset:16384\n"
"MF v[200:203],a[168:171],a[56:59],v[200:203]\n"
"TR a[166:167],v13 offset:18432\n"
"MF v[232:235],a[168:171],a[60:63],v[232:235]\n"
"WT lgkmcnt(12)\n"
"s_add_u32 s66, s67, 65536\n"
"TR a[168:169],v14 offset:16384\n"
"MF v[204:207],a[172:175],a[56:59],v[204:207]\n"
"LD a[196:199],v1,s[40:43],s66 offen offset:0 sc1\n"
"MF v[236:239],a[172:175],a[60:63],v[236:239]\n"
"WT lgkmcnt(11)\n"
"TR a[170:171],v14 offset:18432\n"
"TR a[172:173],v15 offset:16384\n"
"MF v[208:211],a[176:179],a[56:59],v[208:211]\n"
"TR a[174:175],v15 offset:18432\n"
"MF v[240:243],a[176:179],a[60:63],v[240:243]\n"
"WT lgkmcnt(12)\n"
"s_add_u32 s66, s67, 131072\n"
"TR a[176:177],v12 offset:20480\n"
"MF v[212:215],a[180:183],a[56:59],v[212:215]\n"
"LD a[200:203],v1,s[40:43],s66 offen offset:0 sc1\n"
"MF v[244:247],a[180:183],a[60:63],v[244:247]\n"
"WT lgkmcnt(11)\n"
"TR a[178:179],v12 offset:22528\n"
"TR a[180:181],v13 offset:20480\n"
"MF v[216:219],a[184:187],a[56:59],v[216:219]\n"
"TR a[182:183],v13 offset:22528\n"
"MF v[248:251],a[184:187],a[60:63],v[248:251]\n"
"WT lgkmcnt(12)\n"
"s_add_u32 s66, s67, 196608\n"
"TR a[184:185],v14 offset:20480\n"
"MF v[220:223],a[188:191],a[56:59],v[220:223]\n"
"LD a[204:207],v1,s[40:43],s66 offen offset:0 sc1\n"
"MF v[252:255],a[188:191],a[60:63],v[252:255]\n"
"WT vmcnt(19) lgkmcnt(11)\n"
"TR a[186:187],v14 offset:22528\n"
"TR a[188:189],v15 offset:20480\n"
"MF v[192:195],a[160:163],a[64:67],v[192:195]\n"
"WT vmcnt(18)\n"
"TR a[190:191],v15 offset:22528\n"
"MF v[224:227],a[160:163],a[68:71],v[224:227]\n"
"WT lgkmcnt(12)\n"
"s_add_u32 s66, s67, 4096\n"
"TR a[160:161],v12 offset:24576\n"
"MF v[196:199],a[164:167],a[64:67],v[196:199]\n"
"LD a[208:211],v1,s[40:43],s66 offen offset:0 sc1\n"
"MF v[228:231],a[164:167],a[68:71],v[228:231]\n"
"WT lgkmcnt(11)\n"
"TR a[162:163],v12 offset:26624\n"
"TR a[164:165],v13 offset:24576\n"
"MF v[200:203],a[168:171],a[64:67],v[200:203]\n"
"TR a[166:167],v13 offset:26624\n"
"MF v[232:235],a[168:171],a[68:71],v[232:235]\n"
"WT lgkmcnt(12)\n"
"s_add_u32 s66, s67, 69632\n"
"TR a[168:169],v14 offset:24576\n"
"MF v[204:207],a[172:175],a[64:67],v[204:207]\n"
"LD a[212:215],v1,s[40:43],s66 offen offset:0 sc1\n"
"MF v[236:239],a[172:175],a[68:71],v[236:239]\n"
"WT lgkmcnt(11)\n"
"TR a[170:171],v14 offset:26624\n"
"TR a[172:173],v15 offset:24576\n"
"MF v[208:211],a[176:179],a[64:67],v[208:211]\n"
"TR a[174:175],v15 offset:26624\n"
"MF v[240:243],a[176:179],a[68:71],v[240:243]\n"
"WT lgkmcnt(12)\n"
"s_add_u32 s66, s67, 135168\n"
"TR a[176:177],v12 offset:28672\n"
"MF v[212:215],a[180:183],a[64:67],v[212:215]\n"
"LD a[216:219],v1,s[40:43],s66 offen offset:0 sc1\n"
"MF v[244:247],a[180:183],a[68:71],v[244:247]\n"
"WT lgkmcnt(11)\n"
"TR a[178:179],v12 offset:30720\n"
"TR a[180:181],v13 offset:28672\n"
"MF v[216:219],a[184:187],a[64:67],v[216:219]\n"
"TR a[182:183],v13 offset:30720\n"
"MF v[248:251],a[184:187],a[68:71],v[248:251]\n"
"WT lgkmcnt(12)\n"
"s_add_u32 s66, s67, 200704\n"
"TR a[184:185],v14 offset:28672\n"
"MF v[220:223],a[188:191],a[64:67],v[220:223]\n"
"LD a[220:223],v1,s[40:43],s66 offen offset:0 sc1\n"
"MF v[252:255],a[188:191],a[68:71],v[252:255]\n"
"WT vmcnt(21) lgkmcnt(11)\n"
"TR a[186:187],v14 offset:30720\n"
"TR a[188:189],v15 offset:28672\n"
"MF v[192:195],a[160:163],a[72:75],v[192:195]\n"
"WT vmcnt(20)\n"
"s_barrier\n"
"TR a[190:191],v15 offset:30720\n"
"MF v[224:227],a[160:163],a[76:79],v[224:227]\n"
"WT lgkmcnt(12)\n"
"s_add_u32 s66, s67, 0\n"
"TR a[160:161],v12 offset:32768\n"
"MF v[196:199],a[164:167],a[72:75],v[196:199]\n"
"LD a[224:227],v1,s[40:43],s66 offen offset:1024 sc1\n"
"MF v[228:231],a[164:167],a[76:79],v[228:231]\n"
"WT lgkmcnt(11)\n"
"TR a[162:163],v12 offset:34816\n"
"TR a[164:165],v13 offset:32768\n"
"MF v[200:203],a[168:171],a[72:75],v[200:203]\n"
"TR a[166:167],v13 offset:34816\n"
"MF v[232:235],a[168:171],a[76:79],v[232:235]\n"
"WT lgkmcnt(12)\n"
"s_add_u32 s66, s67, 65536\n"
"TR a[168:169],v14 offset:32768\n"
"MF v[204:207],a[172:175],a[72:75],v[204:207]\n"
"LD a[228:231],v1,s[40:43],s66 offen offset:1024 sc1\n"
"MF v[236:239],a[172:175],a[76:79],v[236:239]\n"
"WT lgkmcnt(11)\n"
"TR a[170:171],v14 offset:34816\n"
"TR a[172:173],v15 offset:32768\n"
"MF v[208:211],a[176:179],a[72:75],v[208:211]\n"
"TR a[174:175],v15 offset:34816\n"
"MF v[240:243],a[176:179],a[76:79],v[240:243]\n"
"WT lgkmcnt(12)\n"
"s_add_u32 s66, s67, 131072\n"
"TR a[176:177],v12 offset:36864\n"
"MF v[212:215],a[180:183],a[72:75],v[212:215]\n"
"LD a[232:235],v1,s[40:43],s66 offen offset:1024 sc1\n"
"MF v[244:247],a[180:183],a[76:79],v[244:247]\n"
"WT lgkmcnt(11)\n"
"TR a[178:179],v12 offset:38912\n"
"TR a[180:181],v13 offset:36864\n"
"MF v[216:219],a[184:187],a[72:75],v[216:219]\n"
"TR a[182:183],v13 offset:38912\n"
"MF v[248:251],a[184:187],a[76:79],v[248:251]\n"
"WT lgkmcnt(12)\n"
"s_add_u32 s66, s67, 196608\n"
"TR a[184:185],v14 offset:36864\n"
"MF v[220:223],a[188:191],a[72:75],v[220:223]\n"
"LD a[236:239],v1,s[40:43],s66 offen offset:1024 sc1\n"
"MF v[252:255],a[188:191],a[76:79],v[252:255]\n"
"WT vmcnt(23) lgkmcnt(11)\n"
"TR a[186:187],v14 offset:38912\n"
"TR a[188:189],v15 offset:36864\n"
"MF v[192:195],a[160:163],a[80:83],v[192:195]\n"
"WT vmcnt(22)\n"
"TR a[190:191],v15 offset:38912\n"
"MF v[224:227],a[160:163],a[84:87],v[224:227]\n"
"WT lgkmcnt(12)\n"
"s_add_u32 s66, s67, 4096\n"
"TR a[160:161],v12 offset:40960\n"
"MF v[196:199],a[164:167],a[80:83],v[196:199]\n"
"LD a[240:243],v1,s[40:43],s66 offen offset:1024 sc1\n"
"MF v[228:231],a[164:167],a[84:87],v[228:231]\n"
"WT lgkmcnt(11)\n"
"TR a[162:163],v12 offset:43008\n"
"TR a[164:165],v13 offset:40960\n"
"MF v[200:203],a[168:171],a[80:83],v[200:203]\n"
"TR a[166:167],v13 offset:43008\n"
"MF v[232:235],a[168:171],a[84:87],v[232:235]\n"
"WT lgkmcnt(12)\n"
"s_add_u32 s66, s67, 69632\n"
"TR a[168:169],v14 offset:40960\n"
"MF v[204:207],a[172:175],a[80:83],v[204:207]\n"
"LD a[244:247],v1,s[40:43],s66 offen offset:1024 sc1\n"
"MF v[236:239],a[172:175],a[84:87],v[236:239]\n"
"WT lgkmcnt(11)\n"
"TR a[170:171],v14 offset:43008\n"
"TR a[172:173],v15 offset:40960\n"
"MF v[208:211],a[176:179],a[80:83],v[208:211]\n"
"TR a[174:175],v15 offset:43008\n"
"MF v[240:243],a[176:179],a[84:87],v[240:243]\n"
"WT lgkmcnt(12)\n"
"s_add_u32 s66, s67, 135168\n"
"TR a[176:177],v12 offset:45056\n"
"MF v[212:215],a[180:183],a[80:83],v[212:215]\n"
"LD a[248:251],v1,s[40:43],s66 offen offset:1024 sc1\n"
"MF v[244:247],a[180:183],a[84:87],v[244:247]\n"
"WT lgkmcnt(11)\n"
"TR a[178:179],v12 offset:47104\n"
"TR a[180:181],v13 offset:45056\n"
"MF v[216:219],a[184:187],a[80:83],v[216:219]\n"
"TR a[182:183],v13 offset:47104\n"
"MF v[248:251],a[184:187],a[84:87],v[248:251]\n"
"WT lgkmcnt(12)\n"
"s_add_u32 s66, s67, 200704\n"
"TR a[184:185],v14 offset:45056\n"
"MF v[220:223],a[188:191],a[80:83],v[220:223]\n"
"LD a[252:255],v1,s[40:43],s66 offen offset:1024 sc1\n"
"MF v[252:255],a[188:191],a[84:87],v[252:255]\n"
"WT vmcnt(25) lgkmcnt(11)\n"
"TR a[186:187],v14 offset:47104\n"
"TR a[188:189],v15 offset:45056\n"
"MF v[192:195],a[160:163],a[88:91],v[192:195]\n"
"WT vmcnt(24)\n"
"TR a[190:191],v15 offset:47104\n"
"MF v[224:227],a[160:163],a[92:95],v[224:227]\n"
"WT lgkmcnt(12)\n"
"s_add_u32 s66, s67, 0\n"
"MF v[196:199],a[164:167],a[88:91],v[196:199]\n"
"LD a[112:115],v1,s[40:43],s66 offen offset:2048 sc1\n"
"MF v[228:231],a[164:167],a[92:95],v[228:231]\n"
"WT lgkmcnt(10)\n"
"MF v[200:203],a[168:171],a[88:91],v[200:203]\n"
"MF v[232:235],a[168:171],a[92:95],v[232:235]\n"
"WT lgkmcnt(8)\n"
"s_add_u32 s66, s67, 65536\n"
"MF v[204:207],a[172:175],a[88:91],v[204:207]\n"
"LD a[116:119],v1,s[40:43],s66 offen offset:2048 sc1\n"
"MF v[236:239],a[172:175],a[92:95],v[236:239]\n"
"WT lgkmcnt(6)\n"
"MF v[208:211],a[176:179],a[88:91],v[208:211]\n"
"MF v[240:243],a[176:179],a[92:95],v[240:243]\n"
"WT lgkmcnt(4)\n"
"s_add_u32 s66, s67, 131072\n"
"MF v[212:215],a[180:183],a[88:91],v[212:215]\n"
"LD a[120:123],v1,s[40:43],s66 offen offset:2048 sc1\n"
"MF v[244:247],a[180:183],a[92:95],v[244:247]\n"
"WT lgkmcnt(2)\n"
"MF v[216:219],a[184:187],a[88:91],v[216:219]\n"
"MF v[248:251],a[184:187],a[92:95],v[248:251]\n"
"WT lgkmcnt(0)\n"
"s_add_u32 s66, s67, 196608\n"
"MF v[220:223],a[188:191],a[88:91],v[220:223]\n"
"LD a[124:127],v1,s[40:43],s66 offen offset:2048 sc1\n"
"MF v[252:255],a[188:191],a[92:95],v[252:255]\n"
"s_mov_b32 s58, 0xbfb8aa3b\n"
"s_mov_b32 s48, s28\n"
"s_and_b32 s49, s29, 0xffff\n"
"s_mov_b32 s50, 0x280000\n"
"s_mov_b32 s51, 0x20000\n"
"v_pk_mul_f32 v[96:97], v[192:193], s[58:59] op_sel_hi:[1,0]\n"
"v_pk_mul_f32 v[98:99], v[194:195], s[58:59] op_sel_hi:[1,0]\n"
"v_pk_mul_f32 v[100:101], v[196:197], s[58:59] op_sel_hi:[1,0]\n"
"v_pk_mul_f32 v[102:103], v[198:199], s[58:59] op_sel_hi:[1,0]\n"
"v_pk_mul_f32 v[104:105], v[200:201], s[58:59] op_sel_hi:[1,0]\n"
"v_pk_mul_f32 v[106:107], v[202:203], s[58:59] op_sel_hi:[1,0]\n"
"v_pk_mul_f32 v[108:109], v[204:205], s[58:59] op_sel_hi:[1,0]\n"
"v_pk_mul_f32 v[110:111], v[206:207], s[58:59] op_sel_hi:[1,0]\n"
"v_pk_mul_f32 v[112:113], v[208:209], s[58:59] op_sel_hi:[1,0]\n"
"v_pk_mul_f32 v[114:115], v[210:211], s[58:59] op_sel_hi:[1,0]\n"
"v_pk_mul_f32 v[116:117], v[212:213], s[58:59] op_sel_hi:[1,0]\n"
"v_pk_mul_f32 v[118:119], v[214:215], s[58:59] op_sel_hi:[1,0]\n"
"v_pk_mul_f32 v[120:121], v[216:217], s[58:59] op_sel_hi:[1,0]\n"
"v_pk_mul_f32 v[122:123], v[218:219], s[58:59] op_sel_hi:[1,0]\n"
"v_pk_mul_f32 v[124:125], v[220:221], s[58:59] op_sel_hi:[1,0]\n"
"v_pk_mul_f32 v[126:127], v[222:223], s[58:59] op_sel_hi:[1,0]\n"
"v_pk_mul_f32 v[128:129], v[224:225], s[58:59] op_sel_hi:[1,0]\n"
"v_pk_mul_f32 v[130:131], v[226:227], s[58:59] op_sel_hi:[1,0]\n"
"v_pk_mul_f32 v[132:133], v[228:229], s[58:59] op_sel_hi:[1,0]\n"
"v_pk_mul_f32 v[134:135], v[230:231], s[58:59] op_sel_hi:[1,0]\n"
"v_pk_mul_f32 v[136:137], v[232:233], s[58:59] op_sel_hi:[1,0]\n"
"v_pk_mul_f32 v[138:139], v[234:235], s[58:59] op_sel_hi:[1,0]\n"
"v_pk_mul_f32 v[140:141], v[236:237], s[58:59] op_sel_hi:[1,0]\n"
"v_pk_mul_f32 v[142:143], v[238:239], s[58:59] op_sel_hi:[1,0]\n"
"v_pk_mul_f32 v[144:145], v[240:241], s[58:59] op_sel_hi:[1,0]\n"
"v_pk_mul_f32 v[146:147], v[242:243], s[58:59] op_sel_hi:[1,0]\n"
"v_pk_mul_f32 v[148:149], v[244:245], s[58:59] op_sel_hi:[1,0]\n"
"v_pk_mul_f32 v[150:151], v[246:247], s[58:59] op_sel_hi:[1,0]\n"
"v_pk_mul_f32 v[152:153], v[248:249], s[58:59] op_sel_hi:[1,0]\n"
"v_pk_mul_f32 v[154:155], v[250:251], s[58:59] op_sel_hi:[1,0]\n"
"v_pk_mul_f32 v[156:157], v[252:253], s[58:59] op_sel_hi:[1,0]\n"
"v_pk_mul_f32 v[158:159], v[254:255], s[58:59] op_sel_hi:[1,0]\n"
"EX v96,v96\n"
"EX v97,v97\n"
"EX v98,v98\n"
"EX v99,v99\n"
"s_add_u32 s66, s67, 4096\n"
"EX v100,v100\n"
"LD a[128:131],v1,s[40:43],s66 offen offset:2048 sc1\n"
"EX v101,v101\n"
"EX v102,v102\n"
"EX v103,v103\n"
"EX v104,v104\n"
"s_add_u32 s66, s67, 69632\n"
"EX v105,v105\n"
"LD a[132:135],v1,s[40:43],s66 offen offset:2048 sc1\n"
"EX v106,v106\n"
"EX v107,v107\n"
"EX v108,v108\n"
"EX v109,v109\n"
"s_add_u32 s66, s67, 135168\n"
"EX v110,v110\n"
"LD a[136:139],v1,s[40:43],s66 offen offset:2048 sc1\n"
"EX v111,v111\n"
"EX v112,v112\n"
"EX v113,v113\n"
"EX v114,v114\n"
"s_add_u32 s66, s67, 200704\n"
"EX v115,v115\n"
"LD a[140:143],v1,s[40:43],s66 offen offset:2048 sc1\n"
"EX v116,v116\n"
"EX v117,v117\n"
"EX v118,v118\n"
"EX v119,v119\n"
"s_add_u32 s66, s67, 0\n"
"EX v120,v120\n"
"LD a[0:3],v1,s[40:43],s66 offen offset:3072 sc1\n"
"EX v121,v121\n"
"EX v122,v122\n"
"EX v123,v123\n"
"EX v124,v124\n"
"s_add_u32 s66, s67, 65536\n"
"EX v125,v125\n"
"LD a[4:7],v1,s[40:43],s66 offen offset:3072 sc1\n"
"EX v126,v126\n"
"EX v127,v127\n"
"EX v128,v128\n"
"EX v129,v129\n"
"s_add_u32 s66, s67, 131072\n"
"EX v130,v130\n"
"LD a[8:11],v1,s[40:43],s66 offen offset:3072 sc1\n"
"EX v131,v131\n"
"EX v132,v132\n"
"EX v133,v133\n"
"EX v134,v134\n"
"s_add_u32 s66, s67, 196608\n"
"EX v135,v135\n"
"LD a[12:15],v1,s[40:43],s66 offen offset:3072 sc1\n"
"EX v136,v136\n"
"EX v137,v137\n"
"EX v138,v138\n"
"EX v139,v139\n"
"s_add_u32 s66, s67, 4096\n"
"EX v140,v140\n"
"LD a[16:19],v1,s[40:43],s66 offen offset:3072 sc1\n"
"EX v141,v141\n"
"EX v142,v142\n"
"EX v143,v143\n"
"EX v144,v144\n"
"s_add_u32 s66, s67, 69632\n"
"EX v145,v145\n"
"LD a[20:23],v1,s[40:43],s66 offen offset:3072 sc1\n"
"EX v146,v146\n"
"EX v147,v147\n"
"EX v148,v148\n"
"EX v149,v149\n"
"s_add_u32 s66, s67, 135168\n"
"EX v150,v150\n"
"LD a[24:27],v1,s[40:43],s66 offen offset:3072 sc1\n"
"EX v151,v151\n"
"EX v152,v152\n"
"EX v153,v153\n"
"EX v154,v154\n"
"s_add_u32 s66, s67, 200704\n"
"EX v155,v155\n"
"LD a[28:31],v1,s[40:43],s66 offen offset:3072 sc1\n"
"EX v156,v156\n"
"EX v157,v157\n"
"EX v158,v158\n"
"EX v159,v159\n"
"v_pk_add_f32 v[96:97], v[96:97], 1.0 op_sel_hi:[1,0]\n"
"v_pk_add_f32 v[98:99], v[98:99], 1.0 op_sel_hi:[1,0]\n"
"v_pk_add_f32 v[100:101], v[100:101], 1.0 op_sel_hi:[1,0]\n"
"v_pk_add_f32 v[102:103], v[102:103], 1.0 op_sel_hi:[1,0]\n"
"v_pk_add_f32 v[104:105], v[104:105], 1.0 op_sel_hi:[1,0]\n"
"v_pk_add_f32 v[106:107], v[106:107], 1.0 op_sel_hi:[1,0]\n"
"v_pk_add_f32 v[108:109], v[108:109], 1.0 op_sel_hi:[1,0]\n"
"v_pk_add_f32 v[110:111], v[110:111], 1.0 op_sel_hi:[1,0]\n"
"v_pk_add_f32 v[112:113], v[112:113], 1.0 op_sel_hi:[1,0]\n"
"v_pk_add_f32 v[114:115], v[114:115], 1.0 op_sel_hi:[1,0]\n"
"v_pk_add_f32 v[116:117], v[116:117], 1.0 op_sel_hi:[1,0]\n"
"v_pk_add_f32 v[118:119], v[118:119], 1.0 op_sel_hi:[1,0]\n"
"v_pk_add_f32 v[120:121], v[120:121], 1.0 op_sel_hi:[1,0]\n"
"v_pk_add_f32 v[122:123], v[122:123], 1.0 op_sel_hi:[1,0]\n"
"v_pk_add_f32 v[124:125], v[124:125], 1.0 op_sel_hi:[1,0]\n"
"v_pk_add_f32 v[126:127], v[126:127], 1.0 op_sel_hi:[1,0]\n"
"v_pk_add_f32 v[128:129], v[128:129], 1.0 op_sel_hi:[1,0]\n"
"v_pk_add_f32 v[130:131], v[130:131], 1.0 op_sel_hi:[1,0]\n"
"v_pk_add_f32 v[132:133], v[132:133], 1.0 op_sel_hi:[1,0]\n"
"v_pk_add_f32 v[134:135], v[134:135], 1.0 op_sel_hi:[1,0]\n"
"v_pk_add_f32 v[136:137], v[136:137], 1.0 op_sel_hi:[1,0]\n"
"v_pk_add_f32 v[138:139], v[138:139], 1.0 op_sel_hi:[1,0]\n"
"v_pk_add_f32 v[140:141], v[140:141], 1.0 op_sel_hi:[1,0]\n"
"v_pk_add_f32 v[142:143], v[142:143], 1.0 op_sel_hi:[1,0]\n"
"v_pk_add_f32 v[144:145], v[144:145], 1.0 op_sel_hi:[1,0]\n"
"v_pk_add_f32 v[146:147], v[146:147], 1.0 op_sel_hi:[1,0]\n"
"v_pk_add_f32 v[148:149], v[148:149], 1.0 op_sel_hi:[1,0]\n"
"v_pk_add_f32 v[150:151], v[150:151], 1.0 op_sel_hi:[1,0]\n"
"v_pk_add_f32 v[152:153], v[152:153], 1.0 op_sel_hi:[1,0]\n"
"v_pk_add_f32 v[154:155], v[154:155], 1.0 op_sel_hi:[1,0]\n"
"v_pk_add_f32 v[156:157], v[156:157], 1.0 op_sel_hi:[1,0]\n"
"v_pk_add_f32 v[158:159], v[158:159], 1.0 op_sel_hi:[1,0]\n"
"WT vmcnt(32)\n"
"WT vmcnt(31)\n"
"MF v[32:35],a[144:147],a[192:195],0\n"
"v_rcp_f32 v96, v96\n"
"MF v[36:39],a[148:151],a[192:195],0\n"
"v_rcp_f32 v97, v97\n"
"WT vmcnt(27)\n"
"MF v[64:67],a[144:147],a[208:211],0\n"
"v_rcp_f32 v98, v98\n"
"MF v[68:71],a[148:151],a[208:211],0\n"
"v_rcp_f32 v99, v99\n"
"MF v[32:35],a[144:147],a[196:199],v[32:35]\n"
"v_rcp_f32 v100, v100\n"
"MF v[36:39],a[148:151],a[196:199],v[36:39]\n"
"v_rcp_f32 v101, v101\n"
"WT vmcnt(26)\n"
"MF v[64:67],a[144:147],a[212:215],v[64:67]\n"
"v_rcp_f32 v102, v102\n"
"MF v[68:71],a[148:151],a[212:215],v[68:71]\n"
"v_rcp_f32 v103, v103\n"
"MF v[32:35],a[144:147],a[200:203],v[32:35]\n"
"v_rcp_f32 v128, v128\n"
"MF v[36:39],a[148:151],a[200:203],v[36:39]\n"
"v_rcp_f32 v129, v129\n"
"WT vmcnt(25)\n"
"MF v[64:67],a[144:147],a[216:219],v[64:67]\n"
"v_rcp_f32 v130, v130\n"
"MF v[68:71],a[148:151],a[216:219],v[68:71]\n"
"v_rcp_f32 v131, v131\n"
"MF v[32:35],a[144:147],a[204:207],v[32:35]\n"
"v_rcp_f32 v132, v132\n"
"MF v[36:39],a[148:151],a[204:207],v[36:39]\n"
"v_rcp_f32 v133, v133\n"
"WT vmcnt(24)\n"
"MF v[64:67],a[144:147],a[220:223],v[64:67]\n"
"v_rcp_f32 v134, v134\n"
"MF v[68:71],a[148:151],a[220:223],v[68:71]\n"
"v_rcp_f32 v135, v135\n"
"v_pk_mul_f32 v[192:193], v[192:193], v[96:97]\n"
"v_pk_mul_f32 v[194:195], v[194:195], v[98:99]\n"
"v_pk_mul_f32 v[196:197], v[196:197], v[100:101]\n"
"v_pk_mul_f32 v[198:199], v[198:199], v[102:103]\n"
"v_pk_mul_f32 v[224:225], v[224:225], v[128:129]\n"
"v_pk_mul_f32 v[226:227], v[226:227], v[130:131]\n"
"v_pk_mul_f32 v[228:229], v[228:229], v[132:133]\n"
"v_pk_mul_f32 v[230:231], v[230:231], v[134:135]\n"
"MF v[96:99],a[144:147],v[160:163],0\n"
"MF v[100:103],a[148:151],v[160:163],0\n"
"MF v[128:131],a[144:147],v[176:179],0\n"
"MF v[132:135],a[148:151],v[176:179],0\n"
"v_cmp_u_f32 vcc, v32, v64\n"
"s_nop 1\n"
"s_cmp_eq_u64 vcc, 0\n"
"s_cbranch_scc0 L_pr_52\n"
"L_po_53:\n"
"v_pk_fma_f32 v[192:193], v[192:193], v[32:33], v[96:97]\n"
"v_pk_fma_f32 v[194:195], v[194:195], v[34:35], v[98:99]\n"
"v_pk_fma_f32 v[196:197], v[196:197], v[36:37], v[100:101]\n"
"v_pk_fma_f32 v[198:199], v[198:199], v[38:39], v[102:103]\n"
"v_pk_fma_f32 v[224:225], v[224:225], v[64:65], v[128:129]\n"
"v_pk_fma_f32 v[226:227], v[226:227], v[66:67], v[130:131]\n"
"v_pk_fma_f32 v[228:229], v[228:229], v[68:69], v[132:133]\n"
"v_pk_fma_f32 v[230:231], v[230:231], v[70:71], v[134:135]\n"
"CV v192,v192,v193\n"
"CV v193,v194,v195\n"
"CV v194,v196,v197\n"
"CV v195,v198,v199\n"
"CV v224,v224,v225\n"
"CV v225,v226,v227\n"
"CV v226,v228,v229\n"
"CV v227,v230,v231\n"
"v_permlane16_swap_b32 v192, v194\n"
"v_permlane16_swap_b32 v193, v195\n"
"v_permlane16_swap_b32 v224, v226\n"
"v_permlane16_swap_b32 v225, v227\n"
"WT vmcnt(0)\n"
"ST v[192:195],v22,s[48:51],s62 offen offset:0\n"
"ST v[224:227],v22,s[48:51],s63 offen offset:0\n"
"MF v[40:43],a[144:147],a[224:227],0\n"
"v_rcp_f32 v104, v104\n"
"MF v[44:47],a[148:151],a[224:227],0\n"
"v_rcp_f32 v105, v105\n"
"MF v[72:75],a[144:147],a[240:243],0\n"
"v_rcp_f32 v106, v106\n"
"MF v[76:79],a[148:151],a[240:243],0\n"
"v_rcp_f32 v107, v107\n"
"MF v[40:43],a[144:147],a[228:231],v[40:43]\n"
"v_rcp_f32 v108, v108\n"
"MF v[44:47],a[148:151],a[228:231],v[44:47]\n"
"v_rcp_f32 v109, v109\n"
"MF v[72:75],a[144:147],a[244:247],v[72:75]\n"
"v_rcp_f32 v110, v110\n"
"MF v[76:79],a[148:151],a[244:247],v[76:79]\n"
"v_rcp_f32 v111, v111\n"
"MF v[40:43],a[144:147],a[232:235],v[40:43]\n"
"v_rcp_f32 v136, v136\n"
"MF v[44:47],a[148:151],a[232:235],v[44:47]\n"
"v_rcp_f32 v137, v137\n"
"MF v[72:75],a[144:147],a[248:251],v[72:75]\n"
"v_rcp_f32 v138, v138\n"
"MF v[76:79],a[148:151],a[248:251],v[76:79]\n"
"v_rcp_f32 v139, v139\n"
"MF v[40:43],a[144:147],a[236:239],v[40:43]\n"
"v_rcp_f32 v140, v140\n"
"MF v[44:47],a[148:151],a[236:239],v[44:47]\n"
"v_rcp_f32 v141, v141\n"
"MF v[72:75],a[144:147],a[252:255],v[72:75]\n"
"v_rcp_f32 v142, v142\n"
"MF v[76:79],a[148:151],a[252:255],v[76:79]\n"
"v_rcp_f32 v143, v143\n"
"v_pk_mul_f32 v[200:201], v[200:201], v[104:105]\n"
"v_pk_mul_f32 v[202:203], v[202:203], v[106:107]\n"
"v_pk_mul_f32 v[204:205], v[204:205], v[108:109]\n"
"v_pk_mul_f32 v[206:207], v[206:207], v[110:111]\n"
"v_pk_mul_f32 v[232:233], v[232:233], v[136:137]\n"
"v_pk_mul_f32 v[234:235], v[234:235], v[138:139]\n"
"v_pk_mul_f32 v[236:237], v[236:237], v[140:141]\n"
"v_pk_mul_f32 v[238:239], v[238:239], v[142:143]\n"
"MF v[104:107],a[144:147],v[164:167],0\n"
"MF v[108:111],a[148:151],v[164:167],0\n"
"MF v[136:139],a[144:147],v[180:183],0\n"
"MF v[140:143],a[148:151],v[180:183],0\n"
"v_cmp_u_f32 vcc, v40, v72\n"
"s_nop 1\n"
"s_cmp_eq_u64 vcc, 0\n"
"s_cbranch_scc0 L_pr_54\n"
"L_po_55:\n"
"v_pk_fma_f32 v[200:201], v[200:201], v[40:41], v[104:105]\n"
"v_pk_fma_f32 v[202:203], v[202:203], v[42:43], v[106:107]\n"
"v_pk_fma_f32 v[204:205], v[204:205], v[44:45], v[108:109]\n"
"v_pk_fma_f32 v[206:207], v[206:207], v[46:47], v[110:111]\n"
"v_pk_fma_f32 v[232:233], v[232:233], v[72:73], v[136:137]\n"
"v_pk_fma_f32 v[234:235], v[234:235], v[74:75], v[138:139]\n"
"v_pk_fma_f32 v[236:237], v[236:237], v[76:77], v[140:141]\n"
"v_pk_fma_f32 v[238:239], v[238:239], v[78:79], v[142:143]\n"
"CV v200,v200,v201\n"
"CV v201,v202,v203\n"
"CV v202,v204,v205\n"
"CV v203,v206,v207\n"
"CV v232,v232,v233\n"
"CV v233,v234,v235\n"
"CV v234,v236,v237\n"
"CV v235,v238,v239\n"
"v_permlane16_swap_b32 v200, v202\n"
"v_permlane16_swap_b32 v201, v203\n"
"v_permlane16_swap_b32 v232, v234\n"
"v_permlane16_swap_b32 v233, v235\n"
"ST v[200:203],v22,s[48:51],s62 offen offset:64\n"
"ST v[232:235],v22,s[48:51],s63 offen offset:64\n"
"MF v[48:51],a[144:147],a[112:115],0\n"
"v_rcp_f32 v112, v112\n"
"MF v[52:55],a[148:151],a[112:115],0\n"
"v_rcp_f32 v113, v113\n"
"MF v[80:83],a[144:147],a[128:131],0\n"
"v_rcp_f32 v114, v114\n"
"MF v[84:87],a[148:151],a[128:131],0\n"
"v_rcp_f32 v115, v115\n"
"MF v[48:51],a[144:147],a[116:119],v[48:51]\n"
"v_rcp_f32 v116, v116\n"
"MF v[52:55],a[148:151],a[116:119],v[52:55]\n"
"v_rcp_f32 v117, v117\n"
"MF v[80:83],a[144:147],a[132:135],v[80:83]\n"
"v_rcp_f32 v118, v118\n"
"MF v[84:87],a[148:151],a[132:135],v[84:87]\n"
"v_rcp_f32 v119, v119\n"
"MF v[48:51],a[144:147],a[120:123],v[48:51]\n"
"v_rcp_f32 v144, v144\n"
"MF v[52:55],a[148:151],a[120:123],v[52:55]\n"
"v_rcp_f32 v145, v145\n"
"MF v[80:83],a[144:147],a[136:139],v[80:83]\n"
"v_rcp_f32 v146, v146\n"
"MF v[84:87],a[148:151],a[136:139],v[84:87]\n"
"v_rcp_f32 v147, v147\n"
"MF v[48:51],a[144:147],a[124:127],v[48:51]\n"
"v_rcp_f32 v148, v148\n"
"MF v[52:55],a[148:151],a[124:127],v[52:55]\n"
"v_rcp_f32 v149, v149\n"
"MF v[80:83],a[144:147],a[140:143],v[80:83]\n"
"v_rcp_f32 v150, v150\n"
"MF v[84:87],a[148:151],a[140:143],v[84:87]\n"
"v_rcp_f32 v151, v151\n"
"v_pk_mul_f32 v[208:209], v[208:209], v[112:113]\n"
"v_pk_mul_f32 v[210:211], v[210:211], v[114:115]\n"
"v_pk_mul_f32 v[212:213], v[212:213], v[116:117]\n"
"v_pk_mul_f32 v[214:215], v[214:215], v[118:119]\n"
"v_pk_mul_f32 v[240:241], v[240:241], v[144:145]\n"
"v_pk_mul_f32 v[242:243], v[242:243], v[146:147]\n"
"v_pk_mul_f32 v[244:245], v[244:245], v[148:149]\n"
"v_pk_mul_f32 v[246:247], v[246:247], v[150:151]\n"
"MF v[112:115],a[144:147],v[168:171],0\n"
"MF v[116:119],a[148:151],v[168:171],0\n"
"MF v[144:147],a[144:147],v[184:187],0\n"
"MF v[148:151],a[148:151],v[184:187],0\n"
"v_cmp_u_f32 vcc, v48, v80\n"
"s_nop 1\n"
"s_cmp_eq_u64 vcc, 0\n"
"s_cbranch_scc0 L_pr_56\n"
"L_po_57:\n"
"v_pk_fma_f32 v[208:209], v[208:209], v[48:49], v[112:113]\n"
"v_pk_fma_f32 v[210:211], v[210:211], v[50:51], v[114:115]\n"
"v_pk_fma_f32 v[212:213], v[212:213], v[52:53], v[116:117]\n"
"v_pk_fma_f32 v[214:215], v[214:215], v[54:55], v[118:119]\n"
"v_pk_fma_f32 v[240:241], v[240:241], v[80:81], v[144:145]\n"
"v_pk_fma_f32 v[242:243], v[242:243], v[82:83], v[146:147]\n"
"v_pk_fma_f32 v[244:245], v[244:245], v[84:85], v[148:149]\n"
"v_pk_fma_f32 v[246:247], v[246:247], v[86:87], v[150:151]\n"
"CV v208,v208,v209\n"
"CV v209,v210,v211\n"
"CV v210,v212,v213\n"
"CV v211,v214,v215\n"
"CV v240,v240,v241\n"
"CV v241,v242,v243\n"
"CV v242,v244,v245\n"
"CV v243,v246,v247\n"
"v_permlane16_swap_b32 v208, v210\n"
"v_permlane16_swap_b32 v209, v211\n"
"v_permlane16_swap_b32 v240, v242\n"
"v_permlane16_swap_b32 v241, v243\n"
"ST v[208:211],v22,s[48:51],s62 offen offset:128\n"
"ST v[240:243],v22,s[48:51],s63 offen offset:128\n"
"MF v[56:59],a[144:147],a[0:3],0\n"
"v_rcp_f32 v120, v120\n"
"MF v[60:63],a[148:151],a[0:3],0\n"
"v_rcp_f32 v121, v121\n"
"MF v[88:91],a[144:147],a[16:19],0\n"
"v_rcp_f32 v122, v122\n"
"MF v[92:95],a[148:151],a[16:19],0\n"
"v_rcp_f32 v123, v123\n"
"MF v[56:59],a[144:147],a[4:7],v[56:59]\n"
"v_rcp_f32 v124, v124\n"
"MF v[60:63],a[148:151],a[4:7],v[60:63]\n"
"v_rcp_f32 v125, v125\n"
"MF v[88:91],a[144:147],a[20:23],v[88:91]\n"
"v_rcp_f32 v126, v126\n"
"MF v[92:95],a[148:151],a[20:23],v[92:95]\n"
"v_rcp_f32 v127, v127\n"
"MF v[56:59],a[144:147],a[8:11],v[56:59]\n"
"v_rcp_f32 v152, v152\n"
"MF v[60:63],a[148:151],a[8:11],v[60:63]\n"
"v_rcp_f32 v153, v153\n"
"MF v[88:91],a[144:147],a[24:27],v[88:91]\n"
"v_rcp_f32 v154, v154\n"
"MF v[92:95],a[148:151],a[24:27],v[92:95]\n"
"v_rcp_f32 v155, v155\n"
"MF v[56:59],a[144:147],a[12:15],v[56:59]\n"
"v_rcp_f32 v156, v156\n"
"MF v[60:63],a[148:151],a[12:15],v[60:63]\n"
"v_rcp_f32 v157, v157\n"
"MF v[88:91],a[144:147],a[28:31],v[88:91]\n"
"v_rcp_f32 v158, v158\n"
"MF v[92:95],a[148:151],a[28:31],v[92:95]\n"
"v_rcp_f32 v159, v159\n"
"v_pk_mul_f32 v[216:217], v[216:217], v[120:121]\n"
"v_pk_mul_f32 v[218:219], v[218:219], v[122:123]\n"
"v_pk_mul_f32 v[220:221], v[220:221], v[124:125]\n"
"v_pk_mul_f32 v[222:223], v[222:223], v[126:127]\n"
"v_pk_mul_f32 v[248:249], v[248:249], v[152:153]\n"
"v_pk_mul_f32 v[250:251], v[250:251], v[154:155]\n"
"v_pk_mul_f32 v[252:253], v[252:253], v[156:157]\n"
"v_pk_mul_f32 v[254:255], v[254:255], v[158:159]\n"
"MF v[120:123],a[144:147],v[172:175],0\n"
"MF v[124:127],a[148:151],v[172:175],0\n"
"MF v[152:155],a[144:147],v[188:191],0\n"
"MF v[156:159],a[148:151],v[188:191],0\n"
"v_cmp_u_f32 vcc, v56, v88\n"
"s_nop 1\n"
"s_cmp_eq_u64 vcc, 0\n"
"s_cbranch_scc0 L_pr_58\n"
"L_po_59:\n"
"v_pk_fma_f32 v[216:217], v[216:217], v[56:57], v[120:121]\n"
"v_pk_fma_f32 v[218:219], v[218:219], v[58:59], v[122:123]\n"
"v_pk_fma_f32 v[220:221], v[220:221], v[60:61], v[124:125]\n"
"v_pk_fma_f32 v[222:223], v[222:223], v[62:63], v[126:127]\n"
"v_pk_fma_f32 v[248:249], v[248:249], v[88:89], v[152:153]\n"
"v_pk_fma_f32 v[250:251], v[250:251], v[90:91], v[154:155]\n"
"v_pk_fma_f32 v[252:253], v[252:253], v[92:93], v[156:157]\n"
"v_pk_fma_f32 v[254:255], v[254:255], v[94:95], v[158:159]\n"
"CV v216,v216,v217\n"
"CV v217,v218,v219\n"
"CV v218,v220,v221\n"
"CV v219,v222,v223\n"
"CV v248,v248,v249\n"
"CV v249,v250,v251\n"
"CV v250,v252,v253\n"
"CV v251,v254,v255\n"
"v_permlane16_swap_b32 v216, v218\n"
"v_permlane16_swap_b32 v217, v219\n"
"v_permlane16_swap_b32 v248, v250\n"
"v_permlane16_swap_b32 v249, v251\n"
"ST v[216:219],v22,s[48:51],s62 offen offset:192\n"
"ST v[248:251],v22,s[48:51],s63 offen offset:192\n"
"s_branch L_end_35\n"
"L_idle_38:\n"
"s_cmp_lg_u32 s33, 255\n"
"s_cbranch_scc1 L_end_35\n"
"s_lshl_b32 s56, s34, 6\n"
"v_add_u32 v21, s56, v0\n"
"v_lshlrev_b32 v21, 7, v21\n"
"v_add_u32 v20, 0x48000, v21\n"
"buffer_load_dwordx2 v[18:19], v20, s[40:43], 0 offen sc1\n"
"WT vmcnt(0)\n"
"v_cmp_ne_u64 vcc, s[38:39], v[18:19]\n"
"s_nop 1\n"
"s_cmp_eq_u64 vcc, 0\n"
"s_cbranch_scc0 L_psp_61\n"
"L_pok_60:\n"
"s_barrier\n"
"s_cmp_lg_u32 s34, 0\n"
"s_cbranch_scc1 L_end_35\n"
"s_mov_b64 exec, 1\n"
"v_mov_b32 v21, 0\n"
"buffer_store_dwordx2 v[16:17], v21, s[40:43], 0 offen sc1\n"
"s_mov_b64 exec, -1\n"
"L_end_35:\n"
"WT vmcnt(0) lgkmcnt(0)\n"
"s_endpgm\n"
"L_p0_2:\n"
"v_and_b32 v120, 15, v0\n"
"v_mul_u32_u24 v121, 320, v120\n"
"v_lshrrev_b32 v23, 4, v0\n"
"v_lshl_add_u32 v121, v23, 3, v121\n"
"v_add_u32 v121, s59, v121\n"
"s_mov_b32 s56, s10\n"
"s_and_b32 s57, s11, 0xffff\n"
"s_mov_b32 s58, 0x200000\n"
"s_mov_b32 s59, 0x20000\n"
"v_min_u32 v23, 15, v0\n"
"v_lshlrev_b32 v23, 7, v23\n"
"s_lshl_b32 s73, s33, 2\n"
"s_add_u32 s73, s73, s34\n"
"s_mul_i32 s73, s73, 2048\n"
"buffer_load_dword a255, v23, s[56:59], s73 offen\n"
"s_mov_b32 s56, s14\n"
"s_and_b32 s57, s15, 0xffff\n"
"s_mov_b32 s58, 0x400000\n"
"s_mov_b32 s59, 0x20000\n"
"v_lshrrev_b32 v23, 3, v0\n"
"v_min_u32 v23, 3, v23\n"
"v_lshlrev_b32 v23, 13, v23\n"
"v_and_b32 v22, 7, v0\n"
"v_lshl_add_u32 v23, v22, 7, v23\n"
"s_lshl_b32 s73, s37, 2\n"
"s_add_u32 s73, s73, s34\n"
"s_mul_i32 s73, s73, 32\n"
"s_add_u32 s73, s73, s72\n"
"s_lshl_b32 s73, s73, 10\n"
"buffer_load_dword a255, v23, s[56:59], s73 offen\n"
"s_lshl_b32 s73, s33, 2\n"
"s_add_u32 s73, s73, s34\n"
"s_mul_i32 s56, s73, 2560\n"
"s_add_u32 s56, s56, 0x700000\n"
"v_min_u32 v23, 19, v0\n"
"v_lshlrev_b32 v23, 7, v23\n"
"v_add_u32 v23, s56, v23\n"
"s_lshl_b32 s56, s33, 7\n"
"s_add_u32 s56, s56, 0xfff40000\n"
"v_lshlrev_b32 v22, 15, v0\n"
"v_add_u32 v22, s56, v22\n"
"v_cmp_lt_u32 vcc, 31, v0\n"
"s_nop 1\n"
"v_cndmask_b32 v23, v23, v22, vcc\n"
"s_lshl_b32 s56, s73, 7\n"
"s_add_u32 s56, s56, 0xffbe0000\n"
"v_min_u32 v22, 39, v0\n"
"v_lshlrev_b32 v22, 17, v22\n"
"v_add_u32 v22, s56, v22\n"
"v_cmp_lt_u32 vcc, 36, v0\n"
"s_nop 1\n"
"v_cndmask_b32 v23, v23, v22, vcc\n"
"buffer_load_dword a255, v23, s[40:43], 0 offen sc1\n"
"s_mov_b32 s59, s33\n"
"v_lshlrev_b32 v120, 5, v120\n"
"v_add_u32 v120, 0x26000, v120\n"
"s_lshl_b32 s56, s34, 3\n"
"v_add_u32 v111, s56, v120\n"
"WT vmcnt(15)\n"
"L_ln_4:\n"
"ds_read_b64 v[32:33], v121 offset:0\n"
"ds_read_b64 v[34:35], v121 offset:32\n"
"ds_read_b64 v[36:37], v121 offset:64\n"
"ds_read_b64 v[38:39], v121 offset:96\n"
"ds_read_b64 v[40:41], v121 offset:128\n"
"ds_read_b64 v[42:43], v121 offset:160\n"
"ds_read_b64 v[44:45], v121 offset:192\n"
"ds_read_b64 v[46:47], v121 offset:224\n"
"ds_read_b64 v[48:49], v121 offset:256\n"
"ds_read_b64 v[50:51], v121 offset:288\n"
"WT lgkmcnt(0)\n"
"s_mul_i32 s57, s59, 20480\n"
"s_mul_i32 s56, s34, 5120\n"
"s_add_u32 s57, s57, s56\n"
"s_add_u32 s58, s57, 0xe00000\n"
"s_add_u32 s56, s58, 4096\n"
"ST v[32:35],v1,s[40:43],s58 offen offset:0 sc1\n"
"ST v[36:39],v1,s[40:43],s58 offen offset:1024 sc1\n"
"ST v[40:43],v1,s[40:43],s58 offen offset:2048 sc1\n"
"ST v[44:47],v1,s[40:43],s58 offen offset:3072 sc1\n"
"ST v[48:51],v1,s[40:43],s56 offen offset:0 sc1\n"
"s_mov_b32 s73, 0x100000\n"
"L_lnrep_63:\n"
"v_lshlrev_b32 v64, 16, v32\n"
"v_and_b32 v65, 0xffff0000, v32\n"
"v_lshlrev_b32 v66, 16, v33\n"
"v_and_b32 v67, 0xffff0000, v33\n"
"v_lshlrev_b32 v68, 16, v34\n"
"v_and_b32 v69, 0xffff0000, v34\n"
"v_lshlrev_b32 v70, 16, v35\n"
"v_and_b32 v71, 0xffff0000, v35\n"
"v_lshlrev_b32 v72, 16, v36\n"
"v_and_b32 v73, 0xffff0000, v36\n"
"v_lshlrev_b32 v74, 16, v37\n"
"v_and_b32 v75, 0xffff0000, v37\n"
"v_lshlrev_b32 v76, 16, v38\n"
"v_and_b32 v77, 0xffff0000, v38\n"
"v_lshlrev_b32 v78, 16, v39\n"
"v_and_b32 v79, 0xffff0000, v39\n"
"v_lshlrev_b32 v80, 16, v40\n"
"v_and_b32 v81, 0xffff0000, v40\n"
"v_lshlrev_b32 v82, 16, v41\n"
"v_and_b32 v83, 0xffff0000, v41\n"
"v_lshlrev_b32 v84, 16, v42\n"
"v_and_b32 v85, 0xffff0000, v42\n"
"v_lshlrev_b32 v86, 16, v43\n"
"v_and_b32 v87, 0xffff0000, v43\n"
"v_lshlrev_b32 v88, 16, v44\n"
"v_and_b32 v89, 0xffff0000, v44\n"
"v_lshlrev_b32 v90, 16, v45\n"
"v_and_b32 v91, 0xffff0000, v45\n"
"v_lshlrev_b32 v92, 16, v46\n"
"v_and_b32 v93, 0xffff0000, v46\n"
"v_lshlrev_b32 v94, 16, v47\n"
"v_and_b32 v95, 0xffff0000, v47\n"
"v_lshlrev_b32 v96, 16, v48\n"
"v_and_b32 v97, 0xffff0000, v48\n"
"v_lshlrev_b32 v98, 16, v49\n"
"v_and_b32 v99, 0xffff0000, v49\n"
"v_lshlrev_b32 v100, 16, v50\n"
"v_and_b32 v101, 0xffff0000, v50\n"
"v_lshlrev_b32 v102, 16, v51\n"
"v_and_b32 v103, 0xffff0000, v51\n"
"v_pk_add_f32 v[104:105], v[64:65], v[66:67]\n"
"v_pk_mul_f32 v[106:107], v[64:65], v[64:65]\n"
"v_pk_fma_f32 v[106:107], v[66:67], v[66:67], v[106:107]\n"
"v_pk_add_f32 v[104:105], v[104:105], v[68:69]\n"
"v_pk_fma_f32 v[106:107], v[68:69], v[68:69], v[106:107]\n"
"v_pk_add_f32 v[104:105], v[104:105], v[70:71]\n"
"v_pk_fma_f32 v[106:107], v[70:71], v[70:71], v[106:107]\n"
"v_pk_add_f32 v[104:105], v[104:105], v[72:73]\n"
"v_pk_fma_f32 v[106:107], v[72:73], v[72:73], v[106:107]\n"
"v_pk_add_f32 v[104:105], v[104:105], v[74:75]\n"
"v_pk_fma_f32 v[106:107], v[74:75], v[74:75], v[106:107]\n"
"v_pk_add_f32 v[104:105], v[104:105], v[76:77]\n"
"v_pk_fma_f32 v[106:107], v[76:77], v[76:77], v[106:107]\n"
"v_pk_add_f32 v[104:105], v[104:105], v[78:79]\n"
"v_pk_fma_f32 v[106:107], v[78:79], v[78:79], v[106:107]\n"
"v_pk_add_f32 v[104:105], v[104:105], v[80:81]\n"
"v_pk_fma_f32 v[106:107], v[80:81], v[80:81], v[106:107]\n"
"v_pk_add_f32 v[104:105], v[104:105], v[82:83]\n"
"v_pk_fma_f32 v[106:107], v[82:83], v[82:83], v[106:107]\n"
"v_pk_add_f32 v[104:105], v[104:105], v[84:85]\n"
"v_pk_fma_f32 v[106:107], v[84:85], v[84:85], v[106:107]\n"
"v_pk_add_f32 v[104:105], v[104:105], v[86:87]\n"
"v_pk_fma_f32 v[106:107], v[86:87], v[86:87], v[106:107]\n"
"v_pk_add_f32 v[104:105], v[104:105], v[88:89]\n"
"v_pk_fma_f32 v[106:107], v[88:89], v[88:89], v[106:107]\n"
"v_pk_add_f32 v[104:105], v[104:105], v[90:91]\n"
"v_pk_fma_f32 v[106:107], v[90:91], v[90:91], v[106:107]\n"
"v_pk_add_f32 v[104:105], v[104:105], v[92:93]\n"
"v_pk_fma_f32 v[106:107], v[92:93], v[92:93], v[106:107]\n"
"v_pk_add_f32 v[104:105], v[104:105], v[94:95]\n"
"v_pk_fma_f32 v[106:107], v[94:95], v[94:95], v[106:107]\n"
"v_pk_add_f32 v[104:105], v[104:105], v[96:97]\n"
"v_pk_fma_f32 v[106:107], v[96:97], v[96:97], v[106:107]\n"
"v_pk_add_f32 v[104:105], v[104:105], v[98:99]\n"
"v_pk_fma_f32 v[106:107], v[98:99], v[98:99], v[106:107]\n"
"v_pk_add_f32 v[104:105], v[104:105], v[100:101]\n"
"v_pk_fma_f32 v[106:107], v[100:101], v[100:101], v[106:107]\n"
"v_pk_add_f32 v[104:105], v[104:105], v[102:103]\n"
"v_pk_fma_f32 v[106:107], v[102:103], v[102:103], v[106:107]\n"
"v_add_f32 v104, v104, v105\n"
"v_add_f32 v105, v106, v107\n"
"v_mov_b32 v106, v104\n"
"v_mov_b32 v107, v105\n"
"s_nop 0\n"
"v_permlane32_swap_b32 v104, v106\n"
"v_permlane32_swap_b32 v105, v107\n"
"v_add_f32 v104, v104, v106\n"
"v_add_f32 v105, v105, v107\n"
"v_mov_b32 v106, v104\n"
"v_mov_b32 v107, v105\n"
"s_nop 0\n"
"v_permlane16_swap_b32 v104, v106\n"
"v_permlane16_swap_b32 v105, v107\n"
"v_add_f32 v104, v104, v106\n"
"v_add_f32 v105, v105, v107\n"
"ds_write_b64 v111, v[104:105] offset:0\n"
"WT lgkmcnt(0)\n"
"s_barrier\n"
"RD v[112:115],v120 offset:0\n"
"RD v[116:119],v120 offset:16\n"
"WT lgkmcnt(1)\n"
"v_pk_add_f32 v[112:113], v[112:113], v[114:115]\n"
"WT lgkmcnt(0)\n"
"v_pk_add_f32 v[116:117], v[116:117], v[118:119]\n"
"v_pk_add_f32 v[112:113], v[112:113], v[116:117]\n"
"v_mul_f32 v112, 0x3acccccd, v112\n"
"v_mul_f32 v113, 0x3acccccd, v113\n"
"v_fma_f32 v113, -v112, v112, v113\n"
"v_max_f32 v113, 0, v113\n"
"v_add_f32 v113, 0x3727c5ac, v113\n"
"v_rsq_f32 v108, v113\n"
"s_nop 0\n"
"v_mul_f32 v109, v112, v108\n"
"v_sub_f32 v109, 0, v109\n"
"v_pk_fma_f32 v[64:65], v[64:65], v[108:109], v[108:109] op_sel:[0,0,1] op_sel_hi:[1,0,1]\n"
"CV v32,v64,v65\n"
"v_pk_fma_f32 v[66:67], v[66:67], v[108:109], v[108:109] op_sel:[0,0,1] op_sel_hi:[1,0,1]\n"
"CV v33,v66,v67\n"
"v_pk_fma_f32 v[68:69], v[68:69], v[108:109], v[108:109] op_sel:[0,0,1] op_sel_hi:[1,0,1]\n"
"CV v34,v68,v69\n"
"v_pk_fma_f32 v[70:71], v[70:71], v[108:109], v[108:109] op_sel:[0,0,1] op_sel_hi:[1,0,1]\n"
"CV v35,v70,v71\n"
"v_pk_fma_f32 v[72:73], v[72:73], v[108:109], v[108:109] op_sel:[0,0,1] op_sel_hi:[1,0,1]\n"
"CV v36,v72,v73\n"
"v_pk_fma_f32 v[74:75], v[74:75], v[108:109], v[108:109] op_sel:[0,0,1] op_sel_hi:[1,0,1]\n"
"CV v37,v74,v75\n"
"v_pk_fma_f32 v[76:77], v[76:77], v[108:109], v[108:109] op_sel:[0,0,1] op_sel_hi:[1,0,1]\n"
"CV v38,v76,v77\n"
"v_pk_fma_f32 v[78:79], v[78:79], v[108:109], v[108:109] op_sel:[0,0,1] op_sel_hi:[1,0,1]\n"
"CV v39,v78,v79\n"
"v_pk_fma_f32 v[80:81], v[80:81], v[108:109], v[108:109] op_sel:[0,0,1] op_sel_hi:[1,0,1]\n"
"CV v40,v80,v81\n"
"v_pk_fma_f32 v[82:83], v[82:83], v[108:109], v[108:109] op_sel:[0,0,1] op_sel_hi:[1,0,1]\n"
"CV v41,v82,v83\n"
"v_pk_fma_f32 v[84:85], v[84:85], v[108:109], v[108:109] op_sel:[0,0,1] op_sel_hi:[1,0,1]\n"
"CV v42,v84,v85\n"
"v_pk_fma_f32 v[86:87], v[86:87], v[108:109], v[108:109] op_sel:[0,0,1] op_sel_hi:[1,0,1]\n"
"CV v43,v86,v87\n"
"v_pk_fma_f32 v[88:89], v[88:89], v[108:109], v[108:109] op_sel:[0,0,1] op_sel_hi:[1,0,1]\n"
"CV v44,v88,v89\n"
"v_pk_fma_f32 v[90:91], v[90:91], v[108:109], v[108:109] op_sel:[0,0,1] op_sel_hi:[1,0,1]\n"
"CV v45,v90,v91\n"
"v_pk_fma_f32 v[92:93], v[92:93], v[108:109], v[108:109] op_sel:[0,0,1] op_sel_hi:[1,0,1]\n"
"CV v46,v92,v93\n"
"v_pk_fma_f32 v[94:95], v[94:95], v[108:109], v[108:109] op_sel:[0,0,1] op_sel_hi:[1,0,1]\n"
"CV v47,v94,v95\n"
"v_pk_fma_f32 v[96:97], v[96:97], v[108:109], v[108:109] op_sel:[0,0,1] op_sel_hi:[1,0,1]\n"
"CV v48,v96,v97\n"
"v_pk_fma_f32 v[98:99], v[98:99], v[108:109], v[108:109] op_sel:[0,0,1] op_sel_hi:[1,0,1]\n"
"CV v49,v98,v99\n"
"v_pk_fma_f32 v[100:101], v[100:101], v[108:109], v[108:109] op_sel:[0,0,1] op_sel_hi:[1,0,1]\n"
"CV v50,v100,v101\n"
"v_pk_fma_f32 v[102:103], v[102:103], v[108:109], v[108:109] op_sel:[0,0,1] op_sel_hi:[1,0,1]\n"
"CV v51,v102,v103\n"
"s_add_u32 s58, s57, s73\n"
"s_add_u32 s56, s58, 4096\n"
"ST v[32:35],v1,s[40:43],s58 offen offset:0 sc1\n"
"ST v[36:39],v1,s[40:43],s58 offen offset:1024 sc1\n"
"ST v[40:43],v1,s[40:43],s58 offen offset:2048 sc1\n"
"ST v[44:47],v1,s[40:43],s58 offen offset:3072 sc1\n"
"ST v[48:51],v1,s[40:43],s56 offen offset:0 sc1\n"
"v_xor_b32 v111, 512, v111\n"
"v_xor_b32 v120, 512, v120\n"
"s_add_u32 s73, s73, 0x300000\n"
"s_cmp_eq_u32 s73, 0x400000\n"
"s_cbranch_scc1 L_lnrep_63\n"
"v_add_u32 v121, 20480, v121\n"
"s_add_u32 s59, s59, 32\n"
"s_lshr_b32 s56, s59, 5\n"
"s_cmp_eq_u32 s56, 3\n"
"s_cbranch_scc1 L_ln_4\n"
"s_cmp_lt_u32 s33, 32\n"
"s_cbranch_scc1 L_p0back_3\n"
"s_cmp_ge_u32 s33, 59\n"
"s_cbranch_scc1 L_p0back_3\n"
"v_lshlrev_b32 v120, 16, v140\n"
"v_and_b32 v121, 0xffff0000, v140\n"
"v_lshlrev_b32 v122, 16, v141\n"
"v_and_b32 v123, 0xffff0000, v141\n"
"v_lshlrev_b32 v124, 16, v142\n"
"v_and_b32 v125, 0xffff0000, v142\n"
"v_lshlrev_b32 v126, 16, v143\n"
"v_and_b32 v127, 0xffff0000, v143\n"
"v_lshlrev_b32 v128, 16, v144\n"
"v_and_b32 v129, 0xffff0000, v144\n"
"v_lshlrev_b32 v130, 16, v145\n"
"v_and_b32 v131, 0xffff0000, v145\n"
"v_lshlrev_b32 v132, 16, v146\n"
"v_and_b32 v133, 0xffff0000, v146\n"
"v_lshlrev_b32 v134, 16, v147\n"
"v_and_b32 v135, 0xffff0000, v147\n"
"v_pk_add_f32 v[120:121], v[120:121], v[128:129]\n"
"v_pk_add_f32 v[122:123], v[122:123], v[130:131]\n"
"v_pk_add_f32 v[124:125], v[124:125], v[132:133]\n"
"v_pk_add_f32 v[126:127], v[126:127], v[134:135]\n"
"v_lshlrev_b32 v128, 16, v148\n"
"v_and_b32 v129, 0xffff0000, v148\n"
"v_lshlrev_b32 v130, 16, v149\n"
"v_and_b32 v131, 0xffff0000, v149\n"
"v_lshlrev_b32 v132, 16, v150\n"
"v_and_b32 v133, 0xffff0000, v150\n"
"v_lshlrev_b32 v134, 16, v151\n"
"v_and_b32 v135, 0xffff0000, v151\n"
"v_pk_add_f32 v[120:121], v[120:121], v[128:129]\n"
"v_pk_add_f32 v[122:123], v[122:123], v[130:131]\n"
"v_pk_add_f32 v[124:125], v[124:125], v[132:133]\n"
"v_pk_add_f32 v[126:127], v[126:127], v[134:135]\n"
"v_lshlrev_b32 v128, 16, v152\n"
"v_and_b32 v129, 0xffff0000, v152\n"
"v_lshlrev_b32 v130, 16, v153\n"
"v_and_b32 v131, 0xffff0000, v153\n"
"v_lshlrev_b32 v132, 16, v154\n"
"v_and_b32 v133, 0xffff0000, v154\n"
"v_lshlrev_b32 v134, 16, v155\n"
"v_and_b32 v135, 0xffff0000, v155\n"
"v_pk_add_f32 v[120:121], v[120:121], v[128:129]\n"
"v_pk_add_f32 v[122:123], v[122:123], v[130:131]\n"
"v_pk_add_f32 v[124:125], v[124:125], v[132:133]\n"
"v_pk_add_f32 v[126:127], v[126:127], v[134:135]\n"
"v_lshlrev_b32 v128, 16, v156\n"
"v_and_b32 v129, 0xffff0000, v156\n"
"v_lshlrev_b32 v130, 16, v157\n"
"v_and_b32 v131, 0xffff0000, v157\n"
"v_lshlrev_b32 v132, 16, v158\n"
"v_and_b32 v133, 0xffff0000, v158\n"
"v_lshlrev_b32 v134, 16, v159\n"
"v_and_b32 v135, 0xffff0000, v159\n"
"v_pk_add_f32 v[120:121], v[120:121], v[128:129]\n"
"v_pk_add_f32 v[122:123], v[122:123], v[130:131]\n"
"v_pk_add_f32 v[124:125], v[124:125], v[132:133]\n"
"v_pk_add_f32 v[126:127], v[126:127], v[134:135]\n"
"v_lshlrev_b32 v128, 16, v160\n"
"v_and_b32 v129, 0xffff0000, v160\n"
"v_lshlrev_b32 v130, 16, v161\n"
"v_and_b32 v131, 0xffff0000, v161\n"
"v_lshlrev_b32 v132, 16, v162\n"
"v_and_b32 v133, 0xffff0000, v162\n"
"v_lshlrev_b32 v134, 16, v163\n"
"v_and_b32 v135, 0xffff0000, v163\n"
"v_pk_add_f32 v[120:121], v[120:121], v[128:129]\n"
"v_pk_add_f32 v[122:123], v[122:123], v[130:131]\n"
"v_pk_add_f32 v[124:125], v[124:125], v[132:133]\n"
"v_pk_add_f32 v[126:127], v[126:127], v[134:135]\n"
"v_lshlrev_b32 v128, 16, v164\n"
"v_and_b32 v129, 0xffff0000, v164\n"
"v_lshlrev_b32 v130, 16, v165\n"
"v_and_b32 v131, 0xffff0000, v165\n"
"v_lshlrev_b32 v132, 16, v166\n"
"v_and_b32 v133, 0xffff0000, v166\n"
"v_lshlrev_b32 v134, 16, v167\n"
"v_and_b32 v135, 0xffff0000, v167\n"
"v_pk_add_f32 v[120:121], v[120:121], v[128:129]\n"
"v_pk_add_f32 v[122:123], v[122:123], v[130:131]\n"
"v_pk_add_f32 v[124:125], v[124:125], v[132:133]\n"
"v_pk_add_f32 v[126:127], v[126:127], v[134:135]\n"
"v_lshlrev_b32 v128, 16, v168\n"
"v_and_b32 v129, 0xffff0000, v168\n"
"v_lshlrev_b32 v130, 16, v169\n"
"v_and_b32 v131, 0xffff0000, v169\n"
"v_lshlrev_b32 v132, 16, v170\n"
"v_and_b32 v133, 0xffff0000, v170\n"
"v_lshlrev_b32 v134, 16, v171\n"
"v_and_b32 v135, 0xffff0000, v171\n"
"v_pk_add_f32 v[120:121], v[120:121], v[128:129]\n"
"v_pk_add_f32 v[122:123], v[122:123], v[130:131]\n"
"v_pk_add_f32 v[124:125], v[124:125], v[132:133]\n"
"v_pk_add_f32 v[126:127], v[126:127], v[134:135]\n"
"v_lshlrev_b32 v128, 16, v172\n"
"v_and_b32 v129, 0xffff0000, v172\n"
"v_lshlrev_b32 v130, 16, v173\n"
"v_and_b32 v131, 0xffff0000, v173\n"
"v_lshlrev_b32 v132, 16, v174\n"
"v_and_b32 v133, 0xffff0000, v174\n"
"v_lshlrev_b32 v134, 16, v175\n"
"v_and_b32 v135, 0xffff0000, v175\n"
"v_pk_add_f32 v[120:121], v[120:121], v[128:129]\n"
"v_pk_add_f32 v[122:123], v[122:123], v[130:131]\n"
"v_pk_add_f32 v[124:125], v[124:125], v[132:133]\n"
"v_pk_add_f32 v[126:127], v[126:127], v[134:135]\n"
"v_lshlrev_b32 v128, 16, v176\n"
"v_and_b32 v129, 0xffff0000, v176\n"
"v_lshlrev_b32 v130, 16, v177\n"
"v_and_b32 v131, 0xffff0000, v177\n"
"v_lshlrev_b32 v132, 16, v178\n"
"v_and_b32 v133, 0xffff0000, v178\n"
"v_lshlrev_b32 v134, 16, v179\n"
"v_and_b32 v135, 0xffff0000, v179\n"
"v_pk_add_f32 v[120:121], v[120:121], v[128:129]\n"
"v_pk_add_f32 v[122:123], v[122:123], v[130:131]\n"
"v_pk_add_f32 v[124:125], v[124:125], v[132:133]\n"
"v_pk_add_f32 v[126:127], v[126:127], v[134:135]\n"
"v_lshlrev_b32 v128, 16, v180\n"
"v_and_b32 v129, 0xffff0000, v180\n"
"v_lshlrev_b32 v130, 16, v181\n"
"v_and_b32 v131, 0xffff0000, v181\n"
"v_lshlrev_b32 v132, 16, v182\n"
"v_and_b32 v133, 0xffff0000, v182\n"
"v_lshlrev_b32 v134, 16, v183\n"
"v_and_b32 v135, 0xffff0000, v183\n"
"v_pk_add_f32 v[120:121], v[120:121], v[128:129]\n"
"v_pk_add_f32 v[122:123], v[122:123], v[130:131]\n"
"v_pk_add_f32 v[124:125], v[124:125], v[132:133]\n"
"v_pk_add_f32 v[126:127], v[126:127], v[134:135]\n"
"v_lshlrev_b32 v128, 16, v184\n"
"v_and_b32 v129, 0xffff0000, v184\n"
"v_lshlrev_b32 v130, 16, v185\n"
"v_and_b32 v131, 0xffff0000, v185\n"
"v_lshlrev_b32 v132, 16, v186\n"
"v_and_b32 v133, 0xffff0000, v186\n"
"v_lshlrev_b32 v134, 16, v187\n"
"v_and_b32 v135, 0xffff0000, v187\n"
"v_pk_add_f32 v[120:121], v[120:121], v[128:129]\n"
"v_pk_add_f32 v[122:123], v[122:123], v[130:131]\n"
"v_pk_add_f32 v[124:125], v[124:125], v[132:133]\n"
"v_pk_add_f32 v[126:127], v[126:127], v[134:135]\n"
"v_lshlrev_b32 v128, 16, v188\n"
"v_and_b32 v129, 0xffff0000, v188\n"
"v_lshlrev_b32 v130, 16, v189\n"
"v_and_b32 v131, 0xffff0000, v189\n"
"v_lshlrev_b32 v132, 16, v190\n"
"v_and_b32 v133, 0xffff0000, v190\n"
"v_lshlrev_b32 v134, 16, v191\n"
"v_and_b32 v135, 0xffff0000, v191\n"
"v_pk_add_f32 v[120:121], v[120:121], v[128:129]\n"
"v_pk_add_f32 v[122:123], v[122:123], v[130:131]\n"
"v_pk_add_f32 v[124:125], v[124:125], v[132:133]\n"
"v_pk_add_f32 v[126:127], v[126:127], v[134:135]\n"
"v_lshlrev_b32 v128, 16, v192\n"
"v_and_b32 v129, 0xffff0000, v192\n"
"v_lshlrev_b32 v130, 16, v193\n"
"v_and_b32 v131, 0xffff0000, v193\n"
"v_lshlrev_b32 v132, 16, v194\n"
"v_and_b32 v133, 0xffff0000, v194\n"
"v_lshlrev_b32 v134, 16, v195\n"
"v_and_b32 v135, 0xffff0000, v195\n"
"v_pk_add_f32 v[120:121], v[120:121], v[128:129]\n"
"v_pk_add_f32 v[122:123], v[122:123], v[130:131]\n"
"v_pk_add_f32 v[124:125], v[124:125], v[132:133]\n"
"v_pk_add_f32 v[126:127], v[126:127], v[134:135]\n"
"v_lshlrev_b32 v128, 16, v196\n"
"v_and_b32 v129, 0xffff0000, v196\n"
"v_lshlrev_b32 v130, 16, v197\n"
"v_and_b32 v131, 0xffff0000, v197\n"
"v_lshlrev_b32 v132, 16, v198\n"
"v_and_b32 v133, 0xffff0000, v198\n"
"v_lshlrev_b32 v134, 16, v199\n"
"v_and_b32 v135, 0xffff0000, v199\n"
"v_pk_add_f32 v[120:121], v[120:121], v[128:129]\n"
"v_pk_add_f32 v[122:123], v[122:123], v[130:131]\n"
"v_pk_add_f32 v[124:125], v[124:125], v[132:133]\n"
"v_pk_add_f32 v[126:127], v[126:127], v[134:135]\n"
"v_lshlrev_b32 v128, 16, v200\n"
"v_and_b32 v129, 0xffff0000, v200\n"
"v_lshlrev_b32 v130, 16, v201\n"
"v_and_b32 v131, 0xffff0000, v201\n"
"v_lshlrev_b32 v132, 16, v202\n"
"v_and_b32 v133, 0xffff0000, v202\n"
"v_lshlrev_b32 v134, 16, v203\n"
"v_and_b32 v135, 0xffff0000, v203\n"
"v_pk_add_f32 v[120:121], v[120:121], v[128:129]\n"
"v_pk_add_f32 v[122:123], v[122:123], v[130:131]\n"
"v_pk_add_f32 v[124:125], v[124:125], v[132:133]\n"
"v_pk_add_f32 v[126:127], v[126:127], v[134:135]\n"
"v_lshlrev_b32 v128, 16, v204\n"
"v_and_b32 v129, 0xffff0000, v204\n"
"v_lshlrev_b32 v130, 16, v205\n"
"v_and_b32 v131, 0xffff0000, v205\n"
"v_lshlrev_b32 v132, 16, v206\n"
"v_and_b32 v133, 0xffff0000, v206\n"
"v_lshlrev_b32 v134, 16, v207\n"
"v_and_b32 v135, 0xffff0000, v207\n"
"v_pk_add_f32 v[120:121], v[120:121], v[128:129]\n"
"v_pk_add_f32 v[122:123], v[122:123], v[130:131]\n"
"v_pk_add_f32 v[124:125], v[124:125], v[132:133]\n"
"v_pk_add_f32 v[126:127], v[126:127], v[134:135]\n"
"v_lshlrev_b32 v128, 16, v208\n"
"v_and_b32 v129, 0xffff0000, v208\n"
"v_lshlrev_b32 v130, 16, v209\n"
"v_and_b32 v131, 0xffff0000, v209\n"
"v_lshlrev_b32 v132, 16, v210\n"
"v_and_b32 v133, 0xffff0000, v210\n"
"v_lshlrev_b32 v134, 16, v211\n"
"v_and_b32 v135, 0xffff0000, v211\n"
"v_pk_add_f32 v[120:121], v[120:121], v[128:129]\n"
"v_pk_add_f32 v[122:123], v[122:123], v[130:131]\n"
"v_pk_add_f32 v[124:125], v[124:125], v[132:133]\n"
"v_pk_add_f32 v[126:127], v[126:127], v[134:135]\n"
"v_lshlrev_b32 v128, 16, v212\n"
"v_and_b32 v129, 0xffff0000, v212\n"
"v_lshlrev_b32 v130, 16, v213\n"
"v_and_b32 v131, 0xffff0000, v213\n"
"v_lshlrev_b32 v132, 16, v214\n"
"v_and_b32 v133, 0xffff0000, v214\n"
"v_lshlrev_b32 v134, 16, v215\n"
"v_and_b32 v135, 0xffff0000, v215\n"
"v_pk_add_f32 v[120:121], v[120:121], v[128:129]\n"
"v_pk_add_f32 v[122:123], v[122:123], v[130:131]\n"
"v_pk_add_f32 v[124:125], v[124:125], v[132:133]\n"
"v_pk_add_f32 v[126:127], v[126:127], v[134:135]\n"
"v_lshlrev_b32 v128, 16, v216\n"
"v_and_b32 v129, 0xffff0000, v216\n"
"v_lshlrev_b32 v130, 16, v217\n"
"v_and_b32 v131, 0xffff0000, v217\n"
"v_lshlrev_b32 v132, 16, v218\n"
"v_and_b32 v133, 0xffff0000, v218\n"
"v_lshlrev_b32 v134, 16, v219\n"
"v_and_b32 v135, 0xffff0000, v219\n"
"v_pk_add_f32 v[120:121], v[120:121], v[128:129]\n"
"v_pk_add_f32 v[122:123], v[122:123], v[130:131]\n"
"v_pk_add_f32 v[124:125], v[124:125], v[132:133]\n"
"v_pk_add_f32 v[126:127], v[126:127], v[134:135]\n"
"v_add_f32_dpp v120, v120, v120 row_ror:8 row_mask:0xf bank_mask:0xf\n"
"v_add_f32_dpp v121, v121, v121 row_ror:8 row_mask:0xf bank_mask:0xf\n"
"v_add_f32_dpp v122, v122, v122 row_ror:8 row_mask:0xf bank_mask:0xf\n"
"v_add_f32_dpp v123, v123, v123 row_ror:8 row_mask:0xf bank_mask:0xf\n"
"v_add_f32_dpp v124, v124, v124 row_ror:8 row_mask:0xf bank_mask:0xf\n"
"v_add_f32_dpp v125, v125, v125 row_ror:8 row_mask:0xf bank_mask:0xf\n"
"v_add_f32_dpp v126, v126, v126 row_ror:8 row_mask:0xf bank_mask:0xf\n"
"v_add_f32_dpp v127, v127, v127 row_ror:8 row_mask:0xf bank_mask:0xf\n"
"v_mov_b32 v128, v120\n"
"v_mov_b32 v129, v121\n"
"v_mov_b32 v130, v122\n"
"v_mov_b32 v131, v123\n"
"v_mov_b32 v132, v124\n"
"v_mov_b32 v133, v125\n"
"v_mov_b32 v134, v126\n"
"v_mov_b32 v135, v127\n"
"v_permlane32_swap_b32 v120, v128\n"
"v_permlane32_swap_b32 v121, v129\n"
"v_permlane32_swap_b32 v122, v130\n"
"v_permlane32_swap_b32 v123, v131\n"
"v_permlane32_swap_b32 v124, v132\n"
"v_permlane32_swap_b32 v125, v133\n"
"v_permlane32_swap_b32 v126, v134\n"
"v_permlane32_swap_b32 v127, v135\n"
"v_add_f32 v120, v120, v128\n"
"v_add_f32 v121, v121, v129\n"
"v_add_f32 v122, v122, v130\n"
"v_add_f32 v123, v123, v131\n"
"v_add_f32 v124, v124, v132\n"
"v_add_f32 v125, v125, v133\n"
"v_add_f32 v126, v126, v134\n"
"v_add_f32 v127, v127, v135\n"
"v_mov_b32 v128, v120\n"
"v_mov_b32 v129, v121\n"
"v_mov_b32 v130, v122\n"
"v_mov_b32 v131, v123\n"
"v_mov_b32 v132, v124\n"
"v_mov_b32 v133, v125\n"
"v_mov_b32 v134, v126\n"
"v_mov_b32 v135, v127\n"
"v_permlane16_swap_b32 v120, v128\n"
"v_permlane16_swap_b32 v121, v129\n"
"v_permlane16_swap_b32 v122, v130\n"
"v_permlane16_swap_b32 v123, v131\n"
"v_permlane16_swap_b32 v124, v132\n"
"v_permlane16_swap_b32 v125, v133\n"
"v_permlane16_swap_b32 v126, v134\n"
"v_permlane16_swap_b32 v127, v135\n"
"v_add_f32 v120, v120, v128\n"
"v_add_f32 v121, v121, v129\n"
"v_add_f32 v122, v122, v130\n"
"v_add_f32 v123, v123, v131\n"
"v_add_f32 v124, v124, v132\n"
"v_add_f32 v125, v125, v133\n"
"v_add_f32 v126, v126, v134\n"
"v_add_f32 v127, v127, v135\n"
"v_and_b32 v22, 7, v0\n"
"v_lshlrev_b32 v22, 5, v22\n"
"s_lshl_b32 s56, s34, 8\n"
"s_add_u32 s56, s56, 0x26000\n"
"v_add_u32 v22, s56, v22\n"
"v_lshlrev_b32 v23, 2, v0\n"
"v_add_u32 v21, 0x26000, v23\n"
"ds_write_b128 v22, v[120:123] offset:2048\n"
"ds_write_b128 v22, v[124:127] offset:2064\n"
"WT lgkmcnt(0)\n"
"s_barrier\n"
"ds_read_b32 v120, v21 offset:2048\n"
"ds_read_b32 v121, v21 offset:2304\n"
"ds_read_b32 v122, v21 offset:2560\n"
"ds_read_b32 v123, v21 offset:2816\n"
"WT lgkmcnt(2)\n"
"v_add_f32 v120, v120, v121\n"
"WT lgkmcnt(0)\n"
"v_add_f32 v122, v122, v123\n"
"v_add_f32 v120, v120, v122\n"
"s_sub_u32 s57, s33, 32\n"
"s_lshl_b32 s57, s57, 8\n"
"s_add_u32 s57, s57, 0x20000\n"
"buffer_store_dword v120, v23, s[40:43], s57 offen sc1\n"
"s_branch L_p0back_3\n"
"L_psp_9:\n"
"s_mov_b32 s56, 0x40000\n"
"L_psp_9l:\n"
"s_sleep 2\n"
"buffer_load_dwordx2 v[18:19], v20, s[40:43], 0 offen sc1\n"
"WT vmcnt(0)\n"
"v_cmp_ne_u64 vcc, s[38:39], v[18:19]\n"
"s_nop 1\n"
"s_cmp_eq_u64 vcc, 0\n"
"s_cbranch_scc1 L_pok_8\n"
"s_sub_u32 s56, s56, 1\n"
"s_cmp_lg_u32 s56, 0\n"
"s_cbranch_scc1 L_psp_9l\n"
"s_branch L_pok_8\n"
"L_p2free_11:\n"
"WT lgkmcnt(15)\n"
"TR v[96:97],v24 offset:32768\n"
"WT lgkmcnt(15)\n"
"TR v[98:99],v24 offset:34816\n"
"WT lgkmcnt(15)\n"
"TR v[100:101],v24 offset:40960\n"
"WT lgkmcnt(15)\n"
"TR v[102:103],v24 offset:43008\n"
"WT lgkmcnt(15)\n"
"TR v[104:105],v24 offset:49152\n"
"WT lgkmcnt(15)\n"
"TR v[106:107],v24 offset:51200\n"
"WT lgkmcnt(15)\n"
"TR v[108:109],v24 offset:57344\n"
"WT lgkmcnt(15)\n"
"TR v[110:111],v24 offset:59392\n"
"WT lgkmcnt(15)\n"
"TR v[112:113],v25 offset:32768\n"
"WT lgkmcnt(15)\n"
"TR v[114:115],v25 offset:34816\n"
"WT lgkmcnt(15)\n"
"TR v[116:117],v25 offset:40960\n"
"WT lgkmcnt(15)\n"
"TR v[118:119],v25 offset:43008\n"
"WT lgkmcnt(15)\n"
"TR v[120:121],v25 offset:49152\n"
"WT lgkmcnt(15)\n"
"TR v[122:123],v25 offset:51200\n"
"WT lgkmcnt(15)\n"
"TR v[124:125],v25 offset:57344\n"
"WT lgkmcnt(15)\n"
"TR v[126:127],v25 offset:59392\n"
"WT vmcnt(8)\n"
"s_add_u32 s64, s80, 8192\n"
"ST v[32:35],v1,s[40:43],s64 offen offset:0 sc1\n"
"ST v[36:39],v1,s[40:43],s64 offen offset:1024 sc1\n"
"ST v[40:43],v1,s[40:43],s64 offen offset:2048 sc1\n"
"ST v[44:47],v1,s[40:43],s64 offen offset:3072 sc1\n"
"s_add_u32 s64, s80, 12288\n"
"ST v[48:51],v1,s[40:43],s64 offen offset:0 sc1\n"
"ST v[52:55],v1,s[40:43],s64 offen offset:1024 sc1\n"
"s_branch L_p2join_12\n"
"L_psp_16:\n"
"s_mov_b32 s56, 0x40000\n"
"L_psp_16l:\n"
"s_sleep 2\n"
"buffer_load_dwordx2 v[18:19], v20, s[40:43], 0 offen sc1\n"
"WT vmcnt(0)\n"
"v_cmp_ne_u64 vcc, s[38:39], v[18:19]\n"
"s_nop 1\n"
"s_cmp_eq_u64 vcc, 0\n"
"s_cbranch_scc1 L_pok_15\n"
"s_sub_u32 s56, s56, 1\n"
"s_cmp_lg_u32 s56, 0\n"
"s_cbranch_scc1 L_psp_16l\n"
"s_branch L_pok_15\n"
"L_psp_20:\n"
"s_mov_b32 s56, 0x40000\n"
"L_psp_20l:\n"
"s_sleep 2\n"
"buffer_load_dwordx2 v[18:19], v20, s[40:43], 0 offen sc1\n"
"WT vmcnt(0)\n"
"v_cmp_ne_u64 vcc, s[38:39], v[18:19]\n"
"s_nop 1\n"
"s_cmp_eq_u64 vcc, 0\n"
"s_cbranch_scc1 L_pok_19\n"
"s_sub_u32 s56, s56, 1\n"
"s_cmp_lg_u32 s56, 0\n"
"s_cbranch_scc1 L_psp_20l\n"
"s_branch L_pok_19\n"
"L_p4free_17:\n"
"buffer_load_dwordx2 v[18:19], v20, s[40:43], 0 offen sc1\n"
"WT vmcnt(0) lgkmcnt(0)\n"
"v_cmp_ne_u64 vcc, s[38:39], v[18:19]\n"
"s_nop 1\n"
"s_cmp_eq_u64 vcc, 0\n"
"s_cbranch_scc0 L_psp_65\n"
"L_pok_64:\n"
"AW a32,0\n"
"AW a33,0\n"
"AW a34,0\n"
"AW a35,0\n"
"s_add_u32 s16, s16, 25856\n"
"s_add_u32 s17, s17, 32768\n"
"s_and_b32 s17, s17, 0x1ffff\n"
"s_mov_b32 m0, s17\n"
"AW a36,0\n"
"LD v1,s[44:47],s16 offen offset:0 sc1 lds\n"
"AW a37,0\n"
"AW a38,0\n"
"AW a39,0\n"
"AW a40,0\n"
"AW a41,0\n"
"LD v1,s[44:47],s16 offen offset:1024 sc1 lds\n"
"AW a42,0\n"
"AW a43,0\n"
"AW a44,0\n"
"AW a45,0\n"
"AW a46,0\n"
"LD v1,s[44:47],s16 offen offset:2048 sc1 lds\n"
"AW a47,0\n"
"AW a48,0\n"
"AW a49,0\n"
"AW a50,0\n"
"AW a51,0\n"
"LD v1,s[44:47],s16 offen offset:3072 sc1 lds\n"
"AW a52,0\n"
"AW a53,0\n"
"AW a54,0\n"
"AW a55,0\n"
"s_add_u32 s16, s16, 8192\n"
"s_add_u32 m0, s17, 8192\n"
"AW a56,0\n"
"LD v1,s[44:47],s16 offen offset:0 sc1 lds\n"
"AW a57,0\n"
"AW a58,0\n"
"AW a59,0\n"
"AW a60,0\n"
"AW a61,0\n"
"LD v1,s[44:47],s16 offen offset:1024 sc1 lds\n"
"AW a62,0\n"
"AW a63,0\n"
"AW a64,0\n"
"AW a65,0\n"
"AW a66,0\n"
"LD v1,s[44:47],s16 offen offset:2048 sc1 lds\n"
"AW a67,0\n"
"AW a68,0\n"
"AW a69,0\n"
"AW a70,0\n"
"AW a71,0\n"
"LD v1,s[44:47],s16 offen offset:3072 sc1 lds\n"
"AW a72,0\n"
"AW a73,0\n"
"AW a74,0\n"
"AW a75,0\n"
"s_add_u32 s16, s16, 25856\n"
"s_add_u32 s17, s17, 32768\n"
"s_and_b32 s17, s17, 0x1ffff\n"
"s_mov_b32 m0, s17\n"
"AW a76,0\n"
"LD v1,s[44:47],s16 offen offset:0 sc1 lds\n"
"AW a77,0\n"
"AW a78,0\n"
"AW a79,0\n"
"AW a80,0\n"
"AW a81,0\n"
"LD v1,s[44:47],s16 offen offset:1024 sc1 lds\n"
"AW a82,0\n"
"AW a83,0\n"
"AW a84,0\n"
"AW a85,0\n"
"AW a86,0\n"
"LD v1,s[44:47],s16 offen offset:2048 sc1 lds\n"
"AW a87,0\n"
"AW a88,0\n"
"AW a89,0\n"
"AW a90,0\n"
"AW a91,0\n"
"LD v1,s[44:47],s16 offen offset:3072 sc1 lds\n"
"AW a92,0\n"
"AW a93,0\n"
"AW a94,0\n"
"AW a95,0\n"
"s_add_u32 s16, s16, 8192\n"
"s_add_u32 m0, s17, 8192\n"
"AW a96,0\n"
"LD v1,s[44:47],s16 offen offset:0 sc1 lds\n"
"AW a97,0\n"
"AW a98,0\n"
"AW a99,0\n"
"AW a100,0\n"
"AW a101,0\n"
"LD v1,s[44:47],s16 offen offset:1024 sc1 lds\n"
"AW a102,0\n"
"AW a103,0\n"
"AW a104,0\n"
"AW a105,0\n"
"AW a106,0\n"
"LD v1,s[44:47],s16 offen offset:2048 sc1 lds\n"
"AW a107,0\n"
"AW a108,0\n"
"AW a109,0\n"
"AW a110,0\n"
"AW a111,0\n"
"LD v1,s[44:47],s16 offen offset:3072 sc1 lds\n"
"AW a112,0\n"
"AW a113,0\n"
"AW a114,0\n"
"AW a115,0\n"
"s_add_u32 s16, s16, 25856\n"
"s_add_u32 s17, s17, 32768\n"
"s_and_b32 s17, s17, 0x1ffff\n"
"s_mov_b32 m0, s17\n"
"AW a116,0\n"
"LD v1,s[44:47],s16 offen offset:0 sc1 lds\n"
"AW a117,0\n"
"AW a118,0\n"
"AW a119,0\n"
"AW a120,0\n"
"AW a121,0\n"
"LD v1,s[44:47],s16 offen offset:1024 sc1 lds\n"
"AW a122,0\n"
"AW a123,0\n"
"AW a124,0\n"
"AW a125,0\n"
"AW a126,0\n"
"LD v1,s[44:47],s16 offen offset:2048 sc1 lds\n"
"AW a127,0\n"
"AW a192,0\n"
"AW a193,0\n"
"AW a194,0\n"
"AW a195,0\n"
"LD v1,s[44:47],s16 offen offset:3072 sc1 lds\n"
"AW a196,0\n"
"AW a197,0\n"
"AW a198,0\n"
"AW a199,0\n"
"s_add_u32 s16, s16, 8192\n"
"s_add_u32 m0, s17, 8192\n"
"AW a200,0\n"
"LD v1,s[44:47],s16 offen offset:0 sc1 lds\n"
"AW a201,0\n"
"AW a202,0\n"
"AW a203,0\n"
"AW a204,0\n"
"AW a205,0\n"
"LD v1,s[44:47],s16 offen offset:1024 sc1 lds\n"
"AW a206,0\n"
"AW a207,0\n"
"AW a208,0\n"
"AW a209,0\n"
"AW a210,0\n"
"LD v1,s[44:47],s16 offen offset:2048 sc1 lds\n"
"AW a211,0\n"
"AW a212,0\n"
"AW a213,0\n"
"AW a214,0\n"
"AW a215,0\n"
"LD v1,s[44:47],s16 offen offset:3072 sc1 lds\n"
"AW a216,0\n"
"AW a217,0\n"
"AW a218,0\n"
"AW a219,0\n"
"s_add_u32 s16, s16, 25856\n"
"s_add_u32 s17, s17, 32768\n"
"s_and_b32 s17, s17, 0x1ffff\n"
"s_mov_b32 m0, s17\n"
"AW a220,0\n"
"LD v1,s[44:47],s16 offen offset:0 sc1 lds\n"
"AW a221,0\n"
"AW a222,0\n"
"AW a223,0\n"
"AW a224,0\n"
"AW a225,0\n"
"LD v1,s[44:47],s16 offen offset:1024 sc1 lds\n"
"AW a226,0\n"
"AW a227,0\n"
"AW a228,0\n"
"AW a229,0\n"
"AW a230,0\n"
"LD v1,s[44:47],s16 offen offset:2048 sc1 lds\n"
"AW a231,0\n"
"AW a232,0\n"
"AW a233,0\n"
"AW a234,0\n"
"AW a235,0\n"
"LD v1,s[44:47],s16 offen offset:3072 sc1 lds\n"
"AW a236,0\n"
"AW a237,0\n"
"AW a238,0\n"
"AW a239,0\n"
"AW a240,0\n"
"AW a241,0\n"
"AW a242,0\n"
"AW a243,0\n"
"AW a244,0\n"
"AW a245,0\n"
"AW a246,0\n"
"AW a247,0\n"
"AW a248,0\n"
"AW a249,0\n"
"AW a250,0\n"
"AW a251,0\n"
"AW a252,0\n"
"AW a253,0\n"
"AW a254,0\n"
"AW a255,0\n"
"s_branch L_p4join_18\n"
"L_hook_22:\n"
"s_cmp_lg_u32 s34, 0\n"
"s_cbranch_scc1 L_pub_66\n"
"s_mov_b64 exec, 1\n"
"s_lshl_b32 s56, s33, 7\n"
"v_lshl_add_u32 v21, v0, 3, s56\n"
"s_mov_b32 s57, 0x58000\n"
"buffer_store_dwordx2 v[16:17], v21, s[40:43], s57 offen sc1\n"
"s_mov_b64 exec, -1\n"
"L_pub_66:\n"
"s_branch L_back_23\n"
"L_hook_25:\n"
"buffer_load_dwordx2 v[18:19], v20, s[40:43], 0 offen sc1\n"
"s_branch L_back_26\n"
"L_psp_28:\n"
"s_mov_b32 s56, 0x40000\n"
"L_psp_28l:\n"
"s_sleep 2\n"
"buffer_load_dwordx2 v[18:19], v20, s[40:43], 0 offen sc1\n"
"WT vmcnt(0)\n"
"v_cmp_ne_u64 vcc, s[38:39], v[18:19]\n"
"s_nop 1\n"
"s_cmp_eq_u64 vcc, 0\n"
"s_cbranch_scc1 L_pok_27\n"
"s_sub_u32 s56, s56, 1\n"
"s_cmp_lg_u32 s56, 0\n"
"s_cbranch_scc1 L_psp_28l\n"
"s_branch L_pok_27\n"
"L_slow_30:\n"
"s_mov_b32 s14, 0\n"
"v_mov_b32 v228, 0x3f803f80\n"
"v_mov_b32 v229, 0x3f803f80\n"
"v_mov_b32 v230, 0x3f803f80\n"
"v_mov_b32 v231, 0x3f803f80\n"
"L_xt_67:\n"
"L_xq_71:\n"
"s_cmp_lg_u32 s14, 0\n"
"s_cbranch_scc1 L_xq_72\n"
"v_mov_b64 v[96:97], v[32:33]\n"
"v_mov_b64 v[98:99], v[34:35]\n"
"v_mov_b64 v[100:101], v[36:37]\n"
"v_mov_b64 v[102:103], v[38:39]\n"
"v_mov_b64 v[104:105], v[40:41]\n"
"v_mov_b64 v[106:107], v[42:43]\n"
"v_mov_b64 v[108:109], v[44:45]\n"
"v_mov_b64 v[110:111], v[46:47]\n"
"s_mov_b32 s20, s60\n"
"s_lshl_b32 s56, s36, 1\n"
"s_add_u32 s56, s56, 0\n"
"s_branch L_xq_75\n"
"L_xq_72:\n"
"s_cmp_lg_u32 s14, 1\n"
"s_cbranch_scc1 L_xq_73\n"
"v_mov_b64 v[96:97], v[48:49]\n"
"v_mov_b64 v[98:99], v[50:51]\n"
"v_mov_b64 v[100:101], v[52:53]\n"
"v_mov_b64 v[102:103], v[54:55]\n"
"v_mov_b64 v[104:105], v[56:57]\n"
"v_mov_b64 v[106:107], v[58:59]\n"
"v_mov_b64 v[108:109], v[60:61]\n"
"v_mov_b64 v[110:111], v[62:63]\n"
"s_mov_b32 s20, s61\n"
"s_lshl_b32 s56, s36, 1\n"
"s_add_u32 s56, s56, 1\n"
"s_branch L_xq_75\n"
"L_xq_73:\n"
"s_cmp_lg_u32 s14, 2\n"
"s_cbranch_scc1 L_xq_74\n"
"v_mov_b64 v[96:97], v[64:65]\n"
"v_mov_b64 v[98:99], v[66:67]\n"
"v_mov_b64 v[100:101], v[68:69]\n"
"v_mov_b64 v[102:103], v[70:71]\n"
"v_mov_b64 v[104:105], v[72:73]\n"
"v_mov_b64 v[106:107], v[74:75]\n"
"v_mov_b64 v[108:109], v[76:77]\n"
"v_mov_b64 v[110:111], v[78:79]\n"
"s_mov_b32 s20, s62\n"
"s_lshl_b32 s56, s36, 1\n"
"s_sub_u32 s56, 30, s56\n"
"s_branch L_xq_75\n"
"L_xq_74:\n"
"v_mov_b64 v[96:97], v[80:81]\n"
"v_mov_b64 v[98:99], v[82:83]\n"
"v_mov_b64 v[100:101], v[84:85]\n"
"v_mov_b64 v[102:103], v[86:87]\n"
"v_mov_b64 v[104:105], v[88:89]\n"
"v_mov_b64 v[106:107], v[90:91]\n"
"v_mov_b64 v[108:109], v[92:93]\n"
"v_mov_b64 v[110:111], v[94:95]\n"
"s_mov_b32 s20, s63\n"
"s_lshl_b32 s56, s36, 1\n"
"s_sub_u32 s56, 31, s56\n"
"s_branch L_xq_75\n"
"L_xq_75:\n"
"s_lshl_b32 s17, s56, 1\n"
"s_add_u32 s17, s17, 2\n"
"s_lshl_b32 s56, s56, 6\n"
"s_lshl_b32 s57, s34, 4\n"
"s_add_u32 s56, s56, s57\n"
"v_and_b32 v21, 15, v0\n"
"v_lshrrev_b32 v22, 4, v0\n"
"v_lshlrev_b32 v22, 2, v22\n"
"v_sub_u32 v21, v21, v22\n"
"v_add_u32 v232, s56, v21\n"
"v_mov_b32 v233, 0xff800000\n"
"s_mov_b32 s15, 0\n"
"v_mov_b64 v[112:113], 0\n"
"v_mov_b64 v[114:115], 0\n"
"v_mov_b64 v[116:117], 0\n"
"v_mov_b64 v[118:119], 0\n"
"v_mov_b64 v[120:121], 0\n"
"v_mov_b64 v[122:123], 0\n"
"v_mov_b64 v[124:125], 0\n"
"v_mov_b64 v[126:127], 0\n"
"v_mov_b64 v[128:129], 0\n"
"v_mov_b64 v[130:131], 0\n"
"v_mov_b64 v[132:133], 0\n"
"v_mov_b64 v[134:135], 0\n"
"v_mov_b64 v[136:137], 0\n"
"v_mov_b64 v[138:139], 0\n"
"v_mov_b64 v[140:141], 0\n"
"v_mov_b64 v[142:143], 0\n"
"v_mov_b64 v[224:225], 0\n"
"v_mov_b64 v[220:221], 0\n"
"v_mov_b64 v[226:227], 0\n"
"v_mov_b64 v[222:223], 0\n"
"L_xp_68:\n"
"s_mov_b32 s16, 0\n"
"L_xc_69:\n"
"s_lshr_b32 s21, s16, 1\n"
"s_mul_i32 s21, s21, 34048\n"
"s_and_b32 s56, s16, 1\n"
"s_lshl_b32 s56, s56, 14\n"
"s_add_u32 s21, s21, s56\n"
"s_add_u32 s22, s21, 0\n"
"LD v[144:147],v1,s[44:47],s22 offen offset:0 sc1\n"
"LD v[148:151],v1,s[44:47],s22 offen offset:1024 sc1\n"
"LD v[152:155],v1,s[44:47],s22 offen offset:2048 sc1\n"
"LD v[156:159],v1,s[44:47],s22 offen offset:3072 sc1\n"
"s_add_u32 s22, s21, 4096\n"
"LD v[160:163],v1,s[44:47],s22 offen offset:0 sc1\n"
"LD v[164:167],v1,s[44:47],s22 offen offset:1024 sc1\n"
"LD v[168:171],v1,s[44:47],s22 offen offset:2048 sc1\n"
"LD v[172:175],v1,s[44:47],s22 offen offset:3072 sc1\n"
"s_add_u32 s22, s21, 8192\n"
"LD v[176:179],v1,s[44:47],s22 offen offset:0 sc1\n"
"LD v[180:183],v1,s[44:47],s22 offen offset:1024 sc1\n"
"LD v[184:187],v1,s[44:47],s22 offen offset:2048 sc1\n"
"LD v[188:191],v1,s[44:47],s22 offen offset:3072 sc1\n"
"s_add_u32 s22, s21, 12288\n"
"LD v[192:195],v1,s[44:47],s22 offen offset:0 sc1\n"
"LD v[196:199],v1,s[44:47],s22 offen offset:1024 sc1\n"
"LD v[200:203],v1,s[44:47],s22 offen offset:2048 sc1\n"
"LD v[204:207],v1,s[44:47],s22 offen offset:3072 sc1\n"
"WT vmcnt(0)\n"
"MF v[208:211],v[144:147],v[96:99],v[220:223]\n"
"MF v[208:211],v[148:151],v[100:103],v[208:211]\n"
"MF v[208:211],v[152:155],v[104:107],v[208:211]\n"
"MF v[208:211],v[156:159],v[108:111],v[208:211]\n"
"MF v[212:215],v[160:163],v[96:99],v[220:223]\n"
"MF v[212:215],v[164:167],v[100:103],v[212:215]\n"
"MF v[212:215],v[168:171],v[104:107],v[212:215]\n"
"MF v[212:215],v[172:175],v[108:111],v[212:215]\n"
"s_lshl_b32 s56, s16, 5\n"
"v_subrev_u32 v21, s56, v232\n"
"v_mov_b32 v22, 0xff800000\n"
"v_cmp_le_i32 vcc, 0, v21\n"
"s_nop 1\n"
"v_cndmask_b32 v208, v22, v208, vcc\n"
"v_cmp_le_i32 vcc, 1, v21\n"
"s_nop 1\n"
"v_cndmask_b32 v209, v22, v209, vcc\n"
"v_cmp_le_i32 vcc, 2, v21\n"
"s_nop 1\n"
"v_cndmask_b32 v210, v22, v210, vcc\n"
"v_cmp_le_i32 vcc, 3, v21\n"
"s_nop 1\n"
"v_cndmask_b32 v211, v22, v211, vcc\n"
"v_cmp_le_i32 vcc, 16, v21\n"
"s_nop 1\n"
"v_cndmask_b32 v212, v22, v212, vcc\n"
"v_cmp_le_i32 vcc, 17, v21\n"
"s_nop 1\n"
"v_cndmask_b32 v213, v22, v213, vcc\n"
"v_cmp_le_i32 vcc, 18, v21\n"
"s_nop 1\n"
"v_cndmask_b32 v214, v22, v214, vcc\n"
"v_cmp_le_i32 vcc, 19, v21\n"
"s_nop 1\n"
"v_cndmask_b32 v215, v22, v215, vcc\n"
"s_cmp_lg_u32 s15, 0\n"
"s_cbranch_scc1 L_xm_76\n"
"v_max_f32 v233, v233, v208\n"
"v_max_f32 v233, v233, v209\n"
"v_max_f32 v233, v233, v210\n"
"v_max_f32 v233, v233, v211\n"
"v_max_f32 v233, v233, v212\n"
"v_max_f32 v233, v233, v213\n"
"v_max_f32 v233, v233, v214\n"
"v_max_f32 v233, v233, v215\n"
"s_branch L_xn_70\n"
"L_xm_76:\n"
"EX v208,v208\n"
"EX v209,v209\n"
"EX v210,v210\n"
"EX v211,v211\n"
"EX v212,v212\n"
"EX v213,v213\n"
"EX v214,v214\n"
"EX v215,v215\n"
"CV v216,v208,v209\n"
"CV v217,v210,v211\n"
"CV v218,v212,v213\n"
"CV v219,v214,v215\n"
"s_nop 1\n"
"MF v[112:115],v[176:179],v[216:219],v[112:115]\n"
"MF v[116:119],v[180:183],v[216:219],v[116:119]\n"
"MF v[120:123],v[184:187],v[216:219],v[120:123]\n"
"MF v[124:127],v[188:191],v[216:219],v[124:127]\n"
"MF v[128:131],v[192:195],v[216:219],v[128:131]\n"
"MF v[132:135],v[196:199],v[216:219],v[132:135]\n"
"MF v[136:139],v[200:203],v[216:219],v[136:139]\n"
"MF v[140:143],v[204:207],v[216:219],v[140:143]\n"
"MF v[224:227],v[228:231],v[216:219],v[224:227]\n"
"L_xn_70:\n"
"s_add_u32 s16, s16, 1\n"
"s_cmp_lt_u32 s16, s17\n"
"s_cbranch_scc1 L_xc_69\n"
"s_cmp_lg_u32 s15, 0\n"
"s_cbranch_scc1 L_xd_77\n"
"v_mov_b32 v21, v233\n"
"s_nop 1\n"
"v_permlane32_swap_b32 v233, v21\n"
"v_max_f32 v233, v233, v21\n"
"v_mov_b32 v21, v233\n"
"s_nop 1\n"
"v_permlane16_swap_b32 v233, v21\n"
"v_max_f32 v233, v233, v21\n"
"v_sub_f32 v220, 0, v233\n"
"v_sub_f32 v221, 0, v233\n"
"v_sub_f32 v222, 0, v233\n"
"v_sub_f32 v223, 0, v233\n"
"s_mov_b32 s15, 1\n"
"s_branch L_xp_68\n"
"L_xd_77:\n"
"s_nop 2\n"
"v_rcp_f32 v216, v224\n"
"s_nop 0\n"
"v_pk_mul_f32 v[112:113], v[112:113], v[216:217] op_sel_hi:[1,0]\n"
"v_pk_mul_f32 v[114:115], v[114:115], v[216:217] op_sel_hi:[1,0]\n"
"v_pk_mul_f32 v[116:117], v[116:117], v[216:217] op_sel_hi:[1,0]\n"
"v_pk_mul_f32 v[118:119], v[118:119], v[216:217] op_sel_hi:[1,0]\n"
"CV v112,v112,v113\n"
"CV v113,v114,v115\n"
"CV v114,v116,v117\n"
"CV v115,v118,v119\n"
"ST v[112:115],v1,s[40:43],s20 offen offset:0 sc1\n"
"v_pk_mul_f32 v[120:121], v[120:121], v[216:217] op_sel_hi:[1,0]\n"
"v_pk_mul_f32 v[122:123], v[122:123], v[216:217] op_sel_hi:[1,0]\n"
"v_pk_mul_f32 v[124:125], v[124:125], v[216:217] op_sel_hi:[1,0]\n"
"v_pk_mul_f32 v[126:127], v[126:127], v[216:217] op_sel_hi:[1,0]\n"
"CV v120,v120,v121\n"
"CV v121,v122,v123\n"
"CV v122,v124,v125\n"
"CV v123,v126,v127\n"
"ST v[120:123],v1,s[40:43],s20 offen offset:1024 sc1\n"
"v_pk_mul_f32 v[128:129], v[128:129], v[216:217] op_sel_hi:[1,0]\n"
"v_pk_mul_f32 v[130:131], v[130:131], v[216:217] op_sel_hi:[1,0]\n"
"v_pk_mul_f32 v[132:133], v[132:133], v[216:217] op_sel_hi:[1,0]\n"
"v_pk_mul_f32 v[134:135], v[134:135], v[216:217] op_sel_hi:[1,0]\n"
"CV v128,v128,v129\n"
"CV v129,v130,v131\n"
"CV v130,v132,v133\n"
"CV v131,v134,v135\n"
"ST v[128:131],v1,s[40:43],s20 offen offset:2048 sc1\n"
"v_pk_mul_f32 v[136:137], v[136:137], v[216:217] op_sel_hi:[1,0]\n"
"v_pk_mul_f32 v[138:139], v[138:139], v[216:217] op_sel_hi:[1,0]\n"
"v_pk_mul_f32 v[140:141], v[140:141], v[216:217] op_sel_hi:[1,0]\n"
"v_pk_mul_f32 v[142:143], v[142:143], v[216:217] op_sel_hi:[1,0]\n"
"CV v136,v136,v137\n"
"CV v137,v138,v139\n"
"CV v138,v140,v141\n"
"CV v139,v142,v143\n"
"ST v[136:139],v1,s[40:43],s20 offen offset:3072 sc1\n"
"WT vmcnt(0)\n"
"s_add_u32 s14, s14, 1\n"
"s_cmp_lt_u32 s14, 4\n"
"s_cbranch_scc1 L_xt_67\n"
"s_branch L_slowback_31\n"
"L_psp_43:\n"
"s_mov_b32 s56, 0x40000\n"
"L_psp_43l:\n"
"s_sleep 2\n"
"buffer_load_dwordx2 v[18:19], v20, s[40:43], 0 offen sc1\n"
"WT vmcnt(0)\n"
"v_cmp_ne_u64 vcc, s[38:39], v[18:19]\n"
"s_nop 1\n"
"s_cmp_eq_u64 vcc, 0\n"
"s_cbranch_scc1 L_pok_42\n"
"s_sub_u32 s56, s56, 1\n"
"s_cmp_lg_u32 s56, 0\n"
"s_cbranch_scc1 L_psp_43l\n"
"s_branch L_pok_42\n"
"L_psp_49:\n"
"s_mov_b32 s56, 0x40000\n"
"L_psp_49l:\n"
"s_sleep 2\n"
"buffer_load_dwordx2 v[18:19], v20, s[40:43], 0 offen sc1\n"
"WT vmcnt(0)\n"
"v_cmp_ne_u64 vcc, s[38:39], v[18:19]\n"
"s_nop 1\n"
"s_cmp_eq_u64 vcc, 0\n"
"s_cbranch_scc1 L_pok_48\n"
"s_sub_u32 s56, s56, 1\n"
"s_cmp_lg_u32 s56, 0\n"
"s_cbranch_scc1 L_psp_49l\n"
"s_branch L_pok_48\n"
"L_psp_51:\n"
"s_mov_b32 s56, 0x40000\n"
"L_psp_51l:\n"
"s_sleep 2\n"
"buffer_load_dwordx2 v[18:19], v20, s[40:43], 0 offen sc1\n"
"WT vmcnt(0)\n"
"v_cmp_ne_u64 vcc, s[38:39], v[18:19]\n"
"s_nop 1\n"
"s_cmp_eq_u64 vcc, 0\n"
"s_cbranch_scc1 L_pok_50\n"
"s_sub_u32 s56, s56, 1\n"
"s_cmp_lg_u32 s56, 0\n"
"s_cbranch_scc1 L_psp_51l\n"
"s_branch L_pok_50\n"
"L_pr_52:\n"
"s_mov_b32 s56, 0x40000\n"
"L_pr_52l:\n"
"buffer_load_dwordx2 v[18:19], v20, s[40:43], 0 offen sc1\n"
"s_add_u32 s66, s67, 0\n"
"LD a[192:195],v1,s[40:43],s66 offen offset:0 sc1\n"
"s_add_u32 s66, s67, 65536\n"
"LD a[196:199],v1,s[40:43],s66 offen offset:0 sc1\n"
"s_add_u32 s66, s67, 131072\n"
"LD a[200:203],v1,s[40:43],s66 offen offset:0 sc1\n"
"s_add_u32 s66, s67, 196608\n"
"LD a[204:207],v1,s[40:43],s66 offen offset:0 sc1\n"
"s_add_u32 s66, s67, 4096\n"
"LD a[208:211],v1,s[40:43],s66 offen offset:0 sc1\n"
"s_add_u32 s66, s67, 69632\n"
"LD a[212:215],v1,s[40:43],s66 offen offset:0 sc1\n"
"s_add_u32 s66, s67, 135168\n"
"LD a[216:219],v1,s[40:43],s66 offen offset:0 sc1\n"
"s_add_u32 s66, s67, 200704\n"
"LD a[220:223],v1,s[40:43],s66 offen offset:0 sc1\n"
"WT vmcnt(0)\n"
"MF v[32:35],a[144:147],a[192:195],0\n"
"MF v[36:39],a[148:151],a[192:195],0\n"
"MF v[64:67],a[144:147],a[208:211],0\n"
"MF v[68:71],a[148:151],a[208:211],0\n"
"MF v[32:35],a[144:147],a[196:199],v[32:35]\n"
"MF v[36:39],a[148:151],a[196:199],v[36:39]\n"
"MF v[64:67],a[144:147],a[212:215],v[64:67]\n"
"MF v[68:71],a[148:151],a[212:215],v[68:71]\n"
"MF v[32:35],a[144:147],a[200:203],v[32:35]\n"
"MF v[36:39],a[148:151],a[200:203],v[36:39]\n"
"MF v[64:67],a[144:147],a[216:219],v[64:67]\n"
"MF v[68:71],a[148:151],a[216:219],v[68:71]\n"
"MF v[32:35],a[144:147],a[204:207],v[32:35]\n"
"MF v[36:39],a[148:151],a[204:207],v[36:39]\n"
"MF v[64:67],a[144:147],a[220:223],v[64:67]\n"
"MF v[68:71],a[148:151],a[220:223],v[68:71]\n"
"v_cmp_ne_u64 vcc, s[38:39], v[18:19]\n"
"s_nop 1\n"
"s_cmp_eq_u64 vcc, 0\n"
"s_cbranch_scc1 L_pr_52d\n"
"s_nop 1\n"
"v_cmp_u_f32 vcc, v32, v64\n"
"s_nop 1\n"
"s_cmp_eq_u64 vcc, 0\n"
"s_cbranch_scc1 L_pr_52d\n"
"s_sub_u32 s56, s56, 1\n"
"s_cmp_lg_u32 s56, 0\n"
"s_cbranch_scc1 L_pr_52l\n"
"L_pr_52d:\n"
"s_add_u32 s66, s67, 0\n"
"LD a[224:227],v1,s[40:43],s66 offen offset:1024 sc1\n"
"s_add_u32 s66, s67, 65536\n"
"LD a[228:231],v1,s[40:43],s66 offen offset:1024 sc1\n"
"s_add_u32 s66, s67, 131072\n"
"LD a[232:235],v1,s[40:43],s66 offen offset:1024 sc1\n"
"s_add_u32 s66, s67, 196608\n"
"LD a[236:239],v1,s[40:43],s66 offen offset:1024 sc1\n"
"s_add_u32 s66, s67, 4096\n"
"LD a[240:243],v1,s[40:43],s66 offen offset:1024 sc1\n"
"s_add_u32 s66, s67, 69632\n"
"LD a[244:247],v1,s[40:43],s66 offen offset:1024 sc1\n"
"s_add_u32 s66, s67, 135168\n"
"LD a[248:251],v1,s[40:43],s66 offen offset:1024 sc1\n"
"s_add_u32 s66, s67, 200704\n"
"LD a[252:255],v1,s[40:43],s66 offen offset:1024 sc1\n"
"s_add_u32 s66, s67, 0\n"
"LD a[112:115],v1,s[40:43],s66 offen offset:2048 sc1\n"
"s_add_u32 s66, s67, 65536\n"
"LD a[116:119],v1,s[40:43],s66 offen offset:2048 sc1\n"
"s_add_u32 s66, s67, 131072\n"
"LD a[120:123],v1,s[40:43],s66 offen offset:2048 sc1\n"
"s_add_u32 s66, s67, 196608\n"
"LD a[124:127],v1,s[40:43],s66 offen offset:2048 sc1\n"
"s_add_u32 s66, s67, 4096\n"
"LD a[128:131],v1,s[40:43],s66 offen offset:2048 sc1\n"
"s_add_u32 s66, s67, 69632\n"
"LD a[132:135],v1,s[40:43],s66 offen offset:2048 sc1\n"
"s_add_u32 s66, s67, 135168\n"
"LD a[136:139],v1,s[40:43],s66 offen offset:2048 sc1\n"
"s_add_u32 s66, s67, 200704\n"
"LD a[140:143],v1,s[40:43],s66 offen offset:2048 sc1\n"
"s_add_u32 s66, s67, 0\n"
"LD a[0:3],v1,s[40:43],s66 offen offset:3072 sc1\n"
"s_add_u32 s66, s67, 65536\n"
"LD a[4:7],v1,s[40:43],s66 offen offset:3072 sc1\n"
"s_add_u32 s66, s67, 131072\n"
"LD a[8:11],v1,s[40:43],s66 offen offset:3072 sc1\n"
"s_add_u32 s66, s67, 196608\n"
"LD a[12:15],v1,s[40:43],s66 offen offset:3072 sc1\n"
"s_add_u32 s66, s67, 4096\n"
"LD a[16:19],v1,s[40:43],s66 offen offset:3072 sc1\n"
"s_add_u32 s66, s67, 69632\n"
"LD a[20:23],v1,s[40:43],s66 offen offset:3072 sc1\n"
"s_add_u32 s66, s67, 135168\n"
"LD a[24:27],v1,s[40:43],s66 offen offset:3072 sc1\n"
"s_add_u32 s66, s67, 200704\n"
"LD a[28:31],v1,s[40:43],s66 offen offset:3072 sc1\n"
"s_branch L_po_53\n"
"L_pr_54:\n"
"s_mov_b32 s56, 0x40000\n"
"L_pr_54l:\n"
"buffer_load_dwordx2 v[18:19], v20, s[40:43], 0 offen sc1\n"
"s_add_u32 s66, s67, 0\n"
"LD a[224:227],v1,s[40:43],s66 offen offset:1024 sc1\n"
"s_add_u32 s66, s67, 65536\n"
"LD a[228:231],v1,s[40:43],s66 offen offset:1024 sc1\n"
"s_add_u32 s66, s67, 131072\n"
"LD a[232:235],v1,s[40:43],s66 offen offset:1024 sc1\n"
"s_add_u32 s66, s67, 196608\n"
"LD a[236:239],v1,s[40:43],s66 offen offset:1024 sc1\n"
"s_add_u32 s66, s67, 4096\n"
"LD a[240:243],v1,s[40:43],s66 offen offset:1024 sc1\n"
"s_add_u32 s66, s67, 69632\n"
"LD a[244:247],v1,s[40:43],s66 offen offset:1024 sc1\n"
"s_add_u32 s66, s67, 135168\n"
"LD a[248:251],v1,s[40:43],s66 offen offset:1024 sc1\n"
"s_add_u32 s66, s67, 200704\n"
"LD a[252:255],v1,s[40:43],s66 offen offset:1024 sc1\n"
"WT vmcnt(0)\n"
"MF v[40:43],a[144:147],a[224:227],0\n"
"MF v[44:47],a[148:151],a[224:227],0\n"
"MF v[72:75],a[144:147],a[240:243],0\n"
"MF v[76:79],a[148:151],a[240:243],0\n"
"MF v[40:43],a[144:147],a[228:231],v[40:43]\n"
"MF v[44:47],a[148:151],a[228:231],v[44:47]\n"
"MF v[72:75],a[144:147],a[244:247],v[72:75]\n"
"MF v[76:79],a[148:151],a[244:247],v[76:79]\n"
"MF v[40:43],a[144:147],a[232:235],v[40:43]\n"
"MF v[44:47],a[148:151],a[232:235],v[44:47]\n"
"MF v[72:75],a[144:147],a[248:251],v[72:75]\n"
"MF v[76:79],a[148:151],a[248:251],v[76:79]\n"
"MF v[40:43],a[144:147],a[236:239],v[40:43]\n"
"MF v[44:47],a[148:151],a[236:239],v[44:47]\n"
"MF v[72:75],a[144:147],a[252:255],v[72:75]\n"
"MF v[76:79],a[148:151],a[252:255],v[76:79]\n"
"v_cmp_ne_u64 vcc, s[38:39], v[18:19]\n"
"s_nop 1\n"
"s_cmp_eq_u64 vcc, 0\n"
"s_cbranch_scc1 L_pr_54d\n"
"s_nop 1\n"
"v_cmp_u_f32 vcc, v40, v72\n"
"s_nop 1\n"
"s_cmp_eq_u64 vcc, 0\n"
"s_cbranch_scc1 L_pr_54d\n"
"s_sub_u32 s56, s56, 1\n"
"s_cmp_lg_u32 s56, 0\n"
"s_cbranch_scc1 L_pr_54l\n"
"L_pr_54d:\n"
"s_add_u32 s66, s67, 0\n"
"LD a[112:115],v1,s[40:43],s66 offen offset:2048 sc1\n"
"s_add_u32 s66, s67, 65536\n"
"LD a[116:119],v1,s[40:43],s66 offen offset:2048 sc1\n"
"s_add_u32 s66, s67, 131072\n"
"LD a[120:123],v1,s[40:43],s66 offen offset:2048 sc1\n"
"s_add_u32 s66, s67, 196608\n"
"LD a[124:127],v1,s[40:43],s66 offen offset:2048 sc1\n"
"s_add_u32 s66, s67, 4096\n"
"LD a[128:131],v1,s[40:43],s66 offen offset:2048 sc1\n"
"s_add_u32 s66, s67, 69632\n"
"LD a[132:135],v1,s[40:43],s66 offen offset:2048 sc1\n"
"s_add_u32 s66, s67, 135168\n"
"LD a[136:139],v1,s[40:43],s66 offen offset:2048 sc1\n"
"s_add_u32 s66, s67, 200704\n"
"LD a[140:143],v1,s[40:43],s66 offen offset:2048 sc1\n"
"s_add_u32 s66, s67, 0\n"
"LD a[0:3],v1,s[40:43],s66 offen offset:3072 sc1\n"
"s_add_u32 s66, s67, 65536\n"
"LD a[4:7],v1,s[40:43],s66 offen offset:3072 sc1\n"
"s_add_u32 s66, s67, 131072\n"
"LD a[8:11],v1,s[40:43],s66 offen offset:3072 sc1\n"
"s_add_u32 s66, s67, 196608\n"
"LD a[12:15],v1,s[40:43],s66 offen offset:3072 sc1\n"
"s_add_u32 s66, s67, 4096\n"
"LD a[16:19],v1,s[40:43],s66 offen offset:3072 sc1\n"
"s_add_u32 s66, s67, 69632\n"
"LD a[20:23],v1,s[40:43],s66 offen offset:3072 sc1\n"
"s_add_u32 s66, s67, 135168\n"
"LD a[24:27],v1,s[40:43],s66 offen offset:3072 sc1\n"
"s_add_u32 s66, s67, 200704\n"
"LD a[28:31],v1,s[40:43],s66 offen offset:3072 sc1\n"
"WT vmcnt(0)\n"
"s_branch L_po_55\n"
"L_pr_56:\n"
"s_mov_b32 s56, 0x40000\n"
"L_pr_56l:\n"
"buffer_load_dwordx2 v[18:19], v20, s[40:43], 0 offen sc1\n"
"s_add_u32 s66, s67, 0\n"
"LD a[112:115],v1,s[40:43],s66 offen offset:2048 sc1\n"
"s_add_u32 s66, s67, 65536\n"
"LD a[116:119],v1,s[40:43],s66 offen offset:2048 sc1\n"
"s_add_u32 s66, s67, 131072\n"
"LD a[120:123],v1,s[40:43],s66 offen offset:2048 sc1\n"
"s_add_u32 s66, s67, 196608\n"
"LD a[124:127],v1,s[40:43],s66 offen offset:2048 sc1\n"
"s_add_u32 s66, s67, 4096\n"
"LD a[128:131],v1,s[40:43],s66 offen offset:2048 sc1\n"
"s_add_u32 s66, s67, 69632\n"
"LD a[132:135],v1,s[40:43],s66 offen offset:2048 sc1\n"
"s_add_u32 s66, s67, 135168\n"
"LD a[136:139],v1,s[40:43],s66 offen offset:2048 sc1\n"
"s_add_u32 s66, s67, 200704\n"
"LD a[140:143],v1,s[40:43],s66 offen offset:2048 sc1\n"
"WT vmcnt(0)\n"
"MF v[48:51],a[144:147],a[112:115],0\n"
"MF v[52:55],a[148:151],a[112:115],0\n"
"MF v[80:83],a[144:147],a[128:131],0\n"
"MF v[84:87],a[148:151],a[128:131],0\n"
"MF v[48:51],a[144:147],a[116:119],v[48:51]\n"
"MF v[52:55],a[148:151],a[116:119],v[52:55]\n"
"MF v[80:83],a[144:147],a[132:135],v[80:83]\n"
"MF v[84:87],a[148:151],a[132:135],v[84:87]\n"
"MF v[48:51],a[144:147],a[120:123],v[48:51]\n"
"MF v[52:55],a[148:151],a[120:123],v[52:55]\n"
"MF v[80:83],a[144:147],a[136:139],v[80:83]\n"
"MF v[84:87],a[148:151],a[136:139],v[84:87]\n"
"MF v[48:51],a[144:147],a[124:127],v[48:51]\n"
"MF v[52:55],a[148:151],a[124:127],v[52:55]\n"
"MF v[80:83],a[144:147],a[140:143],v[80:83]\n"
"MF v[84:87],a[148:151],a[140:143],v[84:87]\n"
"v_cmp_ne_u64 vcc, s[38:39], v[18:19]\n"
"s_nop 1\n"
"s_cmp_eq_u64 vcc, 0\n"
"s_cbranch_scc1 L_pr_56d\n"
"s_nop 1\n"
"v_cmp_u_f32 vcc, v48, v80\n"
"s_nop 1\n"
"s_cmp_eq_u64 vcc, 0\n"
"s_cbranch_scc1 L_pr_56d\n"
"s_sub_u32 s56, s56, 1\n"
"s_cmp_lg_u32 s56, 0\n"
"s_cbranch_scc1 L_pr_56l\n"
"L_pr_56d:\n"
"s_add_u32 s66, s67, 0\n"
"LD a[0:3],v1,s[40:43],s66 offen offset:3072 sc1\n"
"s_add_u32 s66, s67, 65536\n"
"LD a[4:7],v1,s[40:43],s66 offen offset:3072 sc1\n"
"s_add_u32 s66, s67, 131072\n"
"LD a[8:11],v1,s[40:43],s66 offen offset:3072 sc1\n"
"s_add_u32 s66, s67, 196608\n"
"LD a[12:15],v1,s[40:43],s66 offen offset:3072 sc1\n"
"s_add_u32 s66, s67, 4096\n"
"LD a[16:19],v1,s[40:43],s66 offen offset:3072 sc1\n"
"s_add_u32 s66, s67, 69632\n"
"LD a[20:23],v1,s[40:43],s66 offen offset:3072 sc1\n"
"s_add_u32 s66, s67, 135168\n"
"LD a[24:27],v1,s[40:43],s66 offen offset:3072 sc1\n"
"s_add_u32 s66, s67, 200704\n"
"LD a[28:31],v1,s[40:43],s66 offen offset:3072 sc1\n"
"WT vmcnt(0)\n"
"s_branch L_po_57\n"
"L_pr_58:\n"
"s_mov_b32 s56, 0x40000\n"
"L_pr_58l:\n"
"buffer_load_dwordx2 v[18:19], v20, s[40:43], 0 offen sc1\n"
"s_add_u32 s66, s67, 0\n"
"LD a[0:3],v1,s[40:43],s66 offen offset:3072 sc1\n"
"s_add_u32 s66, s67, 65536\n"
"LD a[4:7],v1,s[40:43],s66 offen offset:3072 sc1\n"
"s_add_u32 s66, s67, 131072\n"
"LD a[8:11],v1,s[40:43],s66 offen offset:3072 sc1\n"
"s_add_u32 s66, s67, 196608\n"
"LD a[12:15],v1,s[40:43],s66 offen offset:3072 sc1\n"
"s_add_u32 s66, s67, 4096\n"
"LD a[16:19],v1,s[40:43],s66 offen offset:3072 sc1\n"
"s_add_u32 s66, s67, 69632\n"
"LD a[20:23],v1,s[40:43],s66 offen offset:3072 sc1\n"
"s_add_u32 s66, s67, 135168\n"
"LD a[24:27],v1,s[40:43],s66 offen offset:3072 sc1\n"
"s_add_u32 s66, s67, 200704\n"
"LD a[28:31],v1,s[40:43],s66 offen offset:3072 sc1\n"
"WT vmcnt(0)\n"
"MF v[56:59],a[144:147],a[0:3],0\n"
"MF v[60:63],a[148:151],a[0:3],0\n"
"MF v[88:91],a[144:147],a[16:19],0\n"
"MF v[92:95],a[148:151],a[16:19],0\n"
"MF v[56:59],a[144:147],a[4:7],v[56:59]\n"
"MF v[60:63],a[148:151],a[4:7],v[60:63]\n"
"MF v[88:91],a[144:147],a[20:23],v[88:91]\n"
"MF v[92:95],a[148:151],a[20:23],v[92:95]\n"
"MF v[56:59],a[144:147],a[8:11],v[56:59]\n"
"MF v[60:63],a[148:151],a[8:11],v[60:63]\n"
"MF v[88:91],a[144:147],a[24:27],v[88:91]\n"
"MF v[92:95],a[148:151],a[24:27],v[92:95]\n"
"MF v[56:59],a[144:147],a[12:15],v[56:59]\n"
"MF v[60:63],a[148:151],a[12:15],v[60:63]\n"
"MF v[88:91],a[144:147],a[28:31],v[88:91]\n"
"MF v[92:95],a[148:151],a[28:31],v[92:95]\n"
"v_cmp_ne_u64 vcc, s[38:39], v[18:19]\n"
"s_nop 1\n"
"s_cmp_eq_u64 vcc, 0\n"
"s_cbranch_scc1 L_pr_58d\n"
"s_nop 1\n"
"v_cmp_u_f32 vcc, v56, v88\n"
"s_nop 1\n"
"s_cmp_eq_u64 vcc, 0\n"
"s_cbranch_scc1 L_pr_58d\n"
"s_sub_u32 s56, s56, 1\n"
"s_cmp_lg_u32 s56, 0\n"
"s_cbranch_scc1 L_pr_58l\n"
"L_pr_58d:\n"
"WT vmcnt(0)\n"
"s_branch L_po_59\n"
"L_psp_61:\n"
"s_mov_b32 s56, 0x40000\n"
"L_psp_61l:\n"
"s_sleep 2\n"
"buffer_load_dwordx2 v[18:19], v20, s[40:43], 0 offen sc1\n"
"WT vmcnt(0)\n"
"v_cmp_ne_u64 vcc, s[38:39], v[18:19]\n"
"s_nop 1\n"
"s_cmp_eq_u64 vcc, 0\n"
"s_cbranch_scc1 L_pok_60\n"
"s_sub_u32 s56, s56, 1\n"
"s_cmp_lg_u32 s56, 0\n"
"s_cbranch_scc1 L_psp_61l\n"
"s_branch L_pok_60\n"
"L_psp_65:\n"
"s_mov_b32 s56, 0x40000\n"
"L_psp_65l:\n"
"s_sleep 2\n"
"buffer_load_dwordx2 v[18:19], v20, s[40:43], 0 offen sc1\n"
"WT vmcnt(0)\n"
"v_cmp_ne_u64 vcc, s[38:39], v[18:19]\n"
"s_nop 1\n"
"s_cmp_eq_u64 vcc, 0\n"
"s_cbranch_scc1 L_pok_64\n"
"s_sub_u32 s56, s56, 1\n"
"s_cmp_lg_u32 s56, 0\n"
"s_cbranch_scc1 L_psp_65l\n"
"s_branch L_pok_64\n"
"L_codeend:\n"
".purgem MF\n"
".purgem TR\n"
".purgem RD\n"
".purgem LD\n"
".purgem ST\n"
".purgem CV\n"
".purgem WT\n"
".purgem EX\n"
".purgem AW\n"
".purgem AR\n"
  :: "s"(__builtin_amdgcn_kernarg_segment_ptr()), "s"(blockIdx.x), "s"(wave), "s"(ldsbase)
  : "v0", "v1", "v2", "v3", "v4", "v5", "v6", "v7", "v8", "v9", "v10", "v11", "v12", "v13", "v14", "v15", "v16", "v17", "v18", "v19", "v20", "v21", "v22", "v23", "v24", "v25", "v26", "v27", "v28", "v29", "v30", "v31", "v32", "v33", "v34", "v35", "v36", "v37", "v38", "v39", "v40", "v41", "v42", "v43", "v44", "v45", "v46", "v47", "v48", "v49", "v50", "v51", "v52", "v53", "v54", "v55", "v56", "v57", "v58", "v59", "v60", "v61", "v62", "v63", "v64", "v65", "v66", "v67", "v68", "v69", "v70", "v71", "v72", "v73", "v74", "v75", "v76", "v77", "v78", "v79", "v80", "v81", "v82", "v83", "v84", "v85", "v86", "v87", "v88", "v89", "v90", "v91", "v92", "v93", "v94", "v95", "v96", "v97", "v98", "v99", "v100", "v101", "v102", "v103", "v104", "v105", "v106", "v107", "v108", "v109", "v110", "v111", "v112", "v113", "v114", "v115", "v116", "v117", "v118", "v119", "v120", "v121", "v122", "v123", "v124", "v125", "v126", "v127", "v128", "v129", "v130", "v131", "v132", "v133", "v134", "v135", "v136", "v137", "v138", "v139", "v140", "v141", "v142", "v143", "v144", "v145", "v146", "v147", "v148", "v149", "v150", "v151", "v152", "v153", "v154", "v155", "v156", "v157", "v158", "v159", "v160", "v161", "v162", "v163", "v164", "v165", "v166", "v167", "v168", "v169", "v170", "v171", "v172", "v173", "v174", "v175", "v176", "v177", "v178", "v179", "v180", "v181", "v182", "v183", "v184", "v185", "v186", "v187", "v188", "v189", "v190", "v191", "v192", "v193", "v194", "v195", "v196", "v197", "v198", "v199", "v200", "v201", "v202", "v203", "v204", "v205", "v206", "v207", "v208", "v209", "v210", "v211", "v212", "v213", "v214", "v215", "v216", "v217", "v218", "v219", "v220", "v221", "v222", "v223", "v224", "v225", "v226", "v227", "v228", "v229", "v230", "v231", "v232", "v233", "v234", "v235", "v236", "v237", "v238", "v239", "v240", "v241", "v242", "v243", "v244", "v245", "v246", "v247", "v248", "v249", "v250", "v251", "v252", "v253", "v254", "v255", "a0", "a1", "a2", "a3", "a4", "a5", "a6", "a7", "a8", "a9", "a10", "a11", "a12", "a13", "a14", "a15", "a16", "a17", "a18", "a19", "a20", "a21", "a22", "a23", "a24", "a25", "a26", "a27", "a28", "a29", "a30", "a31", "a32", "a33", "a34", "a35", "a36", "a37", "a38", "a39", "a40", "a41", "a42", "a43", "a44", "a45", "a46", "a47", "a48", "a49", "a50", "a51", "a52", "a53", "a54", "a55", "a56", "a57", "a58", "a59", "a60", "a61", "a62", "a63", "a64", "a65", "a66", "a67", "a68", "a69", "a70", "a71", "a72", "a73", "a74", "a75", "a76", "a77", "a78", "a79", "a80", "a81", "a82", "a83", "a84", "a85", "a86", "a87", "a88", "a89", "a90", "a91", "a92", "a93", "a94", "a95", "a96", "a97", "a98", "a99", "a100", "a101", "a102", "a103", "a104", "a105", "a106", "a107", "a108", "a109", "a110", "a111", "a112", "a113", "a114", "a115", "a116", "a117", "a118", "a119", "a120", "a121", "a122", "a123", "a124", "a125", "a126", "a127", "a128", "a129", "a130", "a131", "a132", "a133", "a134", "a135", "a136", "a137", "a138", "a139", "a140", "a141", "a142", "a143", "a144", "a145", "a146", "a147", "a148", "a149", "a150", "a151", "a152", "a153", "a154", "a155", "a156", "a157", "a158", "a159", "a160", "a161", "a162", "a163", "a164", "a165", "a166", "a167", "a168", "a169", "a170", "a171", "a172", "a173", "a174", "a175", "a176", "a177", "a178", "a179", "a180", "a181", "a182", "a183", "a184", "a185", "a186", "a187", "a188", "a189", "a190", "a191", "a192", "a193", "a194", "a195", "a196", "a197", "a198", "a199", "a200", "a201", "a202", "a203", "a204", "a205", "a206", "a207", "a208", "a209", "a210", "a211", "a212", "a213", "a214", "a215", "a216", "a217", "a218", "a219", "a220", "a221", "a222", "a223", "a224", "a225", "a226", "a227", "a228", "a229", "a230", "a231", "a232", "a233", "a234", "a235", "a236", "a237", "a238", "a239", "a240", "a241", "a242", "a243", "a244", "a245", "a246", "a247", "a248", "a249", "a250", "a251", "a252", "a253", "a254", "a255", "s8", "s9", "s10", "s11", "s12", "s13", "s14", "s15", "s16", "s17", "s18", "s19", "s20", "s21", "s22", "s23", "s24", "s25", "s26", "s27", "s28", "s29", "s30", "s31", "s33", "s34", "s35", "s36", "s37", "s38", "s39", "s40", "s41", "s42", "s43", "s44", "s45", "s46", "s47", "s48", "s49", "s50", "s51", "s52", "s53", "s54", "s55", "s56", "s57", "s58", "s59", "s60", "s61", "s62", "s63", "s64", "s65", "s66", "s67", "s68", "s69", "s70", "s71", "s72", "s73", "s74", "s75", "s76", "s77", "s78", "s79", "s80", "s81", "s82", "s83", "s84", "s85", "s86", "s87", "s88", "s89", "s90", "s91", "s92", "s93", "s94", "s95", "s96", "s97", "s98", "s99", "vcc", "scc", "memory");
}

extern "C" void kernel_launch(void* const* d_in, const int* in_sizes, int n_in, void* d_out, int out_size,
                              void* d_ws, size_t ws_size, hipStream_t stream) {
  Params prm;
  prm.p[0] = d_in[0]; prm.p[1] = d_in[1]; prm.p[2] = d_in[2]; prm.p[3] = d_in[3]; prm.p[4] = d_in[4]; prm.p[5] = d_in[5];
  prm.p[6] = d_in[7]; prm.p[7] = d_in[8]; prm.p[8] = d_in[9]; prm.p[9] = d_in[10]; prm.p[10] = d_out; prm.p[11] = d_ws;
  fused_kernel<<<256, 256, 0, stream>>>(prm);
}
```
